```python
import torch

def _softmax(x, axis=-1):
    return torch.softmax(x, dim=axis)

def _gelu(x):
    return torch.nn.functional.gelu(x, approximate='tanh')

def _silu(x):
    return torch.nn.functional.silu(x)

def _sigmoid(x):
    return torch.sigmoid(x)

def _layernorm(x, axis=-1, eps=1e-05):
    m = x.mean(dim=axis, keepdim=True)
    v = x.var(dim=axis, keepdim=True, unbiased=False)
    return (x - m) / torch.sqrt(v + eps)

def _scatter_add(tgt, idx, src, axis=0):
    return tgt.clone().index_add_(axis, idx, src)

def _segment_sum(src, seg, n):
    out = torch.zeros((n,) + src.shape[1:], dtype=src.dtype)
    return out.index_add_(0, seg, src)

def _pad(x, pw):
    flat = tuple((v for pair in reversed(pw) for v in pair))
    return torch.nn.functional.pad(x, flat)
kernel_name = 'interleave_layers__memory_aug_recurrent_mem_tr__ssm_conv_h3__prompt'

def setup_inputs():

    _w = lambda a: (lambda t: t.to(torch.bfloat16) if t.is_floating_point() else t)(torch.as_tensor(a))

    def _A_setup():
        B, S, Dm = (1, 2048, 640)
        H, Dh = (32, 128)
        M, seg = (8, 32)
        seg = min(seg, S)
        S = S // seg * seg
        return {'x': _w((lambda *s: torch.randn(*s, dtype=torch.bfloat16) * 0.05)(B, S, Dm)), 'mem0': _w((lambda *s: torch.randn(*s, dtype=torch.bfloat16) * 0.4)(B, M, Dm)), 'Wq': _w((lambda *s: torch.randn(*s, dtype=torch.bfloat16) * 1.6)(Dm, H, Dh) * Dm ** (-0.5)), 'Wk': _w((lambda *s: torch.randn(*s, dtype=torch.bfloat16) * 1.6)(Dm, H, Dh) * Dm ** (-0.5)), 'Wv': _w((lambda *s: torch.randn(*s, dtype=torch.bfloat16) * 0.4)(Dm, H, Dh) * Dm ** (-0.5)), 'Wo': _w((lambda *s: torch.randn(*s, dtype=torch.bfloat16) * 0.4)(H, Dh, Dm) * (H * Dh) ** (-0.5)), 'seg_len': seg}
    _A_in = _A_setup()

    def _B_setup():
        B, S, D = (1, 2048, 640)
        ks = 4
        return {'q': _w((lambda *s: torch.randn(*s, dtype=torch.bfloat16) * 0.05)(B, S, D)), 'k': _w((lambda *s: torch.randn(*s, dtype=torch.bfloat16) * 0.4)(B, S, D)), 'v': _w((lambda *s: torch.randn(*s, dtype=torch.bfloat16) * 0.4)(B, S, D)), 'shift_k': _w((lambda *s: torch.randn(*s, dtype=torch.bfloat16) * 0.4)(D, ks) * 0.5), 'ssm_k': _w((lambda *s: torch.randn(*s, dtype=torch.bfloat16) * 0.8)(D, S) * (1.0 / S ** 0.5)), 'ssm_D': _w((lambda *s: torch.randn(*s, dtype=torch.bfloat16) * 0.4)(D))}
    _B_in = _B_setup()
    return {'A_x': _A_in['x'], 'A_mem0': _A_in['mem0'], 'A_Wq': _A_in['Wq'], 'A_Wk': _A_in['Wk'], 'A_Wv': _A_in['Wv'], 'A_Wo': _A_in['Wo'], 'A_seg_len': _A_in['seg_len'], 'B_q': _B_in['q'], 'B_k': _B_in['k'], 'B_v': _B_in['v'], 'B_shift_k': _B_in['shift_k'], 'B_ssm_k': _B_in['ssm_k'], 'B_ssm_D': _B_in['ssm_D']}

def reference(A_x, A_mem0, A_Wq, A_Wk, A_Wv, A_Wo, A_seg_len, B_q, B_k, B_v, B_shift_k, B_ssm_k, B_ssm_D):

    def _A_ref(x, mem0, Wq, Wk, Wv, Wo, seg_len):
        B, S, Dm = x.shape
        H, Dh = (Wq.shape[1], Wq.shape[2])
        M = mem0.shape[1]
        scale = Dh ** (-0.5)
        n_seg = S // seg_len
        L = M + seg_len
        idx = torch.arange(L)
        mask = (idx[:, None] < M) | (idx[:, None] >= idx[None, :])
        mem = mem0
        outs = []
        for s in range(n_seg):
            xs = x[:, s * seg_len:(s + 1) * seg_len, :].clone()
            h = torch.concatenate([mem, xs], axis=1)
            q = torch.einsum('btd,dhe->bhte', h, Wq)
            k = torch.einsum('btd,dhe->bhte', h, Wk)
            v = torch.einsum('btd,dhe->bhte', h, Wv)
            scores = torch.einsum('bhqe,bhke->bhqk', q, k) * scale
            scores = torch.where(mask[None, None], scores, -1000000000.0)
            p = _softmax(scores, axis=-1)
            o = torch.einsum('bhqk,bhke->bhqe', p, v)
            o = torch.einsum('bhte,hed->btd', o, Wo)
            mem = o[:, :M, :].clone()
            outs.append(o[:, M:, :])
        return torch.concatenate(outs, axis=1)

    def _B_ref(q, k, v, shift_k, ssm_k, ssm_D):
        B, S, D = q.shape
        ks = shift_k.shape[-1]
        k_pad = _pad(k, ((0, 0), (ks - 1, 0), (0, 0)))
        k_shift = torch.zeros_like(k)
        for t in range(ks):
            k_shift = k_shift + k_pad[:, t:t + S, :] * shift_k[:, ks - 1 - t][None, None, :]
        kv = k_shift * v
        L = 2 * S
        kv_pad = _pad(kv, ((0, 0), (0, S), (0, 0)))
        kf_pad = _pad(ssm_k, ((0, 0), (0, S)))
        kv_f = (lambda x, axes=(-2, -1): torch.fft.rfft2(x.float(), dim=axes))(kv_pad[:, None, :, :], axes=(1, 2))
        kf_f = (lambda x, axes=(-2, -1): torch.fft.rfft2(x.float(), dim=axes))(kf_pad[:, None, :], axes=(1, 2))
        kf_f = (lambda x, *d: x.permute(*d))(kf_f, 1, 2, 0)
        y_f = kv_f * kf_f[None, :, :, :]
        y = (lambda x, s, axes=(-2, -1): torch.fft.irfft2(x, s=s, dim=axes).to(torch.bfloat16))(y_f, (1, L), axes=(1, 2))[:, 0, :S, :]
        y = y + kv * ssm_D[None, None, :]
        return q * y
    x = A_x
    x = x + _A_ref(_layernorm(x, axis=-1), A_mem0, A_Wq, A_Wk, A_Wv, A_Wo, A_seg_len)
    x = x + _B_ref(_layernorm(x, axis=-1), B_k, B_v, B_shift_k, B_ssm_k, B_ssm_D)
    x = x + _A_ref(_layernorm(x, axis=-1), A_mem0, A_Wq, A_Wk, A_Wv, A_Wo, A_seg_len)
    x = x + _B_ref(_layernorm(x, axis=-1), B_k, B_v, B_shift_k, B_ssm_k, B_ssm_D)
    return x
```

```cpp
#include <hip/hip_runtime.h>
#include <hip/hip_bf16.h>

typedef unsigned short u16;
typedef unsigned int u32;
typedef u32 v2u __attribute__((ext_vector_type(2)));
typedef u32 v4u __attribute__((ext_vector_type(4)));
typedef u32 v8u __attribute__((ext_vector_type(8)));
typedef float f32x2 __attribute__((ext_vector_type(2)));
typedef float f32x4 __attribute__((ext_vector_type(4)));
typedef float f32x16 __attribute__((ext_vector_type(16)));
typedef __bf16 bf16x2 __attribute__((ext_vector_type(2)));
typedef __bf16 bf16x8 __attribute__((ext_vector_type(8)));
typedef short s16x4 __attribute__((ext_vector_type(4)));
#define LAS __attribute__((address_space(3)))
#define GAS __attribute__((address_space(1)))
typedef LAS unsigned char* lptr;
#define DEV __device__ __forceinline__

constexpr int SEQ = 2048, DM = 640, NH = 32, DH = 128, NSEG = 64, NQ = NH * DH;

constexpr u32 MB = 1u << 20;
constexpr u32 W_ORING = 0;
constexpr u32 W_MRING = MB;
constexpr u32 W_CTRL = 4 * MB;
constexpr u32 ACT = SEQ * DM * 2;
constexpr u32 H_TILE = 128 * 1024;
constexpr u32 W_H2 = 8 * MB;
constexpr u32 W_KQ = 16 * MB;
constexpr u32 W_VO = W_KQ + 64 * MB;
constexpr u32 W_P = W_VO + 64 * MB;
constexpr u32 P_ITEM = 512 * 1024;
constexpr u32 W_H1 = W_P + 44 * MB, W_Y = W_H1 + 8 * MB, W_X2 = W_Y + ACT;
constexpr u32 W_KVT = W_X2 + ACT;
constexpr u32 W_END = W_KVT + 3 * MB;
constexpr u32 NREG = 12;
static_assert((size_t)W_END + 2 * MB <= 256ull * MB && W_H1 >= (NREG - 1) * 16 * MB + 2 * MB && W_H1 <= NREG * 16 * MB, "workspace");
constexpr u32 F_ERR = 0, F_OVF = 2, F_TK = 8, F_POL = 32, F_QKV = 64, F_O = F_QKV + 192, F_P = F_O + 64, F_H2 = F_P + 64, F_LN = F_H2 + 64, F_RG = F_LN + 96, F_KV = F_RG + 32,
              F_CV = F_KV + 64, F_N = F_CV + 192;
DEV u32 foff(u32 idx) { return W_CTRL + (idx >> 6) * 16384u + (idx & 63u) * 128u; }
DEV u32 near_(u32 o, u32 side, u32 pol) { return o + ((side ^ (pol >> (o >> 24)) ^ (o >> 21)) & 1u) * 8192u; }
DEV u32 p_item(int s) { return W_P + (u32)s * P_ITEM; }

constexpr u32 OOB = 0x80000000u;
constexpr u32 PSENT = 0x7F817F81u;
constexpr int SPIN_CAP = 40000;
constexpr int LDS_BYTES = 160 * 1024;
#define ST_HEAD 0
#define ST_WO 0

#define EV(k) do {} while (0)
#define LST(idx, v) do {} while (0)
#define LS2(cond, idx) do {} while (0)
struct Params {
    const u16 *x, *mem0, *Wq, *Wk, *Wv, *Wo, *bk, *bv, *shift, *ssmk, *ssmD;
    u16* out;
    unsigned char* ws;
};

typedef __amdgpu_buffer_rsrc_t rsrc_t;
DEV rsrc_t mk_rsrc(const void* p, u32 bytes) { return __builtin_amdgcn_make_buffer_rsrc((void*)p, 0, bytes, 0x00020000); }
DEV v4u ld16s(rsrc_t r, u32 off, u32 so) { return __builtin_amdgcn_raw_buffer_load_b128(r, off, so, 16); }
DEV v2u ld8s(rsrc_t r, u32 off, u32 so) { return __builtin_amdgcn_raw_buffer_load_b64(r, off, so, 16); }
DEV v4u ldin16(rsrc_t r, u32 off, u32 so) { return __builtin_amdgcn_raw_buffer_load_b128(r, off, so, 0); }
DEV v2u ldin8(rsrc_t r, u32 off, u32 so) { return __builtin_amdgcn_raw_buffer_load_b64(r, off, so, 0); }
DEV u32 ld4(rsrc_t r, u32 off) { return __builtin_amdgcn_raw_buffer_load_b32(r, off, 0, 16); }
DEV void st16(rsrc_t r, u32 off, v4u v) { __builtin_amdgcn_raw_buffer_store_b128(v, r, off, 0, 16); }
DEV void st8s(rsrc_t r, u32 off, u32 so, v2u v) { __builtin_amdgcn_raw_buffer_store_b64(v, r, off, so, 16); }
DEV void st4s(rsrc_t r, u32 off, u32 so, u32 v) { __builtin_amdgcn_raw_buffer_store_b32(v, r, off, so, 16); }
#define DRAIN() asm volatile("s_waitcnt vmcnt(0)" ::: "memory")
#define VMW(n) asm volatile("s_waitcnt vmcnt(%0)" :: "i"(n) : "memory")
#define BAR() asm volatile("s_waitcnt lgkmcnt(0)\n\ts_barrier" ::: "memory")
#define SCHED() __builtin_amdgcn_sched_barrier(0)
#define CFENCE() asm volatile("" ::: "memory")
#define NOPS(n) asm volatile("s_nop %0" :: "i"(n))
#define PIN(x) asm volatile("" : "+v"(x))
#define PINS(x) asm volatile("" : "+s"(x))

template <int V> struct IC { static constexpr int value = V; };
template <int I, int N, class F> DEV void static_for(F&& f) { if constexpr (I < N) { f(IC<I>{}); static_for<I + 1, N>(f); } }
#define SFOR(var, n) static_for<0, n>([&](auto ic_) __attribute__((always_inline)) { constexpr int var = decltype(ic_)::value;
#define SEND });

DEV u32 pk(float a, float b) { bf16x2 v = {(__bf16)a, (__bf16)b}; return __builtin_bit_cast(u32, v); }
DEV float lo_f(u32 u) { return __builtin_bit_cast(float, u << 16); }
DEV float hi_f(u32 u) { return __builtin_bit_cast(float, u & 0xffff0000u); }
DEV f32x2 up2(u32 u) { return (f32x2){lo_f(u), hi_f(u)}; }
DEV u32 pk2(f32x2 v) { return pk(v.x, v.y); }
DEV float rbf(float a) { return (float)(__bf16)a; }
DEV v4u lds_r16(lptr p) { return *(LAS v4u*)p; }
DEV void lds_w16(lptr p, v4u v) { *(LAS v4u*)p = v; }
DEV v2u lds_tr(lptr p) { return __builtin_bit_cast(v2u, __builtin_amdgcn_ds_read_tr16_b64_v4i16((LAS s16x4*)p)); }
DEV v4u tr2(lptr p0, lptr p1) { const v2u a = lds_tr(p0), b = lds_tr(p1); return (v4u){a.x, a.y, b.x, b.y}; }
DEV v8u cat8(v4u a, v4u b) { return __builtin_shufflevector(a, b, 0, 1, 2, 3, 4, 5, 6, 7); }
DEV f32x4 mfma16(v4u a, v4u b, f32x4 c) { return __builtin_amdgcn_mfma_f32_16x16x32_bf16(__builtin_bit_cast(bf16x8, a), __builtin_bit_cast(bf16x8, b), c, 0, 0, 0); }
DEV f32x16 mfma32(v4u a, v4u b, f32x16 c) { return __builtin_amdgcn_mfma_f32_32x32x16_bf16(__builtin_bit_cast(bf16x8, a), __builtin_bit_cast(bf16x8, b), c, 0, 0, 0); }

DEV float f_(u32 u) { return __builtin_bit_cast(float, u); }
DEV u32 u_(float f) { return __builtin_bit_cast(u32, f); }
template <int CTRL> DEV float dpp(float v) { return f_((u32)__builtin_amdgcn_update_dpp(0, (int)u_(v), CTRL, 0xf, 0xf, false)); }
DEV float max16(float v) { auto r = __builtin_amdgcn_permlane16_swap(u_(v), u_(v), false, false); return fmaxf(f_(r[0]), f_(r[1])); }
DEV float max32(float v) { auto r = __builtin_amdgcn_permlane32_swap(u_(v), u_(v), false, false); return fmaxf(f_(r[0]), f_(r[1])); }
DEV float add16(float v) { auto r = __builtin_amdgcn_permlane16_swap(u_(v), u_(v), false, false); return f_(r[0]) + f_(r[1]); }
DEV float add32(float v) { auto r = __builtin_amdgcn_permlane32_swap(u_(v), u_(v), false, false); return f_(r[0]) + f_(r[1]); }
DEV float wave_sum(float v) {
    v += dpp<0xB1>(v);
    v += dpp<0x4E>(v);
    v += dpp<0x124>(v);
    v += dpp<0x128>(v);
    return add32(add16(v));
}
DEV bool all_(bool c) { return __builtin_amdgcn_ballot_w64(!c) == 0ull; }
DEV int uni(int v) { return __builtin_amdgcn_readfirstlane(v); }
DEV u32 uu(u32 v) { return (u32)__builtin_amdgcn_readfirstlane((int)v); }
DEV u32 rdl(u32 v, int l) { return (u32)__builtin_amdgcn_readlane((int)v, l); }
DEV u32 rt32() { return (u32)__builtin_amdgcn_s_memrealtime(); }

DEV void wait_flags(rsrc_t rs, u32 off, u32 want, bool& dead) {
    if (dead) return;
#pragma unroll 1
    for (int n = 0;; ++n) {
        CFENCE();
        const u32 v = ld4(rs, off);
        if (all_(off >= OOB || v >= want)) break;
        __builtin_amdgcn_s_sleep(1);
        if ((n & 63) == 63) { if (uni((int)ld4(rs, foff(F_ERR))) != 0) { dead = true; break; } }
        if (n > SPIN_CAP) { st4s(rs, foff(F_ERR), 0, 1u); dead = true; break; }
    }
    CFENCE();
}

DEV u32 pol_off() { const int lane = threadIdx.x & 63; return lane < 32 ? foff(F_POL + lane) : OOB; }
DEV u32 pol_of(u32 raw) {
    const int lane = threadIdx.x & 63;
    const float v = add16(__builtin_amdgcn_fmed3f((float)((int)raw - (1 << 20)), -250.f, 250.f));
    const float lo = dpp<0x121>(v), hi = dpp<0x12F>(v), ex = dpp<0x122>(v) + dpp<0x123>(v) + dpp<0x124>(v);
    const float sum = v + lo + ((lane & 15) == 11 ? 0.f : hi) + ((lane & 15) == 0 ? ex : 0.f);
    return (u32)__builtin_amdgcn_ballot_w64(sum >= 0.f) & 0xFFFFu;
}
DEV u32 get_pol(rsrc_t rs) { return pol_of(ld4(rs, pol_off())); }
DEV u32 hblk(u32 base, int ls, int h, u32 side, u32 pol) { return near_(base + (u32)(ls * NH + h) * 16384u, side, pol); }
DEV f32x2 ln_stats(const f32x2 (&x)[5]) {
    const float c = f_(uu(u_(x[0].x)));
    f32x2 d[5];
#pragma unroll
    for (int i = 0; i < 5; ++i) d[i] = x[i] - c;
    const f32x2 s2 = (d[0] + d[1]) + (d[2] + d[3]) + d[4], q = (d[0] * d[0] + d[1] * d[1]) + (d[2] * d[2] + (d[3] * d[3] + d[4] * d[4]));
    const float dm = wave_sum(s2.x + s2.y) * (1.0f / DM), mean = c + dm;
    const float var = fmaxf(wave_sum(q.x + q.y) * (1.0f / DM) - dm * dm, 0.f);
    const float sd = rbf(__builtin_amdgcn_sqrtf(rbf(rbf(var) + 1e-5f)));
    return (f32x2){rbf(mean), __builtin_amdgcn_rcpf(sd)};
}
DEV void store_h(rsrc_t rs, u32 tile, int row, const u32 (&hp)[5]) {
    const u32 lane = threadIdx.x & 63;
    const u32 sw = (u32)(row & 15) * 16u, o8 = (lane >> 5) * 16384u + (((lane & 31u) * 8u) ^ sw), hb = tile + (u32)(row & 31) * 256u;
    st8s(rs, o8, hb, (v2u){hp[0], hp[1]});
    st8s(rs, o8, hb + 32768, (v2u){hp[2], hp[3]});
    st4s(rs, (lane * 4) ^ sw, hb + 65536, hp[4]);
}

#define DMA16(ldsb, vo, so) asm volatile("s_mov_b32 m0, %0\n\ts_nop 0\n\tbuffer_load_dwordx4 %1, %2, %3 offen sc1 lds" :: "s"(ldsb), "v"(vo), "s"(srd), "s"(so) : "memory")
#define DMA16I(ldsb, lo, vo, so, go) do { u32 so_; asm volatile("s_add_u32 m0, %1, %5\n\ts_add_u32 %0, %4, %6\n\tbuffer_load_dwordx4 %2, %3, %0 offen sc1 lds" \
                         : "=&s"(so_) : "s"(ldsb), "v"(vo), "s"(srd), "s"(so), "i"(lo), "i"(go) : "memory", "scc"); } while (0)
#define DMA32(ldsb, vo, so) asm volatile("s_mov_b32 m0, %0\n\ts_nop 0\n\tbuffer_load_dwordx4 %1, %2, %3 offen sc1 lds\n\tbuffer_load_dwordx4 %1, %2, %3 offen offset:1024 sc1 lds" \
                         :: "s"(ldsb), "v"(vo), "s"(srd), "s"(so) : "memory")
#define INPOL "nt"
#define INAUX 2
#define DMANT(sr, ldsb, vo, so) asm volatile("s_mov_b32 m0, %0\n\ts_nop 0\n\tbuffer_load_dwordx4 %1, %2, %3 offen " INPOL " lds" :: "s"(ldsb), "v"(vo), "s"(sr), "s"(so) : "memory")
#define DMAIN(sr, ldsb, vo, so) asm volatile("s_mov_b32 m0, %0\n\ts_nop 0\n\tbuffer_load_dwordx4 %1, %2, %3 offen lds" :: "s"(ldsb), "v"(vo), "s"(sr), "s"(so) : "memory")
DEV v4u mk_srd(const void* q, u32 bytes) { return (v4u){uu((u32)(size_t)q), uu((u32)((size_t)q >> 32) & 0xffffu), bytes, 0x00020000u}; }
#define MK_SRD() const v4u srd = mk_srd(p.ws, W_END)

constexpr int CV_PAD = 40;
constexpr int CV_PL = 4416;
constexpr int CV_F = 8 * CV_PL;
constexpr int CV_KB = 80;
constexpr int CV_KV = 65 * CV_KB;
constexpr int CV_CH = CV_F + CV_KV;
static_assert(CV_CH % 16 == 0 && 4 * CV_CH <= LDS_BYTES, "conv lds");

struct Filt { v4u a[5], b[5]; };
DEV int chan(int grp, int w) { return 16 * (grp >> 2) + (grp & 3) + 4 * w; }
template <int N = 5> DEV void filt_load(const Params& p, int grp, Filt& f) {
    const int tid = threadIdx.x, lane = tid & 63, d = chan(grp, tid >> 6);
    const rsrc_t rf = mk_rsrc(p.ssmk, ACT);
#pragma unroll
    for (int i = 0; i < N; ++i) {
        const u32 s0 = (u32)(8 * (lane + 64 * i) - CV_PAD);
        f.a[i] = __builtin_amdgcn_raw_buffer_load_b128(rf, s0 < SEQ ? (u32)(d * SEQ + s0) * 2u : OOB, 0, INAUX);
        f.b[i] = __builtin_amdgcn_raw_buffer_load_b128(rf, s0 + 8 < SEQ ? (u32)(d * SEQ + s0 + 8) * 2u : OOB, 0, INAUX);
    }
}
constexpr int KT_BYTES = 18 * 1024, KT_X = 64 * 144, NKV = 44, KT_DUMP = 8 * KT_BYTES + KT_X;
DEV void kv_fetch(const Params& p, int tile, u32 dst) {
    const int lane = threadIdx.x & 63, w = uni(threadIdx.x >> 6);
    const v4u sk = mk_srd(p.bk, ACT), sv = mk_srd(p.bv, ACT), ss = mk_srd(w == 0 ? p.bk : p.shift, w == 0 ? ACT : DM * 8);
    const u32 vo = tile < 320 ? (u32)((lane >> 3) * DM + (tile % 10) * 64 + (lane & 7) * 8) * 2u : OOB, r0 = uu((u32)((tile / 10) * 64 + 8 * w) * (DM * 2)), d = uu(dst + w * 1024);
    DMANT(sk, d + 1024, vo, r0); DMANT(sk, d + 5120, vo, r0 + 32 * DM * 2); DMANT(sv, d + 9216, vo, r0); DMANT(sv, d + 13312, vo, r0 + 32 * DM * 2);
    DMAIN(ss, uu(w == 0 ? dst : w == 1 ? dst + 17408 : KT_DUMP), w == 0 ? (tile >= 10 ? vo : OOB) : w == 1 && lane < 32 ? (u32)lane * 16u : OOB,
          uu(w == 0 ? (tile >= 10 ? r0 - 8 * DM * 2 : 0u) : (u32)(tile % 10) * 512u));
}
DEV void kv_tile(const Params& p, rsrc_t rs, lptr src, lptr xt, int tile) {
    const int tid = threadIdx.x;
    const v4u sv = lds_r16(src + 17408 + (tid & 31) * 16);
    lptr kp = src + ((tid >> 5) * 8 + 5) * 128 + (tid & 31) * 4;
    f32x2 kf[11];
#pragma unroll
    for (int i = 0; i < 11; ++i) kf[i] = up2(*(LAS u32*)(kp + i * 128));
    const f32x2 sh[4] = {{lo_f(sv.x), lo_f(sv.z)}, {hi_f(sv.x), hi_f(sv.z)}, {lo_f(sv.y), lo_f(sv.w)}, {hi_f(sv.y), hi_f(sv.w)}};
    u32 kv[8];
#pragma unroll
    for (int i = 0; i < 8; ++i) {
        const f32x2 acc = (kf[i] * sh[3] + kf[i + 1] * sh[2]) + (kf[i + 2] * sh[1] + kf[i + 3] * sh[0]);
        kv[i] = pk2(acc * up2(*(LAS u32*)(kp + 9216 + (i - 5) * 128)));
    }
    lds_w16(xt + (2 * (tid & 31)) * 144 + (tid >> 5) * 16, (v4u){__builtin_amdgcn_perm(kv[6], kv[7], 0x05040100u), __builtin_amdgcn_perm(kv[4], kv[5], 0x05040100u),
                                                             __builtin_amdgcn_perm(kv[2], kv[3], 0x05040100u), __builtin_amdgcn_perm(kv[0], kv[1], 0x05040100u)});
    lds_w16(xt + (2 * (tid & 31) + 1) * 144 + (tid >> 5) * 16, (v4u){__builtin_amdgcn_perm(kv[6], kv[7], 0x07060302u), __builtin_amdgcn_perm(kv[4], kv[5], 0x07060302u),
                                                                 __builtin_amdgcn_perm(kv[2], kv[3], 0x07060302u), __builtin_amdgcn_perm(kv[0], kv[1], 0x07060302u)});
    BAR();
#pragma unroll
    for (int i = 0; i < 2; ++i) {
        const int ch = i * 32 + (tid >> 3);
        st16(rs, W_KVT + (u32)(((tile % 10) * 64 + ch) * SEQ + (tile / 10) * 64 + (tid & 7) * 8) * 2u, lds_r16(xt + ch * 144 + (tid & 7) * 16));
    }
}
template <int MODE> DEV void conv_job(const Params& p, rsrc_t rs, lptr lds, int grp, Filt& f, int next) {
    const int tid = threadIdx.x, lane = tid & 63, w = uni(tid >> 6), bq = lane & 31, hh = lane >> 5;
    lptr F = lds + w * CV_CH;
    lptr fa = F + (bq & 7) * CV_PL + ((bq >> 3) - hh + 4) * 16;
    lptr kb = F + CV_F + CV_KB + hh * 16, kh = kb + (bq + 32) * CV_KB;
    bool dead = false;
    v4u kvr[4];
    const u32 D = p.ssmD[chan(grp, w)];
    auto kv_load = [&]() {
        if (MODE != 2) wait_flags(rs, lane < NKV ? foff(F_KV + lane) : OOB, 1u, dead);
#pragma unroll
        for (int i = 0; i < (MODE == 1 ? 2 : 4); ++i) kvr[i] = ld16s(rs, (u32)lane * 16u, W_KVT + (u32)(chan(grp, w) * SEQ + i * 512) * 2u);
    };
    auto windows = [&](int i) {
        if (i < 4 || lane < 16) {
            const u32 e[8] = {f.a[i].x, f.a[i].y, f.a[i].z, f.a[i].w, f.b[i].x, f.b[i].y, f.b[i].z, f.b[i].w};
            u32 o[7];
#pragma unroll
            for (int q = 0; q < 7; ++q) o[q] = __builtin_amdgcn_alignbit(e[q + 1], e[q], 16);
#pragma unroll
            for (int q = 0; q < 4; ++q) {
                lds_w16(F + (2 * q) * CV_PL + (64 * i + lane) * 16, (v4u){o[q], o[q + 1], o[q + 2], o[q + 3]});
                lds_w16(F + (2 * q + 1) * CV_PL + (64 * i + lane) * 16, (v4u){e[q + 1], e[q + 2], e[q + 3], e[q + 4]});
            }
        }
    };
    lptr yw = lds + (lane >> 4) * CV_CH + 3072 + (lane & 15) * 64 + w * 16, yr = F + 3072 + lane * 16;
    const u32 yo = W_Y + (u32)((((w & 1) * 16 + (lane >> 2)) * 8 + (w >> 1)) * DM + (grp & 3) * 160 + 4 * (grp >> 2) + (lane & 3)) * 16u;
    auto put = [&](const f32x16& ac, int q) {
        const int r = 4 * (q & 3);
        const auto x = __builtin_amdgcn_permlane32_swap(pk(ac[r], ac[r + 1]), pk(ac[r + 4], ac[r + 5]), false, false);
        const auto y = __builtin_amdgcn_permlane32_swap(pk(ac[r + 2], ac[r + 3]), pk(ac[r + 6], ac[r + 7]), false, false);
        lds_w16(yw + (q >> 1) * CV_PL, (v4u){x[0], y[0], x[1], y[1]});
    };
    if (MODE != 2) EV(8);
    if (MODE != 0) kv_load();
    if (lane < CV_KB / 16) lds_w16(F + CV_F + lane * 16, (v4u){0u, 0u, 0u, 0u});
    windows(0);
    if (MODE != 1) { windows(1); windows(2); windows(4); }
    if (MODE == 0) kv_load();
    if (MODE == 2) windows(3);
    if (MODE != 2) EV(9);
#pragma unroll
    for (int i = 0; i < (MODE == 1 ? 2 : 4); ++i) lds_w16(F + CV_F + (16 * i + (lane >> 2) + 1) * CV_KB + (lane & 3) * 16, kvr[i]);
    v4u dg[2];
#pragma unroll
    for (int ks = 0; ks < 2; ++ks)
#pragma unroll
        for (int q = 0; q < 4; ++q) { const int j = 16 * ks + 8 * hh + 7 - bq; dg[ks][q] = j == 2 * q ? D : j == 2 * q + 1 ? D << 16 : 0u; }
    f32x16 acc0 = {}, acc1 = {};
    auto first = [&]() { asm volatile("s_waitcnt vmcnt(0)\n\ts_barrier" ::: "memory"); st4s(rs, tid == 0 ? foff(F_CV + grp) : OOB, 0, 1u); EV(12); };
    if (MODE != 2) {
        EV(10);
        acc0 = mfma32(lds_r16(kb + bq * CV_KB), dg[0], acc0); acc0 = mfma32(lds_r16(kb + bq * CV_KB + 32), dg[1], acc0);
#pragma unroll
        for (int m = 0; m < 8; ++m) {
            lptr kl = kb + max(bq - m, -1) * CV_KB;
            acc0 = mfma32(lds_r16(kl), lds_r16(fa + m * 64), acc0); acc0 = mfma32(lds_r16(kl + 32), lds_r16(fa + m * 64 - 32), acc0);
        }
        put(acc0, 0);
        BAR();
        st16(rs, w < 2 ? yo : OOB, lds_r16(yr));
        EV(11);
    }
    if (MODE == 1) {
        windows(1); windows(2);
        first();
        v4u a[2][2], l[2][2];
#pragma unroll
        for (int u = 0; u < 2; ++u) { lptr kl = kb + max(bq - 8 - u, -1) * CV_KB; a[u][0] = lds_r16(fa + (8 + u) * 64); l[u][0] = lds_r16(kl); a[u][1] = lds_r16(fa + (8 + u) * 64 - 32); l[u][1] = lds_r16(kl + 32); }
#pragma unroll 1
        for (int m = 8; m < 32; m += 2) {
#pragma unroll
            for (int u = 0; u < 2; ++u) {
                lptr kl = kb + max(bq - m - u - 2, -1) * CV_KB;
                acc0 = mfma32(l[u][0], a[u][0], acc0); a[u][0] = lds_r16(fa + (m + u + 2) * 64); l[u][0] = lds_r16(kl); SCHED();
                acc0 = mfma32(l[u][1], a[u][1], acc0); a[u][1] = lds_r16(fa + (m + u + 2) * 64 - 32); l[u][1] = lds_r16(kl + 32); SCHED();
            }
        }
    } else {
        if (MODE == 0) windows(3); else filt_load(p, next, f);
        v4u a0 = lds_r16(fa), a1 = lds_r16(fa - 32), c0 = lds_r16(fa + 2048), c1 = lds_r16(fa + 2048 - 32);
        v4u h0 = lds_r16(kh), h1 = lds_r16(kh + 32), l0 = lds_r16(kb + bq * CV_KB), l1 = lds_r16(kb + bq * CV_KB + 32);
        acc1 = mfma32(h0, dg[0], acc1); acc1 = mfma32(h1, dg[1], acc1);
#pragma unroll 2
        for (int m = 0; m < (MODE == 0 ? 8 : 32); ++m) {
            lptr kl = kb + max(bq - m - 1, -1) * CV_KB;
            if (MODE == 0 && m == 4) first();
            acc1 = mfma32(h0, a0, acc1); h0 = lds_r16(kh - (m + 1) * CV_KB); a0 = lds_r16(fa + (m + 1) * 64); SCHED();
            acc1 = mfma32(l0, c0, acc1); c0 = lds_r16(fa + (m + 1) * 64 + 2048); l0 = lds_r16(kl); SCHED();
            acc1 = mfma32(h1, a1, acc1); h1 = lds_r16(kh - (m + 1) * CV_KB + 32); a1 = lds_r16(fa + (m + 1) * 64 - 32); SCHED();
            acc1 = mfma32(l1, c1, acc1); c1 = lds_r16(fa + (m + 1) * 64 + 2048 - 32); l1 = lds_r16(kl + 32); SCHED();
        }
        if (MODE == 0) {
#pragma unroll 2
            for (int m = 8; m < 32; ++m) {
                lptr kl = kb + max(bq - m - 1, -1) * CV_KB;
                acc1 = mfma32(h0, a0, acc1); h0 = lds_r16(kh - (m + 1) * CV_KB); SCHED();
                acc0 = mfma32(l0, a0, acc0); a0 = lds_r16(fa + (m + 1) * 64); SCHED();
                acc1 = mfma32(l0, c0, acc1); c0 = lds_r16(fa + (m + 1) * 64 + 2048); l0 = lds_r16(kl); SCHED();
                acc1 = mfma32(h1, a1, acc1); h1 = lds_r16(kh - (m + 1) * CV_KB + 32); SCHED();
                acc0 = mfma32(l1, a1, acc0); a1 = lds_r16(fa + (m + 1) * 64 - 32); SCHED();
                acc1 = mfma32(l1, c1, acc1); c1 = lds_r16(fa + (m + 1) * 64 + 2048 - 32); l1 = lds_r16(kl + 32); SCHED();
            }
        }
        BAR();
    }
    if (MODE != 2) { EV(13); put(acc0, 0); put(acc0, 2); }
    if (MODE != 1) { put(acc1, 4); put(acc1, 6); }
    BAR();
#pragma unroll
    for (int q = (MODE == 2 ? 2 : 0); q < (MODE == 1 ? 2 : 4); ++q) st16(rs, q == 0 && w < 2 ? OOB : yo + (u32)q * (2 * DM * 16), lds_r16(yr + q * CV_PL));
    BAR();
    if (MODE != 2) EV(14);
}

DEV void pre_rings(const Params& p, rsrc_t rs, int j) {
    const u32 gt = (u32)(j * 256 + threadIdx.x) * 16u, n = ((gt >> 14) << 9) + ((gt & 8191u) >> 4);
    const v4u ps = {PSENT, PSENT, PSENT, PSENT};
    v4u m = ps;
    if (n < 640u) m = *(const v4u*)(p.mem0 + (2 * ((n >> 2) & 3) + (n & 1)) * DM + (n >> 4) * 16 + (n & 2) * 4);
#pragma unroll
    for (u32 i = 0; i < 8; ++i) st16(rs, W_ORING + gt + i * 131072u, ps);
#pragma unroll
    for (u32 i = 0; i < 2; ++i) st16(rs, gt < 16384u ? W_MRING + i * (MB / 4) + 131072u + gt : OOB, ps);
    if (n < 640u) {
#pragma unroll
        for (int i = 0; i < 4; ++i) m[i] = m[i] == PSENT ? 0x7FC17FC1u : m[i];
    }
    st16(rs, W_MRING + gt, m);
    st16(rs, W_MRING + MB / 4 + gt, m);
}

#define M32A(first, d, a, b) do { if (first) asm volatile("v_mfma_f32_32x32x16_bf16 %0, %1, %2, 0" : "=&v"(d) : "a"(a), "v"(b)); \
                                  else asm volatile("v_mfma_f32_32x32x16_bf16 %0, %1, %2, %0" : "+v"(d) : "a"(a), "v"(b)); } while (0)
#define M32B(first, d, a, b) do { if (first) asm volatile("v_mfma_f32_32x32x16_bf16 %0, %1, %2, 0" : "=&v"(d) : "v"(a), "a"(b)); \
                                  else asm volatile("v_mfma_f32_32x32x16_bf16 %0, %1, %2, %0" : "+v"(d) : "v"(a), "a"(b)); } while (0)
#define VCVT(d, a, b) asm volatile("v_cvt_pk_bf16_f32 %0, %1, %2" : "=v"(d) : "v"(a), "v"(b))
#define LD4A(d, vo, so) asm volatile("buffer_load_dword %0, %1, %2, %3 offen sc1" : "=v"(d) : "v"(vo), "s"(srd), "s"(so) : "memory")
DEV u32 wst_src(int lane, int ld) { return (u32)((lane >> 4) * ld + 8 * ((lane & 15) ^ ((lane >> 4) << 2))) * 2u; }
DEV u32 wst_rd(int lane, int w) { const int q4 = (lane >> 2) & 3; return (u32)(q4 * 256 + ((w ^ q4) * 64) + ((lane >> 4) & 1) * 32 + (lane & 3) * 8); }

constexpr int QK_A = 65536;
#define QLEAD 8
#define QFIRST 4
#define QW0 10
#define QW1 3
#define QSAMP 23
#define QPUB 26
DEV void role_qkv(const Params& p, lptr lds, int c) {
    const int grp = 64 + c;
    const int tid = threadIdx.x, lane = tid & 63, w = uni(tid >> 6), hh = lane >> 5, r32 = lane & 31;
    const int m = c >> 5, h = c & 31;
    const rsrc_t rs = mk_rsrc(p.ws, W_END);
    v4u wt[40];
    Filt flt;
    bool dead = false;
    u32 pol;
    const u32 t16 = (u32)tid * 16u, lnf = w < 2 ? foff(F_LN + tid) : OOB;
    MK_SRD();
#define QDMA(i, dst, vo, so) DMA16I(dst, (i) * 4096, vo, so, ((i) >> 1) * 16384 + ((i) & 1) * 4096)
    {
        const v4u sw = mk_srd(m == 0 ? p.Wq : m == 1 ? p.Wk : p.Wv, DM * NQ * 2), sx = mk_srd(p.x, ACT);
        const u32 vo = wst_src(lane, NQ) + (u32)h * (DH * 2), x8 = (u32)lane * 8u, x4 = (u32)lane * 4u;
        v2u xa[6], xb[6]; u32 xc[6];
#pragma unroll
        for (int i = 0; i < 6; ++i)
            asm volatile("buffer_load_dwordx2 %0, %3, %5, %6 offen " INPOL "\n\tbuffer_load_dwordx2 %1, %3, %5, %6 offen offset:512 " INPOL "\n\tbuffer_load_dword %2, %4, %5, %6 offen offset:1024 " INPOL
                         : "=&v"(xa[i]), "=&v"(xb[i]), "=&v"(xc[i]) : "v"(x8), "v"(x4), "s"(sx), "s"(uu((4 * c + w + 384 * i) * (DM * 2))) : "memory");
        EV(1);
        auto wfill = [&](int i0, int n) {
#pragma unroll 1
            for (int i = i0; i < i0 + n; ++i) DMAIN(sw, uu((4 * i + w) * 1024), vo, uu((16 * i + 4 * w) * (NQ * 2)));
        };
        wfill(0, QW0);
        asm volatile("s_waitcnt vmcnt(%0) ; HZCLR" :: "i"(QW0) : "memory");
        EV(2);
#pragma unroll
        for (int i = 0; i < 6; ++i) {
            wfill(QW0 + QW1 * i, QW1);
            if (i == 2) { VMW(2 * QW1 + 3); BAR(); st4s(rs, tid == 0 ? foff(F_LN + c) : OOB, 0, 1u); }
            asm volatile("" : "+v"(xa[i]), "+v"(xb[i]), "+v"(xc[i]));
            const int row = 4 * c + w + 384 * i;
            if (i < 5 || row < SEQ) {
                const f32x2 x[5] = {up2(xa[i].x), up2(xa[i].y), up2(xb[i].x), up2(xb[i].y), up2(xc[i])};
                const f32x2 st = ln_stats(x);
                u32 hp[5];
#pragma unroll
                for (int q = 0; q < 5; ++q) hp[q] = pk2(up2(pk2(x[q] - st.x)) * st.y);
                store_h(rs, W_H1 + (u32)(row >> 5) * H_TILE, row, hp);
            }
        }
        EV(3);
        constexpr int K0 = QW0 + 6 * QW1;
        u32 fv, praw;
        { const u32 po = pol_off(); LD4A(praw, po, 0); LD4A(fv, lnf, 0); }
        wfill(K0, 40 - K0);
        asm volatile("s_waitcnt vmcnt(%2) ; HZCLR\n\ts_barrier" : "+v"(praw), "+v"(fv) : "i"(40 - K0) : "memory");
        st4s(rs, tid == 0 ? foff(F_LN + c) : OOB, 0, 2u);
        EV(4);
        lptr tb = lds + hh * 2048 + wst_rd(lane, w);
        SFOR(k, 40)
            if constexpr (k == K0) {
                if (w < 2 && !all_(fv != 0u)) wait_flags(rs, lnf, 1u, dead);
                BAR();
                EV(5);
                const u32 dst = uu(w * 1024), so = W_H1;
                SFOR(i, 10) QDMA(i, dst, t16, so); SEND
                VMW(10); BAR();
            }
            lptr q = tb + (k >> 3) * 32768 + (k & 7) * 4096; wt[k] = tr2(q, q + 1024); asm volatile("" : "+a"(wt[k]));
        SEND
        pol = pol_of(praw);
        VMW(6);
    }
    EV(6);
    u32 ar[8];
#pragma unroll
    for (int j = 0; j < 8; ++j) ar[j] = (u32)(r32 * 256 + ((2 * j + hh) ^ (r32 & 15)) * 16);
#define XRD(ks) lds_r16(lds + ar[(ks) & 7] + ((ks) >> 3) * 8192)
    const u32 h2flag = tid < 8 ? near_(foff(F_H2 + lane), 1u, pol) : OOB;
    const u32 ol = (m == 2 ? (u32)(2 * w + (r32 >> 4)) * 1024u : (u32)((r32 >> 4) * 4 + w) * 1024u) + (u32)(hh * 16 + (r32 & 15)) * 16u;
    const u32 fo = tid == 192 ? near_(foff(F_QKV + m * 64 + h), 0u, pol) : OOB;
    v4u x[8];
    u32 fill = uu(QK_A + w * 1024);
    int n0 = 0, n1 = -3, lim0 = QFIRST;
    auto run = [&](auto isv) __attribute__((always_inline)) {
      constexpr bool V = decltype(isv)::value;
      f32x16 a0, a1 = {};
      u32 oa = OOB, pfo = n1 == -1 && tid == 192 ? foff(F_CV + grp) : OOB, pfv = 2, qfo = OOB, qfv = 0;
      v4u pp, pq;
      int cur;
      u32 fv = 0;
#define QS(k) do {} while (0)
#define QCV(d, i, j) VCVT(d, a1[i], a1[j])
#define QPK1(d, a_, q_) do { if constexpr (V) QCV(d[q_], 4 * (a_) + 8 * ((q_) >> 1) + 2 * ((q_) & 1), 4 * (a_) + 8 * ((q_) >> 1) + 2 * ((q_) & 1) + 1); else QCV(d[q_], 4 * (a_) + (q_), 4 * (a_) + (q_) + 8); } while (0)
#define QPK(d, a_) do { QPK1(d, a_, 0); QPK1(d, a_, 1); QPK1(d, a_, 2); QPK1(d, a_, 3); } while (0)
      auto stream = [&](auto kind, int n1) __attribute__((always_inline)) {
            constexpr bool P = decltype(kind)::value;
            u32 nsrc, nvo, hb;
#define QT(k) QS((k) + (P ? 3 : 9))
            QT(0);
            SCHED();
            SFOR(ks, 40)
                if constexpr (ks < 20) { if constexpr (V) M32B(ks == 0, a0, x[ks & 7], wt[ks]); else M32A(ks == 0, a0, wt[ks], x[ks & 7]); }
                else if constexpr (ks == 20) {
                    if constexpr (V) asm volatile("v_mfma_f32_32x32x16_bf16 %0, %1, %2, %3" : "+v"(a1) : "v"(x[ks & 7]), "a"(wt[ks]), "v"(a0));
                    else asm volatile("v_mfma_f32_32x32x16_bf16 %0, %1, %2, %3" : "+v"(a1) : "a"(wt[ks]), "v"(x[ks & 7]), "v"(a0));
                } else { if constexpr (V) M32B(false, a1, x[ks & 7], wt[ks]); else M32A(false, a1, wt[ks], x[ks & 7]); }
                if constexpr (ks < 35 || P) x[(ks + 5) & 7] = XRD(ks < 35 ? ks + 5 : ks - 35);
                SCHED();
                if constexpr (P) {
                    if constexpr (ks == 0) { n0 += (cur >> 6) ^ 1; nsrc = W_H1 + (u32)n0 * H_TILE; PINS(n0); PINS(nsrc); }
                    if constexpr (ks == 1) { nvo = t16 | (n0 >= lim0 ? OOB : 0u); PIN(nvo); }
                    if constexpr (ks >= 2 && ks < 22 && ks % 2 == 0) QDMA(ks / 2 - 1, fill, nvo, nsrc);
                    if constexpr (ks == 9) { QT(1); asm volatile("s_waitcnt vmcnt(4)\n\ts_barrier" ::: "memory"); QT(2); }
                    if constexpr (ks == 11) { if (__builtin_expect(n0 == lim0 && n0 < NSEG, 0)) filt_load<3>(p, grp, flt); }
                    if constexpr (ks == 13) QPK(pp, 0);
                    if constexpr (ks == 17) QPK(pq, 1);
                    if constexpr (ks == 21) { st4s(rs, pfo, 0, pfv); pfo = qfo; pfv = qfv; }
                    if constexpr (ks == 22) st16(rs, oa, pp);
                    if constexpr (ks == 23) st16(rs, oa + 512, pq);
                    if constexpr (ks == 34) { QT(3); asm volatile("s_waitcnt vmcnt(5) lgkmcnt(0)\n\ts_barrier" ::: "memory"); QT(4); }
                } else {
                    if constexpr (ks == 0) { ++n0; PINS(n0); NOPS(3); }
                    if constexpr (ks == 1) { QPK(pp, 0); st16(rs, oa, pp); }
                    if constexpr (ks == 2) { QPK(pq, 1); st16(rs, oa + 512, pq); }
                    if constexpr (ks == QSAMP) LD4A(fv, h2flag, 0);
                    if constexpr (ks == QPUB) { QT(1); asm volatile("s_waitcnt vmcnt(1)\n\ts_barrier" ::: "memory"); QT(2); st4s(rs, qfo, 0, qfv); }
                    if constexpr (ks == 35) { QT(3); QT(4); }
                    if constexpr (ks == 39) asm volatile("s_waitcnt vmcnt(1)" : "+v"(fv) :: "memory");
                }
                if constexpr (ks == 24) { hb = (m == 2 ? W_VO : W_KQ) + (u32)(cur * NH + h) * 16384u; PINS(hb); }
                if constexpr (ks == 25) { asm volatile("" :: "v"(a0)); hb = near_(hb, m == 1 ? 0u : 1u, pol); PINS(hb); }
                if constexpr (ks == 26) { oa = ol + hb; PIN(oa); }
                if constexpr (ks >= 27 && ks < 35) asm volatile("v_xor_b32 %0, 0x10000, %0" : "+v"(ar[(ks + 5) & 7]));
                if constexpr (ks == 38) { qfo = fo + (u32)(cur >> 6) * (32 * 128); qfv = (u32)(cur & 63) + 1; asm volatile("" : "+v"(qfo), "+v"(qfv)); }
                if constexpr (ks == 39) { fill ^= QK_A; cur = n0; PINS(fill); }
                SCHED();
            SEND
            QT(5);
      };
#pragma unroll 1
      for (const int end = n1 < -1 ? -1 : NSEG; n1 < end; ++n1) {
        QS(0);
        fill = uu(fill);
        {
            cur = n0;
            if (n1 >= 0) {
                if (w == 0 && ((u32)__builtin_amdgcn_ballot_w64(fv <= (u32)n1) & 255u) != 0u) wait_flags(rs, h2flag, (u32)n1 + 1, dead);
                BAR();
                cur = 64 | n1;
            }
            QS(1);
            if (n1 >= -1) {
                const u32 so = uu(n1 < 0 ? W_H1 + QFIRST * H_TILE : near_(W_H2 + (u32)n1 * H_TILE, 1u, pol)), dst = fill ^ QK_A;
                SFOR(i, 10) QDMA(i, dst, t16, so); SEND
                QPK(pp, 0); st16(rs, oa, pp);
                QPK(pq, 1); st16(rs, oa + 512, pq);
                if (n1 >= 0) { pfo = qfo; pfv = qfv; }
                oa = OOB; qfo = OOB;
            }
            VMW(8);
            BAR();
            QS(2);
#pragma unroll
            for (int ks = 0; ks < 5; ++ks) x[ks] = XRD(ks);
        }
        int cnt = n1 == -2 ? QFIRST - 1 : n1 == -1 ? QLEAD - QFIRST - 1 : 1;
        do stream(IC<1>{}, 0); while (--cnt > 0);
        int kq_ = (n0 - lim0) & ~(n1 + 1);
        if (kq_ < 0) {
            stream(IC<0>{}, n1 + 1);
        }
        PINS(kq_);
        if (kq_ >= 0) {
            NOPS(9);
            QPK(pp, 0); st16(rs, oa, pp);
            QPK(pq, 1); st16(rs, oa + 512, pq);
            DRAIN(); BAR();
            st4s(rs, qfo, 0, qfv); st4s(rs, pfo == qfo ? OOB : pfo, 0, pfv);
            oa = OOB; pfo = OOB; qfo = OOB; fv = 0;
            if (n1 == -3) EV(7);
        }
      }
    };
#pragma unroll 1
    for (;;) {
        if (m == 2) run(IC<1>{}); else run(IC<0>{});
        if (n1 > 0) break;

        wait_flags(rs, tid < 96 ? lnf : OOB, 2u, dead);
        conv_job<1>(p, rs, lds, grp, flt, 0);
        lim0 = NSEG;
    }
}

constexpr int OU_SEL = 131072;

DEV void role_out(const Params& p, lptr lds, int L, int kq, int ng) {
    const int tid = threadIdx.x, lane = tid & 63, w = uni(tid >> 6), hh = lane >> 5, r32 = lane & 31;
    const rsrc_t rs = mk_rsrc(p.ws, W_END);
    v4u wo[64];
    Filt flt;
    bool dead = false;
    {
        const v4u sw = mk_srd(p.Wo, DM * NQ * 2);
        const u32 vo = wst_src(lane, DM) + (ng == 4 ? 1024u : (u32)((ng >> 1) * 512 + (ng & 1) * 128 + ((lane ^ (lane >> 2)) & 8) * 16));
        auto fill = [&](int b) {
#pragma unroll 1
            for (int i = 0; i < 8; ++i) DMAIN(sw, uu((b % 5) * 32768 + (4 * i + w) * 1024), vo, uu((kq * 1024 + b * 128 + 16 * i + 4 * w) * (DM * 2)));
        };
#pragma unroll 1
        for (int b = 0; b < 5; ++b) fill(b);
        lptr tb = lds + hh * 1024 + wst_rd(lane, w);
        SFOR(b, 8)
            VMW(b < 3 ? 32 : 66 - 8 * b); BAR();
            lptr q = tb + (b % 5) * 32768;
#pragma unroll
            for (int k = 0; k < 8; ++k) { lptr a = q + (k >> 1) * 8192 + (k & 1) * 2048; wo[8 * b + k] = tr2(a, a + 4096); if (b < 7) asm volatile("" : "+a"(wo[8 * b + k])); else PIN(wo[8 * b + k]); }
            if constexpr (b < 3) { BAR(); fill(b + 5); }
            if constexpr (b == 2) filt_load(p, L == 0 ? 44 + kq * 5 + ng : 1 << 12, flt);
        SEND
    }
    EV(6);
    if (L == 0) {
        BAR();
        conv_job<0>(p, rs, lds, 44 + kq * 5 + ng, flt, 0);
        DRAIN(); BAR();
        st4s(rs, tid == 0 ? foff(F_CV + 44 + kq * 5 + ng) : OOB, 0, 2u);
    }
    EV(15);
    MK_SRD();
    const u32 pol = get_pol(rs);
    const u32 ofl = (lane < 8 && w == 0) ? near_(foff(F_O + L * 32 + kq * 8 + lane), 0u, pol) : OOB;
    const u32 l16 = (u32)lane * 16u;
    const u32 fo = (tid == 192) ? near_(foff(F_P + L * 32 + kq * 5 + ng), 1u, pol) : OOB;
    auto osrc = [&](int s) { return uu(hblk(W_VO, L * NSEG + s, kq * 8, 0u, pol) + w * 1024); };
#define ODMA(i, dst, vo, so) DMA16I(dst, (i) * 4096, vo, so, ((i) >> 1) * 16384 + ((i) & 1) * 4096)
    const u32 pvo = (u32)(kq * 5 + ng) * 16384u + (u32)w * 2048u + l16;
    lptr tw = lds + OU_SEL + 256 + r32 * 272 + (ng == 4 ? hh * 32 + w * 4 : (w & 1) * 128 + hh * 16 + (w >> 1) * 8), tr = lds + OU_SEL + 256 + (8 * w + (lane >> 4)) * 272 + (lane & 15) * 16;
    lptr selw = lds + OU_SEL + (w == 0 ? 0 : 64);
    u32 pfo = OOB;
    u32 buf = 0;
    if (w == 0) wait_flags(rs, ofl, 1u, dead);
    BAR();
    for (int s = 0; s < NSEG; ++s) {
        u32 src = osrc(s), dst = uu(buf + w * 1024);
        lptr A = lds + buf + l16;
        asm volatile("" : "+s"(src), "+s"(dst));
#define OS(k) do {} while (0)
        OS(0);
        OS(1);
        SFOR(i, 10) ODMA(i, dst, l16, src); SEND
        OS(2);
        asm volatile("s_waitcnt vmcnt(7)\n\ts_barrier" ::: "memory");
        OS(3);
        LS2(L == 0 && w == 1, 96 + kq * 5 + ng);
        v4u o[64];
#pragma unroll
        for (int k = 0; k < 3; ++k) o[k] = lds_r16(A + k * 1024);
        st4s(rs, pfo, 0, (u32)s);
        u32 fv, pa; int rdy;
        f32x16 acc;
        SCHED();
        SFOR(k, 64)
            if constexpr (k < 56) M32A(k == 0, acc, wo[k], o[k]); else asm volatile("v_mfma_f32_32x32x16_bf16 %0, %1, %2, %0" : "+v"(acc) : "v"(wo[k]), "v"(o[k]));
            if constexpr (k == 61) rdy = *(LAS int*)(lds + OU_SEL);
            if constexpr (k < 61) o[k + 3] = lds_r16(A + (k + 3) * 1024);
            SCHED();
            if constexpr (k < 12 && (k & 1) == 0) ODMA(10 + (k >> 1), dst, l16, src);
            if constexpr (k == 7) OS(4);
            if constexpr (k == 8) OS(5);
            if constexpr (k == 23) OS(6);
            if constexpr (k == 24) OS(7);
            if constexpr (k == 43) OS(8);
            if constexpr (k == 44) OS(9);
            if constexpr (k == 59) OS(10);
            if constexpr (k == 63) OS(11);
            if constexpr (k == 7) asm volatile("s_waitcnt vmcnt(8)\n\ts_barrier" ::: "memory");
            if constexpr (k == 23) asm volatile("s_waitcnt vmcnt(5)\n\ts_barrier" ::: "memory");
            if constexpr (k == 30) { pa = pvo + near_(p_item(s), (u32)L, pol); PIN(pa); }
            if constexpr (k == 22) LD4A(fv, ofl, 0);
            if constexpr (k == 42) {
                asm volatile("s_waitcnt vmcnt(0)" : "+v"(fv) :: "memory");
                *(LAS int*)selw = (int)((u32)__builtin_amdgcn_ballot_w64(fv < (u32)s + 2) & 255u);
            }
            if constexpr (k == 43) asm volatile("s_waitcnt lgkmcnt(1)\n\ts_barrier" ::: "memory");
            SCHED();
        SEND
        const bool ready = uni(rdy) == 0;
        asm volatile("s_nop 11" : "+v"(acc));
        u32 pc[8];
#pragma unroll
        for (int i = 0; i < 8; ++i) pc[i] = pk(acc[2 * i], acc[2 * i + 1]);
        if (ng != 4) {
#pragma unroll
            for (int i = 0; i < 4; ++i) *(LAS v2u*)(tw + i * 32) = (v2u){pc[2 * i], pc[2 * i + 1]};
        } else {
#pragma unroll
            for (int i = 0; i < 8; ++i) *(LAS u32*)(tw + (i >> 1) * 64 + (i & 1) * 16) = pc[i];
        }
        BAR();
        { const v4u p0 = lds_r16(tr), p1 = lds_r16(tr + 4 * 272); st16(rs, pa, p0); st16(rs, pa + 1024, p1); }
        SCHED();
        OS(12);
        if (ready) pfo = fo;
        else {
            LD4A(fv, ofl, 0);
            asm volatile("s_waitcnt vmcnt(1)\n\ts_barrier" ::: "memory");
            st4s(rs, fo, 0, (u32)s + 1); pfo = OOB;
            OS(14);
            LS2(L == 0 && w == 1, 160 + kq * 5 + ng);
            asm volatile("s_waitcnt vmcnt(1)" : "+v"(fv) :: "memory");
            if (w == 0 && s + 1 < NSEG && ((u32)__builtin_amdgcn_ballot_w64(fv < (u32)s + 2) & 255u) != 0u) wait_flags(rs, ofl, (u32)s + 2, dead);
            BAR();
        }
        OS(13);
        buf ^= 65536u;
    }
}

template <int L> DEV void role_row(const Params& p, lptr lds, int ri) {
    const int lane = threadIdx.x & 63, w = uni(threadIdx.x >> 6);
    const rsrc_t rs = mk_rsrc(p.ws, W_END), rx = mk_rsrc(p.x, ACT);
    bool dead = false;
    const u32 pol = get_pol(rs);
    const u32 fp = threadIdx.x < 20 ? near_(foff(F_P + L * 32 + lane), 1u, pol) : OOB, fo = threadIdx.x == 192 && L == 0 ? near_(foff(F_H2 + (ri >> 2)), 1u, pol) : OOB;
    u32 fv = 0;
    MK_SRD();
    auto yfill = [&](int b, int i) { DMA32(uu(w * 20480 + (b & 1) * 10240 + i * 2048), (u32)lane * 16u, uu(W_Y + (u32)(ri * 8 + b) * 10240u + i * 2048)); };
    auto ywait = [&](u32 v) {
#pragma unroll 1
        for (u32 i = v == 3u ? 160u : 0u; i < (v == 3u ? 168u : 160u); i += 64) wait_flags(rs, i + lane < (v == 3u ? 168u : 160u) ? foff(F_CV + i + lane) : OOB, v, dead);
    };
    v2u xa = {}, xb = {}; u32 xc = 0;
    auto xload = [&](int s) { const u32 ro = (u32)(s * 32 + ri) * (DM * 2); xa = ldin8(rx, (u32)lane * 8u, ro); xb = ldin8(rx, (u32)lane * 8u + 512u, ro); xc = __builtin_amdgcn_raw_buffer_load_b32(rx, (u32)lane * 4u + 1024u, ro, 0); };
    if (L == 0) xload(0);
    ywait(1u);
    EV(15);
#pragma unroll 1
    for (int i = 0; i < 5; ++i) yfill(0, i);
    DRAIN();
    for (int s = 0; s < NSEG; ++s) {
        f32x2 y[5];
        {
            lptr ya = lds + w * 20480 + ((s >> 3) & 1) * 10240 + (s & 7) * 2 + lane * 16;
            auto yr = [&](lptr q) { return lo_f(*(LAS u16*)q); };
            y[0] = (f32x2){yr(ya), yr(ya + 2560)}; y[1] = (f32x2){yr(ya + 5120), yr(ya + 7680)};
            y[2] = (f32x2){yr(ya + 1024), yr(ya + 3584)}; y[3] = (f32x2){yr(ya + 6144), yr(ya + 8704)};
            lptr yc = ya - lane * 16 + 2048 + (lane >> 1) * 16 + (lane & 1) * 5120;
            y[4] = (f32x2){yr(yc), yr(yc + 2560)};
            asm volatile("" : "+v"(y[0]), "+v"(y[1]), "+v"(y[2]), "+v"(y[3]), "+v"(y[4]));
        }
        u32 xi[5] = {xa.x, xa.y, xb.x, xb.y, xc};
        u32 pa = near_(p_item(s), (u32)L, pol) + (u32)ri * 256u, pc = pa + 65536u + (u32)(lane >> 4) * 81920u + (u32)(lane & 15) * 16u;
        pa += (u32)(lane >> 5) * 81920u + (u32)((lane >> 4) & 1) * 16384u + (u32)(lane & 15) * 16u;
        asm volatile("" : "+v"(xi[0]), "+v"(xi[1]), "+v"(xi[2]), "+v"(xi[3]), "+v"(xi[4]), "+v"(pa), "+v"(pc));
        if (s == 2 || s == 26) ywait(s == 2 ? 2u : 3u);
#define RS(k) do {} while (0)
        RS(0);
        if (w == 0 && ((u32)__builtin_amdgcn_ballot_w64(fv <= (u32)s) & 0xFFFFFu) != 0u) wait_flags(rs, fp, (u32)s + 1, dead);
        BAR();
        RS(1);
        v4u d[5];
#pragma unroll
        for (int i = 0; i < 4; ++i) d[i] = ld16s(rs, pa, (i >> 1) * 32768 + (i & 1) * 163840);
        d[4] = ld16s(rs, pc, 0);
        if (L == 0) xload(s + 1);
        else {
            const u32 ro = W_X2 + (u32)(s * 32 + ri) * (DM * 2);
            const v2u a = ld8s(rs, (u32)lane * 8u, ro), b = ld8s(rs, (u32)lane * 8u + 512u, ro);
            xi[0] = a.x; xi[1] = a.y; xi[2] = b.x; xi[3] = b.y; xi[4] = ld4(rs, (u32)lane * 4u + 1024u + ro);
        }
        RS(2);
        LS2(L == 0, 192 + ri);
        f32x2 o[5];
        {
            f32x2 t[5][2];
#pragma unroll
            for (int i = 0; i < 5; ++i) {
                const auto e = __builtin_amdgcn_permlane32_swap(d[i].x, d[i].z, false, false), f = __builtin_amdgcn_permlane32_swap(d[i].y, d[i].w, false, false);
                t[i][0] = up2(e[0]) + up2(e[1]); t[i][1] = up2(f[0]) + up2(f[1]);
            }
            o[0] = t[0][0] + t[1][0]; o[1] = t[0][1] + t[1][1]; o[2] = t[2][0] + t[3][0]; o[3] = t[2][1] + t[3][1];
            const auto e = __builtin_amdgcn_permlane16_swap(u_(t[4][0].x), u_(t[4][1].x), false, false), f = __builtin_amdgcn_permlane16_swap(u_(t[4][0].y), u_(t[4][1].y), false, false);
            o[4] = (f32x2){f_(e[0]) + f_(e[1]), f_(f[0]) + f_(f[1])};
        }
        f32x2 x1[5];
#pragma unroll
        for (int i = 0; i < 5; ++i) x1[i] = up2(pk2(up2(xi[i]) + up2(pk2(o[i]))));
        asm volatile("" : "+v"(x1[0]), "+v"(x1[1]), "+v"(x1[2]), "+v"(x1[3]), "+v"(x1[4]));
        RS(3);
        if ((s & 7) - 2u < 5u && s < NSEG - 8) yfill((s >> 3) + 1, (s & 7) - 2);
        SCHED();
        const f32x2 st1 = ln_stats(x1);
        u32 xp[5];
#pragma unroll
        for (int i = 0; i < 5; ++i) xp[i] = pk2(x1[i] + ((x1[i] - st1.x) * st1.y) * y[i]);
        const int row = s * 32 + ri;
        RS(4);
        if (L == 0) {
            const u32 ro = (u32)row * (DM * 2);
            f32x2 x2[5];
#pragma unroll
            for (int i = 0; i < 5; ++i) x2[i] = up2(xp[i]);
            st8s(rs, (u32)lane * 8, W_X2 + ro, (v2u){xp[0], xp[1]});
            st8s(rs, (u32)lane * 8 + 512, W_X2 + ro, (v2u){xp[2], xp[3]});
            st4s(rs, (u32)lane * 4 + 1024, W_X2 + ro, xp[4]);
            SCHED();
            const f32x2 st2 = ln_stats(x2);
            u32 hp[5];
#pragma unroll
            for (int i = 0; i < 5; ++i) hp[i] = pk2((x2[i] - st2.x) * st2.y);
            store_h(rs, near_(W_H2 + (u32)s * H_TILE, 1u, pol), row, hp);
        } else {
            u16* o_ = p.out + (size_t)row * DM;
            *(v2u*)(o_ + 4 * lane) = (v2u){xp[0], xp[1]};
            *(v2u*)(o_ + 256 + 4 * lane) = (v2u){xp[2], xp[3]};
            *(u32*)(o_ + 512 + 2 * lane) = xp[4];
        }
        RS(5);
        LD4A(fv, fp, 0);
        if (L == 0) asm volatile("s_waitcnt vmcnt(1)\n\ts_barrier" ::: "memory");
        st4s(rs, fo, 0, (u32)s + 1);
        RS(6);
        asm volatile("s_waitcnt vmcnt(1)" : "+v"(fv) :: "memory");
        RS(7);
        if (ri == 0) LST(320 + L * 64 + s, rt32());
        LS2(L == 0, 224 + ri);
    }
}

constexpr float SCALE = 0.08838834764831845f, LOG2E = 1.4426950408889634f;
constexpr int C_K = 0, C_V = 8192, C_QX = 16384, C_MEM = 24576, C_Q = C_MEM + 10240, C_KM = C_Q + 2304, C_VM = C_KM + 2304, C_DUMP = C_VM + 2048,
              C_ZERO = C_DUMP + 1024, C_WL = C_ZERO + 128;
constexpr int NLF = 14;
static_assert(C_WL % 16 == 0 && C_WL + 4 * NLF * 2048 <= LDS_BYTES, "head lds");
#define CH_VIS 10
#define CH_H0 124
#define CH_W0 88
#define CH_MH 4
#define CH_MW 4
#define CH_REC 20
#define CH_LATE 30
#define CH_UP 4
#define CH_WX 6
DEV void wait_until(u32 t) {
    unsigned long long c;
    asm volatile("1:\n\ts_memrealtime %0\n\ts_waitcnt lgkmcnt(0)\n\ts_mov_b64 vcc, %0\n\ts_sub_u32 vcc_lo, %1, vcc_lo\n\ts_cmpk_lt_u32 vcc_lo, 400\n\ts_cbranch_scc1 1b" : "=&s"(c) : "s"(uu(t - 1u)) : "vcc", "scc", "memory");
}
#define WAIT_TXT(tmp, t1) "s_memrealtime vcc\n1:\n\ts_waitcnt lgkmcnt(0)\n\ts_sub_u32 " tmp ", " t1 ", vcc_lo\n\ts_memrealtime vcc\n\ts_cmpk_lt_u32 " tmp ", 400\n\ts_cbranch_scc1 1b\n\t"
#define SMF_A(acc, a, b) asm volatile("v_smfmac_f32_16x16x64_bf16 %0, %1, %2, %3" : "+v"(acc) : "v"(a), "a"(b), "v"(idx))
#define SMF_V(acc, a, b) asm volatile("v_smfmac_f32_16x16x64_bf16 %0, %1, %2, %3" : "+v"(acc) : "v"(a), "v"(b), "v"(idx))
#define MF0(d, a, b) asm volatile("v_mfma_f32_16x16x32_bf16 %0, %1, %2, 0" : "=&v"(d) : "v"(a), "v"(b))
#define MF1(d, a, b) asm volatile("v_mfma_f32_16x16x32_bf16 %0, %1, %2, %0" : "+v"(d) : "v"(a), "v"(b))
#define MF0H(d, a, b) asm volatile("v_mfma_f32_16x16x16_bf16 %0, %1, %2, 0" : "=&v"(d) : "v"(a), "v"(b))
#define MF1H(d, a, b) asm volatile("v_mfma_f32_16x16x16_bf16 %0, %1, %2, %0" : "+v"(d) : "v"(a), "v"(b))
DEV v8u tr_frag(lptr p0, lptr p1, lptr p2, lptr p3) { return cat8(tr2(p0, p1), tr2(p2, p3)); }
constexpr int qslot(int ks) { return (ks & 1) && ks < 8 ? ks >> 1 : -1; }
constexpr int kslot(int ks) { return ks < 4 ? 4 + ks : -1; }
constexpr int vslot(int ks) { return ks >= 4 ? 4 + ks : -1; }
DEV u32 ring_map(u32 off, u32 half) { return ((off >> 13) << 14) + (half << 13) + (off & 8191u); }
#define ZRD(x, i) x = __builtin_bit_cast(f32x4, lds_r16(ZP + 16 * (i)))
#define VMAX(d, a, b) asm volatile("v_max_f32 %0, %1, %2" : "=v"(d) : "v"(a), "v"(b))
#define VMAX3(d, a, b, c_) asm volatile("v_max3_f32 %0, %1, %2, %3" : "=v"(d) : "v"(a), "v"(b), "v"(c_))
#define VADD(d, a, b) asm volatile("v_add_f32 %0, %1, %2" : "=v"(d) : "v"(a), "v"(b))
#define VEXP(d, a) asm volatile("v_exp_f32 %0, %1" : "=v"(d) : "v"(a))
#define VFMC(d, a, b) asm volatile("v_fmamk_f32 %0, %1, 0x3e0293ee, %2" : "=v"(d) : "v"(a), "v"(b))
#define SEL(d, a, b) asm volatile("v_cndmask_b32 %0, %1, %2, %3" : "=v"(d) : "v"(a), "v"(b), "s"(himask))
#define QPERM(d, a, q) asm volatile("v_mov_b32_dpp %0, %1 quad_perm:" q " row_mask:0xf bank_mask:0xf" : "=v"(d) : "v"(a))

#define HSAMP 58
DEV void role_head(const Params& p, lptr lds, int L, int h) {
    const int tid = threadIdx.x, lane = tid & 63, w = uni(tid >> 6), g = lane >> 4, r = lane & 15;
    const rsrc_t rs = mk_rsrc(p.ws, W_END);
    v8u wq0[10], wq1[10], wk0[10], wk1[10], wv0[10], wv1[10];
    lptr WL = lds + C_WL + w * (NLF * 2048) + lane * 16;
    {
        constexpr int GS = 1056, CS = 48 * GS;
        const v4u sq = mk_srd(p.Wq, DM * NQ * 2), sk = mk_srd(p.Wk, DM * NQ * 2), sv = mk_srd(p.Wv, DM * NQ * 2);
        const u32 vo = (u32)(g * NQ + h * DH + 8 * (r ^ ((g & 1) << 3))) * 2u;
        auto fill = [&](int ch) {
#pragma unroll 1
            for (int i = 0; i < 4; ++i) {
                const u32 dst = uu((ch % 3) * CS + (4 * i + w) * GS), so = uu((ch * 64 + 16 * i + 4 * w) * (NQ * 2));
                DMAIN(sq, dst, vo, so); DMAIN(sk, dst + 16 * GS, vo, so); DMAIN(sv, dst + 32 * GS, vo, so);
            }
        };
        if (L == 1) pre_rings(p, rs, h); else __builtin_amdgcn_s_sleep(ST_HEAD);
        fill(0); fill(1); fill(2);
        const int q4 = r >> 2;
        lptr tb = lds + (4 * (g >> 1) + 2 * (q4 >> 1) + (g & 1)) * GS + (q4 & 1) * 256 + ((w * 64 + (r & 3) * 8) ^ ((q4 & 1) << 7));
        SFOR(ch, 10)
            VMW(ch == 1 || ch == 2 ? 25 : ch < 8 ? 24 : 108 - 12 * ch);
            BAR();
            if constexpr (ch == 0) st4s(rs, tid == 0 && L == 1 ? foff(F_RG + h) : OOB, 0, 1u);
            auto frag = [&](int m, int t) { lptr b = tb + (ch % 3) * CS + m * 16 * GS + t * 32; return tr_frag(b, b + 512, b + 8 * GS, b + 8 * GS + 512); };
            wq0[ch] = frag(0, 0); wq1[ch] = frag(0, 1); wk0[ch] = frag(1, 0); wk1[ch] = frag(1, 1); wv0[ch] = frag(2, 0); wv1[ch] = frag(2, 1);
            asm volatile("" : "+a"(wq0[ch]), "+a"(wk0[ch]), "+v"(wv0[ch]), "+v"(wv1[ch]));
            if constexpr (qslot(ch) < 0) asm volatile("" : "+a"(wq1[ch])); else asm volatile("" : "+v"(wq1[ch]));
            if constexpr (kslot(ch) < 0) asm volatile("" : "+a"(wk1[ch])); else asm volatile("" : "+v"(wk1[ch]));
            BAR();
            if constexpr (ch < 7) fill(ch + 3);
        SEND
#pragma unroll
        for (int ch = 0; ch < 10; ++ch) {
            if (qslot(ch) >= 0) { lds_w16(WL + qslot(ch) * 2048, wq1[ch].lo); lds_w16(WL + qslot(ch) * 2048 + 1024, wq1[ch].hi); }
            if (kslot(ch) >= 0) { lds_w16(WL + kslot(ch) * 2048, wk1[ch].lo); lds_w16(WL + kslot(ch) * 2048 + 1024, wk1[ch].hi); }
            if (vslot(ch) >= 0) { lds_w16(WL + vslot(ch) * 2048, wv1[ch].lo); lds_w16(WL + vslot(ch) * 2048 + 1024, wv1[ch].hi); }
        }
        CFENCE();
    }
    const u32 idx = (r & 2) ? 0xEEEEu : 0x4444u;
    const unsigned long long himask = 0xCCCCCCCCCCCCCCCCull;
    bool dead = false;
    EV(6);
    const u32 pol = get_pol(rs);
    const u32 pol0 = pol & 1u;
    const u32 l16 = (u32)lane * 16u;
    const u32 mring = W_MRING + (u32)L * (MB / 4) + pol0 * 8192u, hint = mring + 4 * 32768u;
    const u32 fkh = lane < 3 ? near_(foff(F_QKV + lane * 64 + L * 32 + h), 0u, pol) : lane < 6 ? hint + (u32)(lane - 3) * 4u : OOB;
    u32 tm = 0;
    MK_SRD();
    const unsigned long long kmask = __builtin_amdgcn_ballot_w64((hblk(W_KQ, L * NSEG + lane, h, 0u, pol) & 8192u) != 0u),
                             vmask = __builtin_amdgcn_ballot_w64((hblk(W_VO, L * NSEG + lane, h, 1u, pol) & 8192u) != 0u);
    u32 kb = uu(W_KQ + (u32)(L * NSEG * NH + h) * 16384u + w * 2048);
    u32 dk = uu(C_K + w * 2048), dv = dk + (C_V - C_K), dq = dk + (C_QX - C_K), dm = uu(C_MEM + w * 2048), dm2 = uu(w < 2 ? C_MEM + 8192 + w * 1024 : C_DUMP);
    asm volatile("" : "+s"(dk), "+s"(dv), "+s"(dq), "+s"(dm), "+s"(dm2));
    wait_flags(rs, lane < 3 ? fkh : OOB, 1u, dead);
    EV(7);
    u32 ksrc = kb + ((u32)kmask & 1u) * 8192u, vsrc = kb + (W_VO - W_KQ) + ((u32)vmask & 1u) * 8192u;
    DMA32(dk, l16, ksrc); DMA32(dv, l16, vsrc); DMA32(dq, l16, ksrc ^ 8192u);
    lptr qw = lds + C_Q + (2 * g) * 272 + (32 * w + 2 * r) * 2;
    lptr qr = lds + C_Q + (2 * (r >> 2) + (r & 1)) * 272 + g * 16;
    lptr LB = lds + l16, VX = lds + C_V + w * 2048 + l16;
    if (tid >= 64 && tid < 70) { const u32 z = tid == 68 ? 0x3F803F80u : tid == 69 ? PSENT : 0u; lds_w16(lds + C_ZERO + (tid - 64) * 16, (v4u){z, z, z, z}); }
    u32 zaddr = 0; PIN(zaddr);
    lptr ZP = lds + zaddr + C_ZERO;
    const float CEXP = SCALE * LOG2E;
#define LF_RD(slot, hf) lf[slot][hf] = lds_r16(WL + (slot) * 2048 + (hf) * 1024)
    BAR();
    const u32 ofl = tid == 0 ? near_(foff(F_O + L * 32 + h), 0u, pol) : OOB;
    u32 oso = (g & 1) ? OOB : W_ORING + (u32)L * (MB / 2) + ring_map((u32)((r >> 3) * 32768 + h * 1024 + w * 256 + (g >> 1) * 128 + (r & 7) * 16), pol0);
    unsigned long long vis[8];
#pragma unroll
    for (int i = 0; i < 8; ++i) vis[i] = __builtin_amdgcn_ballot_w64(16 * (i >> 2) + 4 * g + (i & 3) <= 16 * (w & 1) + r);
    u32 missed = 0;
    EV(8);
    for (int s = 0; s < NSEG;) {
        u32 ms = mring + (u32)(s & 3) * 32768u + w * 2048, ms2 = mring + (u32)(s & 3) * 32768u + 16384u + (w & 1) * 1024, sv = (u32)s;
        asm volatile("" : "+s"(ms), "+s"(ms2)); PIN(sv);
        f32x4 aq0, aq1, ak0, ak1;
        v4u lf[NLF][2];
        ZRD(aq0, 0); ZRD(aq1, 1); ZRD(ak0, 2); ZRD(ak1, 3);
#pragma unroll
        for (int i = 0; i < 4; ++i) { LF_RD(i, 0); LF_RD(i, 1); }
        SCHED();
        v4u A[10];
#define HT(k) do {} while (0)
        v4u kx[8], qf[4], kmf[4], vx[2], ones, P;
        float fa, fb, fc, fd, mx, my, sx[4], t[4], tm0, tm1; u32 pa, pb, fl, e01, e23, pmm; unsigned long long nanq;
        f32x4 av0, av1, s0, s1, sm, oa, ob, rsum;
        v2u vm, pm;
#define LW(sl) cat8(lf[sl][0], lf[sl][1])
#define PQ(ks, tl) do { if constexpr (!(tl)) SMF_A(aq0, A[ks], wq0[ks]); else if constexpr (qslot(ks) < 0) SMF_A(aq1, A[ks], wq1[ks]); else { const v8u lw_ = LW(qslot(ks)); SMF_V(aq1, A[ks], lw_); } } while (0)
#define PK(ks, tl) do { if constexpr (!(tl)) SMF_A(ak0, A[ks], wk0[ks]); else if constexpr (kslot(ks) < 0) SMF_A(ak1, A[ks], wk1[ks]); else { const v8u lw_ = LW(kslot(ks)); SMF_V(ak1, A[ks], lw_); } } while (0)
#define PV(ks, tl) do { if constexpr (!(tl)) SMF_V(av0, A[ks], wv0[ks]); else if constexpr (vslot(ks) < 0) SMF_V(av1, A[ks], wv1[ks]); else { const v8u lw_ = LW(vslot(ks)); SMF_V(av1, A[ks], lw_); } } while (0)
#define KXR(i) kx[i] = lds_r16(LB + C_K + ((i) & 1) * 4096 + ((i) >> 1) * 1024)
#define XW(q_) do { *(LAS u32*)(q_) = pa; *(LAS u32*)((q_) + 272) = pb; } while (0)
        auto slot = [&](auto zc) __attribute__((always_inline)) {
            constexpr int z = decltype(zc)::value;
            if constexpr (z < 14) PQ(z >> 1, z & 1);
            else if constexpr (z < 17) PQ(z - 7, 0);
            else if constexpr (z < 20) PQ(z - 10, 1);
            else if constexpr (z < 33) PK((z - 20) >> 1, z & 1);
            else if constexpr (z < 35) { if constexpr (z == 33) MF0(s0, kx[0], qf[0]); else MF0(s1, kx[1], qf[0]); }
            else if constexpr (z < 41) { if constexpr (z & 1) MF1(s0, kx[z - 33], qf[(z - 33) >> 1]); else MF1(s1, kx[z - 33], qf[(z - 33) >> 1]); }
            else if constexpr (z == 41) PK(6, 1);
            else if constexpr (z < 45) PK(z - 35, 0);
            else if constexpr (z < 48) PK(z - 38, 1);
            else if constexpr (z < 58) PV(z - 48, 0);
            else if constexpr (z < 61) PV(z < 60 ? z - 54 : 0, 1);
            else if constexpr (z == 61) MF0(sm, kmf[0], qf[0]);
            else if constexpr (z < 65) MF1(sm, kmf[z - 61], qf[z - 61]);
            else if constexpr (z == 65) MF0(oa, vx[0], P);
            else if constexpr (z == 66) MF0(ob, vx[1], P);
            else PV(6, 1);
            if constexpr (z < 6) A[4 + z] = lds_r16(LB + C_MEM + (4 + z) * 1024);
            if constexpr (z == 6 || z == 7) LF_RD(4, z - 6);
            if constexpr (z == 8 || z == 9) LF_RD(5, z - 8);
            if constexpr (z == 12 || z == 13) LF_RD(6, z - 12);
            if constexpr (z == 16 || z == 17) KXR(z - 16);
            if constexpr (z == 18 || z == 19) LF_RD(7, z - 18);
            if constexpr (z >= 20 && z < 24) KXR(z - 18);
            if constexpr (z == 32 || z == 33) KXR(z - 26);
            if constexpr (z == 24) XW(qw);
            if constexpr (z == 25) ZRD(av0, 0);
            if constexpr (z == 26) ZRD(av1, 1);
            if constexpr (z == 27) asm volatile("s_waitcnt lgkmcnt(2)\n\ts_barrier" ::: "memory");
            if constexpr (z >= 28 && z < 32) qf[z - 28] = lds_r16(qr + (z - 28) * 64);
            if constexpr (z == HSAMP) fl = ld4(rs, fkh);
            if constexpr (z == 52) XW(qw + (C_KM - C_Q));
            if constexpr (z == 53 || z == 54) vx[z - 53] = lds_r16(VX + (z - 53) * 1024);
            if constexpr (z == 55) asm volatile("s_waitcnt lgkmcnt(2)\n\ts_barrier" ::: "memory");
            if constexpr (z >= 56 && z < 60) kmf[z - 56] = lds_r16(qr + (C_KM - C_Q) + (z - 56) * 64);
            if constexpr (z >= 48 && z < 52) LF_RD(8 + ((z - 48) >> 1), z & 1);
            if constexpr (z >= 60) LF_RD(10 + ((z - 60) >> 1), z & 1);
            SCHED();
            if constexpr (z == 19) HT(2);
            if constexpr (z == 32) HT(3);
            if constexpr (z == 47) HT(4);
            if constexpr (z == 64) HT(5);
            if constexpr (z == 20) VADD(fa, aq0[0], aq0[2]);
            if constexpr (z == 21) VADD(fb, aq0[1], aq0[3]);
            if constexpr (z == 22) { VADD(fc, aq1[0], aq1[2]); VADD(fd, aq1[1], aq1[3]); }
            if constexpr (z == 23) { asm volatile("v_cmp_u_f32 %0, %1, %2" : "=s"(nanq) : "v"(fa), "v"(fb)); VCVT(pa, fa, fc); VCVT(pb, fb, fd); }
            if constexpr (z == 39 || z == 40 || z == 41) NOPS(1);
            if constexpr (z == 42) VMAX3(mx, s0[0], s0[1], s0[2]);
            if constexpr (z == 43) { VMAX3(mx, mx, s0[3], s1[0]); VMAX3(my, s1[1], s1[2], s1[3]); }
            if constexpr (z == 44 || z == 46) { VMAX(fa, mx, my); VMAX(my, mx, my); mx = fa; }
            if constexpr (z == 45) { SEL(sx[0], s0[0], s1[0]); asm volatile("v_permlane16_swap_b32 %0, %1" : "+v"(mx), "+v"(my)); }
            if constexpr (z == 47) { SEL(sx[1], s0[1], s1[1]); asm volatile("v_permlane32_swap_b32 %0, %1" : "+v"(mx), "+v"(my)); }
            if constexpr (z == 48) asm volatile("v_max_f32 %0, %0, %1\n\tv_mul_f32 %0, 0xbe0293ee, %0" : "+v"(mx) : "v"(my));
            if constexpr (z == 49) { VADD(fa, ak0[0], ak0[2]); VADD(fb, ak0[1], ak0[3]); }
            if constexpr (z == 50) { VADD(fc, ak1[0], ak1[2]); VADD(fd, ak1[1], ak1[3]); }
            if constexpr (z == 51) { VCVT(pa, fa, fc); VCVT(pb, fb, fd); }
            if constexpr (z == 52) SEL(sx[2], s0[2], s1[2]);
            if constexpr (z == 53) { SEL(sx[3], s0[3], s1[3]); VFMC(t[0], sx[0], mx); }
            if constexpr (z == 54) { VFMC(t[1], sx[1], mx); VFMC(t[2], sx[2], mx); }
            if constexpr (z == 55) VFMC(t[3], sx[3], mx);
            if constexpr (z >= 56 && z < 60) VEXP(t[z - 56], t[z - 56]);
            if constexpr (z == 60) { VCVT(e01, t[0], t[1]); VCVT(e23, t[2], t[3]); }
            if constexpr (z == 61) { QPERM(P.x, e01, "[0,1,0,1]"); QPERM(P.z, e01, "[2,3,2,3]"); }
            if constexpr (z == 62) { QPERM(P.y, e23, "[0,1,0,1]"); QPERM(P.w, e23, "[2,3,2,3]"); }
            if constexpr (z == 63) { VADD(fa, av0[0], av0[2]); VADD(fb, av0[1], av0[3]); }
            if constexpr (z == 64) VCVT(vm.x, fa, fb);
            if constexpr (z == 67) { asm volatile("" :: "v"(sm)); VFMC(tm0, sm[0], mx); VFMC(tm1, sm[1], mx); }
            SCHED();
        };
        {
            u32 tmp;
            asm volatile("s_mov_b32 m0, %2\n\t" WAIT_TXT("%0", "%1") "buffer_load_dwordx4 %4, %5, %6 offen sc1 lds\n\tbuffer_load_dwordx4 %4, %5, %6 offen offset:1024 sc1 lds\n\t"
                         "s_mov_b32 m0, %3\n\ts_nop 0\n\tbuffer_load_dwordx4 %4, %5, %7 offen sc1 lds\n\ts_waitcnt vmcnt(0) lgkmcnt(0)\n\ts_barrier"
                         : "=&s"(tmp) : "s"(uu(tm - 1u)), "s"(dm), "s"(dm2), "v"(l16), "s"(srd), "s"(ms), "s"(ms2) : "vcc", "scc", "memory");
        }
#pragma unroll
        for (int ks = 0; ks < 4; ++ks) A[ks] = lds_r16(LB + C_MEM + ks * 1024);
        SCHED();
        st4s(rs, ofl, 0, sv);
        SCHED();
        static_for<0, 24>(slot);
        if (__builtin_expect(nanq != 0ull, 0)) {
            u32 mn = ~0u;
#pragma unroll
            for (int i = 0; i < 10; ++i) mn = min(min(min(mn, A[i].x ^ PSENT), A[i].y ^ PSENT), min(A[i].z ^ PSENT, A[i].w ^ PSENT));
            nanq = missed < SPIN_CAP ? __builtin_amdgcn_ballot_w64(mn == 0u) : 0ull;
        }
        if (__builtin_expect(nanq == 0ull, 1)) {
        static_for<24, 68>(slot);
        PV(7, 1); ones = lds_r16(ZP + 64); SCHED();
        VMAX(fa, tm0, tm1);
        if (__builtin_expect(__builtin_amdgcn_ballot_w64(fa > 24.0f) != 0ull, 0)) {
            const float up = fmaxf(max32(max16(fmaxf(tm0, tm1))), 0.f);
            tm0 -= up; tm1 -= up;
#pragma unroll
            for (int i = 0; i < 4; ++i) t[i] = __builtin_amdgcn_exp2f(__builtin_fmaf(sx[i], CEXP, mx) - up);
            e01 = pk(t[0], t[1]); e23 = pk(t[2], t[3]);
            asm volatile("s_nop 1" : "+v"(e01), "+v"(e23));
            QPERM(P.x, e01, "[0,1,0,1]"); QPERM(P.z, e01, "[2,3,2,3]"); QPERM(P.y, e23, "[0,1,0,1]"); QPERM(P.w, e23, "[2,3,2,3]");
            NOPS(1);
            MF0(oa, vx[0], P); MF0(ob, vx[1], P);
            NOPS(7);
        }
        SCHED();
        PV(1, 1); VEXP(tm0, tm0); SEL(oa[0], oa[0], ob[0]); SCHED();
        PV(8, 1); VEXP(tm1, tm1); SEL(oa[1], oa[1], ob[1]); SCHED();
        PV(9, 1); VCVT(pmm, tm0, tm1); SEL(oa[2], oa[2], ob[2]); SCHED();
        PV(2, 1);
        asm volatile("v_cndmask_b32 %0, %2, 0, %3\n\tv_cndmask_b32 %1, 0, %2, %3" : "=&v"(pm.x), "=&v"(pm.y) : "v"(pmm), "s"(himask)); SCHED();
        PV(3, 1); SEL(oa[3], oa[3], ob[3]); SCHED();
        MF0(rsum, ones, P); NOPS(4);
        VADD(fc, av1[0], av1[2]); VADD(fd, av1[1], av1[3]);
        { const v2u ones2 = ones.lo; MF1H(rsum, ones2, pm); }
        VCVT(vm.y, fc, fd); NOPS(1);
        MF1H(oa, vm, pm);
        {
            float inv; v4u oc;
            asm volatile("s_nop 3\n\tv_rcp_f32 %0, %1\n\ts_nop 2" : "=v"(inv) : "v"(rsum[0]), "v"(rsum));
            PIN(oa);
            const f32x2 i2 = {inv, inv};
            const f32x2 u0 = oa.lo * i2, u1 = oa.hi * i2;
            asm volatile("v_cvt_pk_bf16_f32 %0, %4, %5\n\tv_cvt_pk_bf16_f32 %2, %4, %5\n\tv_cvt_pk_bf16_f32 %1, %6, %7\n\tv_cvt_pk_bf16_f32 %3, %6, %7\n\t"
                         "v_permlane16_swap_b32 %0, %2\n\ts_nop 0\n\tv_permlane16_swap_b32 %1, %3"
                         : "=&v"(oc.x), "=&v"(oc.y), "=&v"(oc.z), "=&v"(oc.w) : "v"(u0.x), "v"(u0.y), "v"(u1.x), "v"(u1.y));
            st16(rs, oso, oc);
        }
        SCHED();
        unsigned long long clk;
        asm volatile("s_memrealtime %0" : "=s"(clk));
        HT(6);
        PIN(fl);
        v4u tq[4], tk[8], tv[2];
        f32x4 c0, c1, cm;
        u32 oo, kvo; bool kvok;
        u32 ln = l16; PIN(ln);
        lptr QX = lds + C_QX + (w & 1) * 4096 + ln, VO = lds + C_V + (w ^ 1) * 2048 + ln, VMX = lds + C_VM + w * 512 + (ln >> 1);
#pragma unroll
        for (int i = 0; i < 2; ++i) { tq[i] = lds_r16(QX + i * 1024); tk[2 * i] = lds_r16(LB + C_K + i * 1024); tk[2 * i + 1] = lds_r16(LB + C_K + 4096 + i * 1024); }
        *(LAS v2u*)VMX = vm;
        SCHED();
        SFOR(i, 12)
            if constexpr (i == 0) MF0(cm, kmf[0], tq[0]); else if constexpr (i == 1) MF0(c0, tk[0], tq[0]); else if constexpr (i == 2) MF0(c1, tk[1], tq[0]);
            else if constexpr (i % 3 == 0) MF1(cm, kmf[i / 3], tq[i / 3]); else if constexpr (i % 3 == 1) MF1(c0, tk[2 * (i / 3)], tq[i / 3]); else MF1(c1, tk[2 * (i / 3) + 1], tq[i / 3]);
            if constexpr (i == 0) { tq[2] = lds_r16(QX + 2048); tk[4] = lds_r16(LB + C_K + 2048); }
            if constexpr (i == 1) { tk[5] = lds_r16(LB + C_K + 6144); tq[3] = lds_r16(QX + 3072); }
            if constexpr (i == 2) { tk[6] = lds_r16(LB + C_K + 3072); tk[7] = lds_r16(LB + C_K + 7168); }
            if constexpr (i == 3) { tv[0] = lds_r16(VO); tv[1] = lds_r16(VO + 1024); }
            if constexpr (i == 4) { oo = ln + (ln & 0x300u) + ((vsrc ^ 8192u) + (u32)((w & 1) * 256)); PIN(oo); kb += NH * 16384; PINS(kb); }
            if constexpr (i == 5) { ksrc = kb + (((kmask >> ((s + 1) & 63)) & 1ull) ? 8192u : 0u); PINS(ksrc); }
            if constexpr (i == 6) { vsrc = kb + (((vmask >> ((s + 1) & 63)) & 1ull) ? W_VO - W_KQ + 8192u : W_VO - W_KQ); PINS(vsrc); }
            if constexpr (i == 7) { kvok = ((u32)__builtin_amdgcn_ballot_w64(fl < (u32)s + 2) & 7u) == 0u; }
            if constexpr (i == 8) { kvo = l16 | (kvok ? 0u : OOB); PIN(kvo); }
            SCHED();
        SEND
        asm volatile("s_waitcnt lgkmcnt(0)\n\ts_barrier" : "+s"(clk) :: "memory");
        const v2u tvm = *(LAS v2u*)(VMX + (512 - (w & 1) * 1024));
        const v4u pv = lds_r16(ZP + 80);
        DMA32(dk, kvo, ksrc);
        SCHED(); asm volatile("" :: "v"(cm));
        float u[10], m, m2;
        {
            float NEG = -1000000000.0f / SCALE; PIN(NEG);
#define MSK(d, a, i) asm volatile("v_cndmask_b32 %0, %1, %2, %3" : "=v"(d) : "v"(NEG), "v"(a), "s"(vis[i]))
#pragma unroll
            for (int i = 0; i < 4; ++i) MSK(u[i], c0[i], i);
#pragma unroll
            for (int i = 0; i < 4; ++i) MSK(u[4 + i], c1[i], 4 + i);
            asm volatile("v_max3_f32 %0, %2, %3, %10\n\tv_max3_f32 %1, %4, %5, %11\n\tv_max3_f32 %0, %0, %6, %7\n\tv_max3_f32 %1, %1, %8, %9\n\t"
                         "v_max_f32 %0, %0, %1\n\tv_mov_b32 %1, %0\n\ts_nop 1\n\tv_permlane16_swap_b32 %0, %1\n\tv_max_f32 %0, %0, %1\n\tv_mov_b32 %1, %0\n\ts_nop 1\n\t"
                         "v_permlane32_swap_b32 %0, %1\n\tv_max_f32 %0, %0, %1\n\tv_mul_f32 %0, 0xbe0293ee, %0"
                         : "=&v"(m), "=&v"(m2) : "v"(u[0]), "v"(u[1]), "v"(u[2]), "v"(u[3]), "v"(u[4]), "v"(u[5]), "v"(u[6]), "v"(u[7]), "v"(cm[0]), "v"(cm[1]));
        }
        DMA32(dv, kvo, vsrc);
        {
            v4u px; v2u p0, p1;
#define EX2(d, a, b) asm volatile("v_fmamk_f32 %0, %2, 0x3e0293ee, %4\n\tv_fmamk_f32 %1, %3, 0x3e0293ee, %4\n\tv_exp_f32 %0, %0\n\tv_exp_f32 %1, %1" : "=&v"(d.x), "=&v"(d.y) : "v"(a), "v"(b), "v"(m))
            f32x2 ex[5];
            EX2(ex[0], u[0], u[1]); EX2(ex[1], u[2], u[3]); EX2(ex[2], u[4], u[5]); EX2(ex[3], u[6], u[7]); EX2(ex[4], cm[0], cm[1]);
            asm volatile("v_cvt_pk_bf16_f32 %0, %8, %9\n\tv_cvt_pk_bf16_f32 %1, %10, %11\n\tv_cvt_pk_bf16_f32 %2, %12, %13\n\tv_cvt_pk_bf16_f32 %3, %14, %15\n\t"
                         "v_mov_b32 %5, 0\n\tv_mov_b32 %6, 0\n\tv_cvt_pk_bf16_f32 %4, %16, %17\n\tv_cvt_pk_bf16_f32 %7, %16, %17"
                         : "=&v"(px.x), "=&v"(px.y), "=&v"(px.z), "=&v"(px.w), "=&v"(p0.x), "=&v"(p0.y), "=&v"(p1.x), "=&v"(p1.y)
                         : "v"(ex[0].x), "v"(ex[0].y), "v"(ex[1].x), "v"(ex[1].y), "v"(ex[2].x), "v"(ex[2].y), "v"(ex[3].x), "v"(ex[3].y), "v"(ex[4].x), "v"(ex[4].y));
            f32x4 o[4], sum;
            const v2u ones2 = ones.lo;
            const u32 now = (u32)clk;
            u32 oz;
            SCHED();
            MF0(sum, ones, px); { const u32 q = ksrc ^ 8192u; DMA32(dq, kvo, q); } SCHED();
            MF0(o[0], vx[0], px);
            MF0(o[1], vx[1], px);
            MF0(o[2], tv[0], px); oz = (oso & ~(3u << 17)) | (u32)((s + 3) & 3) << 17; PIN(oz); SCHED();
            MF0(o[3], tv[1], px); oso = (oso & ~(3u << 17)) | (u32)((s + 1) & 3) << 17; PIN(oso); SCHED();
            MF1H(sum, ones2, p0);
            MF1H(o[0], vm, p0);
            MF1H(o[1], vm, p1); st16(rs, oz, pv); SCHED();
            MF1H(o[2], tvm, p0);
            MF1H(o[3], tvm, p1);
            float inv;
            asm volatile("s_nop 2\n\tv_rcp_f32 %0, %1\n\ts_nop 0" : "=v"(inv) : "v"(sum[0]), "v"(sum));
            asm volatile("" : "+v"(o[0]), "+v"(o[1]));
            st16(rs, oo, (v4u){pk2(o[0].lo * inv), pk2(o[0].hi * inv), pk2(o[1].lo * inv), pk2(o[1].hi * inv)});
            asm volatile("" : "+v"(o[2]), "+v"(o[3]));
            st16(rs, oo ^ 2048u, (v4u){pk2(o[2].lo * inv), pk2(o[2].hi * inv), pk2(o[3].lo * inv), pk2(o[3].hi * inv)});
            SCHED();
            if (__builtin_expect(h == 0 && w == 3, 0)) {
                st4s(rs, ln == 0 ? hint + 128u : OOB, 0, (u32)s << 16 | min(missed, 1u) << 14 | min(now - tm, 0x3FFFu));
                LST(L * 64 + s, now); LST(128 + L * 64 + s, ((int)(rdl(fl, 3) - now) >= CH_VIS - 4) | ((int)kvok << 1) | ((int)min(missed, 3u) << 2)); LST(640 + L * 64 + s, tm); LST(1728 + L * 64 + s, __builtin_amdgcn_s_memtime());
            }
            tm = rdl(fl, 5);
            missed = missed == SPIN_CAP ? missed : 0u;
            if (__builtin_expect(rdl(fl, 3) - now + CH_LATE > 4000u, 0)) {
#pragma unroll 1
                for (int n = 0; n < 64; ++n) {
                    CFENCE();
                    const u32 hv = ld4(rs, fkh);
                    tm = rdl(hv, 5);
                    if (rdl(hv, 3) - now + CH_LATE <= 4000u) break;
                }
            }
        }
        LS2(L == 0 && w == 0, h);
        if (__builtin_expect(!kvok, 0) && s + 1 < NSEG) {
            st4s(rs, (ln == 0 && w == 0) ? hint + 4096u + (u32)h * 128u : OOB, 0, (u32)s + 1u);
            wait_flags(rs, ln < 48u ? fkh : OOB, (u32)s + 2, dead);
            DMA32(dk, l16, ksrc); DMA32(dv, l16, vsrc); DMA32(dq, l16, ksrc ^ 8192u);
        }
        ++s;
        } else {
            u32 ln = l16; PIN(ln);
            const u32 hv = ld4(rs, ln == 96u ? foff(F_ERR) : fkh);
            tm = rdl(hv, 5);
            *(LAS u32*)(lds + C_DUMP + (w == 0 ? 0 : 64)) = rdl(hv, 6);
            BAR();
            missed = uu(*(LAS u32*)(lds + C_DUMP)) != 0u ? SPIN_CAP : missed + 1;
            st4s(rs, missed == SPIN_CAP ? foff(F_ERR) : OOB, 0, 1u);
            BAR();
        }
    }
    DRAIN(); BAR();
    st4s(rs, ofl, 0, NSEG);
}

#define WO_GAP 6
DEV void role_wo(const Params& p, lptr lds, int j) {
    const int tid = threadIdx.x, lane = tid & 63, w = uni(tid >> 6), g = lane >> 4, r = lane & 15;
#define WROT 0
    const int rg = j & 1, cg = j >> 1, rot = cg & WROT;
    const rsrc_t rs = mk_rsrc(p.ws, W_END);
    v8u wo[8][5];
    {
        const v4u so_ = mk_srd(p.Wo, DM * NQ * 2);
        u32 vo[5];
#pragma unroll
        for (int i = 0; i < 5; ++i) { const int n = 64 * i + lane; vo[i] = (u32)((n / 5) * DM + cg * 40 + (n % 5) * 8) * 2u; }
        auto fill = [&](int b) {
            const u32 dst = uu(w * 40960 + (b & 3) * 10240), src = uu((8 * w + ((b + rot) & 7)) * (DH * DM * 2));
#pragma unroll
            for (int i = 0; i < 10; ++i) DMAIN(so_, dst + i * 1024, vo[i % 5], src + (i / 5) * (64 * DM * 2));
        };
        __builtin_amdgcn_s_sleep(ST_WO);
#pragma unroll 1
        for (int b = 0; b < 4; ++b) fill(b);
        const int q4 = r >> 2;
        lptr tb = lds + w * 40960 + (32 * (g >> 1) + 16 * (q4 >> 1) + 8 * ((r >> 1) & 1) + 4 * (g & 1) + (q4 & 1)) * 80 + (r & 1) * 8;
        SFOR(b, 8)
            VMW(b < 5 ? 30 : 70 - 10 * b);
#pragma unroll
            for (int t = 0; t < 5; ++t) {
                lptr q = tb + (b & 3) * 10240 + t * 16;
                wo[b][t] = tr_frag(q, q + 160, q + 64 * 80, q + 64 * 80 + 160);
                if (b * 5 + t < 32) asm volatile("" : "+a"(wo[b][t])); else asm volatile("" : "+v"(wo[b][t]));
            }
            CFENCE();
            if constexpr (b < 4) fill(b + 4);
        SEND
    }
    BAR();
    EV(6);
    const u32 idx = (r & 2) ? 0xEEEEu : 0x4444u;
    bool dead = false;
    const u32 pol0 = get_pol(rs) & 1u;
    const u32 l16 = (u32)lane * 16u;
    u32 oo[8];
#pragma unroll
    for (int i = 0; i < 8; ++i) oo[i] = uu(W_ORING + ring_map((u32)(rg * 32768 + (8 * w + ((i + rot) & 7)) * 1024), pol0));
    const u32 hint = W_MRING + 4 * 32768u + pol0 * 8192u;
    u32 hvo[2];
#pragma unroll
    for (int l = 0; l < 2; ++l) hvo[l] = lane < 2 ? hint + (u32)(l ^ 1) * (MB / 4) + (u32)lane * 4u : lane == 2 ? hint + (u32)l * (MB / 4) + 128u : lane == 3 ? foff(F_ERR) :
                 (lane >= 32 && j == 0 && w == 3) ? hint + (u32)l * (MB / 4) + (u32)lane * 128u : OOB;
    lptr xw = r < 8 ? lds + w * 640 + (2 * (g & 1) + (g >> 1)) * 32 + r * 4 : lds + 8192 + lane * 4;
    const int G = ((5 * cg) >> 1) + w, tt = 2 * G + ((lane >> 3) & 1) - 5 * cg;
    const bool pub = w < 3 && lane < 32 && tt >= 0 && tt < 5;
    lptr xr = lds + (pub ? tt * 128 + (2 * (lane >> 4) + ((lane >> 2) & 1)) * 32 + (lane & 3) * 8 : 0);
    const u32 mpo = pub ? W_MRING + ring_map((u32)(G * 256 + rg * 128 + lane * 4), pol0) : OOB;
    wait_flags(rs, lane == 0 ? near_(foff(F_QKV), 0u, pol0) : OOB, 1u, dead);
    constexpr u32 PER0 = CH_W0 + CH_MW + CH_H0 + CH_MH + 2 * CH_VIS;
    const bool lead = j == 0 && w == 3;
    const u32 hso = (lead && lane == 0) ? hint : OOB;
    MK_SRD();
    u32 t = 0, tn = 0;
    if (j == 0) {
        for (int n = 0; n < SPIN_CAP && !dead; ++n) {
            CFENCE();
            if (all_(ld4(rs, W_ORING + pol0 * 8192u + (u32)w * 16384u + (u32)lane * 128u) != PSENT)) break;
            __builtin_amdgcn_s_sleep(1);
        }
        BAR();
    }
    if (lead) {
        t = rt32() + 40u; tn = t + PER0 / 2 + CH_REC;
        st16(rs, lane < 2 ? hint + (u32)lane * (MB / 4) : OOB, (v4u){lane == 0 ? t : tn, PER0, (lane == 0 ? t : tn) + (CH_W0 + CH_VIS + CH_MW + CH_REC), 0u});
    } else {
        for (int n = 0; n < SPIN_CAP && !dead; ++n) {
            CFENCE();
            const u32 hv = ld4(rs, hvo[1]);
            t = rdl(hv, 0);
            if (rdl(hv, 1) < 400u) break;
            __builtin_amdgcn_s_sleep(1);
        }
    }
    int sn[2] = {0, 0};
    u32 hav = 4 * CH_H0, wav = 4 * CH_W0, mh = 8 * CH_MH, mw = 8 * CH_MW, ok[2] = {0u, 0u}, dw[2] = {CH_W0 + CH_VIS + CH_MW + CH_REC, CH_W0 + CH_VIS + CH_MW + CH_REC};
    u32 zero = 0, hv = 0xFFFFu, pst = 1, pdur = 0, pmp = OOB;
    const u32 psent = PSENT;
    lptr sw = lds + 2560 + w * 4;
    bool stop = false;
    asm volatile("" : "+v"(zero), "+v"(sw));
    auto turn = [&](auto lc) __attribute__((always_inline)) {
        constexpr int L = decltype(lc)::value, M = L ^ 1;
        u32 now, done, rep, rd, lost, dh, dn, nt, za; v4u hw;
        const int sc = sn[L];
        auto books = [&](auto ic) __attribute__((always_inline)) {
            constexpr int i = decltype(ic)::value;
            if constexpr (i == 0) { rep = rdl(hv, 2); PINS(rep); }
            if constexpr (i == 1) { lost = (u32)(__builtin_amdgcn_ballot_w64(hv == (u32)sn[M]) >> 32); PINS(lost); }
            if constexpr (i == 2) { lost |= (rep >> 16) ^ (u32)sn[M]; PINS(lost); }
            if constexpr (i == 3) { lost |= ok[M] ^ 2u; rd = rep & 0xFFFFu; PINS(lost); PINS(rd); }
            if constexpr (i == 4) { lost = lost == 0u ? CH_UP * 8u : 0u; PINS(lost); }
            if constexpr (i == 5) { dn = wav >> 2; PINS(dn); pdur = min(pdur, dn + 8u); PINS(pdur); }
            if constexpr (i == 6) { pdur = (done & ok[M]) ? pdur : dn; PINS(pdur); }
            if constexpr (i == 7) { wav += pdur - dn; PINS(wav); }
            if constexpr (i == 8) { dh = hav >> 2; PINS(dh); nt = min(rd, dh + 8u); PINS(nt); }
            if constexpr (i == 9) { nt = rd < 0x3FFFu ? nt : dh; PINS(nt); }
            if constexpr (i == 10) { hav += nt - dh; PINS(hav); }
            if constexpr (i == 11) { mw = max(mw - 1u, CH_MW * 8u); PINS(mw); }
            if constexpr (i == 12) { mh = max(mh - 1u, CH_MH * 8u); PINS(mh); }
            if constexpr (i == 13) { rd = rd >> 14 == 1u ? lost : 0u; PINS(rd); }
            if constexpr (i == 14) { mw += rd; mh += lost - rd; PINS(mw); PINS(mh); }
            if constexpr (i == 15) { ok[M] = done ? 1u : (ok[M] & 1u) << 1; PINS(ok[M]); }
            if constexpr (i == 16) { dh = (2u * hav + mh) / 8u + CH_VIS; PINS(dh); }
            if constexpr (i == 17) { dn = (2u * wav + mw) / 8u + CH_VIS; PINS(dn); }
            if constexpr (i == 18) { nt = dw[L] + dh; PINS(nt); }
            if constexpr (i == 19) { dh += dn; dw[L] = dn; sn[M] += (int)done; PINS(dh); PINS(sn[M]); }
            if constexpr (i == 20) { rd = tn - t + (dh >> 1); PINS(rd); }
            if constexpr (i == 21) { rd = sn[M] + 1 < NSEG ? rd : 0u; PINS(rd); }
            if constexpr (i == 22) { nt = t + (u32)max((int)nt, (int)rd); PINS(nt); }
            if constexpr (i == 23) { hw.x = nt; hw.y = dh; PIN(hw.x); PIN(hw.y); }
            if constexpr (i == 24) { hw.z = nt + dn; hw.w = 0u; PIN(hw); }
            if constexpr (i == 25) st16(rs, hso + L * (MB / 4), hw);
        };
        if (__builtin_expect(sc + 1 < NSEG, 1)) {
            u32 vo = l16 + (L * (MB / 2) + (u32)(sc & 3) * 131072u), mp, hn;
            PIN(vo);
#define WT(k) do {} while (0)
            WT(0);
            v4u d[8];
            {
                u32 tmp;
                asm volatile(WAIT_TXT("%1", "%2") "buffer_load_dwordx4 %0, %3, %4, %5 offen sc1\n\ts_waitcnt lgkmcnt(0)"
                             : "=&v"(d[0]), "=&s"(tmp) : "s"(uu(t - 1u)), "v"(vo), "s"(srd), "s"(oo[0]) : "vcc", "scc", "memory");
            }
            SFOR(i, 7)
                asm volatile("buffer_load_dwordx4 %0, %1, %2, %3 offen sc1" : "=v"(d[i + 1]) : "v"(vo), "s"(srd), "s"(oo[i + 1]) : "memory");
                SCHED();
                if constexpr (i == 0) { done = uu(pst) ^ 1u; PINS(done); }
                static_for<3 * i, 3 * i + 3>(books);
                SCHED();
            SEND
            hn = ld4(rs, hvo[L]);
            SCHED();
            WT(1);
            static_for<21, 26>(books);
            za = done ? pmp : OOB;
            st4s(rs, za, 0, psent);
            *(LAS u32*)(sw + L * 4096) = zero;
            f32x4 acc[5] = {};
            mp = mpo + (L * (MB / 4) + (u32)((sc + 1) & 3) * 32768u); pmp = mpo + (L * (MB / 4) + (u32)(sc & 3) * 32768u);
            asm volatile("" : "+v"(acc[0]), "+v"(acc[1]), "+v"(acc[2]), "+v"(acc[3]), "+v"(acc[4]), "+v"(mp), "+v"(pmp));
            SCHED();
            WT(2);
            float f0[5], f1[5]; unsigned long long bad;
#define WSW(o_) asm volatile("v_permlane32_swap_b32 %0, %1" : "+v"(f0[o_]), "+v"(f1[o_]))
#define WAD(o_) asm volatile("v_add_f32_dpp %0, %1, %0 row_ror:8 row_mask:0xf bank_mask:0xf" : "+v"(f0[o_]) : "v"(f1[o_]))
#define WFO(o_) do { VADD(f0[o_], acc[o_][0], acc[o_][2]); VADD(f1[o_], acc[o_][1], acc[o_][3]); } while (0)
#define WWR(o_) *(LAS float*)(xw + L * 4096 + (o_) * 128) = f0[o_]
            asm volatile("s_waitcnt vmcnt(10)" : "+v"(d[0]));
            if (__builtin_expect(__builtin_amdgcn_ballot_w64(d[0].x == PSENT) != 0ull, 0)) {
                asm volatile("s_waitcnt vmcnt(3)" : "+v"(d[1]), "+v"(d[2]), "+v"(d[3]), "+v"(d[4]), "+v"(d[5]), "+v"(d[6]), "+v"(d[7]));
#pragma unroll
                for (int i = 0; i < 5; ++i) *(LAS u32*)(xw + L * 4096 + i * 128) = psent;
                *(LAS u32*)(sw + L * 4096) = 1u;
            } else {
            SFOR(i, 40)
                constexpr int pc = i < 30 ? i / 5 : 6 + (i & 1), oc = i < 30 ? i % 5 : (i - 30) >> 1;
                if constexpr (oc == 0 && pc > 0) asm volatile("s_waitcnt vmcnt(%1)" : "+v"(d[pc]) : "i"(10 - pc));
                if constexpr (pc * 5 + oc < 32) SMF_A(acc[oc], d[pc], wo[pc][oc]); else SMF_V(acc[oc], d[pc], wo[pc][oc]);
                if constexpr (i == 0) WT(3);
                if constexpr (i == 20) WT(4);
                if constexpr (i == 31 || i == 33) NOPS(1);
                if constexpr (i == 32) NOPS(0);
                if constexpr (i == 34) WFO(0);
                if constexpr (i == 35) { asm volatile("v_cmp_u_f32 %0, %1, %2" : "=s"(bad) : "v"(f0[0]), "v"(f1[0])); WSW(0); }
                if constexpr (i == 36) WFO(1);
                if constexpr (i == 37) { WAD(0); WSW(1); }
                if constexpr (i == 38) { WWR(0); SCHED(); WFO(2); }
                if constexpr (i == 39) { WAD(1); WSW(2); }
                SCHED();
            SEND
            WT(5);
            WWR(1); SCHED(); WFO(3); WAD(2); WWR(2); SCHED(); WSW(3);
            if (__builtin_expect(bad != 0ull, 0)) {
                u32 mn = ~0u;
#pragma unroll
                for (int i = 0; i < 8; ++i) mn = min(mn, d[i].x ^ PSENT);
                if (all_(mn != 0u)) {
#pragma unroll
                    for (int i = 0; i < 8; ++i) mn = min(min(mn, d[i].y ^ PSENT), min(d[i].z ^ PSENT, d[i].w ^ PSENT));
                }
                *(LAS u32*)(sw + L * 4096) = all_(mn != 0u) ? 0u : 1u;
            }
            WFO(4); WAD(3); WWR(3); SCHED();
            asm volatile("v_permlane32_swap_b32 %0, %1\n\ts_nop 1\n\tv_add_f32_dpp %0, %1, %0 row_ror:8 row_mask:0xf bank_mask:0xf" : "+v"(f0[4]), "+v"(f1[4]));
            WWR(4);
            }
            WT(6);
            f32x2 a, b, c, e; u32 cw, mq; v4u sv;
            asm volatile("s_waitcnt lgkmcnt(0)\n\ts_barrier\n\tds_read_b64 %0, %5 offset:%7\n\tds_read_b64 %1, %5 offset:%7+640\n\tds_read_b64 %2, %5 offset:%7+1280\n\tds_read_b64 %3, %5 offset:%7+1920\n\t"
                         "ds_read_b128 %4, %6 offset:%7+2560\n\t"
                         "s_waitcnt lgkmcnt(3)\n\tv_pk_add_f32 %0, %0, %1\n\ts_waitcnt lgkmcnt(1)\n\tv_pk_add_f32 %2, %2, %3\n\ts_waitcnt lgkmcnt(0)\n\tv_pk_add_f32 %0, %0, %2"
                         : "=&v"(a), "=&v"(b), "=&v"(c), "=&v"(e), "=&v"(sv) : "v"(xr), "v"(zero), "i"(L * 4096) : "memory");
            asm volatile("v_cmp_u_f32 vcc, %2, %3\n\tv_cvt_pk_bf16_f32 %0, %2, %3\n\tv_cndmask_b32 %1, %4, %5, vcc" : "=&v"(cw), "=&v"(mq) : "v"(a.x), "v"(a.y), "v"(mp), "v"(OOB) : "vcc");
            st4s(rs, mq, 0, cw);
            SCHED();
            now = rt32();
            PIN(hn); hv = hn;
            WT(7);
            pst = sv.x | sv.y | sv.z | sv.w;
            if (__builtin_expect(__builtin_amdgcn_ballot_w64(mq != mp && pst == 0u) != 0ull, 0)) st4s(rs, mp, 0, cw);
        } else {
            done = uu(pst) ^ 1u;
            st4s(rs, done ? pmp : OOB, 0, psent);
            static_for<0, 25>(books);
            pst = 1;
            hv = ld4(rs, hvo[L]);
            PIN(hv);
            now = rt32();
        }
        pdur = now - t;
        const u32 per = rdl(hv, 1), th = rdl(hv, 0);
        t = lead ? tn : (int)(th - now) < -(int)(per >> 1) ? th + per : th;
        tn = nt;
        dw[M] += max((int)(now + CH_WX - t), 0);
        stop = stop || rdl(hv, 3) != 0u;
    };
    for (int k = 0; k < 2 * SPIN_CAP && !stop && (sn[0] + 1 < NSEG || sn[1] + 1 < NSEG); ++k) { turn(IC<0>{}); turn(IC<1>{}); }
    if (sn[0] + 1 < NSEG || sn[1] + 1 < NSEG) st4s(rs, tid == 0 ? foff(F_ERR) : OOB, 0, 1u);
}

__global__ void __launch_bounds__(256, 1) k_main(Params p) {
    lptr lds = (lptr)(u32)16 - 16;
    if (blockIdx.x == 0) LST(256, rt32());
    unsigned long long pc; int len;
    asm volatile("s_getpc_b64 %0\n.Lk_main_here:\n\ts_mov_b32 %1, .Lk_init_here-.Lk_main_here" : "=s"(pc), "=s"(len));
    const v4u code = ldin16(mk_rsrc((const void*)(pc & ~15ull), (u32)max(len, 0)), (((blockIdx.x >> 3) & 31u) * 256u + threadIdx.x) * 16u, 0);
    CFENCE();
    if (threadIdx.x == 0) {
        GAS u32* ctl = (GAS u32*)(p.ws + W_CTRL);
        const u32 xcc = (u32)__builtin_amdgcn_s_getreg(20 | (3 << 11)) & 7u;
        u32 role = xcc * 32u + __hip_atomic_fetch_add(ctl + (F_TK + xcc) * 32, 1u, __ATOMIC_RELAXED, __HIP_MEMORY_SCOPE_AGENT);
        if (role >= xcc * 32u + 32u) {
            u32 k = __hip_atomic_fetch_add(ctl + F_OVF * 32, 1u, __ATOMIC_RELAXED, __HIP_MEMORY_SCOPE_AGENT);
            for (int n = 0; n < SPIN_CAP; ++n) {
                u32 tot = 0;
                for (int j = 0; j < 8; ++j) tot += __hip_atomic_load(ctl + (F_TK + j) * 32, __ATOMIC_RELAXED, __HIP_MEMORY_SCOPE_AGENT);
                if (tot >= 256u) break;
                __builtin_amdgcn_s_sleep(2);
            }
            role = 255u;
            for (int j = 0; j < 8; ++j) {
                const u32 cn = __hip_atomic_load(ctl + (F_TK + j) * 32, __ATOMIC_RELAXED, __HIP_MEMORY_SCOPE_AGENT);
                const u32 fr = cn < 32u ? 32u - cn : 0u;
                if (k < fr) { role = (u32)j * 32u + cn + k; break; }
                k -= fr;
            }
        }
        *(LAS u32*)lds = role;
    }
    __syncthreads();
    const int role = uni((int)*(LAS u32*)lds), x = role >> 5, i = role & 31;
    asm volatile("" :: "v"(code));
    __syncthreads();
    LST(8192 + blockIdx.x * 16, role);
    if (x < 2) { role_head(p, lds, i >> 4, x * 16 + (i & 15)); __builtin_amdgcn_endpgm(); }
    if (x == 2) { role_wo(p, lds, i); __builtin_amdgcn_endpgm(); }
    const int no = x == 3 ? 30 : x < 6 ? 5 : 0;
    if (x > 3 && i >= 8) { role_qkv(p, lds, (x - 4) * 24 + i - 8); __builtin_amdgcn_endpgm(); }
    const bool isout = i < no;
    const int og = x == 3 ? i / 5 : x + 2, ng = i % 5, L = og >> 2, kq = og & 3;
    const int k = i - no, w = uni(threadIdx.x >> 6);
    const int j = isout ? (L == 1 ? 0 : 44) + kq * 5 + ng : x == 3 ? 36 + k : k < 2 ? 12 + 2 * x + k : x < 6 ? 24 + x : k < 5 ? 10 + 3 * x + k : 15 + 3 * x + k;
    const rsrc_t rs = mk_rsrc(p.ws, W_END);
    if (j < NKV) {
        const int nt = j < 320 - 7 * NKV ? 8 : 7;
#pragma unroll 1
        for (int q = 0; q < 4; ++q) kv_fetch(p, j + NKV * q, uu(q * KT_BYTES));
        Filt f;
#pragma unroll 1
        for (int q = 0; q < nt; ++q) {
            VMW(15); BAR();
            if (q == 0) EV(2);
            kv_fetch(p, j + NKV * (q + 4), uu(((q + 4) & 7) * KT_BYTES));
            kv_tile(p, rs, lds + q * KT_BYTES, lds + (q == 0 ? 8 : q - 1) * KT_BYTES, j + NKV * q);
        }
        filt_load(p, j, f);
        VMW(10); BAR();
        st4s(rs, threadIdx.x == 0 ? foff(F_KV + j) : OOB, 0, 1u);
        EV(4);
        conv_job<0>(p, rs, lds, j, f, 0);
        if (j >= 36) filt_load(p, 28 + j, f);
        DRAIN(); BAR();
        st4s(rs, threadIdx.x == 0 ? foff(F_CV + j) : OOB, 0, 2u);
        if (j >= 36) {
#pragma unroll 1
            for (int g = 28 + j; g < 160; g += 8) conv_job<2>(p, rs, lds, g, f, min(g + 8, 159));
            DRAIN(); BAR();
            st4s(rs, threadIdx.x == 0 ? foff(F_CV + 124 + j) : OOB, 0, 3u);
            EV(15);
            __builtin_amdgcn_endpgm();
        }
    }
    if (!isout) { if (j < 28) role_row<0>(p, lds, (j - 20) * 4 + w); else role_row<1>(p, lds, (j - 28) * 4 + w); __builtin_amdgcn_endpgm(); }
    role_out(p, lds, L, kq, ng);
}

__global__ void __launch_bounds__(256) k_init(unsigned char* ws) {
    __shared__ int vote[4];
    asm volatile(".Lk_init_here:");
    const int c = blockIdx.x, lane = threadIdx.x & 63, w = uni(threadIdx.x >> 6);
    const rsrc_t rs = mk_rsrc(ws, W_END);
    const u32 k = (u32)(c & 15) < NREG ? (u32)(c & 15) : 0u;
    const u32 off0 = k * (16 * MB) + 2 * MB - 65536 + ((u32)(c & 15) >= NREG ? (u32)(c & 3) + 1u : 0u) * 256 + (u32)(c >> 4) * 128 + (u32)w * 16384;
    st4s(rs, lane < 8 ? off0 + (u32)lane * 2048 : OOB, 0, 0u); DRAIN();
    int d0 = 1 << 16, d1 = 1 << 16;
#pragma unroll 1
    for (int q = 0; q < 5; ++q) {
        const unsigned long long t0 = __builtin_amdgcn_s_memtime();
        const u32 v = ld4(rs, lane == 0 ? off0 + (u32)((q ^ w) & 1) * 8192 + (u32)(q >> 1) * 2048 : OOB);
        asm volatile("s_waitcnt vmcnt(0)" :: "v"(v) : "memory");
        const int dt = (int)(__builtin_amdgcn_s_memtime() - t0);
        if (q & 1) d1 = min(d1, dt); else d0 = min(d0, dt);
    }
    const u32 xcc = (u32)__builtin_amdgcn_s_getreg(20 | (3 << 11)) & 0xfu;
    if (lane == 0) vote[w] = min(max((d0 - d1) * ((((xcc >> 2) ^ (u32)w) & 1u) ? -1 : 1), -250), 250);
    static_assert(2 * F_N <= 32 * 64, "clear");
    const u32 o = (u32)(c * 64 + lane);
    st4s(rs, w == 0 && (o < F_POL || o >= 64u) ? W_CTRL + o * 128u : OOB, 0, 0u);
    __syncthreads();
    st4s(rs, threadIdx.x == 0 ? foff(F_POL + c) : OOB, 0, (u32)((1 << 20) + (vote[0] + vote[1] + vote[2] + vote[3]) / 4));
}

extern "C" void kernel_launch(void* const* d_in, const int* in_sizes, int n_in, void* d_out, int out_size, void* d_ws, size_t ws_size,
                              hipStream_t stream) {
    Params p;
    p.x = (const u16*)d_in[0]; p.mem0 = (const u16*)d_in[1]; p.Wq = (const u16*)d_in[2]; p.Wk = (const u16*)d_in[3];
    p.Wv = (const u16*)d_in[4]; p.Wo = (const u16*)d_in[5];
    p.bk = (const u16*)d_in[8]; p.bv = (const u16*)d_in[9]; p.shift = (const u16*)d_in[10]; p.ssmk = (const u16*)d_in[11]; p.ssmD = (const u16*)d_in[12];
    p.out = (u16*)d_out;
    p.ws = (unsigned char*)(((size_t)d_ws + 2 * MB - 1) & ~(size_t)(2 * MB - 1));
    (void)hipFuncSetAttribute((const void*)k_main, hipFuncAttributeMaxDynamicSharedMemorySize, LDS_BYTES);
    hipLaunchKernelGGL(k_init, dim3(32), dim3(256), 0, stream, p.ws);
    hipLaunchKernelGGL(k_main, dim3(256), dim3(256), LDS_BYTES, stream, p);
}
```

```cpp
#include <hip/hip_runtime.h>
#include <hip/hip_bf16.h>

typedef unsigned short u16;
typedef unsigned int u32;
typedef u32 v2u __attribute__((ext_vector_type(2)));
typedef u32 v4u __attribute__((ext_vector_type(4)));
typedef u32 v8u __attribute__((ext_vector_type(8)));
typedef float f32x2 __attribute__((ext_vector_type(2)));
typedef float f32x4 __attribute__((ext_vector_type(4)));
typedef float f32x16 __attribute__((ext_vector_type(16)));
typedef __bf16 bf16x2 __attribute__((ext_vector_type(2)));
typedef __bf16 bf16x8 __attribute__((ext_vector_type(8)));
typedef short s16x4 __attribute__((ext_vector_type(4)));
#define LAS __attribute__((address_space(3)))
#define GAS __attribute__((address_space(1)))
typedef LAS unsigned char* lptr;
#define DEV __device__ __forceinline__

constexpr int SEQ = 2048, DM = 640, NH = 32, DH = 128, NSEG = 64, NQ = NH * DH;

constexpr u32 MB = 1u << 20;
constexpr u32 W_ORING = 0;
constexpr u32 W_MRING = MB;
constexpr u32 W_CTRL = 4 * MB;
constexpr u32 ACT = SEQ * DM * 2;
constexpr u32 H_TILE = 128 * 1024;
constexpr u32 W_H2 = 8 * MB;
constexpr u32 W_KQ = 16 * MB;
constexpr u32 W_VO = W_KQ + 64 * MB;
constexpr u32 W_P = W_VO + 64 * MB;
constexpr u32 P_ITEM = 512 * 1024;
constexpr u32 W_H1 = W_P + 44 * MB, W_Y = W_H1 + 8 * MB, W_X2 = W_Y + ACT;
constexpr u32 W_KVT = W_X2 + ACT;
constexpr u32 W_END = W_KVT + 3 * MB;
constexpr u32 NREG = 12;
static_assert((size_t)W_END + 2 * MB <= 256ull * MB && W_H1 >= (NREG - 1) * 16 * MB + 2 * MB && W_H1 <= NREG * 16 * MB, "workspace");
constexpr u32 F_ERR = 0, F_OVF = 2, F_TK = 8, F_POL = 32, F_QKV = 64, F_O = F_QKV + 192, F_P = F_O + 64, F_H2 = F_P + 64, F_LN = F_H2 + 64, F_RG = F_LN + 96, F_KV = F_RG + 32,
              F_CV = F_KV + 64, F_N = F_CV + 192;
DEV u32 foff(u32 idx) { return W_CTRL + (idx >> 6) * 16384u + (idx & 63u) * 128u; }
DEV u32 near_(u32 o, u32 side, u32 pol) { return o + ((side ^ (pol >> (o >> 24)) ^ (o >> 21)) & 1u) * 8192u; }
DEV u32 p_item(int s) { return W_P + (u32)s * P_ITEM; }

constexpr u32 OOB = 0x80000000u;
constexpr u32 PSENT = 0x7F817F81u;
constexpr int SPIN_CAP = 40000;
constexpr int LDS_BYTES = 160 * 1024;
#define ST_HEAD 0
#define ST_WO 0

#define EV(k) do {} while (0)
#define LST(idx, v) do {} while (0)
#define LS2(cond, idx) do {} while (0)
struct Params {
    const u16 *x, *mem0, *Wq, *Wk, *Wv, *Wo, *bk, *bv, *shift, *ssmk, *ssmD;
    u16* out;
    unsigned char* ws;
};

typedef __amdgpu_buffer_rsrc_t rsrc_t;
DEV rsrc_t mk_rsrc(const void* p, u32 bytes) { return __builtin_amdgcn_make_buffer_rsrc((void*)p, 0, bytes, 0x00020000); }
DEV v4u ld16s(rsrc_t r, u32 off, u32 so) { return __builtin_amdgcn_raw_buffer_load_b128(r, off, so, 16); }
DEV v2u ld8s(rsrc_t r, u32 off, u32 so) { return __builtin_amdgcn_raw_buffer_load_b64(r, off, so, 16); }
DEV v4u ldin16(rsrc_t r, u32 off, u32 so) { return __builtin_amdgcn_raw_buffer_load_b128(r, off, so, 0); }
DEV v2u ldin8(rsrc_t r, u32 off, u32 so) { return __builtin_amdgcn_raw_buffer_load_b64(r, off, so, 0); }
DEV u32 ld4(rsrc_t r, u32 off) { return __builtin_amdgcn_raw_buffer_load_b32(r, off, 0, 16); }
DEV void st16(rsrc_t r, u32 off, v4u v) { __builtin_amdgcn_raw_buffer_store_b128(v, r, off, 0, 16); }
DEV void st8s(rsrc_t r, u32 off, u32 so, v2u v) { __builtin_amdgcn_raw_buffer_store_b64(v, r, off, so, 16); }
DEV void st4s(rsrc_t r, u32 off, u32 so, u32 v) { __builtin_amdgcn_raw_buffer_store_b32(v, r, off, so, 16); }
#define DRAIN() asm volatile("s_waitcnt vmcnt(0)" ::: "memory")
#define VMW(n) asm volatile("s_waitcnt vmcnt(%0)" :: "i"(n) : "memory")
#define BAR() asm volatile("s_waitcnt lgkmcnt(0)\n\ts_barrier" ::: "memory")
#define SCHED() __builtin_amdgcn_sched_barrier(0)
#define CFENCE() asm volatile("" ::: "memory")
#define NOPS(n) asm volatile("s_nop %0" :: "i"(n))
#define PIN(x) asm volatile("" : "+v"(x))
#define PINS(x) asm volatile("" : "+s"(x))

template <int V> struct IC { static constexpr int value = V; };
template <int I, int N, class F> DEV void static_for(F&& f) { if constexpr (I < N) { f(IC<I>{}); static_for<I + 1, N>(f); } }
#define SFOR(var, n) static_for<0, n>([&](auto ic_) __attribute__((always_inline)) { constexpr int var = decltype(ic_)::value;
#define SEND });

DEV u32 pk(float a, float b) { bf16x2 v = {(__bf16)a, (__bf16)b}; return __builtin_bit_cast(u32, v); }
DEV float lo_f(u32 u) { return __builtin_bit_cast(float, u << 16); }
DEV float hi_f(u32 u) { return __builtin_bit_cast(float, u & 0xffff0000u); }
DEV f32x2 up2(u32 u) { return (f32x2){lo_f(u), hi_f(u)}; }
DEV u32 pk2(f32x2 v) { return pk(v.x, v.y); }
DEV float rbf(float a) { return (float)(__bf16)a; }
DEV v4u lds_r16(lptr p) { return *(LAS v4u*)p; }
DEV void lds_w16(lptr p, v4u v) { *(LAS v4u*)p = v; }
DEV v2u lds_tr(lptr p) { return __builtin_bit_cast(v2u, __builtin_amdgcn_ds_read_tr16_b64_v4i16((LAS s16x4*)p)); }
DEV v4u tr2(lptr p0, lptr p1) { const v2u a = lds_tr(p0), b = lds_tr(p1); return (v4u){a.x, a.y, b.x, b.y}; }
DEV v8u cat8(v4u a, v4u b) { return __builtin_shufflevector(a, b, 0, 1, 2, 3, 4, 5, 6, 7); }
DEV f32x4 mfma16(v4u a, v4u b, f32x4 c) { return __builtin_amdgcn_mfma_f32_16x16x32_bf16(__builtin_bit_cast(bf16x8, a), __builtin_bit_cast(bf16x8, b), c, 0, 0, 0); }
DEV f32x16 mfma32(v4u a, v4u b, f32x16 c) { return __builtin_amdgcn_mfma_f32_32x32x16_bf16(__builtin_bit_cast(bf16x8, a), __builtin_bit_cast(bf16x8, b), c, 0, 0, 0); }

DEV float f_(u32 u) { return __builtin_bit_cast(float, u); }
DEV u32 u_(float f) { return __builtin_bit_cast(u32, f); }
template <int CTRL> DEV float dpp(float v) { return f_((u32)__builtin_amdgcn_update_dpp(0, (int)u_(v), CTRL, 0xf, 0xf, false)); }
DEV float max16(float v) { auto r = __builtin_amdgcn_permlane16_swap(u_(v), u_(v), false, false); return fmaxf(f_(r[0]), f_(r[1])); }
DEV float max32(float v) { auto r = __builtin_amdgcn_permlane32_swap(u_(v), u_(v), false, false); return fmaxf(f_(r[0]), f_(r[1])); }
DEV float add16(float v) { auto r = __builtin_amdgcn_permlane16_swap(u_(v), u_(v), false, false); return f_(r[0]) + f_(r[1]); }
DEV float add32(float v) { auto r = __builtin_amdgcn_permlane32_swap(u_(v), u_(v), false, false); return f_(r[0]) + f_(r[1]); }
DEV float wave_sum(float v) {
    v += dpp<0xB1>(v);
    v += dpp<0x4E>(v);
    v += dpp<0x124>(v);
    v += dpp<0x128>(v);
    return add32(add16(v));
}
DEV bool all_(bool c) { return __builtin_amdgcn_ballot_w64(!c) == 0ull; }
DEV int uni(int v) { return __builtin_amdgcn_readfirstlane(v); }
DEV u32 uu(u32 v) { return (u32)__builtin_amdgcn_readfirstlane((int)v); }
DEV u32 rdl(u32 v, int l) { return (u32)__builtin_amdgcn_readlane((int)v, l); }
DEV u32 rt32() { return (u32)__builtin_amdgcn_s_memrealtime(); }

DEV void wait_flags(rsrc_t rs, u32 off, u32 want, bool& dead) {
    if (dead) return;
#pragma unroll 1
    for (int n = 0;; ++n) {
        CFENCE();
        const u32 v = ld4(rs, off);
        if (all_(off >= OOB || v >= want)) break;
        __builtin_amdgcn_s_sleep(1);
        if ((n & 63) == 63) { if (uni((int)ld4(rs, foff(F_ERR))) != 0) { dead = true; break; } }
        if (n > SPIN_CAP) { st4s(rs, foff(F_ERR), 0, 1u); dead = true; break; }
    }
    CFENCE();
}

DEV u32 pol_off() { const int lane = threadIdx.x & 63; return lane < 32 ? foff(F_POL + lane) : OOB; }
DEV u32 pol_of(u32 raw) {
    const int lane = threadIdx.x & 63;
    const float v = add16(__builtin_amdgcn_fmed3f((float)((int)raw - (1 << 20)), -250.f, 250.f));
    const float lo = dpp<0x121>(v), hi = dpp<0x12F>(v), ex = dpp<0x122>(v) + dpp<0x123>(v) + dpp<0x124>(v);
    const float sum = v + lo + ((lane & 15) == 11 ? 0.f : hi) + ((lane & 15) == 0 ? ex : 0.f);
    return (u32)__builtin_amdgcn_ballot_w64(sum >= 0.f) & 0xFFFFu;
}
DEV u32 get_pol(rsrc_t rs) { return pol_of(ld4(rs, pol_off())); }
DEV u32 hblk(u32 base, int ls, int h, u32 side, u32 pol) { return near_(base + (u32)(ls * NH + h) * 16384u, side, pol); }
DEV f32x2 ln_stats(const f32x2 (&x)[5]) {
    const float c = f_(uu(u_(x[0].x)));
    f32x2 d[5];
#pragma unroll
    for (int i = 0; i < 5; ++i) d[i] = x[i] - c;
    const f32x2 s2 = (d[0] + d[1]) + (d[2] + d[3]) + d[4], q = (d[0] * d[0] + d[1] * d[1]) + (d[2] * d[2] + (d[3] * d[3] + d[4] * d[4]));
    const float dm = wave_sum(s2.x + s2.y) * (1.0f / DM), mean = c + dm;
    const float var = fmaxf(wave_sum(q.x + q.y) * (1.0f / DM) - dm * dm, 0.f);
    const float sd = rbf(__builtin_amdgcn_sqrtf(rbf(rbf(var) + 1e-5f)));
    return (f32x2){rbf(mean), __builtin_amdgcn_rcpf(sd)};
}
DEV void store_h(rsrc_t rs, u32 tile, int row, const u32 (&hp)[5]) {
    const u32 lane = threadIdx.x & 63;
    const u32 sw = (u32)(row & 15) * 16u, o8 = (lane >> 5) * 16384u + (((lane & 31u) * 8u) ^ sw), hb = tile + (u32)(row & 31) * 256u;
    st8s(rs, o8, hb, (v2u){hp[0], hp[1]});
    st8s(rs, o8, hb + 32768, (v2u){hp[2], hp[3]});
    st4s(rs, (lane * 4) ^ sw, hb + 65536, hp[4]);
}

#define DMA16(ldsb, vo, so) asm volatile("s_mov_b32 m0, %0\n\ts_nop 0\n\tbuffer_load_dwordx4 %1, %2, %3 offen sc1 lds" :: "s"(ldsb), "v"(vo), "s"(srd), "s"(so) : "memory")
#define DMA16I(ldsb, lo, vo, so, go) do { u32 so_; asm volatile("s_add_u32 m0, %1, %5\n\ts_add_u32 %0, %4, %6\n\tbuffer_load_dwordx4 %2, %3, %0 offen sc1 lds" \
                         : "=&s"(so_) : "s"(ldsb), "v"(vo), "s"(srd), "s"(so), "i"(lo), "i"(go) : "memory", "scc"); } while (0)
#define DMA32(ldsb, vo, so) asm volatile("s_mov_b32 m0, %0\n\ts_nop 0\n\tbuffer_load_dwordx4 %1, %2, %3 offen sc1 lds\n\tbuffer_load_dwordx4 %1, %2, %3 offen offset:1024 sc1 lds" \
                         :: "s"(ldsb), "v"(vo), "s"(srd), "s"(so) : "memory")
#define INPOL "nt"
#define INAUX 2
#define DMANT(sr, ldsb, vo, so) asm volatile("s_mov_b32 m0, %0\n\ts_nop 0\n\tbuffer_load_dwordx4 %1, %2, %3 offen " INPOL " lds" :: "s"(ldsb), "v"(vo), "s"(sr), "s"(so) : "memory")
#define DMAIN(sr, ldsb, vo, so) asm volatile("s_mov_b32 m0, %0\n\ts_nop 0\n\tbuffer_load_dwordx4 %1, %2, %3 offen lds" :: "s"(ldsb), "v"(vo), "s"(sr), "s"(so) : "memory")
DEV v4u mk_srd(const void* q, u32 bytes) { return (v4u){uu((u32)(size_t)q), uu((u32)((size_t)q >> 32) & 0xffffu), bytes, 0x00020000u}; }
#define MK_SRD() const v4u srd = mk_srd(p.ws, W_END)

constexpr int CV_PAD = 40;
constexpr int CV_PL = 4416;
constexpr int CV_F = 8 * CV_PL;
constexpr int CV_KB = 80;
constexpr int CV_KV = 65 * CV_KB;
constexpr int CV_CH = CV_F + CV_KV;
static_assert(CV_CH % 16 == 0 && 4 * CV_CH <= LDS_BYTES, "conv lds");

struct Filt { v4u a[5], b[5]; };
DEV int chan(int grp, int w) { return 16 * (grp >> 2) + (grp & 3) + 4 * w; }
template <int N = 5> DEV void filt_load(const Params& p, int grp, Filt& f) {
    const int tid = threadIdx.x, lane = tid & 63, d = chan(grp, tid >> 6);
    const rsrc_t rf = mk_rsrc(p.ssmk, ACT);
#pragma unroll
    for (int i = 0; i < N; ++i) {
        const u32 s0 = (u32)(8 * (lane + 64 * i) - CV_PAD);
        f.a[i] = __builtin_amdgcn_raw_buffer_load_b128(rf, s0 < SEQ ? (u32)(d * SEQ + s0) * 2u : OOB, 0, INAUX);
        f.b[i] = __builtin_amdgcn_raw_buffer_load_b128(rf, s0 + 8 < SEQ ? (u32)(d * SEQ + s0 + 8) * 2u : OOB, 0, INAUX);
    }
}
constexpr int KT_BYTES = 18 * 1024, KT_X = 64 * 144, NKV = 44, KT_DUMP = 8 * KT_BYTES + KT_X;
DEV void kv_fetch(const Params& p, int tile, u32 dst) {
    const int lane = threadIdx.x & 63, w = uni(threadIdx.x >> 6);
    const v4u sk = mk_srd(p.bk, ACT), sv = mk_srd(p.bv, ACT), ss = mk_srd(w == 0 ? p.bk : p.shift, w == 0 ? ACT : DM * 8);
    const u32 vo = tile < 320 ? (u32)((lane >> 3) * DM + (tile % 10) * 64 + (lane & 7) * 8) * 2u : OOB, r0 = uu((u32)((tile / 10) * 64 + 8 * w) * (DM * 2)), d = uu(dst + w * 1024);
    DMANT(sk, d + 1024, vo, r0); DMANT(sk, d + 5120, vo, r0 + 32 * DM * 2); DMANT(sv, d + 9216, vo, r0); DMANT(sv, d + 13312, vo, r0 + 32 * DM * 2);
    DMAIN(ss, uu(w == 0 ? dst : w == 1 ? dst + 17408 : KT_DUMP), w == 0 ? (tile >= 10 ? vo : OOB) : w == 1 && lane < 32 ? (u32)lane * 16u : OOB,
          uu(w == 0 ? (tile >= 10 ? r0 - 8 * DM * 2 : 0u) : (u32)(tile % 10) * 512u));
}
DEV void kv_tile(const Params& p, rsrc_t rs, lptr src, lptr xt, int tile) {
    const int tid = threadIdx.x;
    const v4u sv = lds_r16(src + 17408 + (tid & 31) * 16);
    lptr kp = src + ((tid >> 5) * 8 + 5) * 128 + (tid & 31) * 4;
    f32x2 kf[11];
#pragma unroll
    for (int i = 0; i < 11; ++i) kf[i] = up2(*(LAS u32*)(kp + i * 128));
    const f32x2 sh[4] = {{lo_f(sv.x), lo_f(sv.z)}, {hi_f(sv.x), hi_f(sv.z)}, {lo_f(sv.y), lo_f(sv.w)}, {hi_f(sv.y), hi_f(sv.w)}};
    u32 kv[8];
#pragma unroll
    for (int i = 0; i < 8; ++i) {
        const f32x2 acc = (kf[i] * sh[3] + kf[i + 1] * sh[2]) + (kf[i + 2] * sh[1] + kf[i + 3] * sh[0]);
        kv[i] = pk2(acc * up2(*(LAS u32*)(kp + 9216 + (i - 5) * 128)));
    }
    lds_w16(xt + (2 * (tid & 31)) * 144 + (tid >> 5) * 16, (v4u){__builtin_amdgcn_perm(kv[6], kv[7], 0x05040100u), __builtin_amdgcn_perm(kv[4], kv[5], 0x05040100u),
                                                             __builtin_amdgcn_perm(kv[2], kv[3], 0x05040100u), __builtin_amdgcn_perm(kv[0], kv[1], 0x05040100u)});
    lds_w16(xt + (2 * (tid & 31) + 1) * 144 + (tid >> 5) * 16, (v4u){__builtin_amdgcn_perm(kv[6], kv[7], 0x07060302u), __builtin_amdgcn_perm(kv[4], kv[5], 0x07060302u),
                                                                 __builtin_amdgcn_perm(kv[2], kv[3], 0x07060302u), __builtin_amdgcn_perm(kv[0], kv[1], 0x07060302u)});
    BAR();
#pragma unroll
    for (int i = 0; i < 2; ++i) {
        const int ch = i * 32 + (tid >> 3);
        st16(rs, W_KVT + (u32)(((tile % 10) * 64 + ch) * SEQ + (tile / 10) * 64 + (tid & 7) * 8) * 2u, lds_r16(xt + ch * 144 + (tid & 7) * 16));
    }
}
template <int MODE> DEV void conv_job(const Params& p, rsrc_t rs, lptr lds, int grp, Filt& f, int next) {
    const int tid = threadIdx.x, lane = tid & 63, w = uni(tid >> 6), bq = lane & 31, hh = lane >> 5;
    lptr F = lds + w * CV_CH;
    lptr fa = F + (bq & 7) * CV_PL + ((bq >> 3) - hh + 4) * 16;
    lptr kb = F + CV_F + CV_KB + hh * 16, kh = kb + (bq + 32) * CV_KB;
    bool dead = false;
    v4u kvr[4];
    const u32 D = p.ssmD[chan(grp, w)];
    auto kv_load = [&]() {
        if (MODE != 2) wait_flags(rs, lane < NKV ? foff(F_KV + lane) : OOB, 1u, dead);
#pragma unroll
        for (int i = 0; i < (MODE == 1 ? 2 : 4); ++i) kvr[i] = ld16s(rs, (u32)lane * 16u, W_KVT + (u32)(chan(grp, w) * SEQ + i * 512) * 2u);
    };
    auto windows = [&](int i) {
        if (i < 4 || lane < 16) {
            const u32 e[8] = {f.a[i].x, f.a[i].y, f.a[i].z, f.a[i].w, f.b[i].x, f.b[i].y, f.b[i].z, f.b[i].w};
            u32 o[7];
#pragma unroll
            for (int q = 0; q < 7; ++q) o[q] = __builtin_amdgcn_alignbit(e[q + 1], e[q], 16);
#pragma unroll
            for (int q = 0; q < 4; ++q) {
                lds_w16(F + (2 * q) * CV_PL + (64 * i + lane) * 16, (v4u){o[q], o[q + 1], o[q + 2], o[q + 3]});
                lds_w16(F + (2 * q + 1) * CV_PL + (64 * i + lane) * 16, (v4u){e[q + 1], e[q + 2], e[q + 3], e[q + 4]});
            }
        }
    };
    lptr yw = lds + (lane >> 4) * CV_CH + 3072 + (lane & 15) * 64 + w * 16, yr = F + 3072 + lane * 16;
    const u32 yo = W_Y + (u32)((((w & 1) * 16 + (lane >> 2)) * 8 + (w >> 1)) * DM + (grp & 3) * 160 + 4 * (grp >> 2) + (lane & 3)) * 16u;
    auto put = [&](const f32x16& ac, int q) {
        const int r = 4 * (q & 3);
        const auto x = __builtin_amdgcn_permlane32_swap(pk(ac[r], ac[r + 1]), pk(ac[r + 4], ac[r + 5]), false, false);
        const auto y = __builtin_amdgcn_permlane32_swap(pk(ac[r + 2], ac[r + 3]), pk(ac[r + 6], ac[r + 7]), false, false);
        lds_w16(yw + (q >> 1) * CV_PL, (v4u){x[0], y[0], x[1], y[1]});
    };
    if (MODE != 2) EV(8);
    if (MODE != 0) kv_load();
    if (lane < CV_KB / 16) lds_w16(F + CV_F + lane * 16, (v4u){0u, 0u, 0u, 0u});
    windows(0);
    if (MODE != 1) { windows(1); windows(2); windows(4); }
    if (MODE == 0) kv_load();
    if (MODE == 2) windows(3);
    if (MODE != 2) EV(9);
#pragma unroll
    for (int i = 0; i < (MODE == 1 ? 2 : 4); ++i) lds_w16(F + CV_F + (16 * i + (lane >> 2) + 1) * CV_KB + (lane & 3) * 16, kvr[i]);
    v4u dg[2];
#pragma unroll
    for (int ks = 0; ks < 2; ++ks)
#pragma unroll
        for (int q = 0; q < 4; ++q) { const int j = 16 * ks + 8 * hh + 7 - bq; dg[ks][q] = j == 2 * q ? D : j == 2 * q + 1 ? D << 16 : 0u; }
    f32x16 acc0 = {}, acc1 = {};
    auto first = [&]() { asm volatile("s_waitcnt vmcnt(0)\n\ts_barrier" ::: "memory"); st4s(rs, tid == 0 ? foff(F_CV + grp) : OOB, 0, 1u); EV(12); };
    if (MODE != 2) {
        EV(10);
        acc0 = mfma32(lds_r16(kb + bq * CV_KB), dg[0], acc0); acc0 = mfma32(lds_r16(kb + bq * CV_KB + 32), dg[1], acc0);
#pragma unroll
        for (int m = 0; m < 8; ++m) {
            lptr kl = kb + max(bq - m, -1) * CV_KB;
            acc0 = mfma32(lds_r16(kl), lds_r16(fa + m * 64), acc0); acc0 = mfma32(lds_r16(kl + 32), lds_r16(fa + m * 64 - 32), acc0);
        }
        put(acc0, 0);
        BAR();
        st16(rs, w < 2 ? yo : OOB, lds_r16(yr));
        EV(11);
    }
    if (MODE == 1) {
        windows(1); windows(2);
        first();
        v4u a[2][2], l[2][2];
#pragma unroll
        for (int u = 0; u < 2; ++u) { lptr kl = kb + max(bq - 8 - u, -1) * CV_KB; a[u][0] = lds_r16(fa + (8 + u) * 64); l[u][0] = lds_r16(kl); a[u][1] = lds_r16(fa + (8 + u) * 64 - 32); l[u][1] = lds_r16(kl + 32); }
#pragma unroll 1
        for (int m = 8; m < 32; m += 2) {
#pragma unroll
            for (int u = 0; u < 2; ++u) {
                lptr kl = kb + max(bq - m - u - 2, -1) * CV_KB;
                acc0 = mfma32(l[u][0], a[u][0], acc0); a[u][0] = lds_r16(fa + (m + u + 2) * 64); l[u][0] = lds_r16(kl); SCHED();
                acc0 = mfma32(l[u][1], a[u][1], acc0); a[u][1] = lds_r16(fa + (m + u + 2) * 64 - 32); l[u][1] = lds_r16(kl + 32); SCHED();
            }
        }
    } else {
        if (MODE == 0) windows(3); else filt_load(p, next, f);
        v4u a0 = lds_r16(fa), a1 = lds_r16(fa - 32), c0 = lds_r16(fa + 2048), c1 = lds_r16(fa + 2048 - 32);
        v4u h0 = lds_r16(kh), h1 = lds_r16(kh + 32), l0 = lds_r16(kb + bq * CV_KB), l1 = lds_r16(kb + bq * CV_KB + 32);
        acc1 = mfma32(h0, dg[0], acc1); acc1 = mfma32(h1, dg[1], acc1);
#pragma unroll 2
        for (int m = 0; m < (MODE == 0 ? 8 : 32); ++m) {
            lptr kl = kb + max(bq - m - 1, -1) * CV_KB;
            if (MODE == 0 && m == 4) first();
            acc1 = mfma32(h0, a0, acc1); h0 = lds_r16(kh - (m + 1) * CV_KB); a0 = lds_r16(fa + (m + 1) * 64); SCHED();
            acc1 = mfma32(l0, c0, acc1); c0 = lds_r16(fa + (m + 1) * 64 + 2048); l0 = lds_r16(kl); SCHED();
            acc1 = mfma32(h1, a1, acc1); h1 = lds_r16(kh - (m + 1) * CV_KB + 32); a1 = lds_r16(fa + (m + 1) * 64 - 32); SCHED();
            acc1 = mfma32(l1, c1, acc1); c1 = lds_r16(fa + (m + 1) * 64 + 2048 - 32); l1 = lds_r16(kl + 32); SCHED();
        }
        if (MODE == 0) {
#pragma unroll 2
            for (int m = 8; m < 32; ++m) {
                lptr kl = kb + max(bq - m - 1, -1) * CV_KB;
                acc1 = mfma32(h0, a0, acc1); h0 = lds_r16(kh - (m + 1) * CV_KB); SCHED();
                acc0 = mfma32(l0, a0, acc0); a0 = lds_r16(fa + (m + 1) * 64); SCHED();
                acc1 = mfma32(l0, c0, acc1); c0 = lds_r16(fa + (m + 1) * 64 + 2048); l0 = lds_r16(kl); SCHED();
                acc1 = mfma32(h1, a1, acc1); h1 = lds_r16(kh - (m + 1) * CV_KB + 32); SCHED();
                acc0 = mfma32(l1, a1, acc0); a1 = lds_r16(fa + (m + 1) * 64 - 32); SCHED();
                acc1 = mfma32(l1, c1, acc1); c1 = lds_r16(fa + (m + 1) * 64 + 2048 - 32); l1 = lds_r16(kl + 32); SCHED();
            }
        }
        BAR();
    }
    if (MODE != 2) { EV(13); put(acc0, 0); put(acc0, 2); }
    if (MODE != 1) { put(acc1, 4); put(acc1, 6); }
    BAR();
#pragma unroll
    for (int q = (MODE == 2 ? 2 : 0); q < (MODE == 1 ? 2 : 4); ++q) st16(rs, q == 0 && w < 2 ? OOB : yo + (u32)q * (2 * DM * 16), lds_r16(yr + q * CV_PL));
    BAR();
    if (MODE != 2) EV(14);
}

DEV void pre_rings(const Params& p, rsrc_t rs, int j) {
    const u32 gt = (u32)(j * 256 + threadIdx.x) * 16u, n = ((gt >> 14) << 9) + ((gt & 8191u) >> 4);
    const v4u ps = {PSENT, PSENT, PSENT, PSENT};
    v4u m = ps;
    if (n < 640u) m = *(const v4u*)(p.mem0 + (2 * ((n >> 2) & 3) + (n & 1)) * DM + (n >> 4) * 16 + (n & 2) * 4);
#pragma unroll
    for (u32 i = 0; i < 8; ++i) st16(rs, W_ORING + gt + i * 131072u, ps);
#pragma unroll
    for (u32 i = 0; i < 2; ++i) st16(rs, gt < 16384u ? W_MRING + i * (MB / 4) + 131072u + gt : OOB, ps);
    if (n < 640u) {
#pragma unroll
        for (int i = 0; i < 4; ++i) m[i] = m[i] == PSENT ? 0x7FC17FC1u : m[i];
    }
    st16(rs, W_MRING + gt, m);
    st16(rs, W_MRING + MB / 4 + gt, m);
}

#define M32A(first, d, a, b) do { if (first) asm volatile("v_mfma_f32_32x32x16_bf16 %0, %1, %2, 0" : "=&v"(d) : "a"(a), "v"(b)); \
                                  else asm volatile("v_mfma_f32_32x32x16_bf16 %0, %1, %2, %0" : "+v"(d) : "a"(a), "v"(b)); } while (0)
#define M32B(first, d, a, b) do { if (first) asm volatile("v_mfma_f32_32x32x16_bf16 %0, %1, %2, 0" : "=&v"(d) : "v"(a), "a"(b)); \
                                  else asm volatile("v_mfma_f32_32x32x16_bf16 %0, %1, %2, %0" : "+v"(d) : "v"(a), "a"(b)); } while (0)
#define VCVT(d, a, b) asm volatile("v_cvt_pk_bf16_f32 %0, %1, %2" : "=v"(d) : "v"(a), "v"(b))
#define LD4A(d, vo, so) asm volatile("buffer_load_dword %0, %1, %2, %3 offen sc1" : "=v"(d) : "v"(vo), "s"(srd), "s"(so) : "memory")
DEV u32 wst_src(int lane, int ld) { return (u32)((lane >> 4) * ld + 8 * ((lane & 15) ^ ((lane >> 4) << 2))) * 2u; }
DEV u32 wst_rd(int lane, int w) { const int q4 = (lane >> 2) & 3; return (u32)(q4 * 256 + ((w ^ q4) * 64) + ((lane >> 4) & 1) * 32 + (lane & 3) * 8); }

constexpr int QK_A = 65536;
#define QLEAD 4
#define QFIRST 2
#define QW0 10
#define QW1 3
#define QSAMP 72
DEV void role_qkv(const Params& p, lptr lds, int c) {
    const int grp = 64 + c;
    const int tid = threadIdx.x, lane = tid & 63, w = uni(tid >> 6), hh = lane >> 5, r32 = lane & 31;
    const int m = c >> 5, h = c & 31;
    const rsrc_t rs = mk_rsrc(p.ws, W_END);
    v4u wt[40];
    Filt flt;
    bool dead = false;
    u32 pol;
    const u32 t16 = (u32)tid * 16u, lnf = w < 2 ? foff(F_LN + tid) : OOB;
    MK_SRD();
#define QDMA(i, dst, vo, so) DMA16I(dst, (i) * 4096, vo, so, ((i) >> 1) * 16384 + ((i) & 1) * 4096)
    {
        const v4u sw = mk_srd(m == 0 ? p.Wq : m == 1 ? p.Wk : p.Wv, DM * NQ * 2), sx = mk_srd(p.x, ACT);
        const u32 vo = wst_src(lane, NQ) + (u32)h * (DH * 2), x8 = (u32)lane * 8u, x4 = (u32)lane * 4u;
        v2u xa[6], xb[6]; u32 xc[6];
#pragma unroll
        for (int i = 0; i < 6; ++i)
            asm volatile("buffer_load_dwordx2 %0, %3, %5, %6 offen " INPOL "\n\tbuffer_load_dwordx2 %1, %3, %5, %6 offen offset:512 " INPOL "\n\tbuffer_load_dword %2, %4, %5, %6 offen offset:1024 " INPOL
                         : "=&v"(xa[i]), "=&v"(xb[i]), "=&v"(xc[i]) : "v"(x8), "v"(x4), "s"(sx), "s"(uu((4 * c + w + 384 * i) * (DM * 2))) : "memory");
        EV(1);
        auto wfill = [&](int i0, int n) {
#pragma unroll 1
            for (int i = i0; i < i0 + n; ++i) DMAIN(sw, uu((4 * i + w) * 1024), vo, uu((16 * i + 4 * w) * (NQ * 2)));
        };
        wfill(0, QW0);
        asm volatile("s_waitcnt vmcnt(%0) ; HZCLR" :: "i"(QW0) : "memory");
        EV(2);
#pragma unroll
        for (int i = 0; i < 6; ++i) {
            wfill(QW0 + QW1 * i, QW1);
            if (i == 2) { VMW(2 * QW1 + 3); BAR(); st4s(rs, tid == 0 ? foff(F_LN + c) : OOB, 0, 1u); }
            asm volatile("" : "+v"(xa[i]), "+v"(xb[i]), "+v"(xc[i]));
            const int row = 4 * c + w + 384 * i;
            if (i < 5 || row < SEQ) {
                const f32x2 x[5] = {up2(xa[i].x), up2(xa[i].y), up2(xb[i].x), up2(xb[i].y), up2(xc[i])};
                const f32x2 st = ln_stats(x);
                u32 hp[5];
#pragma unroll
                for (int q = 0; q < 5; ++q) hp[q] = pk2(up2(pk2(x[q] - st.x)) * st.y);
                store_h(rs, W_H1 + (u32)(row >> 5) * H_TILE, row, hp);
            }
        }
        EV(3);
        constexpr int K0 = QW0 + 6 * QW1;
        static_assert(K0 * 4096 >= QK_A + 40960, "the first tile's place is free");
        u32 fv, praw;
        { const u32 po = pol_off(); LD4A(praw, po, 0); LD4A(fv, lnf, 0); }
        wfill(K0, 40 - K0);
        asm volatile("s_waitcnt vmcnt(%2) ; HZCLR\n\ts_barrier" : "+v"(praw), "+v"(fv) : "i"(40 - K0) : "memory");
        st4s(rs, tid == 0 ? foff(F_LN + c) : OOB, 0, 2u);
        EV(4);
        lptr tb = lds + hh * 2048 + wst_rd(lane, w);
        SFOR(k, 40)
            if constexpr (k == K0) {
                if (w < 2 && !all_(fv != 0u)) wait_flags(rs, lnf, 1u, dead);
                BAR();
                EV(5);
                const u32 dst = uu(QK_A + w * 1024), so = W_H1;
                SFOR(i, 10) QDMA(i, dst, t16, so); SEND
                VMW(10); BAR();
            }
            lptr q = tb + (k >> 3) * 32768 + (k & 7) * 4096; wt[k] = tr2(q, q + 1024); asm volatile("" : "+a"(wt[k]));
        SEND
        pol = pol_of(praw);
    }
    EV(6);
    u32 ar[8];
#pragma unroll
    for (int j = 0; j < 8; ++j) ar[j] = (u32)(r32 * 256 + ((2 * j + hh) ^ (r32 & 15)) * 16);
#define XRD(ks) lds_r16(lds + ar[(ks) & 7] + ((ks) >> 3) * 8192)
    const u32 h2flag = tid < 8 ? near_(foff(F_H2 + lane), 1u, pol) : OOB;
    const u32 ol = (m == 2 ? (u32)(2 * w + (r32 >> 4)) * 1024u : (u32)((r32 >> 4) * 4 + w) * 1024u) + (u32)(hh * 16 + (r32 & 15)) * 16u;
    const u32 fo = near_(foff(F_QKV + m * 64 + h), 0u, pol);
    const u32 ds = uu(w * 1024), dx = ds + QK_A;
    int n0 = -1, n1 = -QLEAD;
    f32x16 a0 = {};
    auto run = [&](auto isv) __attribute__((always_inline)) {
      constexpr bool V = decltype(isv)::value;
      v4u x[8];
      u32 fv = 0, cvo = n1 > -QLEAD && tid == 194 ? foff(F_CV + grp) : OOB;
#pragma unroll
      for (int ks = 20; ks < 25; ++ks) x[ks & 7] = XRD(ks);
#define QS(k) do {} while (0)
#define QPK1(d, a, a_, q_) do { if constexpr (V) VCVT(d[q_], a[4 * (a_) + 8 * ((q_) >> 1) + 2 * ((q_) & 1)], a[4 * (a_) + 8 * ((q_) >> 1) + 2 * ((q_) & 1) + 1]); else VCVT(d[q_], a[4 * (a_) + (q_)], a[4 * (a_) + (q_) + 8]); } while (0)
#define QPK(d, a, a_) do { QPK1(d, a, a_, 0); QPK1(d, a, a_, 1); QPK1(d, a, a_, 2); QPK1(d, a, a_, 3); } while (0)
#pragma unroll 1
      for (const int end = n1 == -QLEAD ? QFIRST - QLEAD : NSEG; n1 < end; ++n1) {
        const int cur = n1 < 0 ? n0 + 1 : 64 | n1, nxt = n1 < 0 ? n0 + 2 : n0 + 1;
        u32 xs = n1 < 0 ? W_H1 + (u32)cur * H_TILE : near_(W_H2 + (u32)n1 * H_TILE, 1u, pol), xd = n0 < 0 ? ds : dx;
        asm volatile("" : "+s"(xs), "+s"(xd));
        QS(0);
        if (w == 0 && n1 >= 0 && ((u32)__builtin_amdgcn_ballot_w64(fv <= (u32)n1) & 255u) != 0u) wait_flags(rs, h2flag, (u32)n1 + 1, dead);
        BAR();
        QS(1);
        f32x16 a1;
        u32 ss, sv, hb, oa, qfo, qfv;
        v4u pp, pq;
        SCHED();
        SFOR(z, 80)
            constexpr int ks = (z + 20) % 40;
            if constexpr (z < 20) { if constexpr (V) M32B(false, a0, x[ks & 7], wt[ks]); else M32A(false, a0, wt[ks], x[ks & 7]); }
            else if constexpr (z < 60) { if constexpr (V) M32B(z == 20, a1, x[ks & 7], wt[ks]); else M32A(z == 20, a1, wt[ks], x[ks & 7]); }
            else if constexpr (z == 60) {
                if constexpr (V) asm volatile("v_mfma_f32_32x32x16_bf16 %0, %1, %2, 0" : "+v"(a0) : "v"(x[ks & 7]), "a"(wt[ks]));
                else asm volatile("v_mfma_f32_32x32x16_bf16 %0, %1, %2, 0" : "+v"(a0) : "a"(wt[ks]), "v"(x[ks & 7]));
            } else { if constexpr (V) M32B(false, a0, x[ks & 7], wt[ks]); else M32A(false, a0, wt[ks], x[ks & 7]); }
            if constexpr (z == 18) { QS(2); asm volatile("s_waitcnt vmcnt(6) lgkmcnt(0)\n\ts_barrier" ::: "memory"); QS(3); }
            if constexpr (z < 15 || z > 20) x[(ks + 5) & 7] = XRD((ks + 5) % 40);
            if constexpr (z >= 18 && z <= 20) { x[2 * (z - 18)] = XRD(2 * (z - 18)); x[2 * (z - 18) + 1] = XRD(2 * (z - 18) + 1); }
            SCHED();
            if constexpr (z < 4) QDMA(z, xd, t16, xs);
            if constexpr (z >= 5 && z < 17 && (z & 1)) QDMA((z + 3) >> 1, xd, t16, xs);
            if constexpr ((z >= 7 && z < 15) || (z >= 47 && z < 55)) asm volatile("v_xor_b32 %0, 0x10000, %0" : "+v"(ar[(ks + 5) & 7]));
            if constexpr (z == 16) { ss = W_H1 + (u32)nxt * H_TILE; PINS(ss); }
            if constexpr (z == 17) { sv = t16 | (nxt >= NSEG ? OOB : 0u); PIN(sv); }
            if constexpr (z >= 21 && z < 41 && (z & 1)) QDMA((z - 21) >> 1, ds, sv, ss);
            if constexpr (z == 24) QPK(pp, a0, 0);
            if constexpr (z == 26) QPK(pq, a0, 1);
            if constexpr (z == 30) { QS(4); asm volatile("s_waitcnt vmcnt(5)\n\ts_barrier" ::: "memory"); QS(5); }
            if constexpr (z == 32 || z == 44) { hb = (m == 2 ? W_VO : W_KQ) + (u32)((z == 32 ? n0 : cur) * NH + h) * 16384u; PINS(hb); }
            if constexpr (z == 34 || z == 45) { hb = near_(hb, m == 1 ? 0u : 1u, pol); PINS(hb); }
            if constexpr (z == 36) { hb = (u32)n0 < NSEG ? hb : OOB; PINS(hb); }
            if constexpr (z == 38 || z == 46) { oa = ol + hb; PIN(oa); }
            if constexpr (z == 41) st16(rs, oa, pp);
            if constexpr (z == 43) st16(rs, oa + 512, pq);
            if constexpr (z == 54) { QS(6); asm volatile("s_waitcnt vmcnt(4)\n\ts_barrier" ::: "memory"); QS(7); }
            if constexpr (z == 56) { if (__builtin_expect(n1 == QFIRST - QLEAD - 1, 0)) filt_load<3>(p, grp, flt); }
            if constexpr (z == 60) NOPS(5);
            if constexpr (z == 61) { QPK(pp, a1, 0); st16(rs, oa, pp); }
            if constexpr (z == 62) { QPK(pq, a1, 1); st16(rs, oa + 512, pq); }
            if constexpr (z == 64) { qfo = tid == 192 ? fo : tid == 193 && n1 >= 0 ? fo + 32 * 128 : cvo; PIN(qfo); }
            if constexpr (z == 66) { qfv = tid == 192 ? (u32)min(nxt, NSEG) : tid == 193 ? (u32)n1 + 1u : 2u; PIN(qfv); }
            if constexpr (z == QSAMP) LD4A(fv, h2flag, 0);
            SCHED();
        SEND
        QS(8);
        asm volatile("s_waitcnt vmcnt(1)\n\ts_barrier" ::: "memory");
        st4s(rs, qfo, 0, qfv);
        QS(9);
        asm volatile("s_waitcnt vmcnt(1)" : "+v"(fv) :: "memory");
        n0 = nxt; cvo = OOB;
      }
      NOPS(7);
    };
    BAR();
#pragma unroll 1
    for (;;) {
        if (m == 2) run(IC<1>{}); else run(IC<0>{});
        if (n1 > 0) break;
        wait_flags(rs, tid < 96 ? lnf : OOB, 2u, dead);
        BAR();
        conv_job<1>(p, rs, lds, grp, flt, 0);
        const u32 so = uu(W_H1 + (u32)n0 * H_TILE);
        SFOR(i, 6) QDMA(i + 4, ds, t16, so); SEND
        DRAIN(); BAR();
    }
}

constexpr int OU_SEL = 131072;

DEV void role_out(const Params& p, lptr lds, int L, int kq, int ng) {
    const int tid = threadIdx.x, lane = tid & 63, w = uni(tid >> 6), hh = lane >> 5, r32 = lane & 31;
    const rsrc_t rs = mk_rsrc(p.ws, W_END);
    v4u wo[64];
    Filt flt;
    bool dead = false;
    {
        const v4u sw = mk_srd(p.Wo, DM * NQ * 2);
        const u32 vo = wst_src(lane, DM) + (ng == 4 ? 1024u : (u32)((ng >> 1) * 512 + (ng & 1) * 128 + ((lane ^ (lane >> 2)) & 8) * 16));
        auto fill = [&](int b) {
#pragma unroll 1
            for (int i = 0; i < 8; ++i) DMAIN(sw, uu((b % 5) * 32768 + (4 * i + w) * 1024), vo, uu((kq * 1024 + b * 128 + 16 * i + 4 * w) * (DM * 2)));
        };
#pragma unroll 1
        for (int b = 0; b < 5; ++b) fill(b);
        lptr tb = lds + hh * 1024 + wst_rd(lane, w);
        SFOR(b, 8)
            VMW(b < 3 ? 32 : 66 - 8 * b); BAR();
            lptr q = tb + (b % 5) * 32768;
#pragma unroll
            for (int k = 0; k < 8; ++k) { lptr a = q + (k >> 1) * 8192 + (k & 1) * 2048; wo[8 * b + k] = tr2(a, a + 4096); if (b < 7) asm volatile("" : "+a"(wo[8 * b + k])); else PIN(wo[8 * b + k]); }
            if constexpr (b < 3) { BAR(); fill(b + 5); }
            if constexpr (b == 2) filt_load(p, L == 0 ? 44 + kq * 5 + ng : 1 << 12, flt);
        SEND
    }
    EV(6);
    if (L == 0) {
        BAR();
        conv_job<0>(p, rs, lds, 44 + kq * 5 + ng, flt, 0);
        DRAIN(); BAR();
        st4s(rs, tid == 0 ? foff(F_CV + 44 + kq * 5 + ng) : OOB, 0, 2u);
    }
    EV(15);
    MK_SRD();
    const u32 pol = get_pol(rs);
    const u32 ofl = (lane < 8 && w == 0) ? near_(foff(F_O + L * 32 + kq * 8 + lane), 0u, pol) : OOB;
    const u32 l16 = (u32)lane * 16u;
    const u32 fo = (tid == 192) ? near_(foff(F_P + L * 32 + kq * 5 + ng), 1u, pol) : OOB;
    auto osrc = [&](int s) { return uu(hblk(W_VO, L * NSEG + s, kq * 8, 0u, pol) + w * 1024); };
#define ODMA(i, dst, vo, so) DMA16I(dst, (i) * 4096, vo, so, ((i) >> 1) * 16384 + ((i) & 1) * 4096)
    const u32 pvo = (u32)(kq * 5 + ng) * 16384u + (u32)w * 2048u + l16;
    lptr tw = lds + OU_SEL + 256 + r32 * 272 + (ng == 4 ? hh * 32 + w * 4 : (w & 1) * 128 + hh * 16 + (w >> 1) * 8), tr = lds + OU_SEL + 256 + (8 * w + (lane >> 4)) * 272 + (lane & 15) * 16;
    lptr selw = lds + OU_SEL + (w == 0 ? 0 : 64);
    u32 pfo = OOB;
    u32 buf = 0;
    if (w == 0) wait_flags(rs, ofl, 1u, dead);
    BAR();
    for (int s = 0; s < NSEG; ++s) {
        u32 src = osrc(s), dst = uu(buf + w * 1024);
        lptr A = lds + buf + l16;
        asm volatile("" : "+s"(src), "+s"(dst));
#define OS(k) do {} while (0)
        OS(0);
        OS(1);
        SFOR(i, 10) ODMA(i, dst, l16, src); SEND
        OS(2);
        asm volatile("s_waitcnt vmcnt(7)\n\ts_barrier" ::: "memory");
        OS(3);
        LS2(L == 0 && w == 1, 96 + kq * 5 + ng);
        v4u o[64];
#pragma unroll
        for (int k = 0; k < 3; ++k) o[k] = lds_r16(A + k * 1024);
        st4s(rs, pfo, 0, (u32)s);
        u32 fv, pa; int rdy;
        f32x16 acc;
        SCHED();
        SFOR(k, 64)
            if constexpr (k < 56) M32A(k == 0, acc, wo[k], o[k]); else asm volatile("v_mfma_f32_32x32x16_bf16 %0, %1, %2, %0" : "+v"(acc) : "v"(wo[k]), "v"(o[k]));
            if constexpr (k == 61) rdy = *(LAS int*)(lds + OU_SEL);
            if constexpr (k < 61) o[k + 3] = lds_r16(A + (k + 3) * 1024);
            SCHED();
            if constexpr (k < 12 && (k & 1) == 0) ODMA(10 + (k >> 1), dst, l16, src);
            if constexpr (k == 7) OS(4);
            if constexpr (k == 8) OS(5);
            if constexpr (k == 23) OS(6);
            if constexpr (k == 24) OS(7);
            if constexpr (k == 43) OS(8);
            if constexpr (k == 44) OS(9);
            if constexpr (k == 59) OS(10);
            if constexpr (k == 63) OS(11);
            if constexpr (k == 7) asm volatile("s_waitcnt vmcnt(8)\n\ts_barrier" ::: "memory");
            if constexpr (k == 23) asm volatile("s_waitcnt vmcnt(5)\n\ts_barrier" ::: "memory");
            if constexpr (k == 30) { pa = pvo + near_(p_item(s), (u32)L, pol); PIN(pa); }
            if constexpr (k == 22) LD4A(fv, ofl, 0);
            if constexpr (k == 42) {
                asm volatile("s_waitcnt vmcnt(0)" : "+v"(fv) :: "memory");
                *(LAS int*)selw = (int)((u32)__builtin_amdgcn_ballot_w64(fv < (u32)s + 2) & 255u);
            }
            if constexpr (k == 43) asm volatile("s_waitcnt lgkmcnt(1)\n\ts_barrier" ::: "memory");
            SCHED();
        SEND
        const bool ready = uni(rdy) == 0;
        asm volatile("s_nop 11" : "+v"(acc));
        u32 pc[8];
#pragma unroll
        for (int i = 0; i < 8; ++i) pc[i] = pk(acc[2 * i], acc[2 * i + 1]);
        if (ng != 4) {
#pragma unroll
            for (int i = 0; i < 4; ++i) *(LAS v2u*)(tw + i * 32) = (v2u){pc[2 * i], pc[2 * i + 1]};
        } else {
#pragma unroll
            for (int i = 0; i < 8; ++i) *(LAS u32*)(tw + (i >> 1) * 64 + (i & 1) * 16) = pc[i];
        }
        BAR();
        { const v4u p0 = lds_r16(tr), p1 = lds_r16(tr + 4 * 272); st16(rs, pa, p0); st16(rs, pa + 1024, p1); }
        SCHED();
        OS(12);
        if (ready) pfo = fo;
        else {
            LD4A(fv, ofl, 0);
            asm volatile("s_waitcnt vmcnt(1)\n\ts_barrier" ::: "memory");
            st4s(rs, fo, 0, (u32)s + 1); pfo = OOB;
            OS(14);
            LS2(L == 0 && w == 1, 160 + kq * 5 + ng);
            asm volatile("s_waitcnt vmcnt(1)" : "+v"(fv) :: "memory");
            if (w == 0 && s + 1 < NSEG && ((u32)__builtin_amdgcn_ballot_w64(fv < (u32)s + 2) & 255u) != 0u) wait_flags(rs, ofl, (u32)s + 2, dead);
            BAR();
        }
        OS(13);
        buf ^= 65536u;
    }
}

template <int L> DEV void role_row(const Params& p, lptr lds, int ri) {
    const int lane = threadIdx.x & 63, w = uni(threadIdx.x >> 6);
    const rsrc_t rs = mk_rsrc(p.ws, W_END), rx = mk_rsrc(p.x, ACT);
    bool dead = false;
    const u32 pol = get_pol(rs);
    const u32 fp = threadIdx.x < 20 ? near_(foff(F_P + L * 32 + lane), 1u, pol) : OOB, fo = threadIdx.x == 192 && L == 0 ? near_(foff(F_H2 + (ri >> 2)), 1u, pol) : OOB;
    u32 fv = 0;
    MK_SRD();
    auto yfill = [&](int b, int i) { DMA32(uu(w * 20480 + (b & 1) * 10240 + i * 2048), (u32)lane * 16u, uu(W_Y + (u32)(ri * 8 + b) * 10240u + i * 2048)); };
    auto ywait = [&](u32 v) {
#pragma unroll 1
        for (u32 i = v == 3u ? 160u : 0u; i < (v == 3u ? 168u : 160u); i += 64) wait_flags(rs, i + lane < (v == 3u ? 168u : 160u) ? foff(F_CV + i + lane) : OOB, v, dead);
    };
    v2u xa = {}, xb = {}; u32 xc = 0;
    auto xload = [&](int s) { const u32 ro = (u32)(s * 32 + ri) * (DM * 2); xa = ldin8(rx, (u32)lane * 8u, ro); xb = ldin8(rx, (u32)lane * 8u + 512u, ro); xc = __builtin_amdgcn_raw_buffer_load_b32(rx, (u32)lane * 4u + 1024u, ro, 0); };
    if (L == 0) xload(0);
    ywait(1u);
    EV(15);
#pragma unroll 1
    for (int i = 0; i < 5; ++i) yfill(0, i);
    DRAIN();
    for (int s = 0; s < NSEG; ++s) {
        f32x2 y[5];
        {
            lptr ya = lds + w * 20480 + ((s >> 3) & 1) * 10240 + (s & 7) * 2 + lane * 16;
            auto yr = [&](lptr q) { return lo_f(*(LAS u16*)q); };
            y[0] = (f32x2){yr(ya), yr(ya + 2560)}; y[1] = (f32x2){yr(ya + 5120), yr(ya + 7680)};
            y[2] = (f32x2){yr(ya + 1024), yr(ya + 3584)}; y[3] = (f32x2){yr(ya + 6144), yr(ya + 8704)};
            lptr yc = ya - lane * 16 + 2048 + (lane >> 1) * 16 + (lane & 1) * 5120;
            y[4] = (f32x2){yr(yc), yr(yc + 2560)};
            asm volatile("" : "+v"(y[0]), "+v"(y[1]), "+v"(y[2]), "+v"(y[3]), "+v"(y[4]));
        }
        u32 xi[5] = {xa.x, xa.y, xb.x, xb.y, xc};
        u32 pa = near_(p_item(s), (u32)L, pol) + (u32)ri * 256u, pc = pa + 65536u + (u32)(lane >> 4) * 81920u + (u32)(lane & 15) * 16u;
        pa += (u32)(lane >> 5) * 81920u + (u32)((lane >> 4) & 1) * 16384u + (u32)(lane & 15) * 16u;
        asm volatile("" : "+v"(xi[0]), "+v"(xi[1]), "+v"(xi[2]), "+v"(xi[3]), "+v"(xi[4]), "+v"(pa), "+v"(pc));
        if (s == 2 || s == 26) ywait(s == 2 ? 2u : 3u);
#define RS(k) do {} while (0)
        RS(0);
        if (w == 0 && ((u32)__builtin_amdgcn_ballot_w64(fv <= (u32)s) & 0xFFFFFu) != 0u) wait_flags(rs, fp, (u32)s + 1, dead);
        BAR();
        RS(1);
        v4u d[5];
#pragma unroll
        for (int i = 0; i < 4; ++i) d[i] = ld16s(rs, pa, (i >> 1) * 32768 + (i & 1) * 163840);
        d[4] = ld16s(rs, pc, 0);
        if (L == 0) xload(s + 1);
        else {
            const u32 ro = W_X2 + (u32)(s * 32 + ri) * (DM * 2);
            const v2u a = ld8s(rs, (u32)lane * 8u, ro), b = ld8s(rs, (u32)lane * 8u + 512u, ro);
            xi[0] = a.x; xi[1] = a.y; xi[2] = b.x; xi[3] = b.y; xi[4] = ld4(rs, (u32)lane * 4u + 1024u + ro);
        }
        RS(2);
        LS2(L == 0, 192 + ri);
        f32x2 o[5];
        {
            f32x2 t[5][2];
#pragma unroll
            for (int i = 0; i < 5; ++i) {
                const auto e = __builtin_amdgcn_permlane32_swap(d[i].x, d[i].z, false, false), f = __builtin_amdgcn_permlane32_swap(d[i].y, d[i].w, false, false);
                t[i][0] = up2(e[0]) + up2(e[1]); t[i][1] = up2(f[0]) + up2(f[1]);
            }
            o[0] = t[0][0] + t[1][0]; o[1] = t[0][1] + t[1][1]; o[2] = t[2][0] + t[3][0]; o[3] = t[2][1] + t[3][1];
            const auto e = __builtin_amdgcn_permlane16_swap(u_(t[4][0].x), u_(t[4][1].x), false, false), f = __builtin_amdgcn_permlane16_swap(u_(t[4][0].y), u_(t[4][1].y), false, false);
            o[4] = (f32x2){f_(e[0]) + f_(e[1]), f_(f[0]) + f_(f[1])};
        }
        f32x2 x1[5];
#pragma unroll
        for (int i = 0; i < 5; ++i) x1[i] = up2(pk2(up2(xi[i]) + up2(pk2(o[i]))));
        asm volatile("" : "+v"(x1[0]), "+v"(x1[1]), "+v"(x1[2]), "+v"(x1[3]), "+v"(x1[4]));
        RS(3);
        if ((s & 7) - 2u < 5u && s < NSEG - 8) yfill((s >> 3) + 1, (s & 7) - 2);
        SCHED();
        const f32x2 st1 = ln_stats(x1);
        u32 xp[5];
#pragma unroll
        for (int i = 0; i < 5; ++i) xp[i] = pk2(x1[i] + ((x1[i] - st1.x) * st1.y) * y[i]);
        const int row = s * 32 + ri;
        RS(4);
        if (L == 0) {
            const u32 ro = (u32)row * (DM * 2);
            f32x2 x2[5];
#pragma unroll
            for (int i = 0; i < 5; ++i) x2[i] = up2(xp[i]);
            st8s(rs, (u32)lane * 8, W_X2 + ro, (v2u){xp[0], xp[1]});
            st8s(rs, (u32)lane * 8 + 512, W_X2 + ro, (v2u){xp[2], xp[3]});
            st4s(rs, (u32)lane * 4 + 1024, W_X2 + ro, xp[4]);
            SCHED();
            const f32x2 st2 = ln_stats(x2);
            u32 hp[5];
#pragma unroll
            for (int i = 0; i < 5; ++i) hp[i] = pk2((x2[i] - st2.x) * st2.y);
            store_h(rs, near_(W_H2 + (u32)s * H_TILE, 1u, pol), row, hp);
        } else {
            u16* o_ = p.out + (size_t)row * DM;
            *(v2u*)(o_ + 4 * lane) = (v2u){xp[0], xp[1]};
            *(v2u*)(o_ + 256 + 4 * lane) = (v2u){xp[2], xp[3]};
            *(u32*)(o_ + 512 + 2 * lane) = xp[4];
        }
        RS(5);
        LD4A(fv, fp, 0);
        if (L == 0) asm volatile("s_waitcnt vmcnt(1)\n\ts_barrier" ::: "memory");
        st4s(rs, fo, 0, (u32)s + 1);
        RS(6);
        asm volatile("s_waitcnt vmcnt(1)" : "+v"(fv) :: "memory");
        RS(7);
        if (ri == 0) LST(320 + L * 64 + s, rt32());
        LS2(L == 0, 224 + ri);
    }
}

constexpr float SCALE = 0.08838834764831845f, LOG2E = 1.4426950408889634f;
constexpr int C_K = 0, C_V = 8192, C_QX = 16384, C_MEM = 24576, C_Q = C_MEM + 10240, C_KM = C_Q + 2304, C_VM = C_KM + 2304, C_DUMP = C_VM + 2048,
              C_ZERO = C_DUMP + 1024, C_WL = C_ZERO + 128;
constexpr int NLF = 14;
static_assert(C_WL % 16 == 0 && C_WL + 4 * NLF * 2048 <= LDS_BYTES, "head lds");
#define CH_VIS 10
#define CH_H0 124
#define CH_W0 88
#define CH_MH 4
#define CH_MW 4
#define CH_REC 20
#define CH_LATE 30
#define CH_UP 4
#define CH_WX 6
DEV void wait_until(u32 t) {
    unsigned long long c;
    asm volatile("1:\n\ts_memrealtime %0\n\ts_waitcnt lgkmcnt(0)\n\ts_mov_b64 vcc, %0\n\ts_sub_u32 vcc_lo, %1, vcc_lo\n\ts_cmpk_lt_u32 vcc_lo, 400\n\ts_cbranch_scc1 1b" : "=&s"(c) : "s"(uu(t - 1u)) : "vcc", "scc", "memory");
}
#define WAIT_TXT(tmp, t1) "s_memrealtime vcc\n1:\n\ts_waitcnt lgkmcnt(0)\n\ts_sub_u32 " tmp ", " t1 ", vcc_lo\n\ts_memrealtime vcc\n\ts_cmpk_lt_u32 " tmp ", 400\n\ts_cbranch_scc1 1b\n\t"
#define SMF_A(acc, a, b) asm volatile("v_smfmac_f32_16x16x64_bf16 %0, %1, %2, %3" : "+v"(acc) : "v"(a), "a"(b), "v"(idx))
#define SMF_V(acc, a, b) asm volatile("v_smfmac_f32_16x16x64_bf16 %0, %1, %2, %3" : "+v"(acc) : "v"(a), "v"(b), "v"(idx))
#define MF0(d, a, b) asm volatile("v_mfma_f32_16x16x32_bf16 %0, %1, %2, 0" : "=&v"(d) : "v"(a), "v"(b))
#define MF1(d, a, b) asm volatile("v_mfma_f32_16x16x32_bf16 %0, %1, %2, %0" : "+v"(d) : "v"(a), "v"(b))
#define MF0H(d, a, b) asm volatile("v_mfma_f32_16x16x16_bf16 %0, %1, %2, 0" : "=&v"(d) : "v"(a), "v"(b))
#define MF1H(d, a, b) asm volatile("v_mfma_f32_16x16x16_bf16 %0, %1, %2, %0" : "+v"(d) : "v"(a), "v"(b))
DEV v8u tr_frag(lptr p0, lptr p1, lptr p2, lptr p3) { return cat8(tr2(p0, p1), tr2(p2, p3)); }
constexpr int qslot(int ks) { return (ks & 1) && ks < 8 ? ks >> 1 : -1; }
constexpr int kslot(int ks) { return ks < 4 ? 4 + ks : -1; }
constexpr int vslot(int ks) { return ks >= 4 ? 4 + ks : -1; }
DEV u32 ring_map(u32 off, u32 half) { return ((off >> 13) << 14) + (half << 13) + (off & 8191u); }
#define ZRD(x, i) x = __builtin_bit_cast(f32x4, lds_r16(ZP + 16 * (i)))
#define VMAX(d, a, b) asm volatile("v_max_f32 %0, %1, %2" : "=v"(d) : "v"(a), "v"(b))
#define VMAX3(d, a, b, c_) asm volatile("v_max3_f32 %0, %1, %2, %3" : "=v"(d) : "v"(a), "v"(b), "v"(c_))
#define VADD(d, a, b) asm volatile("v_add_f32 %0, %1, %2" : "=v"(d) : "v"(a), "v"(b))
#define VEXP(d, a) asm volatile("v_exp_f32 %0, %1" : "=v"(d) : "v"(a))
#define VFMC(d, a, b) asm volatile("v_fmamk_f32 %0, %1, 0x3e0293ee, %2" : "=v"(d) : "v"(a), "v"(b))
#define SEL(d, a, b) asm volatile("v_cndmask_b32 %0, %1, %2, %3" : "=v"(d) : "v"(a), "v"(b), "s"(himask))
#define QPERM(d, a, q) asm volatile("v_mov_b32_dpp %0, %1 quad_perm:" q " row_mask:0xf bank_mask:0xf" : "=v"(d) : "v"(a))

#define HSAMP 58
DEV void role_head(const Params& p, lptr lds, int L, int h) {
    const int tid = threadIdx.x, lane = tid & 63, w = uni(tid >> 6), g = lane >> 4, r = lane & 15;
    const rsrc_t rs = mk_rsrc(p.ws, W_END);
    v8u wq0[10], wq1[10], wk0[10], wk1[10], wv0[10], wv1[10];
    lptr WL = lds + C_WL + w * (NLF * 2048) + lane * 16;
    {
        constexpr int GS = 1056, CS = 48 * GS;
        const v4u sq = mk_srd(p.Wq, DM * NQ * 2), sk = mk_srd(p.Wk, DM * NQ * 2), sv = mk_srd(p.Wv, DM * NQ * 2);
        const u32 vo = (u32)(g * NQ + h * DH + 8 * (r ^ ((g & 1) << 3))) * 2u;
        auto fill = [&](int ch) {
#pragma unroll 1
            for (int i = 0; i < 4; ++i) {
                const u32 dst = uu((ch % 3) * CS + (4 * i + w) * GS), so = uu((ch * 64 + 16 * i + 4 * w) * (NQ * 2));
                DMAIN(sq, dst, vo, so); DMAIN(sk, dst + 16 * GS, vo, so); DMAIN(sv, dst + 32 * GS, vo, so);
            }
        };
        if (L == 1) pre_rings(p, rs, h); else __builtin_amdgcn_s_sleep(ST_HEAD);
        fill(0); fill(1); fill(2);
        const int q4 = r >> 2;
        lptr tb = lds + (4 * (g >> 1) + 2 * (q4 >> 1) + (g & 1)) * GS + (q4 & 1) * 256 + ((w * 64 + (r & 3) * 8) ^ ((q4 & 1) << 7));
        SFOR(ch, 10)
            VMW(ch == 1 || ch == 2 ? 25 : ch < 8 ? 24 : 108 - 12 * ch);
            BAR();
            if constexpr (ch == 0) st4s(rs, tid == 0 && L == 1 ? foff(F_RG + h) : OOB, 0, 1u);
            auto frag = [&](int m, int t) { lptr b = tb + (ch % 3) * CS + m * 16 * GS + t * 32; return tr_frag(b, b + 512, b + 8 * GS, b + 8 * GS + 512); };
            wq0[ch] = frag(0, 0); wq1[ch] = frag(0, 1); wk0[ch] = frag(1, 0); wk1[ch] = frag(1, 1); wv0[ch] = frag(2, 0); wv1[ch] = frag(2, 1);
            asm volatile("" : "+a"(wq0[ch]), "+a"(wk0[ch]), "+v"(wv0[ch]), "+v"(wv1[ch]));
            if constexpr (qslot(ch) < 0) asm volatile("" : "+a"(wq1[ch])); else asm volatile("" : "+v"(wq1[ch]));
            if constexpr (kslot(ch) < 0) asm volatile("" : "+a"(wk1[ch])); else asm volatile("" : "+v"(wk1[ch]));
            BAR();
            if constexpr (ch < 7) fill(ch + 3);
        SEND
#pragma unroll
        for (int ch = 0; ch < 10; ++ch) {
            if (qslot(ch) >= 0) { lds_w16(WL + qslot(ch) * 2048, wq1[ch].lo); lds_w16(WL + qslot(ch) * 2048 + 1024, wq1[ch].hi); }
            if (kslot(ch) >= 0) { lds_w16(WL + kslot(ch) * 2048, wk1[ch].lo); lds_w16(WL + kslot(ch) * 2048 + 1024, wk1[ch].hi); }
            if (vslot(ch) >= 0) { lds_w16(WL + vslot(ch) * 2048, wv1[ch].lo); lds_w16(WL + vslot(ch) * 2048 + 1024, wv1[ch].hi); }
        }
        CFENCE();
    }
    const u32 idx = (r & 2) ? 0xEEEEu : 0x4444u;
    const unsigned long long himask = 0xCCCCCCCCCCCCCCCCull;
    bool dead = false;
    EV(6);
    const u32 pol = get_pol(rs);
    const u32 pol0 = pol & 1u;
    const u32 l16 = (u32)lane * 16u;
    const u32 mring = W_MRING + (u32)L * (MB / 4) + pol0 * 8192u, hint = mring + 4 * 32768u;
    const u32 fkh = lane < 3 ? near_(foff(F_QKV + lane * 64 + L * 32 + h), 0u, pol) : lane < 6 ? hint + (u32)(lane - 3) * 4u : OOB;
    u32 tm = 0;
    MK_SRD();
    const unsigned long long kmask = __builtin_amdgcn_ballot_w64((hblk(W_KQ, L * NSEG + lane, h, 0u, pol) & 8192u) != 0u),
                             vmask = __builtin_amdgcn_ballot_w64((hblk(W_VO, L * NSEG + lane, h, 1u, pol) & 8192u) != 0u);
    u32 kb = uu(W_KQ + (u32)(L * NSEG * NH + h) * 16384u + w * 2048);
    u32 dk = uu(C_K + w * 2048), dv = dk + (C_V - C_K), dq = dk + (C_QX - C_K), dm = uu(C_MEM + w * 2048), dm2 = uu(w < 2 ? C_MEM + 8192 + w * 1024 : C_DUMP);
    asm volatile("" : "+s"(dk), "+s"(dv), "+s"(dq), "+s"(dm), "+s"(dm2));
    wait_flags(rs, lane < 3 ? fkh : OOB, 1u, dead);
    EV(7);
    u32 ksrc = kb + ((u32)kmask & 1u) * 8192u, vsrc = kb + (W_VO - W_KQ) + ((u32)vmask & 1u) * 8192u;
    DMA32(dk, l16, ksrc); DMA32(dv, l16, vsrc); DMA32(dq, l16, ksrc ^ 8192u);
    lptr qw = lds + C_Q + (2 * g) * 272 + (32 * w + 2 * r) * 2;
    lptr qr = lds + C_Q + (2 * (r >> 2) + (r & 1)) * 272 + g * 16;
    lptr LB = lds + l16, VX = lds + C_V + w * 2048 + l16;
    if (tid >= 64 && tid < 70) { const u32 z = tid == 68 ? 0x3F803F80u : tid == 69 ? PSENT : 0u; lds_w16(lds + C_ZERO + (tid - 64) * 16, (v4u){z, z, z, z}); }
    u32 zaddr = 0; PIN(zaddr);
    lptr ZP = lds + zaddr + C_ZERO;
    const float CEXP = SCALE * LOG2E;
#define LF_RD(slot, hf) lf[slot][hf] = lds_r16(WL + (slot) * 2048 + (hf) * 1024)
    BAR();
    const u32 ofl = tid == 0 ? near_(foff(F_O + L * 32 + h), 0u, pol) : OOB;
    u32 oso = (g & 1) ? OOB : W_ORING + (u32)L * (MB / 2) + ring_map((u32)((r >> 3) * 32768 + h * 1024 + w * 256 + (g >> 1) * 128 + (r & 7) * 16), pol0);
    unsigned long long vis[8];
#pragma unroll
    for (int i = 0; i < 8; ++i) vis[i] = __builtin_amdgcn_ballot_w64(16 * (i >> 2) + 4 * g + (i & 3) <= 16 * (w & 1) + r);
    u32 missed = 0;
    EV(8);
    for (int s = 0; s < NSEG;) {
        u32 ms = mring + (u32)(s & 3) * 32768u + w * 2048, ms2 = mring + (u32)(s & 3) * 32768u + 16384u + (w & 1) * 1024, sv = (u32)s;
        asm volatile("" : "+s"(ms), "+s"(ms2)); PIN(sv);
        f32x4 aq0, aq1, ak0, ak1;
        v4u lf[NLF][2];
        ZRD(aq0, 0); ZRD(aq1, 1); ZRD(ak0, 2); ZRD(ak1, 3);
#pragma unroll
        for (int i = 0; i < 4; ++i) { LF_RD(i, 0); LF_RD(i, 1); }
        SCHED();
        v4u A[10];
#define HT(k) do {} while (0)
        v4u kx[8], qf[4], kmf[4], vx[2], ones, P;
        float fa, fb, fc, fd, mx, my, sx[4], t[4], tm0, tm1; u32 pa, pb, fl, e01, e23, pmm; unsigned long long nanq;
        f32x4 av0, av1, s0, s1, sm, oa, ob, rsum;
        v2u vm, pm;
#define LW(sl) cat8(lf[sl][0], lf[sl][1])
#define PQ(ks, tl) do { if constexpr (!(tl)) SMF_A(aq0, A[ks], wq0[ks]); else if constexpr (qslot(ks) < 0) SMF_A(aq1, A[ks], wq1[ks]); else { const v8u lw_ = LW(qslot(ks)); SMF_V(aq1, A[ks], lw_); } } while (0)
#define PK(ks, tl) do { if constexpr (!(tl)) SMF_A(ak0, A[ks], wk0[ks]); else if constexpr (kslot(ks) < 0) SMF_A(ak1, A[ks], wk1[ks]); else { const v8u lw_ = LW(kslot(ks)); SMF_V(ak1, A[ks], lw_); } } while (0)
#define PV(ks, tl) do { if constexpr (!(tl)) SMF_V(av0, A[ks], wv0[ks]); else if constexpr (vslot(ks) < 0) SMF_V(av1, A[ks], wv1[ks]); else { const v8u lw_ = LW(vslot(ks)); SMF_V(av1, A[ks], lw_); } } while (0)
#define KXR(i) kx[i] = lds_r16(LB + C_K + ((i) & 1) * 4096 + ((i) >> 1) * 1024)
#define XW(q_) do { *(LAS u32*)(q_) = pa; *(LAS u32*)((q_) + 272) = pb; } while (0)
        auto slot = [&](auto zc) __attribute__((always_inline)) {
            constexpr int z = decltype(zc)::value;
            if constexpr (z < 14) PQ(z >> 1, z & 1);
            else if constexpr (z < 17) PQ(z - 7, 0);
            else if constexpr (z < 20) PQ(z - 10, 1);
            else if constexpr (z < 33) PK((z - 20) >> 1, z & 1);
            else if constexpr (z < 35) { if constexpr (z == 33) MF0(s0, kx[0], qf[0]); else MF0(s1, kx[1], qf[0]); }
            else if constexpr (z < 41) { if constexpr (z & 1) MF1(s0, kx[z - 33], qf[(z - 33) >> 1]); else MF1(s1, kx[z - 33], qf[(z - 33) >> 1]); }
            else if constexpr (z == 41) PK(6, 1);
            else if constexpr (z < 45) PK(z - 35, 0);
            else if constexpr (z < 48) PK(z - 38, 1);
            else if constexpr (z < 58) PV(z - 48, 0);
            else if constexpr (z < 61) PV(z < 60 ? z - 54 : 0, 1);
            else if constexpr (z == 61) MF0(sm, kmf[0], qf[0]);
            else if constexpr (z < 65) MF1(sm, kmf[z - 61], qf[z - 61]);
            else if constexpr (z == 65) MF0(oa, vx[0], P);
            else if constexpr (z == 66) MF0(ob, vx[1], P);
            else PV(6, 1);
            if constexpr (z < 6) A[4 + z] = lds_r16(LB + C_MEM + (4 + z) * 1024);
            if constexpr (z == 6 || z == 7) LF_RD(4, z - 6);
            if constexpr (z == 8 || z == 9) LF_RD(5, z - 8);
            if constexpr (z == 12 || z == 13) LF_RD(6, z - 12);
            if constexpr (z == 16 || z == 17) KXR(z - 16);
            if constexpr (z == 18 || z == 19) LF_RD(7, z - 18);
            if constexpr (z >= 20 && z < 24) KXR(z - 18);
            if constexpr (z == 32 || z == 33) KXR(z - 26);
            if constexpr (z == 24) XW(qw);
            if constexpr (z == 25) ZRD(av0, 0);
            if constexpr (z == 26) ZRD(av1, 1);
            if constexpr (z == 27) asm volatile("s_waitcnt lgkmcnt(2)\n\ts_barrier" ::: "memory");
            if constexpr (z >= 28 && z < 32) qf[z - 28] = lds_r16(qr + (z - 28) * 64);
            if constexpr (z == HSAMP) fl = ld4(rs, fkh);
            if constexpr (z == 52) XW(qw + (C_KM - C_Q));
            if constexpr (z == 53 || z == 54) vx[z - 53] = lds_r16(VX + (z - 53) * 1024);
            if constexpr (z == 55) asm volatile("s_waitcnt lgkmcnt(2)\n\ts_barrier" ::: "memory");
            if constexpr (z >= 56 && z < 60) kmf[z - 56] = lds_r16(qr + (C_KM - C_Q) + (z - 56) * 64);
            if constexpr (z >= 48 && z < 52) LF_RD(8 + ((z - 48) >> 1), z & 1);
            if constexpr (z >= 60) LF_RD(10 + ((z - 60) >> 1), z & 1);
            SCHED();
            if constexpr (z == 19) HT(2);
            if constexpr (z == 32) HT(3);
            if constexpr (z == 47) HT(4);
            if constexpr (z == 64) HT(5);
            if constexpr (z == 20) VADD(fa, aq0[0], aq0[2]);
            if constexpr (z == 21) VADD(fb, aq0[1], aq0[3]);
            if constexpr (z == 22) { VADD(fc, aq1[0], aq1[2]); VADD(fd, aq1[1], aq1[3]); }
            if constexpr (z == 23) { asm volatile("v_cmp_u_f32 %0, %1, %2" : "=s"(nanq) : "v"(fa), "v"(fb)); VCVT(pa, fa, fc); VCVT(pb, fb, fd); }
            if constexpr (z == 39 || z == 40 || z == 41) NOPS(1);
            if constexpr (z == 42) VMAX3(mx, s0[0], s0[1], s0[2]);
            if constexpr (z == 43) { VMAX3(mx, mx, s0[3], s1[0]); VMAX3(my, s1[1], s1[2], s1[3]); }
            if constexpr (z == 44 || z == 46) { VMAX(fa, mx, my); VMAX(my, mx, my); mx = fa; }
            if constexpr (z == 45) { SEL(sx[0], s0[0], s1[0]); asm volatile("v_permlane16_swap_b32 %0, %1" : "+v"(mx), "+v"(my)); }
            if constexpr (z == 47) { SEL(sx[1], s0[1], s1[1]); asm volatile("v_permlane32_swap_b32 %0, %1" : "+v"(mx), "+v"(my)); }
            if constexpr (z == 48) asm volatile("v_max_f32 %0, %0, %1\n\tv_mul_f32 %0, 0xbe0293ee, %0" : "+v"(mx) : "v"(my));
            if constexpr (z == 49) { VADD(fa, ak0[0], ak0[2]); VADD(fb, ak0[1], ak0[3]); }
            if constexpr (z == 50) { VADD(fc, ak1[0], ak1[2]); VADD(fd, ak1[1], ak1[3]); }
            if constexpr (z == 51) { VCVT(pa, fa, fc); VCVT(pb, fb, fd); }
            if constexpr (z == 52) SEL(sx[2], s0[2], s1[2]);
            if constexpr (z == 53) { SEL(sx[3], s0[3], s1[3]); VFMC(t[0], sx[0], mx); }
            if constexpr (z == 54) { VFMC(t[1], sx[1], mx); VFMC(t[2], sx[2], mx); }
            if constexpr (z == 55) VFMC(t[3], sx[3], mx);
            if constexpr (z >= 56 && z < 60) VEXP(t[z - 56], t[z - 56]);
            if constexpr (z == 60) { VCVT(e01, t[0], t[1]); VCVT(e23, t[2], t[3]); }
            if constexpr (z == 61) { QPERM(P.x, e01, "[0,1,0,1]"); QPERM(P.z, e01, "[2,3,2,3]"); }
            if constexpr (z == 62) { QPERM(P.y, e23, "[0,1,0,1]"); QPERM(P.w, e23, "[2,3,2,3]"); }
            if constexpr (z == 63) { VADD(fa, av0[0], av0[2]); VADD(fb, av0[1], av0[3]); }
            if constexpr (z == 64) VCVT(vm.x, fa, fb);
            if constexpr (z == 67) { asm volatile("" :: "v"(sm)); VFMC(tm0, sm[0], mx); VFMC(tm1, sm[1], mx); }
            SCHED();
        };
        {
            u32 tmp;
            asm volatile("s_mov_b32 m0, %2\n\t" WAIT_TXT("%0", "%1") "buffer_load_dwordx4 %4, %5, %6 offen sc1 lds\n\tbuffer_load_dwordx4 %4, %5, %6 offen offset:1024 sc1 lds\n\t"
                         "s_mov_b32 m0, %3\n\ts_nop 0\n\tbuffer_load_dwordx4 %4, %5, %7 offen sc1 lds\n\ts_waitcnt vmcnt(0) lgkmcnt(0)\n\ts_barrier"
                         : "=&s"(tmp) : "s"(uu(tm - 1u)), "s"(dm), "s"(dm2), "v"(l16), "s"(srd), "s"(ms), "s"(ms2) : "vcc", "scc", "memory");
        }
#pragma unroll
        for (int ks = 0; ks < 4; ++ks) A[ks] = lds_r16(LB + C_MEM + ks * 1024);
        SCHED();
        st4s(rs, ofl, 0, sv);
        SCHED();
        static_for<0, 24>(slot);
        if (__builtin_expect(nanq != 0ull, 0)) {
            u32 mn = ~0u;
#pragma unroll
            for (int i = 0; i < 10; ++i) mn = min(min(min(mn, A[i].x ^ PSENT), A[i].y ^ PSENT), min(A[i].z ^ PSENT, A[i].w ^ PSENT));
            nanq = missed < SPIN_CAP ? __builtin_amdgcn_ballot_w64(mn == 0u) : 0ull;
        }
        if (__builtin_expect(nanq == 0ull, 1)) {
        static_for<24, 68>(slot);
        PV(7, 1); ones = lds_r16(ZP + 64); SCHED();
        VMAX(fa, tm0, tm1);
        if (__builtin_expect(__builtin_amdgcn_ballot_w64(fa > 24.0f) != 0ull, 0)) {
            const float up = fmaxf(max32(max16(fmaxf(tm0, tm1))), 0.f);
            tm0 -= up; tm1 -= up;
#pragma unroll
            for (int i = 0; i < 4; ++i) t[i] = __builtin_amdgcn_exp2f(__builtin_fmaf(sx[i], CEXP, mx) - up);
            e01 = pk(t[0], t[1]); e23 = pk(t[2], t[3]);
            asm volatile("s_nop 1" : "+v"(e01), "+v"(e23));
            QPERM(P.x, e01, "[0,1,0,1]"); QPERM(P.z, e01, "[2,3,2,3]"); QPERM(P.y, e23, "[0,1,0,1]"); QPERM(P.w, e23, "[2,3,2,3]");
            NOPS(1);
            MF0(oa, vx[0], P); MF0(ob, vx[1], P);
            NOPS(7);
        }
        SCHED();
        PV(1, 1); VEXP(tm0, tm0); SEL(oa[0], oa[0], ob[0]); SCHED();
        PV(8, 1); VEXP(tm1, tm1); SEL(oa[1], oa[1], ob[1]); SCHED();
        PV(9, 1); VCVT(pmm, tm0, tm1); SEL(oa[2], oa[2], ob[2]); SCHED();
        PV(2, 1);
        asm volatile("v_cndmask_b32 %0, %2, 0, %3\n\tv_cndmask_b32 %1, 0, %2, %3" : "=&v"(pm.x), "=&v"(pm.y) : "v"(pmm), "s"(himask)); SCHED();
        PV(3, 1); SEL(oa[3], oa[3], ob[3]); SCHED();
        MF0(rsum, ones, P); NOPS(4);
        VADD(fc, av1[0], av1[2]); VADD(fd, av1[1], av1[3]);
        { const v2u ones2 = ones.lo; MF1H(rsum, ones2, pm); }
        VCVT(vm.y, fc, fd); NOPS(1);
        MF1H(oa, vm, pm);
        {
            float inv; v4u oc;
            asm volatile("s_nop 3\n\tv_rcp_f32 %0, %1\n\ts_nop 2" : "=v"(inv) : "v"(rsum[0]), "v"(rsum));
            PIN(oa);
            const f32x2 i2 = {inv, inv};
            const f32x2 u0 = oa.lo * i2, u1 = oa.hi * i2;
            asm volatile("v_cvt_pk_bf16_f32 %0, %4, %5\n\tv_cvt_pk_bf16_f32 %2, %4, %5\n\tv_cvt_pk_bf16_f32 %1, %6, %7\n\tv_cvt_pk_bf16_f32 %3, %6, %7\n\t"
                         "v_permlane16_swap_b32 %0, %2\n\ts_nop 0\n\tv_permlane16_swap_b32 %1, %3"
                         : "=&v"(oc.x), "=&v"(oc.y), "=&v"(oc.z), "=&v"(oc.w) : "v"(u0.x), "v"(u0.y), "v"(u1.x), "v"(u1.y));
            st16(rs, oso, oc);
        }
        SCHED();
        unsigned long long clk;
        asm volatile("s_memrealtime %0" : "=s"(clk));
        HT(6);
        PIN(fl);
        v4u tq[4], tk[8], tv[2];
        f32x4 c0, c1, cm;
        u32 oo, kvo; bool kvok;
        u32 ln = l16; PIN(ln);
        lptr QX = lds + C_QX + (w & 1) * 4096 + ln, VO = lds + C_V + (w ^ 1) * 2048 + ln, VMX = lds + C_VM + w * 512 + (ln >> 1);
#pragma unroll
        for (int i = 0; i < 2; ++i) { tq[i] = lds_r16(QX + i * 1024); tk[2 * i] = lds_r16(LB + C_K + i * 1024); tk[2 * i + 1] = lds_r16(LB + C_K + 4096 + i * 1024); }
        *(LAS v2u*)VMX = vm;
        SCHED();
        SFOR(i, 12)
            if constexpr (i == 0) MF0(cm, kmf[0], tq[0]); else if constexpr (i == 1) MF0(c0, tk[0], tq[0]); else if constexpr (i == 2) MF0(c1, tk[1], tq[0]);
            else if constexpr (i % 3 == 0) MF1(cm, kmf[i / 3], tq[i / 3]); else if constexpr (i % 3 == 1) MF1(c0, tk[2 * (i / 3)], tq[i / 3]); else MF1(c1, tk[2 * (i / 3) + 1], tq[i / 3]);
            if constexpr (i == 0) { tq[2] = lds_r16(QX + 2048); tk[4] = lds_r16(LB + C_K + 2048); }
            if constexpr (i == 1) { tk[5] = lds_r16(LB + C_K + 6144); tq[3] = lds_r16(QX + 3072); }
            if constexpr (i == 2) { tk[6] = lds_r16(LB + C_K + 3072); tk[7] = lds_r16(LB + C_K + 7168); }
            if constexpr (i == 3) { tv[0] = lds_r16(VO); tv[1] = lds_r16(VO + 1024); }
            if constexpr (i == 4) { oo = ln + (ln & 0x300u) + ((vsrc ^ 8192u) + (u32)((w & 1) * 256)); PIN(oo); kb += NH * 16384; PINS(kb); }
            if constexpr (i == 5) { ksrc = kb + (((kmask >> ((s + 1) & 63)) & 1ull) ? 8192u : 0u); PINS(ksrc); }
            if constexpr (i == 6) { vsrc = kb + (((vmask >> ((s + 1) & 63)) & 1ull) ? W_VO - W_KQ + 8192u : W_VO - W_KQ); PINS(vsrc); }
            if constexpr (i == 7) { kvok = ((u32)__builtin_amdgcn_ballot_w64(fl < (u32)s + 2) & 7u) == 0u; }
            if constexpr (i == 8) { kvo = l16 | (kvok ? 0u : OOB); PIN(kvo); }
            SCHED();
        SEND
        asm volatile("s_waitcnt lgkmcnt(0)\n\ts_barrier" : "+s"(clk) :: "memory");
        const v2u tvm = *(LAS v2u*)(VMX + (512 - (w & 1) * 1024));
        const v4u pv = lds_r16(ZP + 80);
        DMA32(dk, kvo, ksrc);
        SCHED(); asm volatile("" :: "v"(cm));
        float u[10], m, m2;
        {
            float NEG = -1000000000.0f / SCALE; PIN(NEG);
#define MSK(d, a, i) asm volatile("v_cndmask_b32 %0, %1, %2, %3" : "=v"(d) : "v"(NEG), "v"(a), "s"(vis[i]))
#pragma unroll
            for (int i = 0; i < 4; ++i) MSK(u[i], c0[i], i);
#pragma unroll
            for (int i = 0; i < 4; ++i) MSK(u[4 + i], c1[i], 4 + i);
            asm volatile("v_max3_f32 %0, %2, %3, %10\n\tv_max3_f32 %1, %4, %5, %11\n\tv_max3_f32 %0, %0, %6, %7\n\tv_max3_f32 %1, %1, %8, %9\n\t"
                         "v_max_f32 %0, %0, %1\n\tv_mov_b32 %1, %0\n\ts_nop 1\n\tv_permlane16_swap_b32 %0, %1\n\tv_max_f32 %0, %0, %1\n\tv_mov_b32 %1, %0\n\ts_nop 1\n\t"
                         "v_permlane32_swap_b32 %0, %1\n\tv_max_f32 %0, %0, %1\n\tv_mul_f32 %0, 0xbe0293ee, %0"
                         : "=&v"(m), "=&v"(m2) : "v"(u[0]), "v"(u[1]), "v"(u[2]), "v"(u[3]), "v"(u[4]), "v"(u[5]), "v"(u[6]), "v"(u[7]), "v"(cm[0]), "v"(cm[1]));
        }
        DMA32(dv, kvo, vsrc);
        {
            v4u px; v2u p0, p1;
#define EX2(d, a, b) asm volatile("v_fmamk_f32 %0, %2, 0x3e0293ee, %4\n\tv_fmamk_f32 %1, %3, 0x3e0293ee, %4\n\tv_exp_f32 %0, %0\n\tv_exp_f32 %1, %1" : "=&v"(d.x), "=&v"(d.y) : "v"(a), "v"(b), "v"(m))
            f32x2 ex[5];
            EX2(ex[0], u[0], u[1]); EX2(ex[1], u[2], u[3]); EX2(ex[2], u[4], u[5]); EX2(ex[3], u[6], u[7]); EX2(ex[4], cm[0], cm[1]);
            asm volatile("v_cvt_pk_bf16_f32 %0, %8, %9\n\tv_cvt_pk_bf16_f32 %1, %10, %11\n\tv_cvt_pk_bf16_f32 %2, %12, %13\n\tv_cvt_pk_bf16_f32 %3, %14, %15\n\t"
                         "v_mov_b32 %5, 0\n\tv_mov_b32 %6, 0\n\tv_cvt_pk_bf16_f32 %4, %16, %17\n\tv_cvt_pk_bf16_f32 %7, %16, %17"
                         : "=&v"(px.x), "=&v"(px.y), "=&v"(px.z), "=&v"(px.w), "=&v"(p0.x), "=&v"(p0.y), "=&v"(p1.x), "=&v"(p1.y)
                         : "v"(ex[0].x), "v"(ex[0].y), "v"(ex[1].x), "v"(ex[1].y), "v"(ex[2].x), "v"(ex[2].y), "v"(ex[3].x), "v"(ex[3].y), "v"(ex[4].x), "v"(ex[4].y));
            f32x4 o[4], sum;
            const v2u ones2 = ones.lo;
            const u32 now = (u32)clk;
            u32 oz;
            SCHED();
            MF0(sum, ones, px); { const u32 q = ksrc ^ 8192u; DMA32(dq, kvo, q); } SCHED();
            MF0(o[0], vx[0], px);
            MF0(o[1], vx[1], px);
            MF0(o[2], tv[0], px); oz = (oso & ~(3u << 17)) | (u32)((s + 3) & 3) << 17; PIN(oz); SCHED();
            MF0(o[3], tv[1], px); oso = (oso & ~(3u << 17)) | (u32)((s + 1) & 3) << 17; PIN(oso); SCHED();
            MF1H(sum, ones2, p0);
            MF1H(o[0], vm, p0);
            MF1H(o[1], vm, p1); st16(rs, oz, pv); SCHED();
            MF1H(o[2], tvm, p0);
            MF1H(o[3], tvm, p1);
            float inv;
            asm volatile("s_nop 2\n\tv_rcp_f32 %0, %1\n\ts_nop 0" : "=v"(inv) : "v"(sum[0]), "v"(sum));
            asm volatile("" : "+v"(o[0]), "+v"(o[1]));
            st16(rs, oo, (v4u){pk2(o[0].lo * inv), pk2(o[0].hi * inv), pk2(o[1].lo * inv), pk2(o[1].hi * inv)});
            asm volatile("" : "+v"(o[2]), "+v"(o[3]));
            st16(rs, oo ^ 2048u, (v4u){pk2(o[2].lo * inv), pk2(o[2].hi * inv), pk2(o[3].lo * inv), pk2(o[3].hi * inv)});
            SCHED();
            if (__builtin_expect(h == 0 && w == 3, 0)) {
                st4s(rs, ln == 0 ? hint + 128u : OOB, 0, (u32)s << 16 | min(missed, 1u) << 14 | min(now - tm, 0x3FFFu));
                LST(L * 64 + s, now); LST(128 + L * 64 + s, ((int)(rdl(fl, 3) - now) >= CH_VIS - 4) | ((int)kvok << 1) | ((int)min(missed, 3u) << 2)); LST(640 + L * 64 + s, tm); LST(1728 + L * 64 + s, __builtin_amdgcn_s_memtime());
            }
            tm = rdl(fl, 5);
            missed = missed == SPIN_CAP ? missed : 0u;
            if (__builtin_expect(rdl(fl, 3) - now + CH_LATE > 4000u, 0)) {
#pragma unroll 1
                for (int n = 0; n < 64; ++n) {
                    CFENCE();
                    const u32 hv = ld4(rs, fkh);
                    tm = rdl(hv, 5);
                    if (rdl(hv, 3) - now + CH_LATE <= 4000u) break;
                }
            }
        }
        LS2(L == 0 && w == 0, h);
        if (__builtin_expect(!kvok, 0) && s + 1 < NSEG) {
            st4s(rs, (ln == 0 && w == 0) ? hint + 4096u + (u32)h * 128u : OOB, 0, (u32)s + 1u);
            wait_flags(rs, ln < 48u ? fkh : OOB, (u32)s + 2, dead);
            DMA32(dk, l16, ksrc); DMA32(dv, l16, vsrc); DMA32(dq, l16, ksrc ^ 8192u);
        }
        ++s;
        } else {
            u32 ln = l16; PIN(ln);
            const u32 hv = ld4(rs, ln == 96u ? foff(F_ERR) : fkh);
            tm = rdl(hv, 5);
            *(LAS u32*)(lds + C_DUMP + (w == 0 ? 0 : 64)) = rdl(hv, 6);
            BAR();
            missed = uu(*(LAS u32*)(lds + C_DUMP)) != 0u ? SPIN_CAP : missed + 1;
            st4s(rs, missed == SPIN_CAP ? foff(F_ERR) : OOB, 0, 1u);
            BAR();
        }
    }
    DRAIN(); BAR();
    st4s(rs, ofl, 0, NSEG);
}

#define WO_GAP 6
DEV void role_wo(const Params& p, lptr lds, int j) {
    const int tid = threadIdx.x, lane = tid & 63, w = uni(tid >> 6), g = lane >> 4, r = lane & 15;
#define WROT 0
    const int rg = j & 1, cg = j >> 1, rot = cg & WROT;
    const rsrc_t rs = mk_rsrc(p.ws, W_END);
    v8u wo[8][5];
    {
        const v4u so_ = mk_srd(p.Wo, DM * NQ * 2);
        u32 vo[5];
#pragma unroll
        for (int i = 0; i < 5; ++i) { const int n = 64 * i + lane; vo[i] = (u32)((n / 5) * DM + cg * 40 + (n % 5) * 8) * 2u; }
        auto fill = [&](int b) {
            const u32 dst = uu(w * 40960 + (b & 3) * 10240), src = uu((8 * w + ((b + rot) & 7)) * (DH * DM * 2));
#pragma unroll
            for (int i = 0; i < 10; ++i) DMAIN(so_, dst + i * 1024, vo[i % 5], src + (i / 5) * (64 * DM * 2));
        };
        __builtin_amdgcn_s_sleep(ST_WO);
#pragma unroll 1
        for (int b = 0; b < 4; ++b) fill(b);
        const int q4 = r >> 2;
        lptr tb = lds + w * 40960 + (32 * (g >> 1) + 16 * (q4 >> 1) + 8 * ((r >> 1) & 1) + 4 * (g & 1) + (q4 & 1)) * 80 + (r & 1) * 8;
        SFOR(b, 8)
            VMW(b < 5 ? 30 : 70 - 10 * b);
#pragma unroll
            for (int t = 0; t < 5; ++t) {
                lptr q = tb + (b & 3) * 10240 + t * 16;
                wo[b][t] = tr_frag(q, q + 160, q + 64 * 80, q + 64 * 80 + 160);
                if (b * 5 + t < 32) asm volatile("" : "+a"(wo[b][t])); else asm volatile("" : "+v"(wo[b][t]));
            }
            CFENCE();
            if constexpr (b < 4) fill(b + 4);
        SEND
    }
    BAR();
    EV(6);
    const u32 idx = (r & 2) ? 0xEEEEu : 0x4444u;
    bool dead = false;
    const u32 pol0 = get_pol(rs) & 1u;
    const u32 l16 = (u32)lane * 16u;
    u32 oo[8];
#pragma unroll
    for (int i = 0; i < 8; ++i) oo[i] = uu(W_ORING + ring_map((u32)(rg * 32768 + (8 * w + ((i + rot) & 7)) * 1024), pol0));
    const u32 hint = W_MRING + 4 * 32768u + pol0 * 8192u;
    u32 hvo[2];
#pragma unroll
    for (int l = 0; l < 2; ++l) hvo[l] = lane < 2 ? hint + (u32)(l ^ 1) * (MB / 4) + (u32)lane * 4u : lane == 2 ? hint + (u32)l * (MB / 4) + 128u : lane == 3 ? foff(F_ERR) :
                 (lane >= 32 && j == 0 && w == 3) ? hint + (u32)l * (MB / 4) + (u32)lane * 128u : OOB;
    lptr xw = r < 8 ? lds + w * 640 + (2 * (g & 1) + (g >> 1)) * 32 + r * 4 : lds + 8192 + lane * 4;
    const int G = ((5 * cg) >> 1) + w, tt = 2 * G + ((lane >> 3) & 1) - 5 * cg;
    const bool pub = w < 3 && lane < 32 && tt >= 0 && tt < 5;
    lptr xr = lds + (pub ? tt * 128 + (2 * (lane >> 4) + ((lane >> 2) & 1)) * 32 + (lane & 3) * 8 : 0);
    const u32 mpo = pub ? W_MRING + ring_map((u32)(G * 256 + rg * 128 + lane * 4), pol0) : OOB;
    wait_flags(rs, lane == 0 ? near_(foff(F_QKV), 0u, pol0) : OOB, 1u, dead);
    constexpr u32 PER0 = CH_W0 + CH_MW + CH_H0 + CH_MH + 2 * CH_VIS;
    const bool lead = j == 0 && w == 3;
    const u32 hso = (lead && lane == 0) ? hint : OOB;
    MK_SRD();
    u32 t = 0, tn = 0;
    if (j == 0) {
        for (int n = 0; n < SPIN_CAP && !dead; ++n) {
            CFENCE();
            if (all_(ld4(rs, W_ORING + pol0 * 8192u + (u32)w * 16384u + (u32)lane * 128u) != PSENT)) break;
            __builtin_amdgcn_s_sleep(1);
        }
        BAR();
    }
    if (lead) {
        t = rt32() + 40u; tn = t + PER0 / 2 + CH_REC;
        st16(rs, lane < 2 ? hint + (u32)lane * (MB / 4) : OOB, (v4u){lane == 0 ? t : tn, PER0, (lane == 0 ? t : tn) + (CH_W0 + CH_VIS + CH_MW + CH_REC), 0u});
    } else {
        for (int n = 0; n < SPIN_CAP && !dead; ++n) {
            CFENCE();
            const u32 hv = ld4(rs, hvo[1]);
            t = rdl(hv, 0);
            if (rdl(hv, 1) < 400u) break;
            __builtin_amdgcn_s_sleep(1);
        }
    }
    int sn[2] = {0, 0};
    u32 hav = 4 * CH_H0, wav = 4 * CH_W0, mh = 8 * CH_MH, mw = 8 * CH_MW, ok[2] = {0u, 0u}, dw[2] = {CH_W0 + CH_VIS + CH_MW + CH_REC, CH_W0 + CH_VIS + CH_MW + CH_REC};
    u32 zero = 0, hv = 0xFFFFu, pst = 1, pdur = 0, pmp = OOB;
    const u32 psent = PSENT;
    lptr sw = lds + 2560 + w * 4;
    bool stop = false;
    asm volatile("" : "+v"(zero), "+v"(sw));
    auto turn = [&](auto lc) __attribute__((always_inline)) {
        constexpr int L = decltype(lc)::value, M = L ^ 1;
        u32 now, done, rep, rd, lost, dh, dn, nt, za; v4u hw;
        const int sc = sn[L];
        auto books = [&](auto ic) __attribute__((always_inline)) {
            constexpr int i = decltype(ic)::value;
            if constexpr (i == 0) { rep = rdl(hv, 2); PINS(rep); }
            if constexpr (i == 1) { lost = (u32)(__builtin_amdgcn_ballot_w64(hv == (u32)sn[M]) >> 32); PINS(lost); }
            if constexpr (i == 2) { lost |= (rep >> 16) ^ (u32)sn[M]; PINS(lost); }
            if constexpr (i == 3) { lost |= ok[M] ^ 2u; rd = rep & 0xFFFFu; PINS(lost); PINS(rd); }
            if constexpr (i == 4) { lost = lost == 0u ? CH_UP * 8u : 0u; PINS(lost); }
            if constexpr (i == 5) { dn = wav >> 2; PINS(dn); pdur = min(pdur, dn + 8u); PINS(pdur); }
            if constexpr (i == 6) { pdur = (done & ok[M]) ? pdur : dn; PINS(pdur); }
            if constexpr (i == 7) { wav += pdur - dn; PINS(wav); }
            if constexpr (i == 8) { dh = hav >> 2; PINS(dh); nt = min(rd, dh + 8u); PINS(nt); }
            if constexpr (i == 9) { nt = rd < 0x3FFFu ? nt : dh; PINS(nt); }
            if constexpr (i == 10) { hav += nt - dh; PINS(hav); }
            if constexpr (i == 11) { mw = max(mw - 1u, CH_MW * 8u); PINS(mw); }
            if constexpr (i == 12) { mh = max(mh - 1u, CH_MH * 8u); PINS(mh); }
            if constexpr (i == 13) { rd = rd >> 14 == 1u ? lost : 0u; PINS(rd); }
            if constexpr (i == 14) { mw += rd; mh += lost - rd; PINS(mw); PINS(mh); }
            if constexpr (i == 15) { ok[M] = done ? 1u : (ok[M] & 1u) << 1; PINS(ok[M]); }
            if constexpr (i == 16) { dh = (2u * hav + mh) / 8u + CH_VIS; PINS(dh); }
            if constexpr (i == 17) { dn = (2u * wav + mw) / 8u + CH_VIS; PINS(dn); }
            if constexpr (i == 18) { nt = dw[L] + dh; PINS(nt); }
            if constexpr (i == 19) { dh += dn; dw[L] = dn; sn[M] += (int)done; PINS(dh); PINS(sn[M]); }
            if constexpr (i == 20) { rd = tn - t + (dh >> 1); PINS(rd); }
            if constexpr (i == 21) { rd = sn[M] + 1 < NSEG ? rd : 0u; PINS(rd); }
            if constexpr (i == 22) { nt = t + (u32)max((int)nt, (int)rd); PINS(nt); }
            if constexpr (i == 23) { hw.x = nt; hw.y = dh; PIN(hw.x); PIN(hw.y); }
            if constexpr (i == 24) { hw.z = nt + dn; hw.w = 0u; PIN(hw); }
            if constexpr (i == 25) st16(rs, hso + L * (MB / 4), hw);
        };
        if (__builtin_expect(sc + 1 < NSEG, 1)) {
            u32 vo = l16 + (L * (MB / 2) + (u32)(sc & 3) * 131072u), mp, hn;
            PIN(vo);
#define WT(k) do {} while (0)
            WT(0);
            v4u d[8];
            {
                u32 tmp;
                asm volatile(WAIT_TXT("%1", "%2") "buffer_load_dwordx4 %0, %3, %4, %5 offen sc1\n\ts_waitcnt lgkmcnt(0)"
                             : "=&v"(d[0]), "=&s"(tmp) : "s"(uu(t - 1u)), "v"(vo), "s"(srd), "s"(oo[0]) : "vcc", "scc", "memory");
            }
            SFOR(i, 7)
                asm volatile("buffer_load_dwordx4 %0, %1, %2, %3 offen sc1" : "=v"(d[i + 1]) : "v"(vo), "s"(srd), "s"(oo[i + 1]) : "memory");
                SCHED();
                if constexpr (i == 0) { done = uu(pst) ^ 1u; PINS(done); }
                static_for<3 * i, 3 * i + 3>(books);
                SCHED();
            SEND
            hn = ld4(rs, hvo[L]);
            SCHED();
            WT(1);
            static_for<21, 26>(books);
            za = done ? pmp : OOB;
            st4s(rs, za, 0, psent);
            *(LAS u32*)(sw + L * 4096) = zero;
            f32x4 acc[5] = {};
            mp = mpo + (L * (MB / 4) + (u32)((sc + 1) & 3) * 32768u); pmp = mpo + (L * (MB / 4) + (u32)(sc & 3) * 32768u);
            asm volatile("" : "+v"(acc[0]), "+v"(acc[1]), "+v"(acc[2]), "+v"(acc[3]), "+v"(acc[4]), "+v"(mp), "+v"(pmp));
            SCHED();
            WT(2);
            float f0[5], f1[5]; unsigned long long bad;
#define WSW(o_) asm volatile("v_permlane32_swap_b32 %0, %1" : "+v"(f0[o_]), "+v"(f1[o_]))
#define WAD(o_) asm volatile("v_add_f32_dpp %0, %1, %0 row_ror:8 row_mask:0xf bank_mask:0xf" : "+v"(f0[o_]) : "v"(f1[o_]))
#define WFO(o_) do { VADD(f0[o_], acc[o_][0], acc[o_][2]); VADD(f1[o_], acc[o_][1], acc[o_][3]); } while (0)
#define WWR(o_) *(LAS float*)(xw + L * 4096 + (o_) * 128) = f0[o_]
            asm volatile("s_waitcnt vmcnt(10)" : "+v"(d[0]));
            if (__builtin_expect(__builtin_amdgcn_ballot_w64(d[0].x == PSENT) != 0ull, 0)) {
                asm volatile("s_waitcnt vmcnt(3)" : "+v"(d[1]), "+v"(d[2]), "+v"(d[3]), "+v"(d[4]), "+v"(d[5]), "+v"(d[6]), "+v"(d[7]));
#pragma unroll
                for (int i = 0; i < 5; ++i) *(LAS u32*)(xw + L * 4096 + i * 128) = psent;
                *(LAS u32*)(sw + L * 4096) = 1u;
            } else {
            SFOR(i, 40)
                constexpr int pc = i < 30 ? i / 5 : 6 + (i & 1), oc = i < 30 ? i % 5 : (i - 30) >> 1;
                if constexpr (oc == 0 && pc > 0) asm volatile("s_waitcnt vmcnt(%1)" : "+v"(d[pc]) : "i"(10 - pc));
                if constexpr (pc * 5 + oc < 32) SMF_A(acc[oc], d[pc], wo[pc][oc]); else SMF_V(acc[oc], d[pc], wo[pc][oc]);
                if constexpr (i == 0) WT(3);
                if constexpr (i == 20) WT(4);
                if constexpr (i == 31 || i == 33) NOPS(1);
                if constexpr (i == 32) NOPS(0);
                if constexpr (i == 34) WFO(0);
                if constexpr (i == 35) { asm volatile("v_cmp_u_f32 %0, %1, %2" : "=s"(bad) : "v"(f0[0]), "v"(f1[0])); WSW(0); }
                if constexpr (i == 36) WFO(1);
                if constexpr (i == 37) { WAD(0); WSW(1); }
                if constexpr (i == 38) { WWR(0); SCHED(); WFO(2); }
                if constexpr (i == 39) { WAD(1); WSW(2); }
                SCHED();
            SEND
            WT(5);
            WWR(1); SCHED(); WFO(3); WAD(2); WWR(2); SCHED(); WSW(3);
            if (__builtin_expect(bad != 0ull, 0)) {
                u32 mn = ~0u;
#pragma unroll
                for (int i = 0; i < 8; ++i) mn = min(mn, d[i].x ^ PSENT);
                if (all_(mn != 0u)) {
#pragma unroll
                    for (int i = 0; i < 8; ++i) mn = min(min(mn, d[i].y ^ PSENT), min(d[i].z ^ PSENT, d[i].w ^ PSENT));
                }
                *(LAS u32*)(sw + L * 4096) = all_(mn != 0u) ? 0u : 1u;
            }
            WFO(4); WAD(3); WWR(3); SCHED();
            asm volatile("v_permlane32_swap_b32 %0, %1\n\ts_nop 1\n\tv_add_f32_dpp %0, %1, %0 row_ror:8 row_mask:0xf bank_mask:0xf" : "+v"(f0[4]), "+v"(f1[4]));
            WWR(4);
            }
            WT(6);
            f32x2 a, b, c, e; u32 cw, mq; v4u sv;
            asm volatile("s_waitcnt lgkmcnt(0)\n\ts_barrier\n\tds_read_b64 %0, %5 offset:%7\n\tds_read_b64 %1, %5 offset:%7+640\n\tds_read_b64 %2, %5 offset:%7+1280\n\tds_read_b64 %3, %5 offset:%7+1920\n\t"
                         "ds_read_b128 %4, %6 offset:%7+2560\n\t"
                         "s_waitcnt lgkmcnt(3)\n\tv_pk_add_f32 %0, %0, %1\n\ts_waitcnt lgkmcnt(1)\n\tv_pk_add_f32 %2, %2, %3\n\ts_waitcnt lgkmcnt(0)\n\tv_pk_add_f32 %0, %0, %2"
                         : "=&v"(a), "=&v"(b), "=&v"(c), "=&v"(e), "=&v"(sv) : "v"(xr), "v"(zero), "i"(L * 4096) : "memory");
            asm volatile("v_cmp_u_f32 vcc, %2, %3\n\tv_cvt_pk_bf16_f32 %0, %2, %3\n\tv_cndmask_b32 %1, %4, %5, vcc" : "=&v"(cw), "=&v"(mq) : "v"(a.x), "v"(a.y), "v"(mp), "v"(OOB) : "vcc");
            st4s(rs, mq, 0, cw);
            SCHED();
            now = rt32();
            PIN(hn); hv = hn;
            WT(7);
            pst = sv.x | sv.y | sv.z | sv.w;
            if (__builtin_expect(__builtin_amdgcn_ballot_w64(mq != mp && pst == 0u) != 0ull, 0)) st4s(rs, mp, 0, cw);
        } else {
            done = uu(pst) ^ 1u;
            st4s(rs, done ? pmp : OOB, 0, psent);
            static_for<0, 25>(books);
            pst = 1;
            hv = ld4(rs, hvo[L]);
            PIN(hv);
            now = rt32();
        }
        pdur = now - t;
        const u32 per = rdl(hv, 1), th = rdl(hv, 0);
        t = lead ? tn : (int)(th - now) < -(int)(per >> 1) ? th + per : th;
        tn = nt;
        dw[M] += max((int)(now + CH_WX - t), 0);
        stop = stop || rdl(hv, 3) != 0u;
    };
    for (int k = 0; k < 2 * SPIN_CAP && !stop && (sn[0] + 1 < NSEG || sn[1] + 1 < NSEG); ++k) { turn(IC<0>{}); turn(IC<1>{}); }
    if (sn[0] + 1 < NSEG || sn[1] + 1 < NSEG) st4s(rs, tid == 0 ? foff(F_ERR) : OOB, 0, 1u);
}

__global__ void __launch_bounds__(256, 1) k_main(Params p) {
    lptr lds = (lptr)(u32)16 - 16;
    if (blockIdx.x == 0) LST(256, rt32());
    unsigned long long pc; int len;
    asm volatile("s_getpc_b64 %0\n.Lk_main_here:\n\ts_mov_b32 %1, .Lk_init_here-.Lk_main_here" : "=s"(pc), "=s"(len));
    const v4u code = ldin16(mk_rsrc((const void*)(pc & ~15ull), (u32)max(len, 0)), (((blockIdx.x >> 3) & 31u) * 256u + threadIdx.x) * 16u, 0);
    CFENCE();
    if (threadIdx.x == 0) {
        GAS u32* ctl = (GAS u32*)(p.ws + W_CTRL);
        const u32 xcc = (u32)__builtin_amdgcn_s_getreg(20 | (3 << 11)) & 7u;
        u32 role = xcc * 32u + __hip_atomic_fetch_add(ctl + (F_TK + xcc) * 32, 1u, __ATOMIC_RELAXED, __HIP_MEMORY_SCOPE_AGENT);
        if (role >= xcc * 32u + 32u) {
            u32 k = __hip_atomic_fetch_add(ctl + F_OVF * 32, 1u, __ATOMIC_RELAXED, __HIP_MEMORY_SCOPE_AGENT);
            for (int n = 0; n < SPIN_CAP; ++n) {
                u32 tot = 0;
                for (int j = 0; j < 8; ++j) tot += __hip_atomic_load(ctl + (F_TK + j) * 32, __ATOMIC_RELAXED, __HIP_MEMORY_SCOPE_AGENT);
                if (tot >= 256u) break;
                __builtin_amdgcn_s_sleep(2);
            }
            role = 255u;
            for (int j = 0; j < 8; ++j) {
                const u32 cn = __hip_atomic_load(ctl + (F_TK + j) * 32, __ATOMIC_RELAXED, __HIP_MEMORY_SCOPE_AGENT);
                const u32 fr = cn < 32u ? 32u - cn : 0u;
                if (k < fr) { role = (u32)j * 32u + cn + k; break; }
                k -= fr;
            }
        }
        *(LAS u32*)lds = role;
    }
    __syncthreads();
    const int role = uni((int)*(LAS u32*)lds), x = role >> 5, i = role & 31;
    asm volatile("" :: "v"(code));
    __syncthreads();
    LST(8192 + blockIdx.x * 16, role);
    if (x < 2) { role_head(p, lds, i >> 4, x * 16 + (i & 15)); __builtin_amdgcn_endpgm(); }
    if (x == 2) { role_wo(p, lds, i); __builtin_amdgcn_endpgm(); }
    const int no = x == 3 ? 30 : x < 6 ? 5 : 0;
    if (x > 3 && i >= 8) { role_qkv(p, lds, (x - 4) * 24 + i - 8); __builtin_amdgcn_endpgm(); }
    const bool isout = i < no;
    const int og = x == 3 ? i / 5 : x + 2, ng = i % 5, L = og >> 2, kq = og & 3;
    const int k = i - no, w = uni(threadIdx.x >> 6);
    const int j = isout ? (L == 1 ? 0 : 44) + kq * 5 + ng : x == 3 ? 36 + k : k < 2 ? 12 + 2 * x + k : x < 6 ? 24 + x : k < 5 ? 10 + 3 * x + k : 15 + 3 * x + k;
    const rsrc_t rs = mk_rsrc(p.ws, W_END);
    if (j < NKV) {
        const int nt = j < 320 - 7 * NKV ? 8 : 7;
#pragma unroll 1
        for (int q = 0; q < 4; ++q) kv_fetch(p, j + NKV * q, uu(q * KT_BYTES));
        Filt f;
#pragma unroll 1
        for (int q = 0; q < nt; ++q) {
            VMW(15); BAR();
            if (q == 0) EV(2);
            kv_fetch(p, j + NKV * (q + 4), uu(((q + 4) & 7) * KT_BYTES));
            kv_tile(p, rs, lds + q * KT_BYTES, lds + (q == 0 ? 8 : q - 1) * KT_BYTES, j + NKV * q);
        }
        filt_load(p, j, f);
        VMW(10); BAR();
        st4s(rs, threadIdx.x == 0 ? foff(F_KV + j) : OOB, 0, 1u);
        EV(4);
        conv_job<0>(p, rs, lds, j, f, 0);
        if (j >= 36) filt_load(p, 28 + j, f);
        DRAIN(); BAR();
        st4s(rs, threadIdx.x == 0 ? foff(F_CV + j) : OOB, 0, 2u);
        if (j >= 36) {
#pragma unroll 1
            for (int g = 28 + j; g < 160; g += 8) conv_job<2>(p, rs, lds, g, f, min(g + 8, 159));
            DRAIN(); BAR();
            st4s(rs, threadIdx.x == 0 ? foff(F_CV + 124 + j) : OOB, 0, 3u);
            EV(15);
            __builtin_amdgcn_endpgm();
        }
    }
    if (!isout) { if (j < 28) role_row<0>(p, lds, (j - 20) * 4 + w); else role_row<1>(p, lds, (j - 28) * 4 + w); __builtin_amdgcn_endpgm(); }
    role_out(p, lds, L, kq, ng);
}

__global__ void __launch_bounds__(256) k_init(unsigned char* ws) {
    __shared__ int vote[4];
    asm volatile(".Lk_init_here:");
    const int c = blockIdx.x, lane = threadIdx.x & 63, w = uni(threadIdx.x >> 6);
    const rsrc_t rs = mk_rsrc(ws, W_END);
    const u32 k = (u32)(c & 15) < NREG ? (u32)(c & 15) : 0u;
    const u32 off0 = k * (16 * MB) + 2 * MB - 65536 + ((u32)(c & 15) >= NREG ? (u32)(c & 3) + 1u : 0u) * 256 + (u32)(c >> 4) * 128 + (u32)w * 16384;
    st4s(rs, lane < 8 ? off0 + (u32)lane * 2048 : OOB, 0, 0u); DRAIN();
    int d0 = 1 << 16, d1 = 1 << 16;
#pragma unroll 1
    for (int q = 0; q < 5; ++q) {
        const unsigned long long t0 = __builtin_amdgcn_s_memtime();
        const u32 v = ld4(rs, lane == 0 ? off0 + (u32)((q ^ w) & 1) * 8192 + (u32)(q >> 1) * 2048 : OOB);
        asm volatile("s_waitcnt vmcnt(0)" :: "v"(v) : "memory");
        const int dt = (int)(__builtin_amdgcn_s_memtime() - t0);
        if (q & 1) d1 = min(d1, dt); else d0 = min(d0, dt);
    }
    const u32 xcc = (u32)__builtin_amdgcn_s_getreg(20 | (3 << 11)) & 0xfu;
    if (lane == 0) vote[w] = min(max((d0 - d1) * ((((xcc >> 2) ^ (u32)w) & 1u) ? -1 : 1), -250), 250);
    static_assert(2 * F_N <= 32 * 64, "clear");
    const u32 o = (u32)(c * 64 + lane);
    st4s(rs, w == 0 && (o < F_POL || o >= 64u) ? W_CTRL + o * 128u : OOB, 0, 0u);
    __syncthreads();
    st4s(rs, threadIdx.x == 0 ? foff(F_POL + c) : OOB, 0, (u32)((1 << 20) + (vote[0] + vote[1] + vote[2] + vote[3]) / 4));
}

extern "C" void kernel_launch(void* const* d_in, const int* in_sizes, int n_in, void* d_out, int out_size, void* d_ws, size_t ws_size,
                              hipStream_t stream) {
    Params p;
    p.x = (const u16*)d_in[0]; p.mem0 = (const u16*)d_in[1]; p.Wq = (const u16*)d_in[2]; p.Wk = (const u16*)d_in[3];
    p.Wv = (const u16*)d_in[4]; p.Wo = (const u16*)d_in[5];
    p.bk = (const u16*)d_in[8]; p.bv = (const u16*)d_in[9]; p.shift = (const u16*)d_in[10]; p.ssmk = (const u16*)d_in[11]; p.ssmD = (const u16*)d_in[12];
    p.out = (u16*)d_out;
    p.ws = (unsigned char*)(((size_t)d_ws + 2 * MB - 1) & ~(size_t)(2 * MB - 1));
    (void)hipFuncSetAttribute((const void*)k_main, hipFuncAttributeMaxDynamicSharedMemorySize, LDS_BYTES);
    hipLaunchKernelGGL(k_init, dim3(32), dim3(256), 0, stream, p.ws);
    hipLaunchKernelGGL(k_main, dim3(256), dim3(256), LDS_BYTES, stream, p);
}
```

```cpp
#include <hip/hip_runtime.h>
#include <hip/hip_bf16.h>

typedef unsigned short u16;
typedef unsigned int u32;
typedef u32 v2u __attribute__((ext_vector_type(2)));
typedef u32 v4u __attribute__((ext_vector_type(4)));
typedef u32 v8u __attribute__((ext_vector_type(8)));
typedef float f32x2 __attribute__((ext_vector_type(2)));
typedef float f32x4 __attribute__((ext_vector_type(4)));
typedef float f32x16 __attribute__((ext_vector_type(16)));
typedef __bf16 bf16x2 __attribute__((ext_vector_type(2)));
typedef __bf16 bf16x8 __attribute__((ext_vector_type(8)));
typedef short s16x4 __attribute__((ext_vector_type(4)));
#define LAS __attribute__((address_space(3)))
#define GAS __attribute__((address_space(1)))
typedef LAS unsigned char* lptr;
#define DEV __device__ __forceinline__

constexpr int SEQ = 2048, DM = 640, NH = 32, DH = 128, NSEG = 64, NQ = NH * DH;

constexpr u32 MB = 1u << 20;
constexpr u32 W_ORING = 0;
constexpr u32 W_MRING = MB;
constexpr u32 W_CTRL = 4 * MB;
constexpr u32 ACT = SEQ * DM * 2;
constexpr u32 H_TILE = 128 * 1024;
constexpr u32 W_H2 = 8 * MB;
constexpr u32 W_KQ = 16 * MB;
constexpr u32 W_VO = W_KQ + 64 * MB;
constexpr u32 W_P = W_VO + 64 * MB;
constexpr u32 P_ITEM = 512 * 1024;
constexpr u32 W_H1 = W_P + 44 * MB, W_Y = W_H1 + 8 * MB, W_X2 = W_Y + ACT;
constexpr u32 W_KVT = W_X2 + ACT;
constexpr u32 W_END = W_KVT + 3 * MB;
constexpr u32 NREG = 12;
static_assert((size_t)W_END + 2 * MB <= 256ull * MB && W_H1 >= (NREG - 1) * 16 * MB + 2 * MB && W_H1 <= NREG * 16 * MB, "workspace");
constexpr u32 F_EP = 0, F_RG = 32, F_LN = 64, F_KV = 192,
              F_QKV = 256, F_O = F_QKV + 192, F_P = F_O + 64, F_H2 = F_P + 64, F_CV = F_H2 + 64, F_ERR = F_CV + 191, F_N = F_CV + 192;
constexpr u32 W_SLOT = 3 * MB;
DEV u32 foff(u32 idx) { return W_CTRL + (idx >> 6) * 16384u + (idx & 63u) * 128u; }
DEV u32 near_(u32 o, u32 side, u32 pol) { return o + ((side ^ (pol >> (o >> 24)) ^ (o >> 21)) & 1u) * 8192u; }
DEV u32 p_item(int s) { return W_P + (u32)s * P_ITEM; }

constexpr u32 OOB = 0x80000000u;
constexpr u32 PSENT = 0x7F817F81u;
constexpr int SPIN_CAP = 40000;
constexpr int LDS_BYTES = 160 * 1024;
#define ST_HEAD 0
#define ST_WO 0

#define EV(k) do {} while (0)
#define LST(idx, v) do {} while (0)
#define LS2(cond, idx) do {} while (0)
struct Args {
    const u16 *x, *mem0, *Wq, *Wk, *Wv, *Wo, *bk, *bv, *shift, *ssmk, *ssmD;
    u16* out;
    unsigned char* ws;
};
struct Params : Args { u32 e0, e1, e2; };

typedef __amdgpu_buffer_rsrc_t rsrc_t;
DEV rsrc_t mk_rsrc(const void* p, u32 bytes) { return __builtin_amdgcn_make_buffer_rsrc((void*)p, 0, bytes, 0x00020000); }
DEV v4u ld16s(rsrc_t r, u32 off, u32 so) { return __builtin_amdgcn_raw_buffer_load_b128(r, off, so, 16); }
DEV v2u ld8s(rsrc_t r, u32 off, u32 so) { return __builtin_amdgcn_raw_buffer_load_b64(r, off, so, 16); }
DEV v4u ldin16(rsrc_t r, u32 off, u32 so) { return __builtin_amdgcn_raw_buffer_load_b128(r, off, so, 0); }
DEV v2u ldin8(rsrc_t r, u32 off, u32 so) { return __builtin_amdgcn_raw_buffer_load_b64(r, off, so, 0); }
DEV u32 ld4(rsrc_t r, u32 off) { return __builtin_amdgcn_raw_buffer_load_b32(r, off, 0, 16); }
DEV void st16(rsrc_t r, u32 off, v4u v) { __builtin_amdgcn_raw_buffer_store_b128(v, r, off, 0, 16); }
DEV void st8s(rsrc_t r, u32 off, u32 so, v2u v) { __builtin_amdgcn_raw_buffer_store_b64(v, r, off, so, 16); }
DEV void st4s(rsrc_t r, u32 off, u32 so, u32 v) { __builtin_amdgcn_raw_buffer_store_b32(v, r, off, so, 16); }
#define DRAIN() asm volatile("s_waitcnt vmcnt(0)" ::: "memory")
#define VMW(n) asm volatile("s_waitcnt vmcnt(%0)" :: "i"(n) : "memory")
#define BAR() asm volatile("s_waitcnt lgkmcnt(0)\n\ts_barrier" ::: "memory")
#define SCHED() __builtin_amdgcn_sched_barrier(0)
#define CFENCE() asm volatile("" ::: "memory")
#define NOPS(n) asm volatile("s_nop %0" :: "i"(n))
#define PIN(x) asm volatile("" : "+v"(x))
#define PINS(x) asm volatile("" : "+s"(x))

template <int V> struct IC { static constexpr int value = V; };
template <int I, int N, class F> DEV void static_for(F&& f) { if constexpr (I < N) { f(IC<I>{}); static_for<I + 1, N>(f); } }
#define SFOR(var, n) static_for<0, n>([&](auto ic_) __attribute__((always_inline)) { constexpr int var = decltype(ic_)::value;
#define SEND });

DEV u32 pk(float a, float b) { bf16x2 v = {(__bf16)a, (__bf16)b}; return __builtin_bit_cast(u32, v); }
DEV float lo_f(u32 u) { return __builtin_bit_cast(float, u << 16); }
DEV float hi_f(u32 u) { return __builtin_bit_cast(float, u & 0xffff0000u); }
DEV f32x2 up2(u32 u) { return (f32x2){lo_f(u), hi_f(u)}; }
DEV u32 pk2(f32x2 v) { return pk(v.x, v.y); }
DEV float rbf(float a) { return (float)(__bf16)a; }
DEV v4u lds_r16(lptr p) { return *(LAS v4u*)p; }
DEV void lds_w16(lptr p, v4u v) { *(LAS v4u*)p = v; }
DEV v2u lds_tr(lptr p) { return __builtin_bit_cast(v2u, __builtin_amdgcn_ds_read_tr16_b64_v4i16((LAS s16x4*)p)); }
DEV v4u tr2(lptr p0, lptr p1) { const v2u a = lds_tr(p0), b = lds_tr(p1); return (v4u){a.x, a.y, b.x, b.y}; }
DEV v8u cat8(v4u a, v4u b) { return __builtin_shufflevector(a, b, 0, 1, 2, 3, 4, 5, 6, 7); }
DEV f32x4 mfma16(v4u a, v4u b, f32x4 c) { return __builtin_amdgcn_mfma_f32_16x16x32_bf16(__builtin_bit_cast(bf16x8, a), __builtin_bit_cast(bf16x8, b), c, 0, 0, 0); }
DEV f32x16 mfma32(v4u a, v4u b, f32x16 c) { return __builtin_amdgcn_mfma_f32_32x32x16_bf16(__builtin_bit_cast(bf16x8, a), __builtin_bit_cast(bf16x8, b), c, 0, 0, 0); }

DEV float f_(u32 u) { return __builtin_bit_cast(float, u); }
DEV u32 u_(float f) { return __builtin_bit_cast(u32, f); }
template <int CTRL> DEV float dpp(float v) { return f_((u32)__builtin_amdgcn_update_dpp(0, (int)u_(v), CTRL, 0xf, 0xf, false)); }
DEV float max16(float v) { auto r = __builtin_amdgcn_permlane16_swap(u_(v), u_(v), false, false); return fmaxf(f_(r[0]), f_(r[1])); }
DEV float max32(float v) { auto r = __builtin_amdgcn_permlane32_swap(u_(v), u_(v), false, false); return fmaxf(f_(r[0]), f_(r[1])); }
DEV float add16(float v) { auto r = __builtin_amdgcn_permlane16_swap(u_(v), u_(v), false, false); return f_(r[0]) + f_(r[1]); }
DEV float add32(float v) { auto r = __builtin_amdgcn_permlane32_swap(u_(v), u_(v), false, false); return f_(r[0]) + f_(r[1]); }
DEV float wave_sum(float v) {
    v += dpp<0xB1>(v);
    v += dpp<0x4E>(v);
    v += dpp<0x124>(v);
    v += dpp<0x128>(v);
    return add32(add16(v));
}
DEV bool all_(bool c) { return __builtin_amdgcn_ballot_w64(!c) == 0ull; }
DEV int uni(int v) { return __builtin_amdgcn_readfirstlane(v); }
DEV u32 uu(u32 v) { return (u32)__builtin_amdgcn_readfirstlane((int)v); }
DEV u32 rdl(u32 v, int l) { return (u32)__builtin_amdgcn_readlane((int)v, l); }
DEV u32 rt32() { return (u32)__builtin_amdgcn_s_memrealtime(); }

DEV void wait_flags(rsrc_t rs, u32 off, u32 want, bool& dead) {
    if (dead) return;
#pragma unroll 1
    for (int n = 0;; ++n) {
        CFENCE();
        const u32 v = ld4(rs, off);
        if (all_(off >= OOB || v >= want)) break;
        __builtin_amdgcn_s_sleep(1);
        if ((n & 63) == 63) { if (uni((int)ld4(rs, foff(F_ERR))) != 0) { dead = true; break; } }
        if (n > SPIN_CAP) { st4s(rs, foff(F_ERR), 0, 1u); dead = true; break; }
    }
    CFENCE();
}

DEV bool boot_ok(const Params& p, u32 off, v4u v, u32 want) { return all_(off >= OOB || (v.x == p.e0 && v.y == p.e1 && v.z == p.e2 && v.w >= want)); }
DEV void boot_set(const Params& p, rsrc_t rs, u32 off, u32 v) { st16(rs, off, (v4u){p.e0, p.e1, p.e2, v}); }
DEV u32 boot_wait(const Params& p, rsrc_t rs, u32 off, u32 want, bool& dead) {
    v4u v;
#pragma unroll 1
    for (int n = 0;; ++n) {
        CFENCE();
        v = ld16s(rs, off, 0);
        if (boot_ok(p, off, v, want) || dead) break;
        __builtin_amdgcn_s_sleep(1);
        if (n > SPIN_CAP) { st4s(rs, foff(F_ERR), 0, 1u); dead = true; }
    }
    CFENCE();
    return v.w;
}

DEV u32 pol_off() { const int lane = threadIdx.x & 63; return lane < 32 ? foff(F_RG + lane) : OOB; }
DEV u32 pol_of(u32 raw) {
    const int lane = threadIdx.x & 63;
    const float v = add16(__builtin_amdgcn_fmed3f((float)((int)raw - (1 << 20)), -250.f, 250.f));
    const float lo = dpp<0x121>(v), hi = dpp<0x12F>(v), ex = dpp<0x122>(v) + dpp<0x123>(v) + dpp<0x124>(v);
    const float sum = v + lo + ((lane & 15) == 11 ? 0.f : hi) + ((lane & 15) == 0 ? ex : 0.f);
    return (u32)__builtin_amdgcn_ballot_w64(sum >= 0.f) & 0xFFFFu;
}
DEV u32 get_pol(const Params& p, rsrc_t rs, bool& dead) { return pol_of(boot_wait(p, rs, pol_off(), 0u, dead)); }
DEV u32 hblk(u32 base, int ls, int h, u32 side, u32 pol) { return near_(base + (u32)(ls * NH + h) * 16384u, side, pol); }
DEV f32x2 ln_stats(const f32x2 (&x)[5]) {
    const float c = f_(uu(u_(x[0].x)));
    f32x2 d[5];
#pragma unroll
    for (int i = 0; i < 5; ++i) d[i] = x[i] - c;
    const f32x2 s2 = (d[0] + d[1]) + (d[2] + d[3]) + d[4], q = (d[0] * d[0] + d[1] * d[1]) + (d[2] * d[2] + (d[3] * d[3] + d[4] * d[4]));
    const float dm = wave_sum(s2.x + s2.y) * (1.0f / DM), mean = c + dm;
    const float var = fmaxf(wave_sum(q.x + q.y) * (1.0f / DM) - dm * dm, 0.f);
    const float sd = rbf(__builtin_amdgcn_sqrtf(rbf(rbf(var) + 1e-5f)));
    return (f32x2){rbf(mean), __builtin_amdgcn_rcpf(sd)};
}
DEV void store_h(rsrc_t rs, u32 tile, int row, const u32 (&hp)[5]) {
    const u32 lane = threadIdx.x & 63;
    const u32 sw = (u32)(row & 15) * 16u, o8 = (lane >> 5) * 16384u + (((lane & 31u) * 8u) ^ sw), hb = tile + (u32)(row & 31) * 256u;
    st8s(rs, o8, hb, (v2u){hp[0], hp[1]});
    st8s(rs, o8, hb + 32768, (v2u){hp[2], hp[3]});
    st4s(rs, (lane * 4) ^ sw, hb + 65536, hp[4]);
}

#define DMA16(ldsb, vo, so) asm volatile("s_mov_b32 m0, %0\n\ts_nop 0\n\tbuffer_load_dwordx4 %1, %2, %3 offen sc1 lds" :: "s"(ldsb), "v"(vo), "s"(srd), "s"(so) : "memory")
#define DMA16I(ldsb, lo, vo, so, go) do { u32 so_; asm volatile("s_add_u32 m0, %1, %5\n\ts_add_u32 %0, %4, %6\n\tbuffer_load_dwordx4 %2, %3, %0 offen sc1 lds" \
                         : "=&s"(so_) : "s"(ldsb), "v"(vo), "s"(srd), "s"(so), "i"(lo), "i"(go) : "memory", "scc"); } while (0)
#define DMA32(ldsb, vo, so) asm volatile("s_mov_b32 m0, %0\n\ts_nop 0\n\tbuffer_load_dwordx4 %1, %2, %3 offen sc1 lds\n\tbuffer_load_dwordx4 %1, %2, %3 offen offset:1024 sc1 lds" \
                         :: "s"(ldsb), "v"(vo), "s"(srd), "s"(so) : "memory")
#define INPOL "nt"
#define INAUX 2
#define DMANT(sr, ldsb, vo, so) asm volatile("s_mov_b32 m0, %0\n\ts_nop 0\n\tbuffer_load_dwordx4 %1, %2, %3 offen " INPOL " lds" :: "s"(ldsb), "v"(vo), "s"(sr), "s"(so) : "memory")
#define DMAIN(sr, ldsb, vo, so) asm volatile("s_mov_b32 m0, %0\n\ts_nop 0\n\tbuffer_load_dwordx4 %1, %2, %3 offen lds" :: "s"(ldsb), "v"(vo), "s"(sr), "s"(so) : "memory")
DEV const u16* sel(bool c, const u16* a, const u16* b) { PINS(a); PINS(b); return c ? a : b; }
DEV v4u mk_srd(const void* q, u32 bytes) { return (v4u){uu((u32)(size_t)q), uu((u32)((size_t)q >> 32) & 0xffffu), bytes, 0x00020000u}; }
#define MK_SRD() const v4u srd = mk_srd(p.ws, W_END)

constexpr int CV_PAD = 40;
constexpr int CV_PL = 4416;
constexpr int CV_F = 8 * CV_PL;
constexpr int CV_KB = 80;
constexpr int CV_KV = 65 * CV_KB;
constexpr int CV_CH = CV_F + CV_KV;
static_assert(CV_CH % 16 == 0 && 4 * CV_CH <= LDS_BYTES, "conv lds");

struct Filt { v4u a[5], b[5]; };
DEV int chan(int grp, int w) { return 16 * (grp >> 2) + (grp & 3) + 4 * w; }
template <int N = 5, int AUX = INAUX> DEV void filt_load(const Params& p, int grp, Filt& f) {
    const int tid = threadIdx.x, lane = tid & 63, d = chan(grp, tid >> 6);
    const rsrc_t rf = mk_rsrc(p.ssmk, ACT);
#pragma unroll
    for (int i = 0; i < N; ++i) {
        const u32 s0 = (u32)(8 * (lane + 64 * i) - CV_PAD);
        f.a[i] = __builtin_amdgcn_raw_buffer_load_b128(rf, s0 < SEQ ? (u32)(d * SEQ + s0) * 2u : OOB, 0, AUX);
        f.b[i] = __builtin_amdgcn_raw_buffer_load_b128(rf, s0 + 8 < SEQ ? (u32)(d * SEQ + s0 + 8) * 2u : OOB, 0, AUX);
    }
}
constexpr int KT_BYTES = 18 * 1024, KT_X = 64 * 144, NKV = 44, KT_DUMP = 8 * KT_BYTES + KT_X;
DEV void kv_fetch(const Params& p, int tile, u32 dst) {
    const int lane = threadIdx.x & 63, w = uni(threadIdx.x >> 6);
    const v4u sk = mk_srd(p.bk, ACT), sv = mk_srd(p.bv, ACT), ss = mk_srd(sel(w == 0, p.bk, p.shift), w == 0 ? ACT : DM * 8);
    const u32 vo = tile < 320 ? (u32)((lane >> 3) * DM + (tile % 10) * 64 + (lane & 7) * 8) * 2u : OOB, r0 = uu((u32)((tile / 10) * 64 + 8 * w) * (DM * 2)), d = uu(dst + w * 1024);
    DMANT(sk, d + 1024, vo, r0); DMANT(sk, d + 5120, vo, r0 + 32 * DM * 2); DMANT(sv, d + 9216, vo, r0); DMANT(sv, d + 13312, vo, r0 + 32 * DM * 2);
    DMAIN(ss, uu(w == 0 ? dst : w == 1 ? dst + 17408 : KT_DUMP), w == 0 ? (tile >= 10 ? vo : OOB) : w == 1 && lane < 32 ? (u32)lane * 16u : OOB,
          uu(w == 0 ? (tile >= 10 ? r0 - 8 * DM * 2 : 0u) : (u32)(tile % 10) * 512u));
}
DEV void kv_tile(const Params& p, rsrc_t rs, lptr src, lptr xt, int tile) {
    const int tid = threadIdx.x;
    const v4u sv = lds_r16(src + 17408 + (tid & 31) * 16);
    lptr kp = src + ((tid >> 5) * 8 + 5) * 128 + (tid & 31) * 4;
    f32x2 kf[11];
#pragma unroll
    for (int i = 0; i < 11; ++i) kf[i] = up2(*(LAS u32*)(kp + i * 128));
    const f32x2 sh[4] = {{lo_f(sv.x), lo_f(sv.z)}, {hi_f(sv.x), hi_f(sv.z)}, {lo_f(sv.y), lo_f(sv.w)}, {hi_f(sv.y), hi_f(sv.w)}};
    u32 kv[8];
#pragma unroll
    for (int i = 0; i < 8; ++i) {
        const f32x2 acc = (kf[i] * sh[3] + kf[i + 1] * sh[2]) + (kf[i + 2] * sh[1] + kf[i + 3] * sh[0]);
        kv[i] = pk2(acc * up2(*(LAS u32*)(kp + 9216 + (i - 5) * 128)));
    }
    lds_w16(xt + (2 * (tid & 31)) * 144 + (tid >> 5) * 16, (v4u){__builtin_amdgcn_perm(kv[6], kv[7], 0x05040100u), __builtin_amdgcn_perm(kv[4], kv[5], 0x05040100u),
                                                             __builtin_amdgcn_perm(kv[2], kv[3], 0x05040100u), __builtin_amdgcn_perm(kv[0], kv[1], 0x05040100u)});
    lds_w16(xt + (2 * (tid & 31) + 1) * 144 + (tid >> 5) * 16, (v4u){__builtin_amdgcn_perm(kv[6], kv[7], 0x07060302u), __builtin_amdgcn_perm(kv[4], kv[5], 0x07060302u),
                                                                 __builtin_amdgcn_perm(kv[2], kv[3], 0x07060302u), __builtin_amdgcn_perm(kv[0], kv[1], 0x07060302u)});
    BAR();
#pragma unroll
    for (int i = 0; i < 2; ++i) {
        const int ch = i * 32 + (tid >> 3);
        st16(rs, W_KVT + (u32)(((tile % 10) * 64 + ch) * SEQ + (tile / 10) * 64 + (tid & 7) * 8) * 2u, lds_r16(xt + ch * 144 + (tid & 7) * 16));
    }
}
template <int MODE> DEV void conv_job(const Params& p, rsrc_t rs, lptr lds, int grp, Filt& f, int next) {
    const int tid = threadIdx.x, lane = tid & 63, w = uni(tid >> 6), bq = lane & 31, hh = lane >> 5;
    lptr F = lds + w * CV_CH;
    lptr fa = F + (bq & 7) * CV_PL + ((bq >> 3) - hh + 4) * 16;
    lptr kb = F + CV_F + CV_KB + hh * 16, kh = kb + (bq + 32) * CV_KB;
    bool dead = false;
    v4u kvr[4];
    const u32 D = p.ssmD[chan(grp, w)];
    auto kv_load = [&]() {
        if (MODE == 0) boot_wait(p, rs, lane < NKV ? foff(F_KV + lane) : OOB, 2u, dead);
#pragma unroll
        for (int i = 0; i < (MODE == 1 ? 2 : 4); ++i) kvr[i] = ld16s(rs, (u32)lane * 16u, W_KVT + (u32)(chan(grp, w) * SEQ + i * 512) * 2u);
    };
    auto windows = [&](int i) {
        if (i < 4 || lane < 16) {
            const u32 e[8] = {f.a[i].x, f.a[i].y, f.a[i].z, f.a[i].w, f.b[i].x, f.b[i].y, f.b[i].z, f.b[i].w};
            u32 o[7];
#pragma unroll
            for (int q = 0; q < 7; ++q) o[q] = __builtin_amdgcn_alignbit(e[q + 1], e[q], 16);
#pragma unroll
            for (int q = 0; q < 4; ++q) {
                lds_w16(F + (2 * q) * CV_PL + (64 * i + lane) * 16, (v4u){o[q], o[q + 1], o[q + 2], o[q + 3]});
                lds_w16(F + (2 * q + 1) * CV_PL + (64 * i + lane) * 16, (v4u){e[q + 1], e[q + 2], e[q + 3], e[q + 4]});
            }
        }
    };
    lptr yw = lds + (lane >> 4) * CV_CH + 3072 + (lane & 15) * 64 + w * 16, yr = F + 3072 + lane * 16;
    const u32 yo = W_Y + (u32)((((w & 1) * 16 + (lane >> 2)) * 8 + (w >> 1)) * DM + (grp & 3) * 160 + 4 * (grp >> 2) + (lane & 3)) * 16u;
    auto put = [&](const f32x16& ac, int q) {
        const int r = 4 * (q & 3);
        const auto x = __builtin_amdgcn_permlane32_swap(pk(ac[r], ac[r + 1]), pk(ac[r + 4], ac[r + 5]), false, false);
        const auto y = __builtin_amdgcn_permlane32_swap(pk(ac[r + 2], ac[r + 3]), pk(ac[r + 6], ac[r + 7]), false, false);
        lds_w16(yw + (q >> 1) * CV_PL, (v4u){x[0], y[0], x[1], y[1]});
    };
    if (MODE != 2) EV(8);
    if (MODE != 0) kv_load();
    if (lane < CV_KB / 16) lds_w16(F + CV_F + lane * 16, (v4u){0u, 0u, 0u, 0u});
    windows(0);
    if (MODE != 1) { windows(1); windows(2); windows(4); }
    if (MODE == 0) kv_load();
    if (MODE == 2) windows(3);
    if (MODE != 2) EV(9);
#pragma unroll
    for (int i = 0; i < (MODE == 1 ? 2 : 4); ++i) lds_w16(F + CV_F + (16 * i + (lane >> 2) + 1) * CV_KB + (lane & 3) * 16, kvr[i]);
    v4u dg[2];
#pragma unroll
    for (int ks = 0; ks < 2; ++ks)
#pragma unroll
        for (int q = 0; q < 4; ++q) { const int j = 16 * ks + 8 * hh + 7 - bq; dg[ks][q] = j == 2 * q ? D : j == 2 * q + 1 ? D << 16 : 0u; }
    f32x16 acc0 = {}, acc1 = {};
    auto first = [&]() { asm volatile("s_waitcnt vmcnt(0)\n\ts_barrier" ::: "memory"); st4s(rs, tid == 0 ? foff(F_CV + grp) : OOB, 0, 1u); EV(12); };
    if (MODE != 2) {
        EV(10);
        acc0 = mfma32(lds_r16(kb + bq * CV_KB), dg[0], acc0); acc0 = mfma32(lds_r16(kb + bq * CV_KB + 32), dg[1], acc0);
#pragma unroll
        for (int m = 0; m < 8; ++m) {
            lptr kl = kb + max(bq - m, -1) * CV_KB;
            acc0 = mfma32(lds_r16(kl), lds_r16(fa + m * 64), acc0); acc0 = mfma32(lds_r16(kl + 32), lds_r16(fa + m * 64 - 32), acc0);
        }
        put(acc0, 0);
        BAR();
        st16(rs, w < 2 ? yo : OOB, lds_r16(yr));
        EV(11);
    }
    if (MODE == 1) {
        windows(1); windows(2);
        first();
        v4u a[2][2], l[2][2];
#pragma unroll
        for (int u = 0; u < 2; ++u) { lptr kl = kb + max(bq - 8 - u, -1) * CV_KB; a[u][0] = lds_r16(fa + (8 + u) * 64); l[u][0] = lds_r16(kl); a[u][1] = lds_r16(fa + (8 + u) * 64 - 32); l[u][1] = lds_r16(kl + 32); }
#pragma unroll 1
        for (int m = 8; m < 32; m += 2) {
#pragma unroll
            for (int u = 0; u < 2; ++u) {
                lptr kl = kb + max(bq - m - u - 2, -1) * CV_KB;
                acc0 = mfma32(l[u][0], a[u][0], acc0); a[u][0] = lds_r16(fa + (m + u + 2) * 64); l[u][0] = lds_r16(kl); SCHED();
                acc0 = mfma32(l[u][1], a[u][1], acc0); a[u][1] = lds_r16(fa + (m + u + 2) * 64 - 32); l[u][1] = lds_r16(kl + 32); SCHED();
            }
        }
    } else {
        if (MODE == 0) windows(3); else filt_load(p, next, f);
        v4u a0 = lds_r16(fa), a1 = lds_r16(fa - 32), c0 = lds_r16(fa + 2048), c1 = lds_r16(fa + 2048 - 32);
        v4u h0 = lds_r16(kh), h1 = lds_r16(kh + 32), l0 = lds_r16(kb + bq * CV_KB), l1 = lds_r16(kb + bq * CV_KB + 32);
        acc1 = mfma32(h0, dg[0], acc1); acc1 = mfma32(h1, dg[1], acc1);
#pragma unroll 2
        for (int m = 0; m < (MODE == 0 ? 8 : 32); ++m) {
            lptr kl = kb + max(bq - m - 1, -1) * CV_KB;
            if (MODE == 0 && m == 4) first();
            acc1 = mfma32(h0, a0, acc1); h0 = lds_r16(kh - (m + 1) * CV_KB); a0 = lds_r16(fa + (m + 1) * 64); SCHED();
            acc1 = mfma32(l0, c0, acc1); c0 = lds_r16(fa + (m + 1) * 64 + 2048); l0 = lds_r16(kl); SCHED();
            acc1 = mfma32(h1, a1, acc1); h1 = lds_r16(kh - (m + 1) * CV_KB + 32); a1 = lds_r16(fa + (m + 1) * 64 - 32); SCHED();
            acc1 = mfma32(l1, c1, acc1); c1 = lds_r16(fa + (m + 1) * 64 + 2048 - 32); l1 = lds_r16(kl + 32); SCHED();
        }
        if (MODE == 0) {
#pragma unroll 2
            for (int m = 8; m < 32; ++m) {
                lptr kl = kb + max(bq - m - 1, -1) * CV_KB;
                acc1 = mfma32(h0, a0, acc1); h0 = lds_r16(kh - (m + 1) * CV_KB); SCHED();
                acc0 = mfma32(l0, a0, acc0); a0 = lds_r16(fa + (m + 1) * 64); SCHED();
                acc1 = mfma32(l0, c0, acc1); c0 = lds_r16(fa + (m + 1) * 64 + 2048); l0 = lds_r16(kl); SCHED();
                acc1 = mfma32(h1, a1, acc1); h1 = lds_r16(kh - (m + 1) * CV_KB + 32); SCHED();
                acc0 = mfma32(l1, a1, acc0); a1 = lds_r16(fa + (m + 1) * 64 - 32); SCHED();
                acc1 = mfma32(l1, c1, acc1); c1 = lds_r16(fa + (m + 1) * 64 + 2048 - 32); l1 = lds_r16(kl + 32); SCHED();
            }
        }
        BAR();
    }
    if (MODE != 2) { EV(13); put(acc0, 0); put(acc0, 2); }
    if (MODE != 1) { put(acc1, 4); put(acc1, 6); }
    BAR();
#pragma unroll
    for (int q = (MODE == 2 ? 2 : 0); q < (MODE == 1 ? 2 : 4); ++q) st16(rs, q == 0 && w < 2 ? OOB : yo + (u32)q * (2 * DM * 16), lds_r16(yr + q * CV_PL));
    BAR();
    if (MODE != 2) EV(14);
}

#define NPROBE 6
DEV int probe(rsrc_t rs, int c) {
    const int lane = threadIdx.x & 63;
    const u32 k = (u32)(c & 15) < NREG ? (u32)(c & 15) : 0u;
    const u32 off0 = k * (16 * MB) + 2 * MB - 65536 + ((u32)(c & 15) >= NREG ? (u32)(c & 3) + 1u : 0u) * 256 + (u32)(c >> 4) * 128;
    st4s(rs, lane == 0 ? off0 : OOB, 0, 0u); DRAIN();
    int d0 = 1 << 16, d1 = 1 << 16;
#pragma unroll 1
    for (int q = 0; q < NPROBE; ++q) {
        const u32 o = lane == 0 ? off0 + (u32)(q & 1) * 8192 + (u32)(q >> 1) * 18432 : OOB;
        const unsigned long long t0 = __builtin_amdgcn_s_memtime();
        st4s(rs, o, 0, 0u); DRAIN();
        const int dt = (int)(__builtin_amdgcn_s_memtime() - t0);
        if (q & 1) d1 = min(d1, dt); else d0 = min(d0, dt);
    }
    const u32 xcc = (u32)__builtin_amdgcn_s_getreg(20 | (3 << 11)) & 0xfu;
    return (d0 - d1) * ((xcc & 4u) ? -1 : 1);
}
DEV void pre_rings(const Params& p, rsrc_t rs, int j) {
    const u32 gt = (u32)(j * 256 + threadIdx.x) * 16u, n = ((gt >> 14) << 9) + ((gt & 8191u) >> 4);
    const v4u ps = {PSENT, PSENT, PSENT, PSENT};
    v4u m = ps;
    if (n < 640u) m = *(const v4u*)(p.mem0 + (2 * ((n >> 2) & 3) + (n & 1)) * DM + (n >> 4) * 16 + (n & 2) * 4);
#pragma unroll
    for (u32 i = 0; i < 8; ++i) st16(rs, W_ORING + gt + i * 131072u, ps);
#pragma unroll
    for (u32 i = 0; i < 2; ++i) st16(rs, gt < 16384u ? W_MRING + i * (MB / 4) + 131072u + gt : OOB, ps);
    if (n < 640u) {
#pragma unroll
        for (int i = 0; i < 4; ++i) m[i] = m[i] == PSENT ? 0x7FC17FC1u : m[i];
    }
    st16(rs, W_MRING + gt, m);
    st16(rs, W_MRING + MB / 4 + gt, m);
    static_assert(2 * (F_N - F_QKV) == 32 * 36, "clear");
    st4s(rs, threadIdx.x < 36 ? foff(F_QKV) + (u32)(j * 36 + threadIdx.x) * 128u : OOB, 0, 0u);
}

#define M32A(first, d, a, b) do { if (first) asm volatile("v_mfma_f32_32x32x16_bf16 %0, %1, %2, 0" : "=&v"(d) : "a"(a), "v"(b)); \
                                  else asm volatile("v_mfma_f32_32x32x16_bf16 %0, %1, %2, %0" : "+v"(d) : "a"(a), "v"(b)); } while (0)
#define M32B(first, d, a, b) do { if (first) asm volatile("v_mfma_f32_32x32x16_bf16 %0, %1, %2, 0" : "=&v"(d) : "v"(a), "a"(b)); \
                                  else asm volatile("v_mfma_f32_32x32x16_bf16 %0, %1, %2, %0" : "+v"(d) : "v"(a), "a"(b)); } while (0)
#define VCVT(d, a, b) asm volatile("v_cvt_pk_bf16_f32 %0, %1, %2" : "=v"(d) : "v"(a), "v"(b))
#define LD4A(d, vo, so) asm volatile("buffer_load_dword %0, %1, %2, %3 offen sc1" : "=v"(d) : "v"(vo), "s"(srd), "s"(so) : "memory")
DEV u32 wst_src(int lane, int ld) { return (u32)((lane >> 4) * ld + 8 * ((lane & 15) ^ ((lane >> 4) << 2))) * 2u; }
DEV u32 wst_rd(int lane, int w) { const int q4 = (lane >> 2) & 3; return (u32)(q4 * 256 + ((w ^ q4) * 64) + ((lane >> 4) & 1) * 32 + (lane & 3) * 8); }

constexpr int QK_A = 65536;
#define QLEAD 4
#define QFIRST 2
#define QW0 10
#define QW1 3
#define QSAMP 72
DEV void role_qkv(const Params& p, lptr lds, int c) {
    const int grp = 64 + c;
    const int tid = threadIdx.x, lane = tid & 63, w = uni(tid >> 6), hh = lane >> 5, r32 = lane & 31;
    const int m = c >> 5, h = c & 31;
    const rsrc_t rs = mk_rsrc(p.ws, W_END);
    v4u wt[40];
    Filt flt;
    bool dead = false;
    u32 pol, h2flag, fo, ol, ar[8];
    const u32 ds = uu(w * 1024), dx = ds + QK_A;
#define XRD(ks) lds_r16(lds + ar[(ks) & 7] + ((ks) >> 3) * 8192)
    const u32 t16 = (u32)tid * 16u, lnf = tid < 96 ? foff(F_LN + tid) : OOB;
    MK_SRD();
#define QDMA(i, dst, vo, so) DMA16I(dst, (i) * 4096, vo, so, ((i) >> 1) * 16384 + ((i) & 1) * 4096)
    {
        const v4u sw = mk_srd(sel(m == 0, p.Wq, sel(m == 1, p.Wk, p.Wv)), DM * NQ * 2), sx = mk_srd(p.x, ACT);
        const u32 vo = wst_src(lane, NQ) + (u32)h * (DH * 2), x8 = (u32)lane * 8u, x4 = (u32)lane * 4u;
        v2u xa[6], xb[6]; u32 xc[6];
#pragma unroll
        for (int i = 0; i < 6; ++i)
            asm volatile("buffer_load_dwordx2 %0, %3, %5, %6 offen " INPOL "\n\tbuffer_load_dwordx2 %1, %3, %5, %6 offen offset:512 " INPOL "\n\tbuffer_load_dword %2, %4, %5, %6 offen offset:1024 " INPOL
                         : "=&v"(xa[i]), "=&v"(xb[i]), "=&v"(xc[i]) : "v"(x8), "v"(x4), "s"(sx), "s"(uu((4 * c + w + 384 * i) * (DM * 2))) : "memory");
        EV(1);
        auto wfill = [&](int i0, int n) {
#pragma unroll 1
            for (int i = i0; i < i0 + n; ++i) DMAIN(sw, uu((4 * i + w) * 1024), vo, uu((16 * i + 4 * w) * (NQ * 2)));
        };
        wfill(0, QW0);
        asm volatile("s_waitcnt vmcnt(%0) ; HZCLR" :: "i"(QW0) : "memory");
        EV(2);
#pragma unroll
        for (int i = 0; i < 6; ++i) {
            wfill(QW0 + QW1 * i, QW1);
            if (i == 2) { VMW(2 * QW1 + 3); BAR(); boot_set(p, rs, tid == 0 ? foff(F_LN + c) : OOB, 1u); }
            asm volatile("" : "+v"(xa[i]), "+v"(xb[i]), "+v"(xc[i]));
            const int row = 4 * c + w + 384 * i;
            if (i < 5 || row < SEQ) {
                const f32x2 x[5] = {up2(xa[i].x), up2(xa[i].y), up2(xb[i].x), up2(xb[i].y), up2(xc[i])};
                const f32x2 st = ln_stats(x);
                u32 hp[5];
#pragma unroll
                for (int q = 0; q < 5; ++q) hp[q] = pk2(up2(pk2(x[q] - st.x)) * st.y);
                store_h(rs, W_H1 + (u32)(row >> 5) * H_TILE, row, hp);
            }
        }
        EV(3);
        constexpr int K0 = QW0 + 6 * QW1;
        static_assert(K0 * 4096 >= QK_A + 40960, "the first tile's place is free");
        v4u fv, praw;
        asm volatile("buffer_load_dwordx4 %0, %2, %4, 0 offen sc1\n\tbuffer_load_dwordx4 %1, %3, %4, 0 offen sc1" : "=&v"(praw), "=&v"(fv) : "v"(pol_off()), "v"(lnf), "s"(srd) : "memory");
        wfill(K0, 40 - K0);
        asm volatile("s_waitcnt vmcnt(%2) ; HZCLR\n\ts_barrier" : "+v"(praw), "+v"(fv) : "i"(40 - K0) : "memory");
        boot_set(p, rs, tid == 0 ? foff(F_LN + c) : OOB, 2u);
        EV(4);
        lptr tb = lds + hh * 2048 + wst_rd(lane, w);
        SFOR(k, 40)
            if constexpr (k == K0) {
                if (!boot_ok(p, lnf, fv, 1u)) boot_wait(p, rs, lnf, 1u, dead);
                if (!boot_ok(p, pol_off(), praw, 0u)) praw.w = boot_wait(p, rs, pol_off(), 0u, dead);
                BAR();
                EV(5);
                const u32 so = W_H1;
                SFOR(i, 10) QDMA(i, dx, t16, so); SEND
                pol = pol_of(praw.w);
                h2flag = tid < 8 ? near_(foff(F_H2 + lane), 1u, pol) : OOB;
                ol = (m == 2 ? (u32)(2 * w + (r32 >> 4)) * 1024u : (u32)((r32 >> 4) * 4 + w) * 1024u) + (u32)(hh * 16 + (r32 & 15)) * 16u;
                fo = near_(foff(F_QKV + m * 64 + h), 0u, pol);
#pragma unroll
                for (int j = 0; j < 8; ++j) { ar[j] = (u32)(QK_A + r32 * 256 + ((2 * j + hh) ^ (r32 & 15)) * 16); PIN(ar[j]); }
                PIN(h2flag); PIN(ol); PINS(fo); PINS(pol);
                VMW(10); BAR();
            }
            lptr q = tb + (k >> 3) * 32768 + (k & 7) * 4096; wt[k] = tr2(q, q + 1024); asm volatile("" : "+a"(wt[k]));
        SEND
    }
    EV(6);
    int n0 = -1, n1 = -QLEAD;
    f32x16 a0 = {};
    const u32 bfo = tid < 96 ? lnf : tid - 128u < NKV ? foff(F_KV - 128 + tid) : OOB;
    v4u brk = {};
    auto run = [&](auto isv) __attribute__((always_inline)) {
      constexpr bool V = decltype(isv)::value;
      v4u x[8];
      u32 fv = 0, cvo = n1 > -QLEAD && tid == 194 ? foff(F_CV + grp) : OOB;
#pragma unroll
      for (int ks = 20; ks < 25; ++ks) x[ks & 7] = XRD(ks);
#define QS(k) do {} while (0)
#define QPK1(d, a, a_, q_) do { if constexpr (V) VCVT(d[q_], a[4 * (a_) + 8 * ((q_) >> 1) + 2 * ((q_) & 1)], a[4 * (a_) + 8 * ((q_) >> 1) + 2 * ((q_) & 1) + 1]); else VCVT(d[q_], a[4 * (a_) + (q_)], a[4 * (a_) + (q_) + 8]); } while (0)
#define QPK(d, a, a_) do { QPK1(d, a, a_, 0); QPK1(d, a, a_, 1); QPK1(d, a, a_, 2); QPK1(d, a, a_, 3); } while (0)
#pragma unroll 1
      for (const int end = n1 == -QLEAD ? QFIRST - QLEAD : NSEG; n1 < end; ++n1) {
        const int cur = n1 < 0 ? n0 + 1 : 64 | n1, nxt = n1 < 0 ? n0 + 2 : n0 + 1;
        u32 xs = n1 < 0 ? W_H1 + (u32)cur * H_TILE : near_(W_H2 + (u32)n1 * H_TILE, 1u, pol);
        PINS(xs);
        QS(0);
        if (w == 0 && n1 >= 0 && ((u32)__builtin_amdgcn_ballot_w64(fv <= (u32)n1) & 255u) != 0u) wait_flags(rs, h2flag, (u32)n1 + 1, dead);
        BAR();
        QS(1);
        f32x16 a1;
        u32 ss, sv, hb, oa, qfo, qfv;
        v4u pp, pq;
        SCHED();
        auto slot = [&](auto zc) __attribute__((always_inline)) {
            constexpr int z = decltype(zc)::value, ks = (z + 20) % 40;
            if constexpr (z < 20) { if constexpr (V) M32B(false, a0, x[ks & 7], wt[ks]); else M32A(false, a0, wt[ks], x[ks & 7]); }
            else if constexpr (z < 60) { if constexpr (V) M32B(z == 20, a1, x[ks & 7], wt[ks]); else M32A(z == 20, a1, wt[ks], x[ks & 7]); }
            else if constexpr (z == 60) {
                if constexpr (V) asm volatile("v_mfma_f32_32x32x16_bf16 %0, %1, %2, 0" : "+v"(a0) : "v"(x[ks & 7]), "a"(wt[ks]));
                else asm volatile("v_mfma_f32_32x32x16_bf16 %0, %1, %2, 0" : "+v"(a0) : "a"(wt[ks]), "v"(x[ks & 7]));
            } else { if constexpr (V) M32B(false, a0, x[ks & 7], wt[ks]); else M32A(false, a0, wt[ks], x[ks & 7]); }
            if constexpr (z == 18) { QS(2); asm volatile("s_waitcnt vmcnt(6) lgkmcnt(0)\n\ts_barrier" ::: "memory"); QS(3); }
            if constexpr (z < 15 || z > 20) x[(ks + 5) & 7] = XRD((ks + 5) % 40);
            if constexpr (z >= 18 && z <= 20) { x[2 * (z - 18)] = XRD(2 * (z - 18)); x[2 * (z - 18) + 1] = XRD(2 * (z - 18) + 1); }
            SCHED();
            if constexpr (z < 4) QDMA(z, dx, t16, xs);
            if constexpr (z >= 5 && z < 17 && (z & 1)) QDMA((z + 3) >> 1, dx, t16, xs);
            if constexpr ((z >= 7 && z < 15) || (z >= 47 && z < 55)) asm volatile("v_xor_b32 %0, 0x10000, %0" : "+v"(ar[(ks + 5) & 7]));
            if constexpr (z == 16) { ss = W_H1 + (u32)nxt * H_TILE; PINS(ss); }
            if constexpr (z == 17) { sv = t16 | (nxt >= NSEG ? OOB : 0u); PIN(sv); }
            if constexpr (z >= 21 && z < 41 && (z & 1)) QDMA((z - 21) >> 1, ds, sv, ss);
            if constexpr (z == 24) QPK(pp, a0, 0);
            if constexpr (z == 26) QPK(pq, a0, 1);
            if constexpr (z == 30) { QS(4); asm volatile("s_waitcnt vmcnt(5)\n\ts_barrier" ::: "memory"); QS(5); }
            if constexpr (z == 32 || z == 44) { hb = (m == 2 ? W_VO : W_KQ) + (u32)((z == 32 ? n0 : cur) * NH + h) * 16384u; PINS(hb); }
            if constexpr (z == 34 || z == 45) { hb = near_(hb, m == 1 ? 0u : 1u, pol); PINS(hb); }
            if constexpr (z == 36) { hb = (u32)n0 < NSEG ? hb : OOB; PINS(hb); }
            if constexpr (z == 38 || z == 46) { oa = ol + hb; PIN(oa); }
            if constexpr (z == 41) st16(rs, oa, pp);
            if constexpr (z == 43) st16(rs, oa + 512, pq);
            if constexpr (z == 54) { QS(6); asm volatile("s_waitcnt vmcnt(4)\n\ts_barrier" ::: "memory"); QS(7); }
            if constexpr (z == 56) { if (__builtin_expect(n1 == QFIRST - QLEAD - 1, 0)) { filt_load<3, 0>(p, grp, flt); brk = ld16s(rs, bfo, 0); } }
            if constexpr (z == 60) NOPS(5);
            if constexpr (z == 61) { QPK(pp, a1, 0); st16(rs, oa, pp); }
            if constexpr (z == 62) { QPK(pq, a1, 1); st16(rs, oa + 512, pq); }
            if constexpr (z == 64) { qfo = tid == 192 ? fo : tid == 193 && n1 >= 0 ? fo + 32 * 128 : cvo; PIN(qfo); }
            if constexpr (z == 66) { qfv = tid == 192 ? (u32)min(nxt, NSEG) : tid == 193 ? (u32)n1 + 1u : 2u; PIN(qfv); }
            if constexpr (z == QSAMP) LD4A(fv, h2flag, 0);
            SCHED();
        };
        if (__builtin_expect(n0 >= 0, 1)) static_for<0, 16>(slot);
        static_for<16, 80>(slot);
        QS(8);
        asm volatile("s_waitcnt vmcnt(1)\n\ts_barrier" ::: "memory");
        st4s(rs, qfo, 0, qfv);
        QS(9);
        asm volatile("s_waitcnt vmcnt(1)" : "+v"(fv) :: "memory");
        n0 = nxt; cvo = OOB;
      }
      NOPS(7);
    };
#pragma unroll 1
    for (;;) {
        if (m == 2) run(IC<1>{}); else run(IC<0>{});
        if (n1 > 0) break;
        if (!boot_ok(p, bfo, brk, 2u)) boot_wait(p, rs, bfo, 2u, dead);
        BAR();
        conv_job<1>(p, rs, lds, grp, flt, 0);
        const u32 so = uu(W_H1 + (u32)n0 * H_TILE);
        SFOR(i, 6) QDMA(i + 4, ds, t16, so); SEND
        DRAIN(); BAR();
    }
}

constexpr int OU_SEL = 131072;

DEV void role_out(const Params& p, lptr lds, int L, int kq, int ng) {
    const int tid = threadIdx.x, lane = tid & 63, w = uni(tid >> 6), hh = lane >> 5, r32 = lane & 31;
    const rsrc_t rs = mk_rsrc(p.ws, W_END);
    v4u wo[64];
    Filt flt;
    bool dead = false;
    {
        const v4u sw = mk_srd(p.Wo, DM * NQ * 2);
        const u32 vo = wst_src(lane, DM) + (ng == 4 ? 1024u : (u32)((ng >> 1) * 512 + (ng & 1) * 128 + ((lane ^ (lane >> 2)) & 8) * 16));
        auto fill = [&](int b) {
#pragma unroll 1
            for (int i = 0; i < 8; ++i) DMAIN(sw, uu((b % 5) * 32768 + (4 * i + w) * 1024), vo, uu((kq * 1024 + b * 128 + 16 * i + 4 * w) * (DM * 2)));
        };
#pragma unroll 1
        for (int b = 0; b < 5; ++b) fill(b);
        lptr tb = lds + hh * 1024 + wst_rd(lane, w);
        SFOR(b, 8)
            VMW(b < 3 ? 32 : 66 - 8 * b); BAR();
            lptr q = tb + (b % 5) * 32768;
#pragma unroll
            for (int k = 0; k < 8; ++k) { lptr a = q + (k >> 1) * 8192 + (k & 1) * 2048; wo[8 * b + k] = tr2(a, a + 4096); if (b < 7) asm volatile("" : "+a"(wo[8 * b + k])); else PIN(wo[8 * b + k]); }
            if constexpr (b < 3) { BAR(); fill(b + 5); }
            if constexpr (b == 2) filt_load(p, L == 0 ? 44 + kq * 5 + ng : 1 << 12, flt);
        SEND
    }
    EV(6);
    const u32 pol = get_pol(p, rs, dead);
    if (L == 0) {
        BAR();
        conv_job<0>(p, rs, lds, 44 + kq * 5 + ng, flt, 0);
        DRAIN(); BAR();
        st4s(rs, tid == 0 ? foff(F_CV + 44 + kq * 5 + ng) : OOB, 0, 2u);
    }
    EV(15);
    MK_SRD();
    const u32 ofl = (lane < 8 && w == 0) ? near_(foff(F_O + L * 32 + kq * 8 + lane), 0u, pol) : OOB;
    const u32 l16 = (u32)lane * 16u;
    const u32 fo = (tid == 192) ? near_(foff(F_P + L * 32 + kq * 5 + ng), 1u, pol) : OOB;
    auto osrc = [&](int s) { return uu(hblk(W_VO, L * NSEG + s, kq * 8, 0u, pol) + w * 1024); };
#define ODMA(i, dst, vo, so) DMA16I(dst, (i) * 4096, vo, so, ((i) >> 1) * 16384 + ((i) & 1) * 4096)
    const u32 pvo = (u32)(kq * 5 + ng) * 16384u + (u32)w * 2048u + l16;
    lptr tw = lds + OU_SEL + 256 + r32 * 272 + (ng == 4 ? hh * 32 + w * 4 : (w & 1) * 128 + hh * 16 + (w >> 1) * 8), tr = lds + OU_SEL + 256 + (8 * w + (lane >> 4)) * 272 + (lane & 15) * 16;
    lptr selw = lds + OU_SEL + (w == 0 ? 0 : 64);
    u32 pfo = OOB;
    u32 buf = 0;
    if (w == 0) wait_flags(rs, ofl, 1u, dead);
    BAR();
    for (int s = 0; s < NSEG; ++s) {
        u32 src = osrc(s), dst = uu(buf + w * 1024);
        lptr A = lds + buf + l16;
        asm volatile("" : "+s"(src), "+s"(dst));
#define OS(k) do {} while (0)
        OS(0);
        OS(1);
        SFOR(i, 10) ODMA(i, dst, l16, src); SEND
        OS(2);
        asm volatile("s_waitcnt vmcnt(7)\n\ts_barrier" ::: "memory");
        OS(3);
        LS2(L == 0 && w == 1, 96 + kq * 5 + ng);
        v4u o[64];
#pragma unroll
        for (int k = 0; k < 3; ++k) o[k] = lds_r16(A + k * 1024);
        st4s(rs, pfo, 0, (u32)s);
        u32 fv, pa; int rdy;
        f32x16 acc;
        SCHED();
        SFOR(k, 64)
            if constexpr (k < 56) M32A(k == 0, acc, wo[k], o[k]); else asm volatile("v_mfma_f32_32x32x16_bf16 %0, %1, %2, %0" : "+v"(acc) : "v"(wo[k]), "v"(o[k]));
            if constexpr (k == 61) rdy = *(LAS int*)(lds + OU_SEL);
            if constexpr (k < 61) o[k + 3] = lds_r16(A + (k + 3) * 1024);
            SCHED();
            if constexpr (k < 12 && (k & 1) == 0) ODMA(10 + (k >> 1), dst, l16, src);
            if constexpr (k == 7) OS(4);
            if constexpr (k == 8) OS(5);
            if constexpr (k == 23) OS(6);
            if constexpr (k == 24) OS(7);
            if constexpr (k == 43) OS(8);
            if constexpr (k == 44) OS(9);
            if constexpr (k == 59) OS(10);
            if constexpr (k == 63) OS(11);
            if constexpr (k == 7) asm volatile("s_waitcnt vmcnt(8)\n\ts_barrier" ::: "memory");
            if constexpr (k == 23) asm volatile("s_waitcnt vmcnt(5)\n\ts_barrier" ::: "memory");
            if constexpr (k == 30) { pa = pvo + near_(p_item(s), (u32)L, pol); PIN(pa); }
            if constexpr (k == 22) LD4A(fv, ofl, 0);
            if constexpr (k == 42) {
                asm volatile("s_waitcnt vmcnt(0)" : "+v"(fv) :: "memory");
                *(LAS int*)selw = (int)((u32)__builtin_amdgcn_ballot_w64(fv < (u32)s + 2) & 255u);
            }
            if constexpr (k == 43) asm volatile("s_waitcnt lgkmcnt(1)\n\ts_barrier" ::: "memory");
            SCHED();
        SEND
        const bool ready = uni(rdy) == 0;
        asm volatile("s_nop 11" : "+v"(acc));
        u32 pc[8];
#pragma unroll
        for (int i = 0; i < 8; ++i) pc[i] = pk(acc[2 * i], acc[2 * i + 1]);
        if (ng != 4) {
#pragma unroll
            for (int i = 0; i < 4; ++i) *(LAS v2u*)(tw + i * 32) = (v2u){pc[2 * i], pc[2 * i + 1]};
        } else {
#pragma unroll
            for (int i = 0; i < 8; ++i) *(LAS u32*)(tw + (i >> 1) * 64 + (i & 1) * 16) = pc[i];
        }
        BAR();
        { const v4u p0 = lds_r16(tr), p1 = lds_r16(tr + 4 * 272); st16(rs, pa, p0); st16(rs, pa + 1024, p1); }
        SCHED();
        OS(12);
        if (ready) pfo = fo;
        else {
            LD4A(fv, ofl, 0);
            asm volatile("s_waitcnt vmcnt(1)\n\ts_barrier" ::: "memory");
            st4s(rs, fo, 0, (u32)s + 1); pfo = OOB;
            OS(14);
            LS2(L == 0 && w == 1, 160 + kq * 5 + ng);
            asm volatile("s_waitcnt vmcnt(1)" : "+v"(fv) :: "memory");
            if (w == 0 && s + 1 < NSEG && ((u32)__builtin_amdgcn_ballot_w64(fv < (u32)s + 2) & 255u) != 0u) wait_flags(rs, ofl, (u32)s + 2, dead);
            BAR();
        }
        OS(13);
        buf ^= 65536u;
    }
}

template <int L> DEV void role_row(const Params& p, lptr lds, int ri) {
    const int lane = threadIdx.x & 63, w = uni(threadIdx.x >> 6);
    const rsrc_t rs = mk_rsrc(p.ws, W_END), rx = mk_rsrc(p.x, ACT);
    bool dead = false;
    const u32 pol = get_pol(p, rs, dead);
    const u32 fp = threadIdx.x < 20 ? near_(foff(F_P + L * 32 + lane), 1u, pol) : OOB, fo = threadIdx.x == 192 && L == 0 ? near_(foff(F_H2 + (ri >> 2)), 1u, pol) : OOB;
    u32 fv = 0;
    MK_SRD();
    auto yfill = [&](int b, int i) { DMA32(uu(w * 20480 + (b & 1) * 10240 + i * 2048), (u32)lane * 16u, uu(W_Y + (u32)(ri * 8 + b) * 10240u + i * 2048)); };
    auto ywait = [&](u32 v) {
        wait_flags(rs, threadIdx.x < (v == 3u ? 8u : 160u) ? foff(F_CV + (v == 3u ? 160u : 0u) + threadIdx.x) : OOB, v, dead);
    };
    v2u xa = {}, xb = {}; u32 xc = 0;
    auto xload = [&](int s) { const u32 ro = (u32)(s * 32 + ri) * (DM * 2); xa = ldin8(rx, (u32)lane * 8u, ro); xb = ldin8(rx, (u32)lane * 8u + 512u, ro); xc = __builtin_amdgcn_raw_buffer_load_b32(rx, (u32)lane * 4u + 1024u, ro, 0); };
    if (L == 0) xload(0);
    ywait(1u);
    BAR();
    EV(15);
#pragma unroll 1
    for (int i = 0; i < 5; ++i) yfill(0, i);
    DRAIN();
    for (int s = 0; s < NSEG; ++s) {
        f32x2 y[5];
        {
            lptr ya = lds + w * 20480 + ((s >> 3) & 1) * 10240 + (s & 7) * 2 + lane * 16;
            auto yr = [&](lptr q) { return lo_f(*(LAS u16*)q); };
            y[0] = (f32x2){yr(ya), yr(ya + 2560)}; y[1] = (f32x2){yr(ya + 5120), yr(ya + 7680)};
            y[2] = (f32x2){yr(ya + 1024), yr(ya + 3584)}; y[3] = (f32x2){yr(ya + 6144), yr(ya + 8704)};
            lptr yc = ya - lane * 16 + 2048 + (lane >> 1) * 16 + (lane & 1) * 5120;
            y[4] = (f32x2){yr(yc), yr(yc + 2560)};
            asm volatile("" : "+v"(y[0]), "+v"(y[1]), "+v"(y[2]), "+v"(y[3]), "+v"(y[4]));
        }
        u32 xi[5] = {xa.x, xa.y, xb.x, xb.y, xc};
        u32 pa = near_(p_item(s), (u32)L, pol) + (u32)ri * 256u, pc = pa + 65536u + (u32)(lane >> 4) * 81920u + (u32)(lane & 15) * 16u;
        pa += (u32)(lane >> 5) * 81920u + (u32)((lane >> 4) & 1) * 16384u + (u32)(lane & 15) * 16u;
        asm volatile("" : "+v"(xi[0]), "+v"(xi[1]), "+v"(xi[2]), "+v"(xi[3]), "+v"(xi[4]), "+v"(pa), "+v"(pc));
        if (s == 2 || s == 26) ywait(s == 2 ? 2u : 3u);
#define RS(k) do {} while (0)
        RS(0);
        if (w == 0 && ((u32)__builtin_amdgcn_ballot_w64(fv <= (u32)s) & 0xFFFFFu) != 0u) wait_flags(rs, fp, (u32)s + 1, dead);
        BAR();
        RS(1);
        v4u d[5];
#pragma unroll
        for (int i = 0; i < 4; ++i) d[i] = ld16s(rs, pa, (i >> 1) * 32768 + (i & 1) * 163840);
        d[4] = ld16s(rs, pc, 0);
        if (L == 0) xload(s + 1);
        else {
            const u32 ro = W_X2 + (u32)(s * 32 + ri) * (DM * 2);
            const v2u a = ld8s(rs, (u32)lane * 8u, ro), b = ld8s(rs, (u32)lane * 8u + 512u, ro);
            xi[0] = a.x; xi[1] = a.y; xi[2] = b.x; xi[3] = b.y; xi[4] = ld4(rs, (u32)lane * 4u + 1024u + ro);
        }
        RS(2);
        LS2(L == 0, 192 + ri);
        f32x2 o[5];
        {
            f32x2 t[5][2];
#pragma unroll
            for (int i = 0; i < 5; ++i) {
                const auto e = __builtin_amdgcn_permlane32_swap(d[i].x, d[i].z, false, false), f = __builtin_amdgcn_permlane32_swap(d[i].y, d[i].w, false, false);
                t[i][0] = up2(e[0]) + up2(e[1]); t[i][1] = up2(f[0]) + up2(f[1]);
            }
            o[0] = t[0][0] + t[1][0]; o[1] = t[0][1] + t[1][1]; o[2] = t[2][0] + t[3][0]; o[3] = t[2][1] + t[3][1];
            const auto e = __builtin_amdgcn_permlane16_swap(u_(t[4][0].x), u_(t[4][1].x), false, false), f = __builtin_amdgcn_permlane16_swap(u_(t[4][0].y), u_(t[4][1].y), false, false);
            o[4] = (f32x2){f_(e[0]) + f_(e[1]), f_(f[0]) + f_(f[1])};
        }
        f32x2 x1[5];
#pragma unroll
        for (int i = 0; i < 5; ++i) x1[i] = up2(pk2(up2(xi[i]) + up2(pk2(o[i]))));
        asm volatile("" : "+v"(x1[0]), "+v"(x1[1]), "+v"(x1[2]), "+v"(x1[3]), "+v"(x1[4]));
        RS(3);
        if ((s & 7) - 2u < 5u && s < NSEG - 8) yfill((s >> 3) + 1, (s & 7) - 2);
        SCHED();
        const f32x2 st1 = ln_stats(x1);
        u32 xp[5];
#pragma unroll
        for (int i = 0; i < 5; ++i) xp[i] = pk2(x1[i] + ((x1[i] - st1.x) * st1.y) * y[i]);
        const int row = s * 32 + ri;
        RS(4);
        if (L == 0) {
            const u32 ro = (u32)row * (DM * 2);
            f32x2 x2[5];
#pragma unroll
            for (int i = 0; i < 5; ++i) x2[i] = up2(xp[i]);
            st8s(rs, (u32)lane * 8, W_X2 + ro, (v2u){xp[0], xp[1]});
            st8s(rs, (u32)lane * 8 + 512, W_X2 + ro, (v2u){xp[2], xp[3]});
            st4s(rs, (u32)lane * 4 + 1024, W_X2 + ro, xp[4]);
            SCHED();
            const f32x2 st2 = ln_stats(x2);
            u32 hp[5];
#pragma unroll
            for (int i = 0; i < 5; ++i) hp[i] = pk2((x2[i] - st2.x) * st2.y);
            store_h(rs, near_(W_H2 + (u32)s * H_TILE, 1u, pol), row, hp);
        } else {
            u16* o_ = p.out + (size_t)row * DM;
            *(v2u*)(o_ + 4 * lane) = (v2u){xp[0], xp[1]};
            *(v2u*)(o_ + 256 + 4 * lane) = (v2u){xp[2], xp[3]};
            *(u32*)(o_ + 512 + 2 * lane) = xp[4];
        }
        RS(5);
        LD4A(fv, fp, 0);
        if (L == 0) asm volatile("s_waitcnt vmcnt(1)\n\ts_barrier" ::: "memory");
        st4s(rs, fo, 0, (u32)s + 1);
        RS(6);
        asm volatile("s_waitcnt vmcnt(1)" : "+v"(fv) :: "memory");
        RS(7);
        if (ri == 0) LST(320 + L * 64 + s, rt32());
        LS2(L == 0, 224 + ri);
    }
}

constexpr float SCALE = 0.08838834764831845f, LOG2E = 1.4426950408889634f;
constexpr int C_K = 0, C_V = 8192, C_QX = 16384, C_MEM = 24576, C_Q = C_MEM + 10240, C_KM = C_Q + 2304, C_VM = C_KM + 2304, C_DUMP = C_VM + 2048,
              C_ZERO = C_DUMP + 1024, C_WL = C_ZERO + 128;
constexpr int NLF = 14;
static_assert(C_WL % 16 == 0 && C_WL + 4 * NLF * 2048 <= LDS_BYTES, "head lds");
#define CH_VIS 10
#define CH_H0 124
#define CH_W0 88
#define CH_MH 4
#define CH_MW 4
#define CH_REC 20
#define CH_LATE 30
#define CH_UP 4
#define CH_WX 6
DEV void wait_until(u32 t) {
    unsigned long long c;
    asm volatile("1:\n\ts_memrealtime %0\n\ts_waitcnt lgkmcnt(0)\n\ts_mov_b64 vcc, %0\n\ts_sub_u32 vcc_lo, %1, vcc_lo\n\ts_cmpk_lt_u32 vcc_lo, 400\n\ts_cbranch_scc1 1b" : "=&s"(c) : "s"(uu(t - 1u)) : "vcc", "scc", "memory");
}
#define WAIT_TXT(tmp, t1) "s_memrealtime vcc\n1:\n\ts_waitcnt lgkmcnt(0)\n\ts_sub_u32 " tmp ", " t1 ", vcc_lo\n\ts_memrealtime vcc\n\ts_cmpk_lt_u32 " tmp ", 400\n\ts_cbranch_scc1 1b\n\t"
#define SMF_A(acc, a, b) asm volatile("v_smfmac_f32_16x16x64_bf16 %0, %1, %2, %3" : "+v"(acc) : "v"(a), "a"(b), "v"(idx))
#define SMF_V(acc, a, b) asm volatile("v_smfmac_f32_16x16x64_bf16 %0, %1, %2, %3" : "+v"(acc) : "v"(a), "v"(b), "v"(idx))
#define MF0(d, a, b) asm volatile("v_mfma_f32_16x16x32_bf16 %0, %1, %2, 0" : "=&v"(d) : "v"(a), "v"(b))
#define MF1(d, a, b) asm volatile("v_mfma_f32_16x16x32_bf16 %0, %1, %2, %0" : "+v"(d) : "v"(a), "v"(b))
#define MF0H(d, a, b) asm volatile("v_mfma_f32_16x16x16_bf16 %0, %1, %2, 0" : "=&v"(d) : "v"(a), "v"(b))
#define MF1H(d, a, b) asm volatile("v_mfma_f32_16x16x16_bf16 %0, %1, %2, %0" : "+v"(d) : "v"(a), "v"(b))
DEV v8u tr_frag(lptr p0, lptr p1, lptr p2, lptr p3) { return cat8(tr2(p0, p1), tr2(p2, p3)); }
constexpr int qslot(int ks) { return (ks & 1) && ks < 8 ? ks >> 1 : -1; }
constexpr int kslot(int ks) { return ks < 4 ? 4 + ks : -1; }
constexpr int vslot(int ks) { return ks >= 4 ? 4 + ks : -1; }
DEV u32 ring_map(u32 off, u32 half) { return ((off >> 13) << 14) + (half << 13) + (off & 8191u); }
#define ZRD(x, i) x = __builtin_bit_cast(f32x4, lds_r16(ZP + 16 * (i)))
#define VMAX(d, a, b) asm volatile("v_max_f32 %0, %1, %2" : "=v"(d) : "v"(a), "v"(b))
#define VMAX3(d, a, b, c_) asm volatile("v_max3_f32 %0, %1, %2, %3" : "=v"(d) : "v"(a), "v"(b), "v"(c_))
#define VADD(d, a, b) asm volatile("v_add_f32 %0, %1, %2" : "=v"(d) : "v"(a), "v"(b))
#define VEXP(d, a) asm volatile("v_exp_f32 %0, %1" : "=v"(d) : "v"(a))
#define VFMC(d, a, b) asm volatile("v_fmamk_f32 %0, %1, 0x3e0293ee, %2" : "=v"(d) : "v"(a), "v"(b))
#define SEL(d, a, b) asm volatile("v_cndmask_b32 %0, %1, %2, %3" : "=v"(d) : "v"(a), "v"(b), "s"(himask))
#define QPERM(d, a, q) asm volatile("v_mov_b32_dpp %0, %1 quad_perm:" q " row_mask:0xf bank_mask:0xf" : "=v"(d) : "v"(a))

#define HSAMP 58
DEV void role_head(const Params& p, lptr lds, int L, int h) {
    const int tid = threadIdx.x, lane = tid & 63, w = uni(tid >> 6), g = lane >> 4, r = lane & 15;
    const rsrc_t rs = mk_rsrc(p.ws, W_END);
    v8u wq0[10], wq1[10], wk0[10], wk1[10], wv0[10], wv1[10];
    lptr WL = lds + C_WL + w * (NLF * 2048) + lane * 16;
    {
        constexpr int GS = 1056, CS = 48 * GS;
        const v4u sq = mk_srd(p.Wq, DM * NQ * 2), sk = mk_srd(p.Wk, DM * NQ * 2), sv = mk_srd(p.Wv, DM * NQ * 2);
        const u32 vo = (u32)(g * NQ + h * DH + 8 * (r ^ ((g & 1) << 3))) * 2u;
        auto fill = [&](int ch) {
#pragma unroll 1
            for (int i = 0; i < 4; ++i) {
                const u32 dst = uu((ch % 3) * CS + (4 * i + w) * GS), so = uu((ch * 64 + 16 * i + 4 * w) * (NQ * 2));
                DMAIN(sq, dst, vo, so); DMAIN(sk, dst + 16 * GS, vo, so); DMAIN(sv, dst + 32 * GS, vo, so);
            }
        };
        int vote = 0;
        if (L == 1) {
            if (w == 0) vote = probe(rs, h);
            BAR();
            EV(2);
            pre_rings(p, rs, h);
        }
        fill(0); fill(1); fill(2);
        const int q4 = r >> 2;
        lptr tb = lds + (4 * (g >> 1) + 2 * (q4 >> 1) + (g & 1)) * GS + (q4 & 1) * 256 + ((w * 64 + (r & 3) * 8) ^ ((q4 & 1) << 7));
        SFOR(ch, 10)
            VMW(ch == 1 || ch == 2 ? 25 : ch < 8 ? 24 : 108 - 12 * ch);
            BAR();
            if constexpr (ch == 0) boot_set(p, rs, tid == 0 && L == 1 ? foff(F_RG + h) : OOB, (u32)((1 << 20) + vote));
            auto frag = [&](int m, int t) { lptr b = tb + (ch % 3) * CS + m * 16 * GS + t * 32; return tr_frag(b, b + 512, b + 8 * GS, b + 8 * GS + 512); };
            wq0[ch] = frag(0, 0); wq1[ch] = frag(0, 1); wk0[ch] = frag(1, 0); wk1[ch] = frag(1, 1); wv0[ch] = frag(2, 0); wv1[ch] = frag(2, 1);
            asm volatile("" : "+a"(wq0[ch]), "+a"(wk0[ch]), "+v"(wv0[ch]), "+v"(wv1[ch]));
            if constexpr (qslot(ch) < 0) asm volatile("" : "+a"(wq1[ch])); else asm volatile("" : "+v"(wq1[ch]));
            if constexpr (kslot(ch) < 0) asm volatile("" : "+a"(wk1[ch])); else asm volatile("" : "+v"(wk1[ch]));
            BAR();
            if constexpr (ch < 7) fill(ch + 3);
        SEND
#pragma unroll
        for (int ch = 0; ch < 10; ++ch) {
            if (qslot(ch) >= 0) { lds_w16(WL + qslot(ch) * 2048, wq1[ch].lo); lds_w16(WL + qslot(ch) * 2048 + 1024, wq1[ch].hi); }
            if (kslot(ch) >= 0) { lds_w16(WL + kslot(ch) * 2048, wk1[ch].lo); lds_w16(WL + kslot(ch) * 2048 + 1024, wk1[ch].hi); }
            if (vslot(ch) >= 0) { lds_w16(WL + vslot(ch) * 2048, wv1[ch].lo); lds_w16(WL + vslot(ch) * 2048 + 1024, wv1[ch].hi); }
        }
        CFENCE();
    }
    const u32 idx = (r & 2) ? 0xEEEEu : 0x4444u;
    const unsigned long long himask = 0xCCCCCCCCCCCCCCCCull;
    bool dead = false;
    EV(6);
    const u32 pol = get_pol(p, rs, dead);
    const u32 pol0 = pol & 1u;
    const u32 l16 = (u32)lane * 16u;
    const u32 mring = W_MRING + (u32)L * (MB / 4) + pol0 * 8192u, hint = mring + 4 * 32768u;
    const u32 fkh = lane < 3 ? near_(foff(F_QKV + lane * 64 + L * 32 + h), 0u, pol) : lane < 6 ? hint + (u32)(lane - 3) * 4u : OOB;
    u32 tm = 0;
    MK_SRD();
    const unsigned long long kmask = __builtin_amdgcn_ballot_w64((hblk(W_KQ, L * NSEG + lane, h, 0u, pol) & 8192u) != 0u),
                             vmask = __builtin_amdgcn_ballot_w64((hblk(W_VO, L * NSEG + lane, h, 1u, pol) & 8192u) != 0u);
    u32 kb = uu(W_KQ + (u32)(L * NSEG * NH + h) * 16384u + w * 2048);
    u32 dk = uu(C_K + w * 2048), dv = dk + (C_V - C_K), dq = dk + (C_QX - C_K), dm = uu(C_MEM + w * 2048), dm2 = uu(w < 2 ? C_MEM + 8192 + w * 1024 : C_DUMP);
    asm volatile("" : "+s"(dk), "+s"(dv), "+s"(dq), "+s"(dm), "+s"(dm2));
    u32 ksrc = kb + ((u32)kmask & 1u) * 8192u, vsrc = kb + (W_VO - W_KQ) + ((u32)vmask & 1u) * 8192u;
    lptr qw = lds + C_Q + (2 * g) * 272 + (32 * w + 2 * r) * 2;
    lptr qr = lds + C_Q + (2 * (r >> 2) + (r & 1)) * 272 + g * 16;
    lptr LB = lds + l16, VX = lds + C_V + w * 2048 + l16;
    if (tid >= 64 && tid < 70) { const u32 z = tid == 68 ? 0x3F803F80u : tid == 69 ? PSENT : 0u; lds_w16(lds + C_ZERO + (tid - 64) * 16, (v4u){z, z, z, z}); }
    u32 zaddr = 0; PIN(zaddr);
    lptr ZP = lds + zaddr + C_ZERO;
    const float CEXP = SCALE * LOG2E;
#define LF_RD(slot, hf) lf[slot][hf] = lds_r16(WL + (slot) * 2048 + (hf) * 1024)
    BAR();
    const u32 ofl = tid == 0 ? near_(foff(F_O + L * 32 + h), 0u, pol) : OOB;
    u32 oso = (g & 1) ? OOB : W_ORING + (u32)L * (MB / 2) + ring_map((u32)((r >> 3) * 32768 + h * 1024 + w * 256 + (g >> 1) * 128 + (r & 7) * 16), pol0);
    unsigned long long vis[8];
#pragma unroll
    for (int i = 0; i < 8; ++i) vis[i] = __builtin_amdgcn_ballot_w64(16 * (i >> 2) + 4 * g + (i & 3) <= 16 * (w & 1) + r);
    u32 missed = 0;
    asm volatile("" : "+v"(oso), "+v"(qw), "+v"(qr), "+v"(VX));
    asm volatile("" : "+s"(vis[0]), "+s"(vis[1]), "+s"(vis[2]), "+s"(vis[3]), "+s"(vis[4]), "+s"(vis[5]), "+s"(vis[6]), "+s"(vis[7]), "+s"(ksrc), "+s"(vsrc));
    wait_flags(rs, lane < 3 ? fkh : OOB, 1u, dead);
    EV(7);
    DMA32(dk, l16, ksrc); DMA32(dv, l16, vsrc); DMA32(dq, l16, ksrc ^ 8192u);
    EV(8);
    for (int s = 0; s < NSEG;) {
        u32 ms = mring + (u32)(s & 3) * 32768u + w * 2048, ms2 = mring + (u32)(s & 3) * 32768u + 16384u + (w & 1) * 1024, sv = (u32)s;
        asm volatile("" : "+s"(ms), "+s"(ms2)); PIN(sv);
        f32x4 aq0, aq1, ak0, ak1;
        v4u lf[NLF][2];
        ZRD(aq0, 0); ZRD(aq1, 1); ZRD(ak0, 2); ZRD(ak1, 3);
#pragma unroll
        for (int i = 0; i < 4; ++i) { LF_RD(i, 0); LF_RD(i, 1); }
        SCHED();
        v4u A[10];
#define HT(k) do {} while (0)
        v4u kx[8], qf[4], kmf[4], vx[2], ones, P;
        float fa, fb, fc, fd, mx, my, sx[4], t[4], tm0, tm1; u32 pa, pb, fl, e01, e23, pmm; unsigned long long nanq;
        f32x4 av0, av1, s0, s1, sm, oa, ob, rsum;
        v2u vm, pm;
#define LW(sl) cat8(lf[sl][0], lf[sl][1])
#define PQ(ks, tl) do { if constexpr (!(tl)) SMF_A(aq0, A[ks], wq0[ks]); else if constexpr (qslot(ks) < 0) SMF_A(aq1, A[ks], wq1[ks]); else { const v8u lw_ = LW(qslot(ks)); SMF_V(aq1, A[ks], lw_); } } while (0)
#define PK(ks, tl) do { if constexpr (!(tl)) SMF_A(ak0, A[ks], wk0[ks]); else if constexpr (kslot(ks) < 0) SMF_A(ak1, A[ks], wk1[ks]); else { const v8u lw_ = LW(kslot(ks)); SMF_V(ak1, A[ks], lw_); } } while (0)
#define PV(ks, tl) do { if constexpr (!(tl)) SMF_V(av0, A[ks], wv0[ks]); else if constexpr (vslot(ks) < 0) SMF_V(av1, A[ks], wv1[ks]); else { const v8u lw_ = LW(vslot(ks)); SMF_V(av1, A[ks], lw_); } } while (0)
#define KXR(i) kx[i] = lds_r16(LB + C_K + ((i) & 1) * 4096 + ((i) >> 1) * 1024)
#define XW(q_) do { *(LAS u32*)(q_) = pa; *(LAS u32*)((q_) + 272) = pb; } while (0)
        auto slot = [&](auto zc) __attribute__((always_inline)) {
            constexpr int z = decltype(zc)::value;
            if constexpr (z < 14) PQ(z >> 1, z & 1);
            else if constexpr (z < 17) PQ(z - 7, 0);
            else if constexpr (z < 20) PQ(z - 10, 1);
            else if constexpr (z < 33) PK((z - 20) >> 1, z & 1);
            else if constexpr (z < 35) { if constexpr (z == 33) MF0(s0, kx[0], qf[0]); else MF0(s1, kx[1], qf[0]); }
            else if constexpr (z < 41) { if constexpr (z & 1) MF1(s0, kx[z - 33], qf[(z - 33) >> 1]); else MF1(s1, kx[z - 33], qf[(z - 33) >> 1]); }
            else if constexpr (z == 41) PK(6, 1);
            else if constexpr (z < 45) PK(z - 35, 0);
            else if constexpr (z < 48) PK(z - 38, 1);
            else if constexpr (z < 58) PV(z - 48, 0);
            else if constexpr (z < 61) PV(z < 60 ? z - 54 : 0, 1);
            else if constexpr (z == 61) MF0(sm, kmf[0], qf[0]);
            else if constexpr (z < 65) MF1(sm, kmf[z - 61], qf[z - 61]);
            else if constexpr (z == 65) MF0(oa, vx[0], P);
            else if constexpr (z == 66) MF0(ob, vx[1], P);
            else PV(6, 1);
            if constexpr (z < 6) A[4 + z] = lds_r16(LB + C_MEM + (4 + z) * 1024);
            if constexpr (z == 6 || z == 7) LF_RD(4, z - 6);
            if constexpr (z == 8 || z == 9) LF_RD(5, z - 8);
            if constexpr (z == 12 || z == 13) LF_RD(6, z - 12);
            if constexpr (z == 16 || z == 17) KXR(z - 16);
            if constexpr (z == 18 || z == 19) LF_RD(7, z - 18);
            if constexpr (z >= 20 && z < 24) KXR(z - 18);
            if constexpr (z == 32 || z == 33) KXR(z - 26);
            if constexpr (z == 24) XW(qw);
            if constexpr (z == 25) ZRD(av0, 0);
            if constexpr (z == 26) ZRD(av1, 1);
            if constexpr (z == 27) asm volatile("s_waitcnt lgkmcnt(2)\n\ts_barrier" ::: "memory");
            if constexpr (z >= 28 && z < 32) qf[z - 28] = lds_r16(qr + (z - 28) * 64);
            if constexpr (z == HSAMP) fl = ld4(rs, fkh);
            if constexpr (z == 52) XW(qw + (C_KM - C_Q));
            if constexpr (z == 53 || z == 54) vx[z - 53] = lds_r16(VX + (z - 53) * 1024);
            if constexpr (z == 55) asm volatile("s_waitcnt lgkmcnt(2)\n\ts_barrier" ::: "memory");
            if constexpr (z >= 56 && z < 60) kmf[z - 56] = lds_r16(qr + (C_KM - C_Q) + (z - 56) * 64);
            if constexpr (z >= 48 && z < 52) LF_RD(8 + ((z - 48) >> 1), z & 1);
            if constexpr (z >= 60) LF_RD(10 + ((z - 60) >> 1), z & 1);
            SCHED();
            if constexpr (z == 19) HT(2);
            if constexpr (z == 32) HT(3);
            if constexpr (z == 47) HT(4);
            if constexpr (z == 64) HT(5);
            if constexpr (z == 20) VADD(fa, aq0[0], aq0[2]);
            if constexpr (z == 21) VADD(fb, aq0[1], aq0[3]);
            if constexpr (z == 22) { VADD(fc, aq1[0], aq1[2]); VADD(fd, aq1[1], aq1[3]); }
            if constexpr (z == 23) { asm volatile("v_cmp_u_f32 %0, %1, %2" : "=s"(nanq) : "v"(fa), "v"(fb)); VCVT(pa, fa, fc); VCVT(pb, fb, fd); }
            if constexpr (z == 39 || z == 40 || z == 41) NOPS(1);
            if constexpr (z == 42) VMAX3(mx, s0[0], s0[1], s0[2]);
            if constexpr (z == 43) { VMAX3(mx, mx, s0[3], s1[0]); VMAX3(my, s1[1], s1[2], s1[3]); }
            if constexpr (z == 44 || z == 46) { VMAX(fa, mx, my); VMAX(my, mx, my); mx = fa; }
            if constexpr (z == 45) { SEL(sx[0], s0[0], s1[0]); asm volatile("v_permlane16_swap_b32 %0, %1" : "+v"(mx), "+v"(my)); }
            if constexpr (z == 47) { SEL(sx[1], s0[1], s1[1]); asm volatile("v_permlane32_swap_b32 %0, %1" : "+v"(mx), "+v"(my)); }
            if constexpr (z == 48) asm volatile("v_max_f32 %0, %0, %1\n\tv_mul_f32 %0, 0xbe0293ee, %0" : "+v"(mx) : "v"(my));
            if constexpr (z == 49) { VADD(fa, ak0[0], ak0[2]); VADD(fb, ak0[1], ak0[3]); }
            if constexpr (z == 50) { VADD(fc, ak1[0], ak1[2]); VADD(fd, ak1[1], ak1[3]); }
            if constexpr (z == 51) { VCVT(pa, fa, fc); VCVT(pb, fb, fd); }
            if constexpr (z == 52) SEL(sx[2], s0[2], s1[2]);
            if constexpr (z == 53) { SEL(sx[3], s0[3], s1[3]); VFMC(t[0], sx[0], mx); }
            if constexpr (z == 54) { VFMC(t[1], sx[1], mx); VFMC(t[2], sx[2], mx); }
            if constexpr (z == 55) VFMC(t[3], sx[3], mx);
            if constexpr (z >= 56 && z < 60) VEXP(t[z - 56], t[z - 56]);
            if constexpr (z == 60) { VCVT(e01, t[0], t[1]); VCVT(e23, t[2], t[3]); }
            if constexpr (z == 61) { QPERM(P.x, e01, "[0,1,0,1]"); QPERM(P.z, e01, "[2,3,2,3]"); }
            if constexpr (z == 62) { QPERM(P.y, e23, "[0,1,0,1]"); QPERM(P.w, e23, "[2,3,2,3]"); }
            if constexpr (z == 63) { VADD(fa, av0[0], av0[2]); VADD(fb, av0[1], av0[3]); }
            if constexpr (z == 64) VCVT(vm.x, fa, fb);
            if constexpr (z == 67) { asm volatile("" :: "v"(sm)); VFMC(tm0, sm[0], mx); VFMC(tm1, sm[1], mx); }
            SCHED();
        };
        {
            u32 tmp;
            asm volatile("s_mov_b32 m0, %2\n\t" WAIT_TXT("%0", "%1") "buffer_load_dwordx4 %4, %5, %6 offen sc1 lds\n\tbuffer_load_dwordx4 %4, %5, %6 offen offset:1024 sc1 lds\n\t"
                         "s_mov_b32 m0, %3\n\ts_nop 0\n\tbuffer_load_dwordx4 %4, %5, %7 offen sc1 lds\n\ts_waitcnt vmcnt(0) lgkmcnt(0)\n\ts_barrier"
                         : "=&s"(tmp) : "s"(uu(tm - 1u)), "s"(dm), "s"(dm2), "v"(l16), "s"(srd), "s"(ms), "s"(ms2) : "vcc", "scc", "memory");
        }
#pragma unroll
        for (int ks = 0; ks < 4; ++ks) A[ks] = lds_r16(LB + C_MEM + ks * 1024);
        SCHED();
        st4s(rs, ofl, 0, sv);
        SCHED();
        static_for<0, 24>(slot);
        if (__builtin_expect(nanq != 0ull, 0)) {
            u32 mn = ~0u;
#pragma unroll
            for (int i = 0; i < 10; ++i) mn = min(min(min(mn, A[i].x ^ PSENT), A[i].y ^ PSENT), min(A[i].z ^ PSENT, A[i].w ^ PSENT));
            nanq = missed < SPIN_CAP ? __builtin_amdgcn_ballot_w64(mn == 0u) : 0ull;
        }
        if (__builtin_expect(nanq == 0ull, 1)) {
        static_for<24, 68>(slot);
        PV(7, 1); ones = lds_r16(ZP + 64); SCHED();
        VMAX(fa, tm0, tm1);
        if (__builtin_expect(__builtin_amdgcn_ballot_w64(fa > 24.0f) != 0ull, 0)) {
            const float up = fmaxf(max32(max16(fmaxf(tm0, tm1))), 0.f);
            tm0 -= up; tm1 -= up;
#pragma unroll
            for (int i = 0; i < 4; ++i) t[i] = __builtin_amdgcn_exp2f(__builtin_fmaf(sx[i], CEXP, mx) - up);
            e01 = pk(t[0], t[1]); e23 = pk(t[2], t[3]);
            asm volatile("s_nop 1" : "+v"(e01), "+v"(e23));
            QPERM(P.x, e01, "[0,1,0,1]"); QPERM(P.z, e01, "[2,3,2,3]"); QPERM(P.y, e23, "[0,1,0,1]"); QPERM(P.w, e23, "[2,3,2,3]");
            NOPS(1);
            MF0(oa, vx[0], P); MF0(ob, vx[1], P);
            NOPS(7);
        }
        SCHED();
        PV(1, 1); VEXP(tm0, tm0); SEL(oa[0], oa[0], ob[0]); SCHED();
        PV(8, 1); VEXP(tm1, tm1); SEL(oa[1], oa[1], ob[1]); SCHED();
        PV(9, 1); VCVT(pmm, tm0, tm1); SEL(oa[2], oa[2], ob[2]); SCHED();
        PV(2, 1);
        asm volatile("v_cndmask_b32 %0, %2, 0, %3\n\tv_cndmask_b32 %1, 0, %2, %3" : "=&v"(pm.x), "=&v"(pm.y) : "v"(pmm), "s"(himask)); SCHED();
        PV(3, 1); SEL(oa[3], oa[3], ob[3]); SCHED();
        MF0(rsum, ones, P); NOPS(4);
        VADD(fc, av1[0], av1[2]); VADD(fd, av1[1], av1[3]);
        { const v2u ones2 = ones.lo; MF1H(rsum, ones2, pm); }
        VCVT(vm.y, fc, fd); NOPS(1);
        MF1H(oa, vm, pm);
        {
            float inv; v4u oc;
            asm volatile("s_nop 3\n\tv_rcp_f32 %0, %1\n\ts_nop 2" : "=v"(inv) : "v"(rsum[0]), "v"(rsum));
            PIN(oa);
            const f32x2 i2 = {inv, inv};
            const f32x2 u0 = oa.lo * i2, u1 = oa.hi * i2;
            asm volatile("v_cvt_pk_bf16_f32 %0, %4, %5\n\tv_cvt_pk_bf16_f32 %2, %4, %5\n\tv_cvt_pk_bf16_f32 %1, %6, %7\n\tv_cvt_pk_bf16_f32 %3, %6, %7\n\t"
                         "v_permlane16_swap_b32 %0, %2\n\ts_nop 0\n\tv_permlane16_swap_b32 %1, %3"
                         : "=&v"(oc.x), "=&v"(oc.y), "=&v"(oc.z), "=&v"(oc.w) : "v"(u0.x), "v"(u0.y), "v"(u1.x), "v"(u1.y));
            st16(rs, oso, oc);
        }
        SCHED();
        unsigned long long clk;
        asm volatile("s_memrealtime %0" : "=s"(clk));
        HT(6);
        PIN(fl);
        v4u tq[4], tk[8], tv[2];
        f32x4 c0, c1, cm;
        u32 oo, kvo; bool kvok;
        u32 ln = l16; PIN(ln);
        lptr QX = lds + C_QX + (w & 1) * 4096 + ln, VO = lds + C_V + (w ^ 1) * 2048 + ln, VMX = lds + C_VM + w * 512 + (ln >> 1);
#pragma unroll
        for (int i = 0; i < 2; ++i) { tq[i] = lds_r16(QX + i * 1024); tk[2 * i] = lds_r16(LB + C_K + i * 1024); tk[2 * i + 1] = lds_r16(LB + C_K + 4096 + i * 1024); }
        *(LAS v2u*)VMX = vm;
        SCHED();
        SFOR(i, 12)
            if constexpr (i == 0) MF0(cm, kmf[0], tq[0]); else if constexpr (i == 1) MF0(c0, tk[0], tq[0]); else if constexpr (i == 2) MF0(c1, tk[1], tq[0]);
            else if constexpr (i % 3 == 0) MF1(cm, kmf[i / 3], tq[i / 3]); else if constexpr (i % 3 == 1) MF1(c0, tk[2 * (i / 3)], tq[i / 3]); else MF1(c1, tk[2 * (i / 3) + 1], tq[i / 3]);
            if constexpr (i == 0) { tq[2] = lds_r16(QX + 2048); tk[4] = lds_r16(LB + C_K + 2048); }
            if constexpr (i == 1) { tk[5] = lds_r16(LB + C_K + 6144); tq[3] = lds_r16(QX + 3072); }
            if constexpr (i == 2) { tk[6] = lds_r16(LB + C_K + 3072); tk[7] = lds_r16(LB + C_K + 7168); }
            if constexpr (i == 3) { tv[0] = lds_r16(VO); tv[1] = lds_r16(VO + 1024); }
            if constexpr (i == 4) { oo = ln + (ln & 0x300u) + ((vsrc ^ 8192u) + (u32)((w & 1) * 256)); PIN(oo); kb += NH * 16384; PINS(kb); }
            if constexpr (i == 5) { ksrc = kb + (((kmask >> ((s + 1) & 63)) & 1ull) ? 8192u : 0u); PINS(ksrc); }
            if constexpr (i == 6) { vsrc = kb + (((vmask >> ((s + 1) & 63)) & 1ull) ? W_VO - W_KQ + 8192u : W_VO - W_KQ); PINS(vsrc); }
            if constexpr (i == 7) { kvok = ((u32)__builtin_amdgcn_ballot_w64(fl < (u32)s + 2) & 7u) == 0u; }
            if constexpr (i == 8) { kvo = l16 | (kvok ? 0u : OOB); PIN(kvo); }
            SCHED();
        SEND
        asm volatile("s_waitcnt lgkmcnt(0)\n\ts_barrier" : "+s"(clk) :: "memory");
        const v2u tvm = *(LAS v2u*)(VMX + (512 - (w & 1) * 1024));
        const v4u pv = lds_r16(ZP + 80);
        DMA32(dk, kvo, ksrc);
        SCHED(); asm volatile("" :: "v"(cm));
        float u[10], m, m2;
        {
            float NEG = -1000000000.0f / SCALE; PIN(NEG);
#define MSK(d, a, i) asm volatile("v_cndmask_b32 %0, %1, %2, %3" : "=v"(d) : "v"(NEG), "v"(a), "s"(vis[i]))
#pragma unroll
            for (int i = 0; i < 4; ++i) MSK(u[i], c0[i], i);
#pragma unroll
            for (int i = 0; i < 4; ++i) MSK(u[4 + i], c1[i], 4 + i);
            asm volatile("v_max3_f32 %0, %2, %3, %10\n\tv_max3_f32 %1, %4, %5, %11\n\tv_max3_f32 %0, %0, %6, %7\n\tv_max3_f32 %1, %1, %8, %9\n\t"
                         "v_max_f32 %0, %0, %1\n\tv_mov_b32 %1, %0\n\ts_nop 1\n\tv_permlane16_swap_b32 %0, %1\n\tv_max_f32 %0, %0, %1\n\tv_mov_b32 %1, %0\n\ts_nop 1\n\t"
                         "v_permlane32_swap_b32 %0, %1\n\tv_max_f32 %0, %0, %1\n\tv_mul_f32 %0, 0xbe0293ee, %0"
                         : "=&v"(m), "=&v"(m2) : "v"(u[0]), "v"(u[1]), "v"(u[2]), "v"(u[3]), "v"(u[4]), "v"(u[5]), "v"(u[6]), "v"(u[7]), "v"(cm[0]), "v"(cm[1]));
        }
        DMA32(dv, kvo, vsrc);
        {
            v4u px; v2u p0, p1;
#define EX2(d, a, b) asm volatile("v_fmamk_f32 %0, %2, 0x3e0293ee, %4\n\tv_fmamk_f32 %1, %3, 0x3e0293ee, %4\n\tv_exp_f32 %0, %0\n\tv_exp_f32 %1, %1" : "=&v"(d.x), "=&v"(d.y) : "v"(a), "v"(b), "v"(m))
            f32x2 ex[5];
            EX2(ex[0], u[0], u[1]); EX2(ex[1], u[2], u[3]); EX2(ex[2], u[4], u[5]); EX2(ex[3], u[6], u[7]); EX2(ex[4], cm[0], cm[1]);
            asm volatile("v_cvt_pk_bf16_f32 %0, %8, %9\n\tv_cvt_pk_bf16_f32 %1, %10, %11\n\tv_cvt_pk_bf16_f32 %2, %12, %13\n\tv_cvt_pk_bf16_f32 %3, %14, %15\n\t"
                         "v_mov_b32 %5, 0\n\tv_mov_b32 %6, 0\n\tv_cvt_pk_bf16_f32 %4, %16, %17\n\tv_cvt_pk_bf16_f32 %7, %16, %17"
                         : "=&v"(px.x), "=&v"(px.y), "=&v"(px.z), "=&v"(px.w), "=&v"(p0.x), "=&v"(p0.y), "=&v"(p1.x), "=&v"(p1.y)
                         : "v"(ex[0].x), "v"(ex[0].y), "v"(ex[1].x), "v"(ex[1].y), "v"(ex[2].x), "v"(ex[2].y), "v"(ex[3].x), "v"(ex[3].y), "v"(ex[4].x), "v"(ex[4].y));
            f32x4 o[4], sum;
            const v2u ones2 = ones.lo;
            const u32 now = (u32)clk;
            u32 oz;
            SCHED();
            MF0(sum, ones, px); { const u32 q = ksrc ^ 8192u; DMA32(dq, kvo, q); } SCHED();
            MF0(o[0], vx[0], px);
            MF0(o[1], vx[1], px);
            MF0(o[2], tv[0], px); oz = (oso & ~(3u << 17)) | (u32)((s + 3) & 3) << 17; PIN(oz); SCHED();
            MF0(o[3], tv[1], px); oso = (oso & ~(3u << 17)) | (u32)((s + 1) & 3) << 17; PIN(oso); SCHED();
            MF1H(sum, ones2, p0);
            MF1H(o[0], vm, p0);
            MF1H(o[1], vm, p1); st16(rs, oz, pv); SCHED();
            MF1H(o[2], tvm, p0);
            MF1H(o[3], tvm, p1);
            float inv;
            asm volatile("s_nop 2\n\tv_rcp_f32 %0, %1\n\ts_nop 0" : "=v"(inv) : "v"(sum[0]), "v"(sum));
            asm volatile("" : "+v"(o[0]), "+v"(o[1]));
            st16(rs, oo, (v4u){pk2(o[0].lo * inv), pk2(o[0].hi * inv), pk2(o[1].lo * inv), pk2(o[1].hi * inv)});
            asm volatile("" : "+v"(o[2]), "+v"(o[3]));
            st16(rs, oo ^ 2048u, (v4u){pk2(o[2].lo * inv), pk2(o[2].hi * inv), pk2(o[3].lo * inv), pk2(o[3].hi * inv)});
            SCHED();
            if (__builtin_expect(h == 0 && w == 3, 0)) {
                st4s(rs, ln == 0 ? hint + 128u : OOB, 0, (u32)s << 16 | min(missed, 1u) << 14 | min(now - tm, 0x3FFFu));
                LST(L * 64 + s, now); LST(128 + L * 64 + s, ((int)(rdl(fl, 3) - now) >= CH_VIS - 4) | ((int)kvok << 1) | ((int)min(missed, 3u) << 2)); LST(640 + L * 64 + s, tm); LST(1728 + L * 64 + s, __builtin_amdgcn_s_memtime());
            }
            tm = rdl(fl, 5);
            missed = missed == SPIN_CAP ? missed : 0u;
            if (__builtin_expect(rdl(fl, 3) - now + CH_LATE > 4000u, 0)) {
#pragma unroll 1
                for (int n = 0; n < 64; ++n) {
                    CFENCE();
                    const u32 hv = ld4(rs, fkh);
                    tm = rdl(hv, 5);
                    if (rdl(hv, 3) - now + CH_LATE <= 4000u) break;
                }
            }
        }
        LS2(L == 0 && w == 0, h);
        if (__builtin_expect(!kvok, 0) && s + 1 < NSEG) {
            st4s(rs, (ln == 0 && w == 0) ? hint + 4096u + (u32)h * 128u : OOB, 0, (u32)s + 1u);
            wait_flags(rs, ln < 48u ? fkh : OOB, (u32)s + 2, dead);
            DMA32(dk, l16, ksrc); DMA32(dv, l16, vsrc); DMA32(dq, l16, ksrc ^ 8192u);
        }
        ++s;
        } else {
            u32 ln = l16; PIN(ln);
            const u32 hv = ld4(rs, ln == 96u ? foff(F_ERR) : fkh);
            tm = rdl(hv, 5);
            *(LAS u32*)(lds + C_DUMP + (w == 0 ? 0 : 64)) = rdl(hv, 6);
            BAR();
            missed = uu(*(LAS u32*)(lds + C_DUMP)) != 0u ? SPIN_CAP : missed + 1;
            st4s(rs, missed == SPIN_CAP ? foff(F_ERR) : OOB, 0, 1u);
            BAR();
        }
    }
    DRAIN(); BAR();
    st4s(rs, ofl, 0, NSEG);
}

#define WO_GAP 6
DEV void role_wo(const Params& p, lptr lds, int j) {
    const int tid = threadIdx.x, lane = tid & 63, w = uni(tid >> 6), g = lane >> 4, r = lane & 15;
#define WROT 0
    const int rg = j & 1, cg = j >> 1, rot = cg & WROT;
    const rsrc_t rs = mk_rsrc(p.ws, W_END);
    v8u wo[8][5];
    {
        const v4u so_ = mk_srd(p.Wo, DM * NQ * 2);
        u32 vo[5];
#pragma unroll
        for (int i = 0; i < 5; ++i) { const int n = 64 * i + lane; vo[i] = (u32)((n / 5) * DM + cg * 40 + (n % 5) * 8) * 2u; }
        auto fill = [&](int b) {
            const u32 dst = uu(w * 40960 + (b & 3) * 10240), src = uu((8 * w + ((b + rot) & 7)) * (DH * DM * 2));
#pragma unroll
            for (int i = 0; i < 10; ++i) DMAIN(so_, dst + i * 1024, vo[i % 5], src + (i / 5) * (64 * DM * 2));
        };
        __builtin_amdgcn_s_sleep(ST_WO);
#pragma unroll 1
        for (int b = 0; b < 4; ++b) fill(b);
        const int q4 = r >> 2;
        lptr tb = lds + w * 40960 + (32 * (g >> 1) + 16 * (q4 >> 1) + 8 * ((r >> 1) & 1) + 4 * (g & 1) + (q4 & 1)) * 80 + (r & 1) * 8;
        SFOR(b, 8)
            VMW(b < 5 ? 30 : 70 - 10 * b);
#pragma unroll
            for (int t = 0; t < 5; ++t) {
                lptr q = tb + (b & 3) * 10240 + t * 16;
                wo[b][t] = tr_frag(q, q + 160, q + 64 * 80, q + 64 * 80 + 160);
                if (b * 5 + t < 32) asm volatile("" : "+a"(wo[b][t])); else asm volatile("" : "+v"(wo[b][t]));
            }
            CFENCE();
            if constexpr (b < 4) fill(b + 4);
        SEND
    }
    BAR();
    EV(6);
    const u32 idx = (r & 2) ? 0xEEEEu : 0x4444u;
    bool dead = false;
    const u32 pol0 = get_pol(p, rs, dead) & 1u;
    const u32 l16 = (u32)lane * 16u;
    u32 oo[8];
#pragma unroll
    for (int i = 0; i < 8; ++i) oo[i] = uu(W_ORING + ring_map((u32)(rg * 32768 + (8 * w + ((i + rot) & 7)) * 1024), pol0));
    const u32 hint = W_MRING + 4 * 32768u + pol0 * 8192u;
    u32 hvo[2];
#pragma unroll
    for (int l = 0; l < 2; ++l) hvo[l] = lane < 2 ? hint + (u32)(l ^ 1) * (MB / 4) + (u32)lane * 4u : lane == 2 ? hint + (u32)l * (MB / 4) + 128u : lane == 3 ? foff(F_ERR) :
                 (lane >= 32 && j == 0 && w == 3) ? hint + (u32)l * (MB / 4) + (u32)lane * 128u : OOB;
    lptr xw = r < 8 ? lds + w * 640 + (2 * (g & 1) + (g >> 1)) * 32 + r * 4 : lds + 8192 + lane * 4;
    const int G = ((5 * cg) >> 1) + w, tt = 2 * G + ((lane >> 3) & 1) - 5 * cg;
    const bool pub = w < 3 && lane < 32 && tt >= 0 && tt < 5;
    lptr xr = lds + (pub ? tt * 128 + (2 * (lane >> 4) + ((lane >> 2) & 1)) * 32 + (lane & 3) * 8 : 0);
    const u32 mpo = pub ? W_MRING + ring_map((u32)(G * 256 + rg * 128 + lane * 4), pol0) : OOB;
    constexpr u32 PER0 = CH_W0 + CH_MW + CH_H0 + CH_MH + 2 * CH_VIS;
    const bool lead = j == 0 && w == 3;
    const u32 hso = (lead && lane == 0) ? hint : OOB;
    MK_SRD();
    u32 t = 0, tn = 0;
    if (j == 0) {
        for (int n = 0; n < SPIN_CAP && !dead; ++n) {
            CFENCE();
            if (all_(ld4(rs, W_ORING + pol0 * 8192u + (u32)w * 16384u + (u32)lane * 128u) != PSENT)) break;
            __builtin_amdgcn_s_sleep(1);
        }
        BAR();
    }
    if (lead) {
        t = rt32() + 40u; tn = t + PER0 / 2 + CH_REC;
        st16(rs, lane < 2 ? hint + (u32)lane * (MB / 4) : OOB, (v4u){lane == 0 ? t : tn, PER0, (lane == 0 ? t : tn) + (CH_W0 + CH_VIS + CH_MW + CH_REC), 0u});
    } else {
        for (int n = 0; n < SPIN_CAP && !dead; ++n) {
            CFENCE();
            const u32 hv = ld4(rs, hvo[1]);
            t = rdl(hv, 0);
            if (rdl(hv, 1) < 400u) break;
            __builtin_amdgcn_s_sleep(1);
        }
    }
    int sn[2] = {0, 0};
    u32 hav = 4 * CH_H0, wav = 4 * CH_W0, mh = 8 * CH_MH, mw = 8 * CH_MW, ok[2] = {0u, 0u}, dw[2] = {CH_W0 + CH_VIS + CH_MW + CH_REC, CH_W0 + CH_VIS + CH_MW + CH_REC};
    u32 zero = 0, hv = 0xFFFFu, pst = 1, pdur = 0, pmp = OOB;
    const u32 psent = PSENT;
    lptr sw = lds + 2560 + w * 4;
    bool stop = false;
    asm volatile("" : "+v"(zero), "+v"(sw));
    auto turn = [&](auto lc) __attribute__((always_inline)) {
        constexpr int L = decltype(lc)::value, M = L ^ 1;
        u32 now, done, rep, rd, lost, dh, dn, nt, za; v4u hw;
        const int sc = sn[L];
        auto books = [&](auto ic) __attribute__((always_inline)) {
            constexpr int i = decltype(ic)::value;
            if constexpr (i == 0) { rep = rdl(hv, 2); PINS(rep); }
            if constexpr (i == 1) { lost = (u32)(__builtin_amdgcn_ballot_w64(hv == (u32)sn[M]) >> 32); PINS(lost); }
            if constexpr (i == 2) { lost |= (rep >> 16) ^ (u32)sn[M]; PINS(lost); }
            if constexpr (i == 3) { lost |= ok[M] ^ 2u; rd = rep & 0xFFFFu; PINS(lost); PINS(rd); }
            if constexpr (i == 4) { lost = lost == 0u ? CH_UP * 8u : 0u; PINS(lost); }
            if constexpr (i == 5) { dn = wav >> 2; PINS(dn); pdur = min(pdur, dn + 8u); PINS(pdur); }
            if constexpr (i == 6) { pdur = (done & ok[M]) ? pdur : dn; PINS(pdur); }
            if constexpr (i == 7) { wav += pdur - dn; PINS(wav); }
            if constexpr (i == 8) { dh = hav >> 2; PINS(dh); nt = min(rd, dh + 8u); PINS(nt); }
            if constexpr (i == 9) { nt = rd < 0x3FFFu ? nt : dh; PINS(nt); }
            if constexpr (i == 10) { hav += nt - dh; PINS(hav); }
            if constexpr (i == 11) { mw = max(mw - 1u, CH_MW * 8u); PINS(mw); }
            if constexpr (i == 12) { mh = max(mh - 1u, CH_MH * 8u); PINS(mh); }
            if constexpr (i == 13) { rd = rd >> 14 == 1u ? lost : 0u; PINS(rd); }
            if constexpr (i == 14) { mw += rd; mh += lost - rd; PINS(mw); PINS(mh); }
            if constexpr (i == 15) { ok[M] = done ? 1u : (ok[M] & 1u) << 1; PINS(ok[M]); }
            if constexpr (i == 16) { dh = (2u * hav + mh) / 8u + CH_VIS; PINS(dh); }
            if constexpr (i == 17) { dn = (2u * wav + mw) / 8u + CH_VIS; PINS(dn); }
            if constexpr (i == 18) { nt = dw[L] + dh; PINS(nt); }
            if constexpr (i == 19) { dh += dn; dw[L] = dn; sn[M] += (int)done; PINS(dh); PINS(sn[M]); }
            if constexpr (i == 20) { rd = tn - t + (dh >> 1); PINS(rd); }
            if constexpr (i == 21) { rd = sn[M] + 1 < NSEG ? rd : 0u; PINS(rd); }
            if constexpr (i == 22) { nt = t + (u32)max((int)nt, (int)rd); PINS(nt); }
            if constexpr (i == 23) { hw.x = nt; hw.y = dh; PIN(hw.x); PIN(hw.y); }
            if constexpr (i == 24) { hw.z = nt + dn; hw.w = 0u; PIN(hw); }
            if constexpr (i == 25) st16(rs, hso + L * (MB / 4), hw);
        };
        if (__builtin_expect(sc + 1 < NSEG, 1)) {
            u32 vo = l16 + (L * (MB / 2) + (u32)(sc & 3) * 131072u), mp, hn;
            PIN(vo);
#define WT(k) do {} while (0)
            WT(0);
            v4u d[8];
            {
                u32 tmp;
                asm volatile(WAIT_TXT("%1", "%2") "buffer_load_dwordx4 %0, %3, %4, %5 offen sc1\n\ts_waitcnt lgkmcnt(0)"
                             : "=&v"(d[0]), "=&s"(tmp) : "s"(uu(t - 1u)), "v"(vo), "s"(srd), "s"(oo[0]) : "vcc", "scc", "memory");
            }
            SFOR(i, 7)
                asm volatile("buffer_load_dwordx4 %0, %1, %2, %3 offen sc1" : "=v"(d[i + 1]) : "v"(vo), "s"(srd), "s"(oo[i + 1]) : "memory");
                SCHED();
                if constexpr (i == 0) { done = uu(pst) ^ 1u; PINS(done); }
                static_for<3 * i, 3 * i + 3>(books);
                SCHED();
            SEND
            hn = ld4(rs, hvo[L]);
            SCHED();
            WT(1);
            static_for<21, 26>(books);
            za = done ? pmp : OOB;
            st4s(rs, za, 0, psent);
            *(LAS u32*)(sw + L * 4096) = zero;
            f32x4 acc[5] = {};
            mp = mpo + (L * (MB / 4) + (u32)((sc + 1) & 3) * 32768u); pmp = mpo + (L * (MB / 4) + (u32)(sc & 3) * 32768u);
            asm volatile("" : "+v"(acc[0]), "+v"(acc[1]), "+v"(acc[2]), "+v"(acc[3]), "+v"(acc[4]), "+v"(mp), "+v"(pmp));
            SCHED();
            WT(2);
            float f0[5], f1[5]; unsigned long long bad;
#define WSW(o_) asm volatile("v_permlane32_swap_b32 %0, %1" : "+v"(f0[o_]), "+v"(f1[o_]))
#define WAD(o_) asm volatile("v_add_f32_dpp %0, %1, %0 row_ror:8 row_mask:0xf bank_mask:0xf" : "+v"(f0[o_]) : "v"(f1[o_]))
#define WFO(o_) do { VADD(f0[o_], acc[o_][0], acc[o_][2]); VADD(f1[o_], acc[o_][1], acc[o_][3]); } while (0)
#define WWR(o_) *(LAS float*)(xw + L * 4096 + (o_) * 128) = f0[o_]
            asm volatile("s_waitcnt vmcnt(10)" : "+v"(d[0]));
            if (__builtin_expect(__builtin_amdgcn_ballot_w64(d[0].x == PSENT) != 0ull, 0)) {
                asm volatile("s_waitcnt vmcnt(3)" : "+v"(d[1]), "+v"(d[2]), "+v"(d[3]), "+v"(d[4]), "+v"(d[5]), "+v"(d[6]), "+v"(d[7]));
#pragma unroll
                for (int i = 0; i < 5; ++i) *(LAS u32*)(xw + L * 4096 + i * 128) = psent;
                *(LAS u32*)(sw + L * 4096) = 1u;
            } else {
            SFOR(i, 40)
                constexpr int pc = i < 30 ? i / 5 : 6 + (i & 1), oc = i < 30 ? i % 5 : (i - 30) >> 1;
                if constexpr (oc == 0 && pc > 0) asm volatile("s_waitcnt vmcnt(%1)" : "+v"(d[pc]) : "i"(10 - pc));
                if constexpr (pc * 5 + oc < 32) SMF_A(acc[oc], d[pc], wo[pc][oc]); else SMF_V(acc[oc], d[pc], wo[pc][oc]);
                if constexpr (i == 0) WT(3);
                if constexpr (i == 20) WT(4);
                if constexpr (i == 31 || i == 33) NOPS(1);
                if constexpr (i == 32) NOPS(0);
                if constexpr (i == 34) WFO(0);
                if constexpr (i == 35) { asm volatile("v_cmp_u_f32 %0, %1, %2" : "=s"(bad) : "v"(f0[0]), "v"(f1[0])); WSW(0); }
                if constexpr (i == 36) WFO(1);
                if constexpr (i == 37) { WAD(0); WSW(1); }
                if constexpr (i == 38) { WWR(0); SCHED(); WFO(2); }
                if constexpr (i == 39) { WAD(1); WSW(2); }
                SCHED();
            SEND
            WT(5);
            WWR(1); SCHED(); WFO(3); WAD(2); WWR(2); SCHED(); WSW(3);
            if (__builtin_expect(bad != 0ull, 0)) {
                u32 mn = ~0u;
#pragma unroll
                for (int i = 0; i < 8; ++i) mn = min(mn, d[i].x ^ PSENT);
                if (all_(mn != 0u)) {
#pragma unroll
                    for (int i = 0; i < 8; ++i) mn = min(min(mn, d[i].y ^ PSENT), min(d[i].z ^ PSENT, d[i].w ^ PSENT));
                }
                *(LAS u32*)(sw + L * 4096) = all_(mn != 0u) ? 0u : 1u;
            }
            WFO(4); WAD(3); WWR(3); SCHED();
            asm volatile("v_permlane32_swap_b32 %0, %1\n\ts_nop 1\n\tv_add_f32_dpp %0, %1, %0 row_ror:8 row_mask:0xf bank_mask:0xf" : "+v"(f0[4]), "+v"(f1[4]));
            WWR(4);
            }
            WT(6);
            f32x2 a, b, c, e; u32 cw, mq; v4u sv;
            asm volatile("s_waitcnt lgkmcnt(0)\n\ts_barrier\n\tds_read_b64 %0, %5 offset:%7\n\tds_read_b64 %1, %5 offset:%7+640\n\tds_read_b64 %2, %5 offset:%7+1280\n\tds_read_b64 %3, %5 offset:%7+1920\n\t"
                         "ds_read_b128 %4, %6 offset:%7+2560\n\t"
                         "s_waitcnt lgkmcnt(3)\n\tv_pk_add_f32 %0, %0, %1\n\ts_waitcnt lgkmcnt(1)\n\tv_pk_add_f32 %2, %2, %3\n\ts_waitcnt lgkmcnt(0)\n\tv_pk_add_f32 %0, %0, %2"
                         : "=&v"(a), "=&v"(b), "=&v"(c), "=&v"(e), "=&v"(sv) : "v"(xr), "v"(zero), "i"(L * 4096) : "memory");
            asm volatile("v_cmp_u_f32 vcc, %2, %3\n\tv_cvt_pk_bf16_f32 %0, %2, %3\n\tv_cndmask_b32 %1, %4, %5, vcc" : "=&v"(cw), "=&v"(mq) : "v"(a.x), "v"(a.y), "v"(mp), "v"(OOB) : "vcc");
            st4s(rs, mq, 0, cw);
            SCHED();
            now = rt32();
            PIN(hn); hv = hn;
            WT(7);
            pst = sv.x | sv.y | sv.z | sv.w;
            if (__builtin_expect(__builtin_amdgcn_ballot_w64(mq != mp && pst == 0u) != 0ull, 0)) st4s(rs, mp, 0, cw);
        } else {
            done = uu(pst) ^ 1u;
            st4s(rs, done ? pmp : OOB, 0, psent);
            static_for<0, 25>(books);
            pst = 1;
            hv = ld4(rs, hvo[L]);
            PIN(hv);
            now = rt32();
        }
        pdur = now - t;
        const u32 per = rdl(hv, 1), th = rdl(hv, 0);
        t = lead ? tn : (int)(th - now) < -(int)(per >> 1) ? th + per : th;
        tn = nt;
        dw[M] += max((int)(now + CH_WX - t), 0);
        stop = stop || rdl(hv, 3) != 0u;
    };
    for (int k = 0; k < 2 * SPIN_CAP && !stop && (sn[0] + 1 < NSEG || sn[1] + 1 < NSEG); ++k) { turn(IC<0>{}); turn(IC<1>{}); }
    if (sn[0] + 1 < NSEG || sn[1] + 1 < NSEG) st4s(rs, tid == 0 ? foff(F_ERR) : OOB, 0, 1u);
}

__global__ void __launch_bounds__(256, 1) k_main(Args args) {
    Params p;
    (Args&)p = args;
    lptr lds = (lptr)(u32)16 - 16;
    if (blockIdx.x == 0) LST(256, rt32());
    unsigned long long pc; int len;
    asm volatile("s_getpc_b64 %0\n.Lk_main_here:\n\ts_mov_b32 %1, .Lk_end_here-.Lk_main_here" : "=s"(pc), "=s"(len));
    const v4u code = ldin16(mk_rsrc((const void*)(pc & ~15ull), (u32)max(len, 0)), (((blockIdx.x >> 3) & 31u) * 256u + threadIdx.x) * 16u, 0);
    CFENCE();
    if (threadIdx.x == 0) {
        const v4u e = ld16s(mk_rsrc(p.ws, W_END), foff(F_EP), 0);
        const unsigned long long tag = (unsigned long long)e.y << 32 | (e.x + 1u);
        u32 role = ((u32)__builtin_amdgcn_s_getreg(20 | (3 << 11)) & 7u) * 32u + ((blockIdx.x >> 3) & 31u);
#pragma unroll 1
        for (int n = 0; n < 256; ++n, role = (role + 1u) & 255u)
            if (__hip_atomic_exchange((GAS unsigned long long*)(p.ws + W_SLOT) + role * 8u, tag, __ATOMIC_RELAXED, __HIP_MEMORY_SCOPE_AGENT) != tag) break;
        lds_w16(lds, (v4u){e.x + 1u, e.y, e.z, role});
    }
    __syncthreads();
    const v4u boot = lds_r16(lds);
    p.e0 = uu(boot.x); p.e1 = uu(boot.y); p.e2 = uu(boot.z);
    const int role = uni((int)boot.w), x = role >> 5, i = role & 31;
    asm volatile("" :: "v"(code));
    __syncthreads();
    LST(8192 + blockIdx.x * 16, role);
    if (x < 2) { role_head(p, lds, i >> 4, x * 16 + (i & 15)); __builtin_amdgcn_endpgm(); }
    if (x == 2) { role_wo(p, lds, i); __builtin_amdgcn_endpgm(); }
    const int no = x == 3 ? 30 : x < 6 ? 5 : 0;
    if (x > 3 && i >= 8) { role_qkv(p, lds, (x - 4) * 24 + i - 8); __builtin_amdgcn_endpgm(); }
    const bool isout = i < no;
    const int og = x == 3 ? i / 5 : x + 2, ng = i % 5, L = og >> 2, kq = og & 3;
    const int k = i - no, w = uni(threadIdx.x >> 6);
    const int j = isout ? (L == 1 ? 0 : 44) + kq * 5 + ng : x == 3 ? 36 + k : k < 2 ? 12 + 2 * x + k : x < 6 ? 24 + x : k < 5 ? 10 + 3 * x + k : 15 + 3 * x + k;
    const rsrc_t rs = mk_rsrc(p.ws, W_END);
    if (j < NKV) {
        const int nt = j < 320 - 7 * NKV ? 8 : 7;
#pragma unroll 1
        for (int q = 0; q < 4; ++q) kv_fetch(p, j + NKV * q, uu(q * KT_BYTES));
        Filt f;
        u32 warm[2];
#pragma unroll 1
        for (int q = 0; q < nt; ++q) {
            VMW(15); BAR();
            if (q == 0) EV(2);
            kv_fetch(p, j + NKV * (q + 4), uu(((q + 4) & 7) * KT_BYTES));
            kv_tile(p, rs, lds + q * KT_BYTES, lds + (q == 0 ? 8 : q - 1) * KT_BYTES, j + NKV * q);
        }
        filt_load(p, j, f);
        VMW(10); BAR();
        boot_set(p, rs, threadIdx.x == 0 ? foff(F_KV + j) : OOB, 2u);
        EV(4);
        { bool dead = false; boot_wait(p, rs, pol_off(), 0u, dead); }
        {
            const u32 n = (u32)(j * 512) + threadIdx.x;
            const rsrc_t rf = mk_rsrc(p.ssmk, ACT);
            warm[0] = __builtin_amdgcn_raw_buffer_load_b32(rf, (256u + n / 48u) * 4096u + (n % 48u) * 64u, 0, 0);
            warm[1] = __builtin_amdgcn_raw_buffer_load_b32(rf, (256u + (n + 256u) / 48u) * 4096u + ((n + 256u) % 48u) * 64u, 0, 0);
        }
        conv_job<0>(p, rs, lds, j, f, 0);
        asm volatile("" :: "v"(warm[0]), "v"(warm[1]));
        if (j >= 36) filt_load(p, 28 + j, f);
        DRAIN(); BAR();
        st4s(rs, threadIdx.x == 0 ? foff(F_CV + j) : OOB, 0, 2u);
        if (j >= 36) {
#pragma unroll 1
            for (int g = 28 + j; g < 160; g += 8) conv_job<2>(p, rs, lds, g, f, min(g + 8, 159));
            DRAIN(); BAR();
            st4s(rs, threadIdx.x == 0 ? foff(F_CV + 124 + j) : OOB, 0, 3u);
            EV(15);
            __builtin_amdgcn_endpgm();
        }
    }
    if (!isout) { if (j < 28) role_row<0>(p, lds, (j - 20) * 4 + w); else role_row<1>(p, lds, (j - 28) * 4 + w);
        boot_set(p, rs, j == 28 && threadIdx.x == 0 ? foff(F_EP) : OOB, 0u);
        __builtin_amdgcn_endpgm();
    }
    role_out(p, lds, L, kq, ng);
}

__global__ void k_end() { asm volatile(".Lk_end_here:"); }

extern "C" void kernel_launch(void* const* d_in, const int* in_sizes, int n_in, void* d_out, int out_size, void* d_ws, size_t ws_size,
                              hipStream_t stream) {
    Args p;
    p.x = (const u16*)d_in[0]; p.mem0 = (const u16*)d_in[1]; p.Wq = (const u16*)d_in[2]; p.Wk = (const u16*)d_in[3];
    p.Wv = (const u16*)d_in[4]; p.Wo = (const u16*)d_in[5];
    p.bk = (const u16*)d_in[8]; p.bv = (const u16*)d_in[9]; p.shift = (const u16*)d_in[10]; p.ssmk = (const u16*)d_in[11]; p.ssmD = (const u16*)d_in[12];
    p.out = (u16*)d_out;
    p.ws = (unsigned char*)(((size_t)d_ws + 2 * MB - 1) & ~(size_t)(2 * MB - 1));
    (void)hipFuncSetAttribute((const void*)k_main, hipFuncAttributeMaxDynamicSharedMemorySize, LDS_BYTES);
    hipLaunchKernelGGL(k_main, dim3(256), dim3(256), LDS_BYTES, stream, p);
}
```

```cpp
#include <hip/hip_runtime.h>
#include <hip/hip_bf16.h>

typedef unsigned short u16;
typedef unsigned int u32;
typedef u32 v2u __attribute__((ext_vector_type(2)));
typedef u32 v4u __attribute__((ext_vector_type(4)));
typedef u32 v8u __attribute__((ext_vector_type(8)));
typedef float f32x2 __attribute__((ext_vector_type(2)));
typedef float f32x4 __attribute__((ext_vector_type(4)));
typedef float f32x16 __attribute__((ext_vector_type(16)));
typedef __bf16 bf16x2 __attribute__((ext_vector_type(2)));
typedef __bf16 bf16x8 __attribute__((ext_vector_type(8)));
typedef short s16x4 __attribute__((ext_vector_type(4)));
#define LAS __attribute__((address_space(3)))
#define GAS __attribute__((address_space(1)))
typedef LAS unsigned char* lptr;
#define DEV __device__ __forceinline__

constexpr int SEQ = 2048, DM = 640, NH = 32, DH = 128, NSEG = 64, NQ = NH * DH;

constexpr u32 MB = 1u << 20;
constexpr u32 W_ORING = 0;
constexpr u32 W_MRING = MB;
constexpr u32 W_CTRL = 4 * MB;
constexpr u32 ACT = SEQ * DM * 2;
constexpr u32 H_TILE = 128 * 1024;
constexpr u32 W_H2 = 8 * MB;
constexpr u32 W_KQ = 16 * MB;
constexpr u32 W_VO = W_KQ + 64 * MB;
constexpr u32 W_P = W_VO + 64 * MB;
constexpr u32 P_ITEM = 512 * 1024;
constexpr u32 W_H1 = W_P + 44 * MB, W_Y = W_H1 + 8 * MB, W_X2 = W_Y + ACT;
constexpr u32 W_KVT = W_X2 + ACT;
constexpr u32 W_END = W_KVT + 3 * MB;
constexpr u32 NREG = 12;
static_assert((size_t)W_END + 2 * MB <= 256ull * MB && W_H1 >= (NREG - 1) * 16 * MB + 2 * MB && W_H1 <= NREG * 16 * MB, "workspace");
constexpr u32 F_EP = 0, F_RG = 32, F_LN = 64, F_KV = 192,
              F_QKV = 256, F_O = F_QKV + 192, F_P = F_O + 64, F_H2 = F_P + 64, F_CV = F_H2 + 64, F_ERR = F_CV + 191, F_N = F_CV + 192;
constexpr u32 W_SLOT = 3 * MB;
DEV u32 foff(u32 idx) { return W_CTRL + (idx >> 6) * 16384u + (idx & 63u) * 128u; }
DEV u32 near_(u32 o, u32 side, u32 pol) { return o + ((side ^ (pol >> (o >> 24)) ^ (o >> 21)) & 1u) * 8192u; }
DEV u32 p_item(int s) { return W_P + (u32)s * P_ITEM; }

constexpr u32 OOB = 0x80000000u;
constexpr u32 PSENT = 0x7F817F81u;
constexpr int SPIN_CAP = 40000;
constexpr int LDS_BYTES = 160 * 1024;
#define ST_HEAD 0
#define ST_WO 0

#define EV(k) do {} while (0)
#define LST(idx, v) do {} while (0)
#define LS2(cond, idx) do {} while (0)
struct Args {
    const u16 *x, *mem0, *Wq, *Wk, *Wv, *Wo, *bk, *bv, *shift, *ssmk, *ssmD;
    u16* out;
    unsigned char* ws;
};
struct Params : Args { u32 e0, e1, e2; };

typedef __amdgpu_buffer_rsrc_t rsrc_t;
DEV rsrc_t mk_rsrc(const void* p, u32 bytes) { return __builtin_amdgcn_make_buffer_rsrc((void*)p, 0, bytes, 0x00020000); }
DEV v4u ld16s(rsrc_t r, u32 off, u32 so) { return __builtin_amdgcn_raw_buffer_load_b128(r, off, so, 16); }
DEV v2u ld8s(rsrc_t r, u32 off, u32 so) { return __builtin_amdgcn_raw_buffer_load_b64(r, off, so, 16); }
DEV v4u ldin16(rsrc_t r, u32 off, u32 so) { return __builtin_amdgcn_raw_buffer_load_b128(r, off, so, 0); }
DEV v2u ldin8(rsrc_t r, u32 off, u32 so) { return __builtin_amdgcn_raw_buffer_load_b64(r, off, so, 0); }
DEV u32 ld4(rsrc_t r, u32 off) { return __builtin_amdgcn_raw_buffer_load_b32(r, off, 0, 16); }
DEV void st16(rsrc_t r, u32 off, v4u v) { __builtin_amdgcn_raw_buffer_store_b128(v, r, off, 0, 16); }
DEV void st8s(rsrc_t r, u32 off, u32 so, v2u v) { __builtin_amdgcn_raw_buffer_store_b64(v, r, off, so, 16); }
DEV void st4s(rsrc_t r, u32 off, u32 so, u32 v) { __builtin_amdgcn_raw_buffer_store_b32(v, r, off, so, 16); }
#define DRAIN() asm volatile("s_waitcnt vmcnt(0)" ::: "memory")
#define VMW(n) asm volatile("s_waitcnt vmcnt(%0)" :: "i"(n) : "memory")
#define BAR() asm volatile("s_waitcnt lgkmcnt(0)\n\ts_barrier" ::: "memory")
#define SCHED() __builtin_amdgcn_sched_barrier(0)
#define CFENCE() asm volatile("" ::: "memory")
#define NOPS(n) asm volatile("s_nop %0" :: "i"(n))
#define PIN(x) asm volatile("" : "+v"(x))
#define PINS(x) asm volatile("" : "+s"(x))

template <int V> struct IC { static constexpr int value = V; };
template <int I, int N, class F> DEV void static_for(F&& f) { if constexpr (I < N) { f(IC<I>{}); static_for<I + 1, N>(f); } }
#define SFOR(var, n) static_for<0, n>([&](auto ic_) __attribute__((always_inline)) { constexpr int var = decltype(ic_)::value;
#define SEND });

DEV u32 pk(float a, float b) { bf16x2 v = {(__bf16)a, (__bf16)b}; return __builtin_bit_cast(u32, v); }
DEV float lo_f(u32 u) { return __builtin_bit_cast(float, u << 16); }
DEV float hi_f(u32 u) { return __builtin_bit_cast(float, u & 0xffff0000u); }
DEV f32x2 up2(u32 u) { return (f32x2){lo_f(u), hi_f(u)}; }
DEV u32 pk2(f32x2 v) { return pk(v.x, v.y); }
DEV float rbf(float a) { return (float)(__bf16)a; }
DEV v4u lds_r16(lptr p) { return *(LAS v4u*)p; }
DEV void lds_w16(lptr p, v4u v) { *(LAS v4u*)p = v; }
DEV v2u lds_tr(lptr p) { return __builtin_bit_cast(v2u, __builtin_amdgcn_ds_read_tr16_b64_v4i16((LAS s16x4*)p)); }
DEV v4u tr2(lptr p0, lptr p1) { const v2u a = lds_tr(p0), b = lds_tr(p1); return (v4u){a.x, a.y, b.x, b.y}; }
DEV v8u cat8(v4u a, v4u b) { return __builtin_shufflevector(a, b, 0, 1, 2, 3, 4, 5, 6, 7); }
DEV f32x4 mfma16(v4u a, v4u b, f32x4 c) { return __builtin_amdgcn_mfma_f32_16x16x32_bf16(__builtin_bit_cast(bf16x8, a), __builtin_bit_cast(bf16x8, b), c, 0, 0, 0); }
DEV f32x16 mfma32(v4u a, v4u b, f32x16 c) { return __builtin_amdgcn_mfma_f32_32x32x16_bf16(__builtin_bit_cast(bf16x8, a), __builtin_bit_cast(bf16x8, b), c, 0, 0, 0); }

DEV float f_(u32 u) { return __builtin_bit_cast(float, u); }
DEV u32 u_(float f) { return __builtin_bit_cast(u32, f); }
template <int CTRL> DEV float dpp(float v) { return f_((u32)__builtin_amdgcn_update_dpp(0, (int)u_(v), CTRL, 0xf, 0xf, false)); }
DEV float max16(float v) { auto r = __builtin_amdgcn_permlane16_swap(u_(v), u_(v), false, false); return fmaxf(f_(r[0]), f_(r[1])); }
DEV float max32(float v) { auto r = __builtin_amdgcn_permlane32_swap(u_(v), u_(v), false, false); return fmaxf(f_(r[0]), f_(r[1])); }
DEV float add16(float v) { auto r = __builtin_amdgcn_permlane16_swap(u_(v), u_(v), false, false); return f_(r[0]) + f_(r[1]); }
DEV float add32(float v) { auto r = __builtin_amdgcn_permlane32_swap(u_(v), u_(v), false, false); return f_(r[0]) + f_(r[1]); }
DEV float wave_sum(float v) {
    v += dpp<0xB1>(v);
    v += dpp<0x4E>(v);
    v += dpp<0x124>(v);
    v += dpp<0x128>(v);
    return add32(add16(v));
}
DEV bool all_(bool c) { return __builtin_amdgcn_ballot_w64(!c) == 0ull; }
DEV int uni(int v) { return __builtin_amdgcn_readfirstlane(v); }
DEV u32 uu(u32 v) { return (u32)__builtin_amdgcn_readfirstlane((int)v); }
DEV u32 rdl(u32 v, int l) { return (u32)__builtin_amdgcn_readlane((int)v, l); }
DEV u32 rt32() { return (u32)__builtin_amdgcn_s_memrealtime(); }

DEV void wait_flags(rsrc_t rs, u32 off, u32 want, bool& dead) {
    if (dead) return;
#pragma unroll 1
    for (int n = 0;; ++n) {
        CFENCE();
        const u32 v = ld4(rs, off);
        if (all_(off >= OOB || v >= want)) break;
        __builtin_amdgcn_s_sleep(1);
        if ((n & 63) == 63) { if (uni((int)ld4(rs, foff(F_ERR))) != 0) { dead = true; break; } }
        if (n > SPIN_CAP) { st4s(rs, foff(F_ERR), 0, 1u); dead = true; break; }
    }
    CFENCE();
}

DEV bool boot_ok(const Params& p, u32 off, v4u v, u32 want) { return all_(off >= OOB || (v.x == p.e0 && v.y == p.e1 && v.z == p.e2 && v.w >= want)); }
DEV void boot_set(const Params& p, rsrc_t rs, u32 off, u32 v) { st16(rs, off, (v4u){p.e0, p.e1, p.e2, v}); }
DEV u32 boot_wait(const Params& p, rsrc_t rs, u32 off, u32 want, bool& dead) {
    v4u v;
#pragma unroll 1
    for (int n = 0;; ++n) {
        CFENCE();
        v = ld16s(rs, off, 0);
        if (boot_ok(p, off, v, want) || dead) break;
        __builtin_amdgcn_s_sleep(1);
        if (n > SPIN_CAP) { st4s(rs, foff(F_ERR), 0, 1u); dead = true; }
    }
    CFENCE();
    return v.w;
}

DEV u32 pol_off() { const int lane = threadIdx.x & 63; return lane < 32 ? foff(F_RG + lane) : OOB; }
DEV u32 pol_of(u32 raw) {
    const int lane = threadIdx.x & 63;
    const float v = add16(__builtin_amdgcn_fmed3f((float)((int)raw - (1 << 20)), -250.f, 250.f));
    const float lo = dpp<0x121>(v), hi = dpp<0x12F>(v), ex = dpp<0x122>(v) + dpp<0x123>(v) + dpp<0x124>(v);
    const float sum = v + lo + ((lane & 15) == 11 ? 0.f : hi) + ((lane & 15) == 0 ? ex : 0.f);
    return (u32)__builtin_amdgcn_ballot_w64(sum >= 0.f) & 0xFFFFu;
}
DEV u32 get_pol(const Params& p, rsrc_t rs, bool& dead) { return pol_of(boot_wait(p, rs, pol_off(), 0u, dead)); }
DEV u32 hblk(u32 base, int ls, int h, u32 side, u32 pol) { return near_(base + (u32)(ls * NH + h) * 16384u, side, pol); }
DEV f32x2 ln_stats(const f32x2 (&x)[5]) {
    const float c = f_(uu(u_(x[0].x)));
    f32x2 d[5];
#pragma unroll
    for (int i = 0; i < 5; ++i) d[i] = x[i] - c;
    const f32x2 s2 = (d[0] + d[1]) + (d[2] + d[3]) + d[4], q = (d[0] * d[0] + d[1] * d[1]) + (d[2] * d[2] + (d[3] * d[3] + d[4] * d[4]));
    const float dm = wave_sum(s2.x + s2.y) * (1.0f / DM), mean = c + dm;
    const float var = fmaxf(wave_sum(q.x + q.y) * (1.0f / DM) - dm * dm, 0.f);
    const float sd = rbf(__builtin_amdgcn_sqrtf(rbf(rbf(var) + 1e-5f)));
    return (f32x2){rbf(mean), __builtin_amdgcn_rcpf(sd)};
}
DEV void store_h(rsrc_t rs, u32 tile, int row, const u32 (&hp)[5]) {
    const u32 lane = threadIdx.x & 63;
    const u32 sw = (u32)(row & 15) * 16u, o8 = (lane >> 5) * 16384u + (((lane & 31u) * 8u) ^ sw), hb = tile + (u32)(row & 31) * 256u;
    st8s(rs, o8, hb, (v2u){hp[0], hp[1]});
    st8s(rs, o8, hb + 32768, (v2u){hp[2], hp[3]});
    st4s(rs, (lane * 4) ^ sw, hb + 65536, hp[4]);
}

#define DMA16(ldsb, vo, so) asm volatile("s_mov_b32 m0, %0\n\ts_nop 0\n\tbuffer_load_dwordx4 %1, %2, %3 offen sc1 lds" :: "s"(ldsb), "v"(vo), "s"(srd), "s"(so) : "memory")
#define DMA16I(ldsb, lo, vo, so, go) do { u32 so_; asm volatile("s_add_u32 m0, %1, %5\n\ts_add_u32 %0, %4, %6\n\tbuffer_load_dwordx4 %2, %3, %0 offen sc1 lds" \
                         : "=&s"(so_) : "s"(ldsb), "v"(vo), "s"(srd), "s"(so), "i"(lo), "i"(go) : "memory", "scc"); } while (0)
#define DMA32(ldsb, vo, so) asm volatile("s_mov_b32 m0, %0\n\ts_nop 0\n\tbuffer_load_dwordx4 %1, %2, %3 offen sc1 lds\n\tbuffer_load_dwordx4 %1, %2, %3 offen offset:1024 sc1 lds" \
                         :: "s"(ldsb), "v"(vo), "s"(srd), "s"(so) : "memory")
#define INPOL "nt"
#define XPOL ""
#define INAUX 2
#define DMANT(sr, ldsb, vo, so) asm volatile("s_mov_b32 m0, %0\n\ts_nop 0\n\tbuffer_load_dwordx4 %1, %2, %3 offen " INPOL " lds" :: "s"(ldsb), "v"(vo), "s"(sr), "s"(so) : "memory")
#define DMAIN(sr, ldsb, vo, so) asm volatile("s_mov_b32 m0, %0\n\ts_nop 0\n\tbuffer_load_dwordx4 %1, %2, %3 offen lds" :: "s"(ldsb), "v"(vo), "s"(sr), "s"(so) : "memory")
DEV const u16* sel(bool c, const u16* a, const u16* b) { PINS(a); PINS(b); return c ? a : b; }
DEV v4u mk_srd(const void* q, u32 bytes) { return (v4u){uu((u32)(size_t)q), uu((u32)((size_t)q >> 32) & 0xffffu), bytes, 0x00020000u}; }
#define MK_SRD() const v4u srd = mk_srd(p.ws, W_END)

constexpr int CV_PAD = 40;
constexpr int CV_PL = 4416;
constexpr int CV_F = 8 * CV_PL;
constexpr int CV_KB = 80;
constexpr int CV_KV = 65 * CV_KB;
constexpr int CV_CH = CV_F + CV_KV;
static_assert(CV_CH % 16 == 0 && 4 * CV_CH <= LDS_BYTES, "conv lds");

struct Filt { v4u a[5], b[5]; };
DEV int chan(int grp, int w) { return 16 * (grp >> 2) + (grp & 3) + 4 * w; }
template <int N = 5, int AUX = INAUX> DEV void filt_load(const Params& p, int grp, Filt& f) {
    const int tid = threadIdx.x, lane = tid & 63, d = chan(grp, tid >> 6);
    const rsrc_t rf = mk_rsrc(p.ssmk, ACT);
#pragma unroll
    for (int i = 0; i < N; ++i) {
        const u32 s0 = (u32)(8 * (lane + 64 * i) - CV_PAD);
        f.a[i] = __builtin_amdgcn_raw_buffer_load_b128(rf, s0 < SEQ ? (u32)(d * SEQ + s0) * 2u : OOB, 0, AUX);
        f.b[i] = __builtin_amdgcn_raw_buffer_load_b128(rf, s0 + 8 < SEQ ? (u32)(d * SEQ + s0 + 8) * 2u : OOB, 0, AUX);
    }
}
constexpr int KT_BYTES = 18 * 1024, KT_X = 64 * 144, NKV = 44, KT_DUMP = 8 * KT_BYTES + KT_X;
DEV void kv_fetch(const Params& p, int tile, u32 dst) {
    const int lane = threadIdx.x & 63, w = uni(threadIdx.x >> 6);
    const v4u sk = mk_srd(p.bk, ACT), sv = mk_srd(p.bv, ACT), ss = mk_srd(sel(w == 0, p.bk, p.shift), w == 0 ? ACT : DM * 8);
    const u32 vo = tile < 320 ? (u32)((lane >> 3) * DM + (tile % 10) * 64 + (lane & 7) * 8) * 2u : OOB, r0 = uu((u32)((tile / 10) * 64 + 8 * w) * (DM * 2)), d = uu(dst + w * 1024);
    DMANT(sk, d + 1024, vo, r0); DMANT(sk, d + 5120, vo, r0 + 32 * DM * 2); DMANT(sv, d + 9216, vo, r0); DMANT(sv, d + 13312, vo, r0 + 32 * DM * 2);
    DMAIN(ss, uu(w == 0 ? dst : w == 1 ? dst + 17408 : KT_DUMP), w == 0 ? (tile >= 10 ? vo : OOB) : w == 1 && lane < 32 ? (u32)lane * 16u : OOB,
          uu(w == 0 ? (tile >= 10 ? r0 - 8 * DM * 2 : 0u) : (u32)(tile % 10) * 512u));
}
DEV void kv_tile(const Params& p, rsrc_t rs, lptr src, lptr xt, int tile) {
    const int tid = threadIdx.x;
    const v4u sv = lds_r16(src + 17408 + (tid & 31) * 16);
    lptr kp = src + ((tid >> 5) * 8 + 5) * 128 + (tid & 31) * 4;
    f32x2 kf[11];
#pragma unroll
    for (int i = 0; i < 11; ++i) kf[i] = up2(*(LAS u32*)(kp + i * 128));
    const f32x2 sh[4] = {{lo_f(sv.x), lo_f(sv.z)}, {hi_f(sv.x), hi_f(sv.z)}, {lo_f(sv.y), lo_f(sv.w)}, {hi_f(sv.y), hi_f(sv.w)}};
    u32 kv[8];
#pragma unroll
    for (int i = 0; i < 8; ++i) {
        const f32x2 acc = (kf[i] * sh[3] + kf[i + 1] * sh[2]) + (kf[i + 2] * sh[1] + kf[i + 3] * sh[0]);
        kv[i] = pk2(acc * up2(*(LAS u32*)(kp + 9216 + (i - 5) * 128)));
    }
    lds_w16(xt + (2 * (tid & 31)) * 144 + (tid >> 5) * 16, (v4u){__builtin_amdgcn_perm(kv[6], kv[7], 0x05040100u), __builtin_amdgcn_perm(kv[4], kv[5], 0x05040100u),
                                                             __builtin_amdgcn_perm(kv[2], kv[3], 0x05040100u), __builtin_amdgcn_perm(kv[0], kv[1], 0x05040100u)});
    lds_w16(xt + (2 * (tid & 31) + 1) * 144 + (tid >> 5) * 16, (v4u){__builtin_amdgcn_perm(kv[6], kv[7], 0x07060302u), __builtin_amdgcn_perm(kv[4], kv[5], 0x07060302u),
                                                                 __builtin_amdgcn_perm(kv[2], kv[3], 0x07060302u), __builtin_amdgcn_perm(kv[0], kv[1], 0x07060302u)});
    BAR();
#pragma unroll
    for (int i = 0; i < 2; ++i) {
        const int ch = i * 32 + (tid >> 3);
        st16(rs, W_KVT + (u32)(((tile % 10) * 64 + ch) * SEQ + (tile / 10) * 64 + (tid & 7) * 8) * 2u, lds_r16(xt + ch * 144 + (tid & 7) * 16));
    }
}
template <int MODE> DEV void conv_job(const Params& p, rsrc_t rs, lptr lds, int grp, Filt& f, int next) {
    const int tid = threadIdx.x, lane = tid & 63, w = uni(tid >> 6), bq = lane & 31, hh = lane >> 5;
    lptr F = lds + w * CV_CH;
    lptr fa = F + (bq & 7) * CV_PL + ((bq >> 3) - hh + 4) * 16;
    lptr kb = F + CV_F + CV_KB + hh * 16, kh = kb + (bq + 32) * CV_KB;
    bool dead = false;
    v4u kvr[4];
    const u32 D = p.ssmD[chan(grp, w)];
    auto kv_load = [&]() {
        if (MODE == 0) boot_wait(p, rs, lane < NKV ? foff(F_KV + lane) : OOB, 2u, dead);
#pragma unroll
        for (int i = 0; i < (MODE == 1 ? 2 : 4); ++i) kvr[i] = ld16s(rs, (u32)lane * 16u, W_KVT + (u32)(chan(grp, w) * SEQ + i * 512) * 2u);
    };
    auto windows = [&](int i) {
        if (i < 4 || lane < 16) {
            const u32 e[8] = {f.a[i].x, f.a[i].y, f.a[i].z, f.a[i].w, f.b[i].x, f.b[i].y, f.b[i].z, f.b[i].w};
            u32 o[7];
#pragma unroll
            for (int q = 0; q < 7; ++q) o[q] = __builtin_amdgcn_alignbit(e[q + 1], e[q], 16);
#pragma unroll
            for (int q = 0; q < 4; ++q) {
                lds_w16(F + (2 * q) * CV_PL + (64 * i + lane) * 16, (v4u){o[q], o[q + 1], o[q + 2], o[q + 3]});
                lds_w16(F + (2 * q + 1) * CV_PL + (64 * i + lane) * 16, (v4u){e[q + 1], e[q + 2], e[q + 3], e[q + 4]});
            }
        }
    };
    lptr yw = lds + (lane >> 4) * CV_CH + 3072 + (lane & 14) * 64 + w * 32 + (lane & 1) * 16, yr = F + 3072 + lane * 16;
    const u32 yo = W_Y + (u32)((((w & 1) * 8 + (lane >> 3)) * 8 + (w >> 1)) * DM + (grp & 3) * 160 + 4 * (grp >> 2)) * 32u + (u32)(lane & 7) * 16u;
    auto put = [&](const f32x16& ac, int q) {
        const int r = 4 * (q & 3);
        const auto x = __builtin_amdgcn_permlane32_swap(pk(ac[r], ac[r + 1]), pk(ac[r + 4], ac[r + 5]), false, false);
        const auto y = __builtin_amdgcn_permlane32_swap(pk(ac[r + 2], ac[r + 3]), pk(ac[r + 6], ac[r + 7]), false, false);
        lds_w16(yw + (q >> 1) * CV_PL, (v4u){x[0], y[0], x[1], y[1]});
    };
    if (MODE != 2) EV(8);
    if (MODE != 0) kv_load();
    if (lane < CV_KB / 16) lds_w16(F + CV_F + lane * 16, (v4u){0u, 0u, 0u, 0u});
    windows(0);
    if (MODE != 1) { windows(1); windows(2); windows(4); }
    if (MODE == 0) kv_load();
    if (MODE == 2) windows(3);
    if (MODE != 2) EV(9);
#pragma unroll
    for (int i = 0; i < (MODE == 1 ? 2 : 4); ++i) lds_w16(F + CV_F + (16 * i + (lane >> 2) + 1) * CV_KB + (lane & 3) * 16, kvr[i]);
    v4u dg[2];
#pragma unroll
    for (int ks = 0; ks < 2; ++ks)
#pragma unroll
        for (int q = 0; q < 4; ++q) { const int j = 16 * ks + 8 * hh + 7 - bq; dg[ks][q] = j == 2 * q ? D : j == 2 * q + 1 ? D << 16 : 0u; }
    f32x16 acc0 = {}, acc1 = {};
    auto first = [&]() { asm volatile("s_waitcnt vmcnt(0)\n\ts_barrier" ::: "memory"); st4s(rs, tid == 0 ? foff(F_CV + grp) : OOB, 0, 1u); EV(12); };
    if (MODE != 2) {
        EV(10);
        acc0 = mfma32(lds_r16(kb + bq * CV_KB), dg[0], acc0); acc0 = mfma32(lds_r16(kb + bq * CV_KB + 32), dg[1], acc0);
#pragma unroll
        for (int m = 0; m < 8; ++m) {
            lptr kl = kb + max(bq - m, -1) * CV_KB;
            acc0 = mfma32(lds_r16(kl), lds_r16(fa + m * 64), acc0); acc0 = mfma32(lds_r16(kl + 32), lds_r16(fa + m * 64 - 32), acc0);
        }
        put(acc0, 0);
        BAR();
        st16(rs, w < 2 ? yo : OOB, lds_r16(yr));
        EV(11);
    }
    if (MODE == 1) {
        windows(1); windows(2);
        first();
        v4u a[2][2], l[2][2];
#pragma unroll
        for (int u = 0; u < 2; ++u) { lptr kl = kb + max(bq - 8 - u, -1) * CV_KB; a[u][0] = lds_r16(fa + (8 + u) * 64); l[u][0] = lds_r16(kl); a[u][1] = lds_r16(fa + (8 + u) * 64 - 32); l[u][1] = lds_r16(kl + 32); }
#pragma unroll 1
        for (int m = 8; m < 32; m += 2) {
#pragma unroll
            for (int u = 0; u < 2; ++u) {
                lptr kl = kb + max(bq - m - u - 2, -1) * CV_KB;
                acc0 = mfma32(l[u][0], a[u][0], acc0); a[u][0] = lds_r16(fa + (m + u + 2) * 64); l[u][0] = lds_r16(kl); SCHED();
                acc0 = mfma32(l[u][1], a[u][1], acc0); a[u][1] = lds_r16(fa + (m + u + 2) * 64 - 32); l[u][1] = lds_r16(kl + 32); SCHED();
            }
        }
    } else {
        if (MODE == 0) windows(3); else filt_load<5, 0>(p, next, f);
        v4u a0 = lds_r16(fa), a1 = lds_r16(fa - 32), c0 = lds_r16(fa + 2048), c1 = lds_r16(fa + 2048 - 32);
        v4u h0 = lds_r16(kh), h1 = lds_r16(kh + 32), l0 = lds_r16(kb + bq * CV_KB), l1 = lds_r16(kb + bq * CV_KB + 32);
        acc1 = mfma32(h0, dg[0], acc1); acc1 = mfma32(h1, dg[1], acc1);
#pragma unroll 2
        for (int m = 0; m < (MODE == 0 ? 8 : 32); ++m) {
            lptr kl = kb + max(bq - m - 1, -1) * CV_KB;
            if (MODE == 0 && m == 4) first();
            acc1 = mfma32(h0, a0, acc1); h0 = lds_r16(kh - (m + 1) * CV_KB); a0 = lds_r16(fa + (m + 1) * 64); SCHED();
            acc1 = mfma32(l0, c0, acc1); c0 = lds_r16(fa + (m + 1) * 64 + 2048); l0 = lds_r16(kl); SCHED();
            acc1 = mfma32(h1, a1, acc1); h1 = lds_r16(kh - (m + 1) * CV_KB + 32); a1 = lds_r16(fa + (m + 1) * 64 - 32); SCHED();
            acc1 = mfma32(l1, c1, acc1); c1 = lds_r16(fa + (m + 1) * 64 + 2048 - 32); l1 = lds_r16(kl + 32); SCHED();
        }
        if (MODE == 0) {
#pragma unroll 2
            for (int m = 8; m < 32; ++m) {
                lptr kl = kb + max(bq - m - 1, -1) * CV_KB;
                acc1 = mfma32(h0, a0, acc1); h0 = lds_r16(kh - (m + 1) * CV_KB); SCHED();
                acc0 = mfma32(l0, a0, acc0); a0 = lds_r16(fa + (m + 1) * 64); SCHED();
                acc1 = mfma32(l0, c0, acc1); c0 = lds_r16(fa + (m + 1) * 64 + 2048); l0 = lds_r16(kl); SCHED();
                acc1 = mfma32(h1, a1, acc1); h1 = lds_r16(kh - (m + 1) * CV_KB + 32); SCHED();
                acc0 = mfma32(l1, a1, acc0); a1 = lds_r16(fa + (m + 1) * 64 - 32); SCHED();
                acc1 = mfma32(l1, c1, acc1); c1 = lds_r16(fa + (m + 1) * 64 + 2048 - 32); l1 = lds_r16(kl + 32); SCHED();
            }
        }
        BAR();
    }
    if (MODE != 2) { EV(13); put(acc0, 0); put(acc0, 2); }
    if (MODE != 1) { put(acc1, 4); put(acc1, 6); }
    BAR();
#pragma unroll
    for (int q = (MODE == 2 ? 2 : 0); q < (MODE == 1 ? 2 : 4); ++q) st16(rs, q == 0 && w < 2 ? OOB : yo + (u32)q * (2 * DM * 32), lds_r16(yr + q * CV_PL));
    BAR();
    if (MODE != 2) EV(14);
}

#define NPROBE 6
DEV int probe(rsrc_t rs, int c) {
    const int lane = threadIdx.x & 63;
    const u32 k = (u32)(c & 15) < NREG ? (u32)(c & 15) : 0u;
    const u32 off0 = k * (16 * MB) + 2 * MB - 65536 + ((u32)(c & 15) >= NREG ? (u32)(c & 3) + 1u : 0u) * 256 + (u32)(c >> 4) * 128;
    st4s(rs, lane == 0 ? off0 : OOB, 0, 0u); DRAIN();
    int d0 = 1 << 16, d1 = 1 << 16;
#pragma unroll 1
    for (int q = 0; q < NPROBE; ++q) {
        const u32 o = lane == 0 ? off0 + (u32)(q & 1) * 8192 + (u32)(q >> 1) * 18432 : OOB;
        const unsigned long long t0 = __builtin_amdgcn_s_memtime();
        st4s(rs, o, 0, 0u); DRAIN();
        const int dt = (int)(__builtin_amdgcn_s_memtime() - t0);
        if (q & 1) d1 = min(d1, dt); else d0 = min(d0, dt);
    }
    const u32 xcc = (u32)__builtin_amdgcn_s_getreg(20 | (3 << 11)) & 0xfu;
    return (d0 - d1) * ((xcc & 4u) ? -1 : 1);
}
DEV void pre_rings(const Params& p, rsrc_t rs, int j) {
    const u32 gt = (u32)(j * 256 + threadIdx.x) * 16u, n = ((gt >> 14) << 9) + ((gt & 8191u) >> 4);
    const v4u ps = {PSENT, PSENT, PSENT, PSENT};
    v4u m = ps;
    if (n < 640u) m = *(const v4u*)(p.mem0 + (2 * ((n >> 2) & 3) + (n & 1)) * DM + (n >> 4) * 16 + (n & 2) * 4);
#pragma unroll
    for (u32 i = 0; i < 8; ++i) st16(rs, W_ORING + gt + i * 131072u, ps);
#pragma unroll
    for (u32 i = 0; i < 2; ++i) st16(rs, gt < 16384u ? W_MRING + i * (MB / 4) + 131072u + gt : OOB, ps);
    if (n < 640u) {
        m = (v4u){__builtin_amdgcn_perm(m.z, m.x, 0x05040100u), __builtin_amdgcn_perm(m.z, m.x, 0x07060302u), __builtin_amdgcn_perm(m.w, m.y, 0x05040100u), __builtin_amdgcn_perm(m.w, m.y, 0x07060302u)};
#pragma unroll
        for (int i = 0; i < 4; ++i) m[i] = m[i] == PSENT ? 0x7FC17FC1u : m[i];
    }
    st16(rs, W_MRING + gt, m);
    st16(rs, W_MRING + MB / 4 + gt, m);
    static_assert(2 * (F_N - F_QKV) == 32 * 36, "clear");
    st4s(rs, threadIdx.x < 36 ? foff(F_QKV) + (u32)(j * 36 + threadIdx.x) * 128u : OOB, 0, 0u);
}

#define M32A(first, d, a, b) do { if (first) asm volatile("v_mfma_f32_32x32x16_bf16 %0, %1, %2, 0" : "=&v"(d) : "a"(a), "v"(b)); \
                                  else asm volatile("v_mfma_f32_32x32x16_bf16 %0, %1, %2, %0" : "+v"(d) : "a"(a), "v"(b)); } while (0)
#define M32B(first, d, a, b) do { if (first) asm volatile("v_mfma_f32_32x32x16_bf16 %0, %1, %2, 0" : "=&v"(d) : "v"(a), "a"(b)); \
                                  else asm volatile("v_mfma_f32_32x32x16_bf16 %0, %1, %2, %0" : "+v"(d) : "v"(a), "a"(b)); } while (0)
#define VCVT(d, a, b) asm volatile("v_cvt_pk_bf16_f32 %0, %1, %2" : "=v"(d) : "v"(a), "v"(b))
#define LD4A(d, vo, so) asm volatile("buffer_load_dword %0, %1, %2, %3 offen sc1" : "=v"(d) : "v"(vo), "s"(srd), "s"(so) : "memory")
DEV u32 wst_src(int lane, int ld) { return (u32)((lane >> 4) * ld + 8 * ((lane & 15) ^ ((lane >> 4) << 2))) * 2u; }
DEV u32 wst_rd(int lane, int w) { const int q4 = (lane >> 2) & 3; return (u32)(q4 * 256 + ((w ^ q4) * 64) + ((lane >> 4) & 1) * 32 + (lane & 3) * 8); }

constexpr int QK_A = 65536;
#define QLEAD 4
#define QFIRST 3
#define QW0 10
#define QW1 3
#define QSAMP 72
DEV void role_qkv(const Params& p, lptr lds, int c) {
    const int grp = 64 + c;
    const int tid = threadIdx.x, lane = tid & 63, w = uni(tid >> 6), hh = lane >> 5, r32 = lane & 31;
    const int m = c >> 5, h = c & 31;
    const rsrc_t rs = mk_rsrc(p.ws, W_END);
    v4u wt[40];
    Filt flt;
    bool dead = false;
    u32 pol, h2flag, fo, ol, ar[8];
    const u32 ds = uu(w * 1024), dx = ds + QK_A;
#define XRD(ks) lds_r16(lds + ar[(ks) & 7] + ((ks) >> 3) * 8192)
    const u32 t16 = (u32)tid * 16u, lnf = tid < 96 ? foff(F_LN + tid) : OOB;
    MK_SRD();
#define QDMA(i, dst, vo, so) DMA16I(dst, (i) * 4096, vo, so, ((i) >> 1) * 16384 + ((i) & 1) * 4096)
    {
        const v4u sw = mk_srd(sel(m == 0, p.Wq, sel(m == 1, p.Wk, p.Wv)), DM * NQ * 2), sx = mk_srd(p.x, ACT);
        const u32 vo = wst_src(lane, NQ) + (u32)h * (DH * 2), x8 = (u32)lane * 8u, x4 = (u32)lane * 4u;
        v2u xa[6], xb[6]; u32 xc[6];
#pragma unroll
        for (int i = 0; i < 6; ++i)
            asm volatile("buffer_load_dwordx2 %0, %3, %5, %6 offen " XPOL "\n\tbuffer_load_dwordx2 %1, %3, %5, %6 offen offset:512 " XPOL "\n\tbuffer_load_dword %2, %4, %5, %6 offen offset:1024 " XPOL
                         : "=&v"(xa[i]), "=&v"(xb[i]), "=&v"(xc[i]) : "v"(x8), "v"(x4), "s"(sx), "s"(uu((4 * c + w + 384 * i) * (DM * 2))) : "memory");
        EV(1);
        auto wfill = [&](int i0, int n) {
#pragma unroll 1
            for (int i = i0; i < i0 + n; ++i) DMAIN(sw, uu((4 * i + w) * 1024), vo, uu((16 * i + 4 * w) * (NQ * 2)));
        };
        wfill(0, QW0);
        asm volatile("s_waitcnt vmcnt(%0) ; HZCLR" :: "i"(QW0) : "memory");
        EV(2);
#pragma unroll
        for (int i = 0; i < 6; ++i) {
            wfill(QW0 + QW1 * i, QW1);
            if (i == 2) { VMW(2 * QW1 + 3); BAR(); boot_set(p, rs, tid == 0 ? foff(F_LN + c) : OOB, 1u); }
            asm volatile("" : "+v"(xa[i]), "+v"(xb[i]), "+v"(xc[i]));
            const int row = 4 * c + w + 384 * i;
            if (i < 5 || row < SEQ) {
                const f32x2 x[5] = {up2(xa[i].x), up2(xa[i].y), up2(xb[i].x), up2(xb[i].y), up2(xc[i])};
                const f32x2 st = ln_stats(x);
                u32 hp[5];
#pragma unroll
                for (int q = 0; q < 5; ++q) hp[q] = pk2(up2(pk2(x[q] - st.x)) * st.y);
                store_h(rs, W_H1 + (u32)(row >> 5) * H_TILE, row, hp);
            }
        }
        EV(3);
        constexpr int K0 = QW0 + 6 * QW1;
        static_assert(K0 * 4096 >= QK_A + 40960, "the first tile's place is free");
        v4u fv, praw;
        asm volatile("buffer_load_dwordx4 %0, %2, %4, 0 offen sc1\n\tbuffer_load_dwordx4 %1, %3, %4, 0 offen sc1" : "=&v"(praw), "=&v"(fv) : "v"(pol_off()), "v"(lnf), "s"(srd) : "memory");
        wfill(K0, 40 - K0);
        asm volatile("s_waitcnt vmcnt(%2) ; HZCLR\n\ts_barrier" : "+v"(praw), "+v"(fv) : "i"(40 - K0) : "memory");
        boot_set(p, rs, tid == 0 ? foff(F_LN + c) : OOB, 2u);
        EV(4);
        lptr tb = lds + hh * 2048 + wst_rd(lane, w);
        SFOR(k, 40)
            if constexpr (k == K0) {
                if (!boot_ok(p, lnf, fv, 1u)) boot_wait(p, rs, lnf, 1u, dead);
                if (!boot_ok(p, pol_off(), praw, 0u)) praw.w = boot_wait(p, rs, pol_off(), 0u, dead);
                BAR();
                EV(5);
                const u32 so = W_H1;
                SFOR(i, 10) QDMA(i, dx, t16, so); SEND
                pol = pol_of(praw.w);
                h2flag = tid < 8 ? near_(foff(F_H2 + lane), 1u, pol) : OOB;
                ol = (m == 2 ? (u32)(2 * w + (r32 >> 4)) * 1024u : (u32)((r32 >> 4) * 4 + w) * 1024u) + (u32)(hh * 16 + (r32 & 15)) * 16u;
                fo = near_(foff(F_QKV + m * 64 + h), 0u, pol);
#pragma unroll
                for (int j = 0; j < 8; ++j) { ar[j] = (u32)(QK_A + r32 * 256 + ((2 * j + hh) ^ (r32 & 15)) * 16); PIN(ar[j]); }
                PIN(h2flag); PIN(ol); PINS(fo); PINS(pol);
                VMW(10); BAR();
            }
            lptr q = tb + (k >> 3) * 32768 + (k & 7) * 4096; wt[k] = tr2(q, q + 1024); asm volatile("" : "+a"(wt[k]));
        SEND
    }
    EV(6);
    int n0 = -1, n1 = -QLEAD;
    f32x16 a0 = {};
    const u32 bfo = tid < 96 ? lnf : tid - 128u < NKV ? foff(F_KV - 128 + tid) : OOB;
    v4u brk = {};
    auto run = [&](auto isv) __attribute__((always_inline)) {
      constexpr bool V = decltype(isv)::value;
      v4u x[8];
      u32 fv = 0, cvo = n1 > -QLEAD && tid == 194 ? foff(F_CV + grp) : OOB;
#pragma unroll
      for (int ks = 20; ks < 25; ++ks) x[ks & 7] = XRD(ks);
#define QS(k) do {} while (0)
#define QPK1(d, a, a_, q_) do { if constexpr (V) VCVT(d[q_], a[4 * (a_) + 8 * ((q_) >> 1) + 2 * ((q_) & 1)], a[4 * (a_) + 8 * ((q_) >> 1) + 2 * ((q_) & 1) + 1]); else VCVT(d[q_], a[4 * (a_) + (q_)], a[4 * (a_) + (q_) + 8]); } while (0)
#define QPK(d, a, a_) do { QPK1(d, a, a_, 0); QPK1(d, a, a_, 1); QPK1(d, a, a_, 2); QPK1(d, a, a_, 3); } while (0)
#pragma unroll 1
      for (const int end = n1 == -QLEAD ? QFIRST - QLEAD : NSEG; n1 < end; ++n1) {
        const int cur = n1 < 0 ? n0 + 1 : 64 | n1, nxt = n1 < 0 ? n0 + 2 : n0 + 1;
        u32 xs = n1 < 0 ? W_H1 + (u32)cur * H_TILE : near_(W_H2 + (u32)n1 * H_TILE, 1u, pol);
        PINS(xs);
        QS(0);
        if (w == 0 && n1 >= 0 && ((u32)__builtin_amdgcn_ballot_w64(fv <= (u32)n1) & 255u) != 0u) wait_flags(rs, h2flag, (u32)n1 + 1, dead);
        BAR();
        QS(1);
        f32x16 a1;
        u32 ss, sv, hb, oa, qfo, qfv;
        v4u pp, pq;
        SCHED();
        auto slot = [&](auto zc) __attribute__((always_inline)) {
            constexpr int z = decltype(zc)::value, ks = (z + 20) % 40;
            if constexpr (z < 20) { if constexpr (V) M32B(false, a0, x[ks & 7], wt[ks]); else M32A(false, a0, wt[ks], x[ks & 7]); }
            else if constexpr (z < 60) { if constexpr (V) M32B(z == 20, a1, x[ks & 7], wt[ks]); else M32A(z == 20, a1, wt[ks], x[ks & 7]); }
            else if constexpr (z == 60) {
                if constexpr (V) asm volatile("v_mfma_f32_32x32x16_bf16 %0, %1, %2, 0" : "+v"(a0) : "v"(x[ks & 7]), "a"(wt[ks]));
                else asm volatile("v_mfma_f32_32x32x16_bf16 %0, %1, %2, 0" : "+v"(a0) : "a"(wt[ks]), "v"(x[ks & 7]));
            } else { if constexpr (V) M32B(false, a0, x[ks & 7], wt[ks]); else M32A(false, a0, wt[ks], x[ks & 7]); }
            if constexpr (z == 18) { QS(2); asm volatile("s_waitcnt vmcnt(6) lgkmcnt(0)\n\ts_barrier" ::: "memory"); QS(3); }
            if constexpr (z < 15 || z > 20) x[(ks + 5) & 7] = XRD((ks + 5) % 40);
            if constexpr (z >= 18 && z <= 20) { x[2 * (z - 18)] = XRD(2 * (z - 18)); x[2 * (z - 18) + 1] = XRD(2 * (z - 18) + 1); }
            SCHED();
            if constexpr (z < 4) QDMA(z, dx, t16, xs);
            if constexpr (z >= 5 && z < 17 && (z & 1)) QDMA((z + 3) >> 1, dx, t16, xs);
            if constexpr ((z >= 7 && z < 15) || (z >= 47 && z < 55)) asm volatile("v_xor_b32 %0, 0x10000, %0" : "+v"(ar[(ks + 5) & 7]));
            if constexpr (z == 16) { ss = W_H1 + (u32)nxt * H_TILE; PINS(ss); }
            if constexpr (z == 17) { sv = t16 | (nxt >= NSEG ? OOB : 0u); PIN(sv); }
            if constexpr (z >= 21 && z < 41 && (z & 1)) QDMA((z - 21) >> 1, ds, sv, ss);
            if constexpr (z == 24) QPK(pp, a0, 0);
            if constexpr (z == 26) QPK(pq, a0, 1);
            if constexpr (z == 30) { QS(4); asm volatile("s_waitcnt vmcnt(5)\n\ts_barrier" ::: "memory"); QS(5); }
            if constexpr (z == 32 || z == 44) { hb = (m == 2 ? W_VO : W_KQ) + (u32)((z == 32 ? n0 : cur) * NH + h) * 16384u; PINS(hb); }
            if constexpr (z == 34 || z == 45) { hb = near_(hb, m == 1 ? 0u : 1u, pol); PINS(hb); }
            if constexpr (z == 36) { hb = (u32)n0 < NSEG ? hb : OOB; PINS(hb); }
            if constexpr (z == 38 || z == 46) { oa = ol + hb; PIN(oa); }
            if constexpr (z == 41) st16(rs, oa, pp);
            if constexpr (z == 43) st16(rs, oa + 512, pq);
            if constexpr (z == 54) { QS(6); asm volatile("s_waitcnt vmcnt(4)\n\ts_barrier" ::: "memory"); QS(7); }
            if constexpr (z == 56) { if (__builtin_expect(n1 == QFIRST - QLEAD - 1, 0)) { filt_load<3, 0>(p, grp, flt); brk = ld16s(rs, bfo, 0); } }
            if constexpr (z == 60) NOPS(5);
            if constexpr (z == 61) { QPK(pp, a1, 0); st16(rs, oa, pp); }
            if constexpr (z == 62) { QPK(pq, a1, 1); st16(rs, oa + 512, pq); }
            if constexpr (z == 64) { qfo = tid == 192 ? fo : tid == 193 && n1 >= 0 ? fo + 32 * 128 : cvo; PIN(qfo); }
            if constexpr (z == 66) { qfv = tid == 192 ? (u32)min(nxt, NSEG) : tid == 193 ? (u32)n1 + 1u : 2u; PIN(qfv); }
            if constexpr (z == QSAMP) LD4A(fv, h2flag, 0);
            SCHED();
        };
        if (__builtin_expect(n0 >= 0, 1)) static_for<0, 16>(slot);
        static_for<16, 80>(slot);
        QS(8);
        asm volatile("s_waitcnt vmcnt(1)\n\ts_barrier" ::: "memory");
        st4s(rs, qfo, 0, qfv);
        QS(9);
        asm volatile("s_waitcnt vmcnt(1)" : "+v"(fv) :: "memory");
        n0 = nxt; cvo = OOB;
      }
      NOPS(7);
    };
#pragma unroll 1
    for (;;) {
        if (m == 2) run(IC<1>{}); else run(IC<0>{});
        if (n1 > 0) break;
        if (!boot_ok(p, bfo, brk, 2u)) boot_wait(p, rs, bfo, 2u, dead);
        BAR();
        conv_job<1>(p, rs, lds, grp, flt, 0);
        const u32 so = uu(W_H1 + (u32)n0 * H_TILE);
        SFOR(i, 6) QDMA(i + 4, ds, t16, so); SEND
        DRAIN(); BAR();
    }
}

constexpr int OU_SEL = 131072;

DEV void role_out(const Params& p, lptr lds, int L, int kq, int ng) {
    const int tid = threadIdx.x, lane = tid & 63, w = uni(tid >> 6), hh = lane >> 5, r32 = lane & 31;
    const rsrc_t rs = mk_rsrc(p.ws, W_END);
    v4u wo[64];
    Filt flt;
    bool dead = false;
    {
        const v4u sw = mk_srd(p.Wo, DM * NQ * 2);
        const u32 vo = wst_src(lane, DM) + (ng == 4 ? 1024u : (u32)((ng >> 1) * 512 + (ng & 1) * 128 + ((lane ^ (lane >> 2)) & 8) * 16));
        auto fill = [&](int b) {
#pragma unroll 1
            for (int i = 0; i < 8; ++i) DMAIN(sw, uu((b % 5) * 32768 + (4 * i + w) * 1024), vo, uu((kq * 1024 + b * 128 + 16 * i + 4 * w) * (DM * 2)));
        };
#pragma unroll 1
        for (int b = 0; b < 5; ++b) fill(b);
        lptr tb = lds + hh * 1024 + wst_rd(lane, w);
        SFOR(b, 8)
            VMW(b < 3 ? 32 : 66 - 8 * b); BAR();
            lptr q = tb + (b % 5) * 32768;
#pragma unroll
            for (int k = 0; k < 8; ++k) { lptr a = q + (k >> 1) * 8192 + (k & 1) * 2048; wo[8 * b + k] = tr2(a, a + 4096); if (b < 7) asm volatile("" : "+a"(wo[8 * b + k])); else PIN(wo[8 * b + k]); }
            if constexpr (b < 3) { BAR(); fill(b + 5); }
            if constexpr (b == 2) filt_load(p, L == 0 ? 44 + kq * 5 + ng : 1 << 12, flt);
        SEND
    }
    EV(6);
    const u32 pol = get_pol(p, rs, dead);
    if (L == 0) {
        BAR();
        conv_job<0>(p, rs, lds, 44 + kq * 5 + ng, flt, 0);
        DRAIN(); BAR();
        st4s(rs, tid == 0 ? foff(F_CV + 44 + kq * 5 + ng) : OOB, 0, 2u);
    }
    EV(15);
    MK_SRD();
    const u32 ofl = (lane < 8 && w == 0) ? near_(foff(F_O + L * 32 + kq * 8 + lane), 0u, pol) : OOB;
    const u32 l16 = (u32)lane * 16u;
    const u32 fo = (tid == 192) ? near_(foff(F_P + L * 32 + kq * 5 + ng), 1u, pol) : OOB;
    auto osrc = [&](int s) { return uu(hblk(W_VO, L * NSEG + s, kq * 8, 0u, pol) + w * 1024); };
#define ODMA(i, dst, vo, so) DMA16I(dst, (i) * 4096, vo, so, ((i) >> 1) * 16384 + ((i) & 1) * 4096)
    const u32 pvo = (u32)(kq * 5 + ng) * 16384u + (u32)w * 2048u + l16;
    lptr tw = lds + OU_SEL + 256 + r32 * 272 + (ng == 4 ? hh * 32 + w * 4 : (w & 1) * 128 + hh * 16 + (w >> 1) * 8), tr = lds + OU_SEL + 256 + (8 * w + (lane >> 4)) * 272 + (lane & 15) * 16;
    lptr selw = lds + OU_SEL + (w == 0 ? 0 : 64);
    u32 pfo = OOB;
    u32 buf = 0;
    if (w == 0) wait_flags(rs, ofl, 1u, dead);
    BAR();
    for (int s = 0; s < NSEG; ++s) {
        u32 src = osrc(s), dst = uu(buf + w * 1024);
        lptr A = lds + buf + l16;
        asm volatile("" : "+s"(src), "+s"(dst));
#define OS(k) do {} while (0)
        OS(0);
        OS(1);
        SFOR(i, 10) ODMA(i, dst, l16, src); SEND
        OS(2);
        asm volatile("s_waitcnt vmcnt(7)\n\ts_barrier" ::: "memory");
        OS(3);
        LS2(L == 0 && w == 1, 96 + kq * 5 + ng);
        v4u o[64];
#pragma unroll
        for (int k = 0; k < 3; ++k) o[k] = lds_r16(A + k * 1024);
        st4s(rs, pfo, 0, (u32)s);
        u32 fv, pa; int rdy;
        f32x16 acc;
        SCHED();
        SFOR(k, 64)
            if constexpr (k < 56) M32A(k == 0, acc, wo[k], o[k]); else asm volatile("v_mfma_f32_32x32x16_bf16 %0, %1, %2, %0" : "+v"(acc) : "v"(wo[k]), "v"(o[k]));
            if constexpr (k == 61) rdy = *(LAS int*)(lds + OU_SEL);
            if constexpr (k < 61) o[k + 3] = lds_r16(A + (k + 3) * 1024);
            SCHED();
            if constexpr (k < 12 && (k & 1) == 0) ODMA(10 + (k >> 1), dst, l16, src);
            if constexpr (k == 7) OS(4);
            if constexpr (k == 8) OS(5);
            if constexpr (k == 23) OS(6);
            if constexpr (k == 24) OS(7);
            if constexpr (k == 43) OS(8);
            if constexpr (k == 44) OS(9);
            if constexpr (k == 59) OS(10);
            if constexpr (k == 63) OS(11);
            if constexpr (k == 7) asm volatile("s_waitcnt vmcnt(8)\n\ts_barrier" ::: "memory");
            if constexpr (k == 23) asm volatile("s_waitcnt vmcnt(5)\n\ts_barrier" ::: "memory");
            if constexpr (k == 30) { pa = pvo + near_(p_item(s), (u32)L, pol); PIN(pa); }
            if constexpr (k == 22) LD4A(fv, ofl, 0);
            if constexpr (k == 42) {
                asm volatile("s_waitcnt vmcnt(0)" : "+v"(fv) :: "memory");
                *(LAS int*)selw = (int)((u32)__builtin_amdgcn_ballot_w64(fv < (u32)s + 2) & 255u);
            }
            if constexpr (k == 43) asm volatile("s_waitcnt lgkmcnt(1)\n\ts_barrier" ::: "memory");
            SCHED();
        SEND
        const bool ready = uni(rdy) == 0;
        asm volatile("s_nop 11" : "+v"(acc));
        u32 pc[8];
#pragma unroll
        for (int i = 0; i < 8; ++i) pc[i] = pk(acc[2 * i], acc[2 * i + 1]);
        if (ng != 4) {
#pragma unroll
            for (int i = 0; i < 4; ++i) *(LAS v2u*)(tw + i * 32) = (v2u){pc[2 * i], pc[2 * i + 1]};
        } else {
#pragma unroll
            for (int i = 0; i < 8; ++i) *(LAS u32*)(tw + (i >> 1) * 64 + (i & 1) * 16) = pc[i];
        }
        BAR();
        { const v4u p0 = lds_r16(tr), p1 = lds_r16(tr + 4 * 272); st16(rs, pa, p0); st16(rs, pa + 1024, p1); }
        SCHED();
        OS(12);
        if (ready) pfo = fo;
        else {
            LD4A(fv, ofl, 0);
            asm volatile("s_waitcnt vmcnt(1)\n\ts_barrier" ::: "memory");
            st4s(rs, fo, 0, (u32)s + 1); pfo = OOB;
            OS(14);
            LS2(L == 0 && w == 1, 160 + kq * 5 + ng);
            asm volatile("s_waitcnt vmcnt(1)" : "+v"(fv) :: "memory");
            if (w == 0 && s + 1 < NSEG && ((u32)__builtin_amdgcn_ballot_w64(fv < (u32)s + 2) & 255u) != 0u) wait_flags(rs, ofl, (u32)s + 2, dead);
            BAR();
        }
        OS(13);
        buf ^= 65536u;
    }
}

template <int L> DEV void role_row(const Params& p, lptr lds, int ri) {
    const int lane = threadIdx.x & 63, w = uni(threadIdx.x >> 6);
    const rsrc_t rs = mk_rsrc(p.ws, W_END), rx = mk_rsrc(p.x, ACT);
    bool dead = false;
    const u32 pol = get_pol(p, rs, dead);
    const u32 fp = threadIdx.x < 20 ? near_(foff(F_P + L * 32 + lane), 1u, pol) : OOB, fo = threadIdx.x == 192 && L == 0 ? near_(foff(F_H2 + (ri >> 2)), 1u, pol) : OOB;
    u32 fv = 0;
    MK_SRD();
    auto yfill = [&](int b, int i) { DMA32(uu((b & 1) * 40960 + w * 10240 + i * 2048), (u32)lane * 16u, uu(W_Y + (u32)((ri >> 1) * 8 + b) * 20480u + (w & 1) * 10240 + i * 2048)); };
    auto ywait = [&](u32 v) {
        wait_flags(rs, threadIdx.x < (v == 3u ? 8u : 160u) ? foff(F_CV + (v == 3u ? 160u : 0u) + threadIdx.x) : OOB, v, dead);
    };
    v2u xa = {}, xb = {}; u32 xc = 0;
    auto xload = [&](int s) { const u32 ro = (u32)(s * 32 + ri) * (DM * 2); xa = ldin8(rx, (u32)lane * 8u, ro); xb = ldin8(rx, (u32)lane * 8u + 512u, ro); xc = __builtin_amdgcn_raw_buffer_load_b32(rx, (u32)lane * 4u + 1024u, ro, 0); };
    if (L == 0) xload(0);
    ywait(1u);
    BAR();
    EV(15);
#pragma unroll 1
    for (int i = 0; i < 5; ++i) yfill(0, i);
    DRAIN(); BAR();
    for (int s = 0; s < NSEG; ++s) {
        u32 xi[5] = {xa.x, xa.y, xb.x, xb.y, xc};
        u32 pa = near_(p_item(s), (u32)L, pol) + (u32)ri * 256u, pc = pa + 65536u + (u32)(lane >> 4) * 81920u + (u32)(lane & 15) * 16u;
        pa += (u32)(lane >> 5) * 81920u + (u32)((lane >> 4) & 1) * 16384u + (u32)(lane & 15) * 16u;
        u32 yf = s == 1 ? foff(F_CV + min(threadIdx.x, 159u)) : s == 25 ? foff(F_CV + 160 + (lane & 7)) : OOB;
        asm volatile("" : "+v"(xi[0]), "+v"(xi[1]), "+v"(xi[2]), "+v"(xi[3]), "+v"(xi[4]), "+v"(pa), "+v"(pc), "+v"(yf));
#define RS(k) do {} while (0)
        RS(0);
        if (w == 0 && ((u32)__builtin_amdgcn_ballot_w64(fv <= (u32)s) & 0xFFFFFu) != 0u) wait_flags(rs, fp, (u32)s + 1, dead);
        BAR();
        RS(1);
        v4u d[5];
#pragma unroll
        for (int i = 0; i < 4; ++i) d[i] = ld16s(rs, pa, (i >> 1) * 32768 + (i & 1) * 163840);
        d[4] = ld16s(rs, pc, 0);
        const u32 yv = ld4(rs, yf);
        if (L == 0) xload(s + 1);
        else {
            const u32 ro = W_X2 + (u32)(s * 32 + ri) * (DM * 2);
            const v2u a = ld8s(rs, (u32)lane * 8u, ro), b = ld8s(rs, (u32)lane * 8u + 512u, ro);
            xi[0] = a.x; xi[1] = a.y; xi[2] = b.x; xi[3] = b.y; xi[4] = ld4(rs, (u32)lane * 4u + 1024u + ro);
        }
        RS(2);
        LS2(L == 0, 192 + ri);
        SCHED();
        f32x2 y[5];
        {
            lptr ya = lds + ((s >> 3) & 1) * 40960 + (w >> 1) * 20480 + (w & 1) * 16 + (s & 7) * 2 + lane * 32;
            auto yr = [&](lptr q) { return lo_f(*(LAS u16*)q); };
            y[0] = (f32x2){yr(ya), yr(ya + 5120)}; y[1] = (f32x2){yr(ya + 10240), yr(ya + 15360)};
            y[2] = (f32x2){yr(ya + 2048), yr(ya + 7168)}; y[3] = (f32x2){yr(ya + 12288), yr(ya + 17408)};
            lptr yc = ya - lane * 32 + 4096 + (lane >> 1) * 32 + (lane & 1) * 10240;
            y[4] = (f32x2){yr(yc), yr(yc + 5120)};
            asm volatile("" : "+v"(y[0]), "+v"(y[1]), "+v"(y[2]), "+v"(y[3]), "+v"(y[4]));
        }
        f32x2 o[5];
        {
            f32x2 t[5][2];
#pragma unroll
            for (int i = 0; i < 5; ++i) {
                const auto e = __builtin_amdgcn_permlane32_swap(d[i].x, d[i].z, false, false), f = __builtin_amdgcn_permlane32_swap(d[i].y, d[i].w, false, false);
                t[i][0] = up2(e[0]) + up2(e[1]); t[i][1] = up2(f[0]) + up2(f[1]);
            }
            o[0] = t[0][0] + t[1][0]; o[1] = t[0][1] + t[1][1]; o[2] = t[2][0] + t[3][0]; o[3] = t[2][1] + t[3][1];
            const auto e = __builtin_amdgcn_permlane16_swap(u_(t[4][0].x), u_(t[4][1].x), false, false), f = __builtin_amdgcn_permlane16_swap(u_(t[4][0].y), u_(t[4][1].y), false, false);
            o[4] = (f32x2){f_(e[0]) + f_(e[1]), f_(f[0]) + f_(f[1])};
        }
        f32x2 x1[5];
#pragma unroll
        for (int i = 0; i < 5; ++i) x1[i] = up2(pk2(up2(xi[i]) + up2(pk2(o[i]))));
        asm volatile("" : "+v"(x1[0]), "+v"(x1[1]), "+v"(x1[2]), "+v"(x1[3]), "+v"(x1[4]));
        RS(3);
        if (__builtin_expect(s == 1 || s == 25, 0) && !all_(yv >= (s == 1 ? 2u : 3u))) ywait(s == 1 ? 2u : 3u);
        if ((s & 7) - 2u < 5u && s < NSEG - 8) yfill((s >> 3) + 1, (s & 7) - 2);
        SCHED();
        const f32x2 st1 = ln_stats(x1);
        u32 xp[5];
#pragma unroll
        for (int i = 0; i < 5; ++i) xp[i] = pk2(x1[i] + ((x1[i] - st1.x) * st1.y) * y[i]);
        const int row = s * 32 + ri;
        RS(4);
        if (L == 0) {
            const u32 ro = (u32)row * (DM * 2);
            f32x2 x2[5];
#pragma unroll
            for (int i = 0; i < 5; ++i) x2[i] = up2(xp[i]);
            st8s(rs, (u32)lane * 8, W_X2 + ro, (v2u){xp[0], xp[1]});
            st8s(rs, (u32)lane * 8 + 512, W_X2 + ro, (v2u){xp[2], xp[3]});
            st4s(rs, (u32)lane * 4 + 1024, W_X2 + ro, xp[4]);
            SCHED();
            const f32x2 st2 = ln_stats(x2);
            u32 hp[5];
#pragma unroll
            for (int i = 0; i < 5; ++i) hp[i] = pk2((x2[i] - st2.x) * st2.y);
            store_h(rs, near_(W_H2 + (u32)s * H_TILE, 1u, pol), row, hp);
        } else {
            u16* o_ = p.out + (size_t)row * DM;
            *(v2u*)(o_ + 4 * lane) = (v2u){xp[0], xp[1]};
            *(v2u*)(o_ + 256 + 4 * lane) = (v2u){xp[2], xp[3]};
            *(u32*)(o_ + 512 + 2 * lane) = xp[4];
        }
        RS(5);
        LD4A(fv, fp, 0);
        if (L == 0) asm volatile("s_waitcnt vmcnt(1)\n\ts_barrier" ::: "memory");
        st4s(rs, fo, 0, (u32)s + 1);
        RS(6);
        asm volatile("s_waitcnt vmcnt(1)" : "+v"(fv) :: "memory");
        RS(7);
        if (ri == 0) LST(320 + L * 64 + s, rt32());
        LS2(L == 0, 224 + ri);
    }
}

constexpr float SCALE = 0.08838834764831845f, LOG2E = 1.4426950408889634f;
constexpr int C_K = 0, C_V = 8192, C_QX = 16384, C_MEM = 24576, C_Q = C_MEM + 10240, C_KM = C_Q + 2304, C_VM = C_KM + 2304, C_DUMP = C_VM + 2048,
              C_ZERO = C_DUMP + 1024, C_WL = C_ZERO + 128;
constexpr int NLF = 14;
static_assert(C_WL % 16 == 0 && C_WL + 4 * NLF * 2048 <= LDS_BYTES, "head lds");
#define CH_VIS 10
#define CH_H0 116
#define CH_W0 82
#define CH_MH 2
#define CH_MW 6
#define CH_REC 8
#define CH_LATE 30
#define CH_SOLO 6
#define CH_KV 12
#define CH_KV0 60
#define CH_M0 6
#define CH_DEC 2u
#define CH_UP 4
#define CH_WX 6
DEV void wait_until(u32 t) {
    unsigned long long c;
    asm volatile("1:\n\ts_memrealtime %0\n\ts_waitcnt lgkmcnt(0)\n\ts_mov_b64 vcc, %0\n\ts_sub_u32 vcc_lo, %1, vcc_lo\n\ts_cmpk_lt_u32 vcc_lo, 400\n\ts_cbranch_scc1 1b" : "=&s"(c) : "s"(uu(t - 1u)) : "vcc", "scc", "memory");
}
#define WAIT_TXT(tmp, t1) "s_memrealtime vcc\n1:\n\ts_waitcnt lgkmcnt(0)\n\ts_sub_u32 " tmp ", " t1 ", vcc_lo\n\ts_memrealtime vcc\n\ts_cmpk_lt_u32 " tmp ", 400\n\ts_cbranch_scc1 1b\n\t"
#define SMF_A(acc, a, b) asm volatile("v_smfmac_f32_16x16x64_bf16 %0, %1, %2, %3" : "+v"(acc) : "v"(a), "a"(b), "v"(idx))
#define SMF_V(acc, a, b) asm volatile("v_smfmac_f32_16x16x64_bf16 %0, %1, %2, %3" : "+v"(acc) : "v"(a), "v"(b), "v"(idx))
#define MF0(d, a, b) asm volatile("v_mfma_f32_16x16x32_bf16 %0, %1, %2, 0" : "=&v"(d) : "v"(a), "v"(b))
#define MF1(d, a, b) asm volatile("v_mfma_f32_16x16x32_bf16 %0, %1, %2, %0" : "+v"(d) : "v"(a), "v"(b))
#define MF0H(d, a, b) asm volatile("v_mfma_f32_16x16x16_bf16 %0, %1, %2, 0" : "=&v"(d) : "v"(a), "v"(b))
#define MF1H(d, a, b) asm volatile("v_mfma_f32_16x16x16_bf16 %0, %1, %2, %0" : "+v"(d) : "v"(a), "v"(b))
DEV v8u tr_frag(lptr p0, lptr p1, lptr p2, lptr p3) { return cat8(tr2(p0, p1), tr2(p2, p3)); }
constexpr int qslot(int ks) { return (ks & 1) && ks < 8 ? ks >> 1 : -1; }
constexpr int kslot(int ks) { return ks < 4 ? 4 + ks : -1; }
constexpr int vslot(int ks) { return ks >= 4 ? 4 + ks : -1; }
DEV u32 ring_map(u32 off, u32 half) { return ((off >> 13) << 14) + (half << 13) + (off & 8191u); }
#define ZRD(x, i) x = __builtin_bit_cast(f32x4, lds_r16(ZP + 16 * (i)))
#define VMAX(d, a, b) asm volatile("v_max_f32 %0, %1, %2" : "=v"(d) : "v"(a), "v"(b))
#define VMAX3(d, a, b, c_) asm volatile("v_max3_f32 %0, %1, %2, %3" : "=v"(d) : "v"(a), "v"(b), "v"(c_))
#define VADD(d, a, b) asm volatile("v_add_f32 %0, %1, %2" : "=v"(d) : "v"(a), "v"(b))
#define VEXP(d, a) asm volatile("v_exp_f32 %0, %1" : "=v"(d) : "v"(a))
#define VFMC(d, a, b) asm volatile("v_fmamk_f32 %0, %1, 0x3e0293ee, %2" : "=v"(d) : "v"(a), "v"(b))
#define SEL(d, a, b) asm volatile("v_cndmask_b32 %0, %1, %2, %3" : "=v"(d) : "v"(a), "v"(b), "s"(himask))
#define QPERM(d, a, q) asm volatile("v_mov_b32_dpp %0, %1 quad_perm:" q " row_mask:0xf bank_mask:0xf" : "=v"(d) : "v"(a))

#define HSAMP 58
DEV void role_head(const Params& p, lptr lds, int L, int h) {
    const int tid = threadIdx.x, lane = tid & 63, w = uni(tid >> 6), g = lane >> 4, r = lane & 15;
    const rsrc_t rs = mk_rsrc(p.ws, W_END);
    v8u wq0[10], wq1[10], wk0[10], wk1[10], wv0[10], wv1[10];
    lptr WL = lds + C_WL + w * (NLF * 2048) + lane * 16;
    {
        constexpr int GS = 1056, CS = 48 * GS;
        const v4u sq = mk_srd(p.Wq, DM * NQ * 2), sk = mk_srd(p.Wk, DM * NQ * 2), sv = mk_srd(p.Wv, DM * NQ * 2);
        const u32 vo = (u32)(g * NQ + h * DH + 8 * (r ^ ((g & 2) << 2))) * 2u;
        auto fill = [&](int ch) {
#pragma unroll 1
            for (int i = 0; i < 4; ++i) {
                const u32 dst = uu((ch % 3) * CS + (4 * i + w) * GS), so = uu((ch * 64 + 16 * i + 4 * w) * (NQ * 2));
                DMAIN(sq, dst, vo, so); DMAIN(sk, dst + 16 * GS, vo, so); DMAIN(sv, dst + 32 * GS, vo, so);
            }
        };
        int vote = 0;
        if (L == 1) {
            if (w == 0) vote = probe(rs, h);
            BAR();
            EV(2);
            pre_rings(p, rs, h);
        }
        fill(0); fill(1); fill(2);
        const int q4 = r >> 2;
        lptr tb = lds + (4 * (g >> 1) + q4) * GS + (g & 1) * 512 + ((w * 64 + (r & 3) * 8) ^ ((g & 1) << 7));
        SFOR(ch, 10)
            VMW(ch == 1 || ch == 2 ? 25 : ch < 8 ? 24 : 108 - 12 * ch);
            BAR();
            if constexpr (ch == 0) boot_set(p, rs, tid == 0 && L == 1 ? foff(F_RG + h) : OOB, (u32)((1 << 20) + vote));
            auto frag = [&](int m, int t) { lptr b = tb + (ch % 3) * CS + m * 16 * GS + t * 32; return tr_frag(b, b + 256, b + 8 * GS, b + 8 * GS + 256); };
            wq0[ch] = frag(0, 0); wq1[ch] = frag(0, 1); wk0[ch] = frag(1, 0); wk1[ch] = frag(1, 1); wv0[ch] = frag(2, 0); wv1[ch] = frag(2, 1);
            asm volatile("" : "+a"(wq0[ch]), "+a"(wk0[ch]), "+v"(wv0[ch]), "+v"(wv1[ch]));
            if constexpr (qslot(ch) < 0) asm volatile("" : "+a"(wq1[ch])); else asm volatile("" : "+v"(wq1[ch]));
            if constexpr (kslot(ch) < 0) asm volatile("" : "+a"(wk1[ch])); else asm volatile("" : "+v"(wk1[ch]));
            BAR();
            if constexpr (ch < 7) fill(ch + 3);
        SEND
#pragma unroll
        for (int ch = 0; ch < 10; ++ch) {
            if (qslot(ch) >= 0) { lds_w16(WL + qslot(ch) * 2048, wq1[ch].lo); lds_w16(WL + qslot(ch) * 2048 + 1024, wq1[ch].hi); }
            if (kslot(ch) >= 0) { lds_w16(WL + kslot(ch) * 2048, wk1[ch].lo); lds_w16(WL + kslot(ch) * 2048 + 1024, wk1[ch].hi); }
            if (vslot(ch) >= 0) { lds_w16(WL + vslot(ch) * 2048, wv1[ch].lo); lds_w16(WL + vslot(ch) * 2048 + 1024, wv1[ch].hi); }
        }
        CFENCE();
    }
    const u32 idx = (r & 2) ? 0xEEEEu : 0x4444u;
    const unsigned long long himask = 0xCCCCCCCCCCCCCCCCull;
    bool dead = false;
    EV(6);
    const u32 pol = get_pol(p, rs, dead);
    const u32 pol0 = pol & 1u;
    const u32 l16 = (u32)lane * 16u;
    const u32 mring = W_MRING + (u32)L * (MB / 4) + pol0 * 8192u, hint = mring + 4 * 32768u;
    const u32 fkh = lane < 3 ? near_(foff(F_QKV + lane * 64 + L * 32 + h), 0u, pol) : lane < 6 ? hint + (u32)(lane - 3) * 4u : OOB;
    u32 tm = 0;
    MK_SRD();
    const unsigned long long kmask = __builtin_amdgcn_ballot_w64((hblk(W_KQ, L * NSEG + lane, h, 0u, pol) & 8192u) != 0u),
                             vmask = __builtin_amdgcn_ballot_w64((hblk(W_VO, L * NSEG + lane, h, 1u, pol) & 8192u) != 0u);
    u32 kb = uu(W_KQ + (u32)(L * NSEG * NH + h) * 16384u + w * 2048);
    u32 dk = uu(C_K + w * 2048), dv = dk + (C_V - C_K), dq = dk + (C_QX - C_K), dm = uu(C_MEM + w * 2048), dm2 = uu(w < 2 ? C_MEM + 8192 + w * 1024 : C_DUMP);
    asm volatile("" : "+s"(dk), "+s"(dv), "+s"(dq), "+s"(dm), "+s"(dm2));
    u32 ksrc = kb + ((u32)kmask & 1u) * 8192u, vsrc = kb + (W_VO - W_KQ) + ((u32)vmask & 1u) * 8192u;
    lptr qw = lds + C_Q + (2 * g) * 272 + (32 * w + 2 * r) * 2;
    lptr qr = lds + C_Q + (2 * (r >> 2) + (r & 1)) * 272 + g * 16;
    lptr LB = lds + l16, VX = lds + C_V + w * 2048 + l16;
    if (tid >= 64 && tid < 70) { const u32 z = tid == 68 ? 0x3F803F80u : tid == 69 ? PSENT : 0u; lds_w16(lds + C_ZERO + (tid - 64) * 16, (v4u){z, z, z, z}); }
    u32 zaddr = 0; PIN(zaddr);
    lptr ZP = lds + zaddr + C_ZERO;
    const float CEXP = SCALE * LOG2E;
#define LF_RD(slot, hf) lf[slot][hf] = lds_r16(WL + (slot) * 2048 + (hf) * 1024)
    BAR();
    const u32 ofl = tid == 0 ? near_(foff(F_O + L * 32 + h), 0u, pol) : OOB;
    u32 oso = (g & 1) ? OOB : W_ORING + (u32)L * (MB / 2) + ring_map((u32)((r >> 3) * 32768 + h * 1024 + w * 256 + (g >> 1) * 128 + (r & 7) * 16), pol0);
    unsigned long long vis[8];
#pragma unroll
    for (int i = 0; i < 8; ++i) vis[i] = __builtin_amdgcn_ballot_w64(16 * (i >> 2) + 4 * g + (i & 3) <= 16 * (w & 1) + r);
    u32 missed = 0, dirty = 1;
    asm volatile("" : "+v"(oso), "+v"(qw), "+v"(qr), "+v"(VX));
    asm volatile("" : "+s"(vis[0]), "+s"(vis[1]), "+s"(vis[2]), "+s"(vis[3]), "+s"(vis[4]), "+s"(vis[5]), "+s"(vis[6]), "+s"(vis[7]), "+s"(ksrc), "+s"(vsrc));
    wait_flags(rs, lane < 3 ? fkh : OOB, 1u, dead);
    EV(7);
    DMA32(dk, l16, ksrc); DMA32(dv, l16, vsrc); DMA32(dq, l16, ksrc ^ 8192u);
    EV(8);
    auto next_slot = [&](u32 now) {
        const u32 hv = ld4(rs, fkh), per = rdl(hv, 4);
        if (per < 400u) { tm = rdl(hv, 5) - per; for (int n = 0; n < 3 && (int)(tm - now) < 0; ++n) tm += per; }
    };
    if (L == 1) { next_slot(rt32() + CH_KV0); tm -= 24u; }
    for (int s = 0; s < NSEG;) {
        {
            *(LAS u32*)(lds + C_DUMP + 128 + w * 4) = tm;
            BAR();
            const v4u c = lds_r16(lds + C_DUMP + 128) - tm;
            const u32 add = uu((u32)max(max((int)c.x, (int)c.y), max((int)c.z, (int)c.w)));
            tm += add; dirty |= add;
        }
        u32 ms = mring + (u32)(s & 3) * 32768u + w * 2048, ms2 = mring + (u32)(s & 3) * 32768u + 16384u + (w & 1) * 1024, sv = (u32)s;
        asm volatile("" : "+s"(ms), "+s"(ms2)); PIN(sv);
        f32x4 aq0, aq1, ak0, ak1;
        v4u lf[NLF][2];
        ZRD(aq0, 0); ZRD(aq1, 1); ZRD(ak0, 2); ZRD(ak1, 3);
#pragma unroll
        for (int i = 0; i < 4; ++i) { LF_RD(i, 0); LF_RD(i, 1); }
        SCHED();
        v4u A[10];
#define HT(k) do {} while (0)
        v4u kx[8], qf[4], kmf[4], vx[2], ones, P;
        float fa, fb, fc, fd, mx, my, sx[4], t[4], tm0, tm1; u32 pa, pb, fl, e01, e23, pmm, tmp = 0; unsigned long long nanq;
        f32x4 av0, av1, s0, s1, sm, oa, ob, rsum;
        v2u vm, pm;
#define LW(sl) cat8(lf[sl][0], lf[sl][1])
#define PQ(ks, tl) do { if constexpr (!(tl)) SMF_A(aq0, A[ks], wq0[ks]); else if constexpr (qslot(ks) < 0) SMF_A(aq1, A[ks], wq1[ks]); else { const v8u lw_ = LW(qslot(ks)); SMF_V(aq1, A[ks], lw_); } } while (0)
#define PK(ks, tl) do { if constexpr (!(tl)) SMF_A(ak0, A[ks], wk0[ks]); else if constexpr (kslot(ks) < 0) SMF_A(ak1, A[ks], wk1[ks]); else { const v8u lw_ = LW(kslot(ks)); SMF_V(ak1, A[ks], lw_); } } while (0)
#define PV(ks, tl) do { if constexpr (!(tl)) SMF_V(av0, A[ks], wv0[ks]); else if constexpr (vslot(ks) < 0) SMF_V(av1, A[ks], wv1[ks]); else { const v8u lw_ = LW(vslot(ks)); SMF_V(av1, A[ks], lw_); } } while (0)
#define KXR(i) kx[i] = lds_r16(LB + C_K + ((i) & 1) * 4096 + ((i) >> 1) * 1024)
#define XW(q_) do { *(LAS u32*)(q_) = pa; *(LAS u32*)((q_) + 272) = pb; } while (0)
        auto slot = [&](auto zc) __attribute__((always_inline)) {
            constexpr int z = decltype(zc)::value;
            if constexpr (z < 14) PQ(z >> 1, z & 1);
            else if constexpr (z < 17) PQ(z - 7, 0);
            else if constexpr (z < 20) PQ(z - 10, 1);
            else if constexpr (z < 33) PK((z - 20) >> 1, z & 1);
            else if constexpr (z < 35) { if constexpr (z == 33) MF0(s0, kx[0], qf[0]); else MF0(s1, kx[1], qf[0]); }
            else if constexpr (z < 41) { if constexpr (z & 1) MF1(s0, kx[z - 33], qf[(z - 33) >> 1]); else MF1(s1, kx[z - 33], qf[(z - 33) >> 1]); }
            else if constexpr (z == 41) PK(6, 1);
            else if constexpr (z < 45) PK(z - 35, 0);
            else if constexpr (z < 48) PK(z - 38, 1);
            else if constexpr (z < 58) PV(z - 48, 0);
            else if constexpr (z < 61) PV(z < 60 ? z - 54 : 0, 1);
            else if constexpr (z == 61) MF0(sm, kmf[0], qf[0]);
            else if constexpr (z < 65) MF1(sm, kmf[z - 61], qf[z - 61]);
            else if constexpr (z == 65) MF0(oa, vx[0], P);
            else if constexpr (z == 66) MF0(ob, vx[1], P);
            else PV(6, 1);
            if constexpr (z < 6) A[4 + z] = lds_r16(LB + C_MEM + (4 + z) * 1024);
            if constexpr (z == 6 || z == 7) LF_RD(4, z - 6);
            if constexpr (z == 8 || z == 9) LF_RD(5, z - 8);
            if constexpr (z == 12 || z == 13) LF_RD(6, z - 12);
            if constexpr (z == 16 || z == 17) KXR(z - 16);
            if constexpr (z == 18 || z == 19) LF_RD(7, z - 18);
            if constexpr (z >= 20 && z < 24) KXR(z - 18);
            if constexpr (z == 32 || z == 33) KXR(z - 26);
            if constexpr (z == 24) XW(qw);
            if constexpr (z == 25) ZRD(av0, 0);
            if constexpr (z == 26) ZRD(av1, 1);
            if constexpr (z == 27) asm volatile("s_waitcnt lgkmcnt(2)\n\ts_barrier" ::: "memory");
            if constexpr (z >= 28 && z < 32) qf[z - 28] = lds_r16(qr + (z - 28) * 64);
            if constexpr (z == HSAMP) fl = ld4(rs, fkh);
            if constexpr (z == 52) XW(qw + (C_KM - C_Q));
            if constexpr (z == 53 || z == 54) vx[z - 53] = lds_r16(VX + (z - 53) * 1024);
            if constexpr (z == 55) asm volatile("s_waitcnt lgkmcnt(2)\n\ts_barrier" ::: "memory");
            if constexpr (z >= 56 && z < 60) kmf[z - 56] = lds_r16(qr + (C_KM - C_Q) + (z - 56) * 64);
            if constexpr (z >= 48 && z < 52) LF_RD(8 + ((z - 48) >> 1), z & 1);
            if constexpr (z >= 60) LF_RD(10 + ((z - 60) >> 1), z & 1);
            SCHED();
            if constexpr (z == 19) HT(2);
            if constexpr (z == 32) HT(3);
            if constexpr (z == 47) HT(4);
            if constexpr (z == 64) HT(5);
            if constexpr (z == 20) VADD(fa, aq0[0], aq0[2]);
            if constexpr (z == 21) VADD(fb, aq0[1], aq0[3]);
            if constexpr (z == 22) { VADD(fc, aq1[0], aq1[2]); VADD(fd, aq1[1], aq1[3]); }
            if constexpr (z == 23) { asm volatile("v_cmp_u_f32 %0, %1, %2" : "=s"(nanq) : "v"(fa), "v"(fb)); VCVT(pa, fa, fc); VCVT(pb, fb, fd); }
            if constexpr (z == 39 || z == 40 || z == 41) NOPS(1);
            if constexpr (z == 42) VMAX3(mx, s0[0], s0[1], s0[2]);
            if constexpr (z == 43) { VMAX3(mx, mx, s0[3], s1[0]); VMAX3(my, s1[1], s1[2], s1[3]); }
            if constexpr (z == 44 || z == 46) { VMAX(fa, mx, my); VMAX(my, mx, my); mx = fa; }
            if constexpr (z == 45) { SEL(sx[0], s0[0], s1[0]); asm volatile("v_permlane16_swap_b32 %0, %1" : "+v"(mx), "+v"(my)); }
            if constexpr (z == 47) { SEL(sx[1], s0[1], s1[1]); asm volatile("v_permlane32_swap_b32 %0, %1" : "+v"(mx), "+v"(my)); }
            if constexpr (z == 48) asm volatile("v_max_f32 %0, %0, %1\n\tv_mul_f32 %0, 0xbe0293ee, %0" : "+v"(mx) : "v"(my));
            if constexpr (z == 49) { VADD(fa, ak0[0], ak0[2]); VADD(fb, ak0[1], ak0[3]); }
            if constexpr (z == 50) { VADD(fc, ak1[0], ak1[2]); VADD(fd, ak1[1], ak1[3]); }
            if constexpr (z == 51) { VCVT(pa, fa, fc); VCVT(pb, fb, fd); }
            if constexpr (z == 52) SEL(sx[2], s0[2], s1[2]);
            if constexpr (z == 53) { SEL(sx[3], s0[3], s1[3]); VFMC(t[0], sx[0], mx); }
            if constexpr (z == 54) { VFMC(t[1], sx[1], mx); VFMC(t[2], sx[2], mx); }
            if constexpr (z == 55) VFMC(t[3], sx[3], mx);
            if constexpr (z >= 56 && z < 60) VEXP(t[z - 56], t[z - 56]);
            if constexpr (z == 60) { VCVT(e01, t[0], t[1]); VCVT(e23, t[2], t[3]); }
            if constexpr (z == 61) { QPERM(P.x, e01, "[0,1,0,1]"); QPERM(P.z, e01, "[2,3,2,3]"); }
            if constexpr (z == 62) { QPERM(P.y, e23, "[0,1,0,1]"); QPERM(P.w, e23, "[2,3,2,3]"); }
            if constexpr (z == 63) { VADD(fa, av0[0], av0[2]); VADD(fb, av0[1], av0[3]); }
            if constexpr (z == 64) VCVT(vm.x, fa, fb);
            if constexpr (z == 67) { asm volatile("" :: "v"(sm)); VFMC(tm0, sm[0], mx); VFMC(tm1, sm[1], mx); }
            SCHED();
        };
        {
            asm volatile("s_mov_b32 m0, %2\n\t" WAIT_TXT("%0", "%1") "buffer_load_dwordx4 %4, %5, %6 offen sc1 lds\n\tbuffer_load_dwordx4 %4, %5, %6 offen offset:1024 sc1 lds\n\t"
                         "s_mov_b32 m0, %3\n\ts_nop 0\n\tbuffer_load_dwordx4 %4, %5, %7 offen sc1 lds\n\ts_waitcnt vmcnt(0) lgkmcnt(0)\n\ts_barrier"
                         : "=&s"(tmp) : "s"(uu(tm - 1u)), "s"(dm), "s"(dm2), "v"(l16), "s"(srd), "s"(ms), "s"(ms2) : "vcc", "scc", "memory");
        }
#pragma unroll
        for (int ks = 0; ks < 4; ++ks) A[ks] = lds_r16(LB + C_MEM + ks * 1024);
        SCHED();
        st4s(rs, ofl, 0, sv);
        if (__builtin_expect(dirty != 0u, 0)) st16(rs, oso, (v4u){PSENT, PSENT, PSENT, PSENT});
        SCHED();
        static_for<0, 24>(slot);
        if (__builtin_expect(nanq != 0ull, 0)) {
            u32 mn = ~0u;
#pragma unroll
            for (int i = 0; i < 10; ++i) mn = min(min(min(mn, A[i].x ^ PSENT), A[i].y ^ PSENT), min(A[i].z ^ PSENT, A[i].w ^ PSENT));
            nanq = missed < SPIN_CAP ? __builtin_amdgcn_ballot_w64(mn == 0u) : 0ull;
        }
        if (__builtin_expect(nanq == 0ull, 1)) {
        static_for<24, 68>(slot);
        PV(7, 1); ones = lds_r16(ZP + 64); SCHED();
        VMAX(fa, tm0, tm1);
        if (__builtin_expect(__builtin_amdgcn_ballot_w64(fa > 24.0f) != 0ull, 0)) {
            const float up = fmaxf(max32(max16(fmaxf(tm0, tm1))), 0.f);
            tm0 -= up; tm1 -= up;
#pragma unroll
            for (int i = 0; i < 4; ++i) t[i] = __builtin_amdgcn_exp2f(__builtin_fmaf(sx[i], CEXP, mx) - up);
            e01 = pk(t[0], t[1]); e23 = pk(t[2], t[3]);
            asm volatile("s_nop 1" : "+v"(e01), "+v"(e23));
            QPERM(P.x, e01, "[0,1,0,1]"); QPERM(P.z, e01, "[2,3,2,3]"); QPERM(P.y, e23, "[0,1,0,1]"); QPERM(P.w, e23, "[2,3,2,3]");
            NOPS(1);
            MF0(oa, vx[0], P); MF0(ob, vx[1], P);
            NOPS(7);
        }
        SCHED();
        PV(1, 1); VEXP(tm0, tm0); SEL(oa[0], oa[0], ob[0]); SCHED();
        PV(8, 1); VEXP(tm1, tm1); SEL(oa[1], oa[1], ob[1]); SCHED();
        PV(9, 1); VCVT(pmm, tm0, tm1); SEL(oa[2], oa[2], ob[2]); SCHED();
        PV(2, 1);
        asm volatile("v_cndmask_b32 %0, %2, 0, %3\n\tv_cndmask_b32 %1, 0, %2, %3" : "=&v"(pm.x), "=&v"(pm.y) : "v"(pmm), "s"(himask)); SCHED();
        PV(3, 1); SEL(oa[3], oa[3], ob[3]); SCHED();
        MF0(rsum, ones, P); NOPS(4);
        VADD(fc, av1[0], av1[2]); VADD(fd, av1[1], av1[3]);
        { const v2u ones2 = ones.lo; MF1H(rsum, ones2, pm); }
        VCVT(vm.y, fc, fd); NOPS(1);
        MF1H(oa, vm, pm);
        {
            float inv; v4u oc;
            asm volatile("s_nop 3\n\tv_rcp_f32 %0, %1\n\ts_nop 2" : "=v"(inv) : "v"(rsum[0]), "v"(rsum));
            PIN(oa);
            const f32x2 i2 = {inv, inv};
            const f32x2 u0 = oa.lo * i2, u1 = oa.hi * i2;
            asm volatile("v_cvt_pk_bf16_f32 %0, %4, %5\n\tv_cvt_pk_bf16_f32 %2, %4, %5\n\tv_cvt_pk_bf16_f32 %1, %6, %7\n\tv_cvt_pk_bf16_f32 %3, %6, %7\n\t"
                         "v_permlane16_swap_b32 %0, %2\n\ts_nop 0\n\tv_permlane16_swap_b32 %1, %3"
                         : "=&v"(oc.x), "=&v"(oc.y), "=&v"(oc.z), "=&v"(oc.w) : "v"(u0.x), "v"(u0.y), "v"(u1.x), "v"(u1.y));
            st16(rs, oso, oc);
        }
        SCHED();
        unsigned long long clk;
        asm volatile("s_memrealtime %0" : "=s"(clk));
        HT(6);
        PIN(fl);
        v4u tq[4], tk[8], tv[2];
        f32x4 c0, c1, cm;
        u32 oo, kvo; bool kvok;
        u32 ln = l16; PIN(ln);
        lptr QX = lds + C_QX + (w & 1) * 4096 + ln, VO = lds + C_V + (w ^ 1) * 2048 + ln, VMX = lds + C_VM + w * 512 + (ln >> 1);
#pragma unroll
        for (int i = 0; i < 2; ++i) { tq[i] = lds_r16(QX + i * 1024); tk[2 * i] = lds_r16(LB + C_K + i * 1024); tk[2 * i + 1] = lds_r16(LB + C_K + 4096 + i * 1024); }
        *(LAS v2u*)VMX = vm;
        SCHED();
        SFOR(i, 12)
            if constexpr (i == 0) MF0(cm, kmf[0], tq[0]); else if constexpr (i == 1) MF0(c0, tk[0], tq[0]); else if constexpr (i == 2) MF0(c1, tk[1], tq[0]);
            else if constexpr (i % 3 == 0) MF1(cm, kmf[i / 3], tq[i / 3]); else if constexpr (i % 3 == 1) MF1(c0, tk[2 * (i / 3)], tq[i / 3]); else MF1(c1, tk[2 * (i / 3) + 1], tq[i / 3]);
            if constexpr (i == 0) { tq[2] = lds_r16(QX + 2048); tk[4] = lds_r16(LB + C_K + 2048); }
            if constexpr (i == 1) { tk[5] = lds_r16(LB + C_K + 6144); tq[3] = lds_r16(QX + 3072); }
            if constexpr (i == 2) { tk[6] = lds_r16(LB + C_K + 3072); tk[7] = lds_r16(LB + C_K + 7168); }
            if constexpr (i == 3) { tv[0] = lds_r16(VO); tv[1] = lds_r16(VO + 1024); }
            if constexpr (i == 4) { oo = ln + (ln & 0x300u) + ((vsrc ^ 8192u) + (u32)((w & 1) * 256)); PIN(oo); kb += NH * 16384; PINS(kb); }
            if constexpr (i == 5) { ksrc = kb + (((kmask >> ((s + 1) & 63)) & 1ull) ? 8192u : 0u); PINS(ksrc); }
            if constexpr (i == 6) { vsrc = kb + (((vmask >> ((s + 1) & 63)) & 1ull) ? W_VO - W_KQ + 8192u : W_VO - W_KQ); PINS(vsrc); }
            if constexpr (i == 7) { kvok = ((u32)__builtin_amdgcn_ballot_w64(fl < (u32)s + 2) & 7u) == 0u; }
            if constexpr (i == 8) { kvo = l16 | (kvok ? 0u : OOB); PIN(kvo); }
            SCHED();
        SEND
        asm volatile("s_waitcnt lgkmcnt(0)\n\ts_barrier" : "+s"(clk) :: "memory");
        const v2u tvm = *(LAS v2u*)(VMX + (512 - (w & 1) * 1024));
        const v4u pv = lds_r16(ZP + 80);
        DMA32(dk, kvo, ksrc);
        SCHED(); asm volatile("" :: "v"(cm));
        float u[10], m, m2;
        {
            float NEG = -1000000000.0f / SCALE; PIN(NEG);
#define MSK(d, a, i) asm volatile("v_cndmask_b32 %0, %1, %2, %3" : "=v"(d) : "v"(NEG), "v"(a), "s"(vis[i]))
#pragma unroll
            for (int i = 0; i < 4; ++i) MSK(u[i], c0[i], i);
#pragma unroll
            for (int i = 0; i < 4; ++i) MSK(u[4 + i], c1[i], 4 + i);
            asm volatile("v_max3_f32 %0, %2, %3, %10\n\tv_max3_f32 %1, %4, %5, %11\n\tv_max3_f32 %0, %0, %6, %7\n\tv_max3_f32 %1, %1, %8, %9\n\t"
                         "v_max_f32 %0, %0, %1\n\tv_mov_b32 %1, %0\n\ts_nop 1\n\tv_permlane16_swap_b32 %0, %1\n\tv_max_f32 %0, %0, %1\n\tv_mov_b32 %1, %0\n\ts_nop 1\n\t"
                         "v_permlane32_swap_b32 %0, %1\n\tv_max_f32 %0, %0, %1\n\tv_mul_f32 %0, 0xbe0293ee, %0"
                         : "=&v"(m), "=&v"(m2) : "v"(u[0]), "v"(u[1]), "v"(u[2]), "v"(u[3]), "v"(u[4]), "v"(u[5]), "v"(u[6]), "v"(u[7]), "v"(cm[0]), "v"(cm[1]));
        }
        DMA32(dv, kvo, vsrc);
        {
            v4u px; v2u p0, p1;
#define EX2(d, a, b) asm volatile("v_fmamk_f32 %0, %2, 0x3e0293ee, %4\n\tv_fmamk_f32 %1, %3, 0x3e0293ee, %4\n\tv_exp_f32 %0, %0\n\tv_exp_f32 %1, %1" : "=&v"(d.x), "=&v"(d.y) : "v"(a), "v"(b), "v"(m))
            f32x2 ex[5];
            EX2(ex[0], u[0], u[1]); EX2(ex[1], u[2], u[3]); EX2(ex[2], u[4], u[5]); EX2(ex[3], u[6], u[7]); EX2(ex[4], cm[0], cm[1]);
            asm volatile("v_cvt_pk_bf16_f32 %0, %8, %9\n\tv_cvt_pk_bf16_f32 %1, %10, %11\n\tv_cvt_pk_bf16_f32 %2, %12, %13\n\tv_cvt_pk_bf16_f32 %3, %14, %15\n\t"
                         "v_mov_b32 %5, 0\n\tv_mov_b32 %6, 0\n\tv_cvt_pk_bf16_f32 %4, %16, %17\n\tv_cvt_pk_bf16_f32 %7, %16, %17"
                         : "=&v"(px.x), "=&v"(px.y), "=&v"(px.z), "=&v"(px.w), "=&v"(p0.x), "=&v"(p0.y), "=&v"(p1.x), "=&v"(p1.y)
                         : "v"(ex[0].x), "v"(ex[0].y), "v"(ex[1].x), "v"(ex[1].y), "v"(ex[2].x), "v"(ex[2].y), "v"(ex[3].x), "v"(ex[3].y), "v"(ex[4].x), "v"(ex[4].y));
            f32x4 o[4], sum;
            const v2u ones2 = ones.lo;
            const u32 now = (u32)clk;
            u32 oz;
            SCHED();
            MF0(sum, ones, px); { const u32 q = ksrc ^ 8192u; DMA32(dq, kvo, q); } SCHED();
            MF0(o[0], vx[0], px);
            MF0(o[1], vx[1], px);
            MF0(o[2], tv[0], px); oz = (oso & ~(3u << 17)) | (u32)((s + 3) & 3) << 17; PIN(oz); SCHED();
            MF0(o[3], tv[1], px); oso = (oso & ~(3u << 17)) | (u32)((s + 1) & 3) << 17; PIN(oso); SCHED();
            MF1H(sum, ones2, p0);
            MF1H(o[0], vm, p0);
            MF1H(o[1], vm, p1); st16(rs, oz, pv); SCHED();
            MF1H(o[2], tvm, p0);
            MF1H(o[3], tvm, p1);
            float inv;
            asm volatile("s_nop 2\n\tv_rcp_f32 %0, %1\n\ts_nop 0" : "=v"(inv) : "v"(sum[0]), "v"(sum));
            asm volatile("" : "+v"(o[0]), "+v"(o[1]));
            st16(rs, oo, (v4u){pk2(o[0].lo * inv), pk2(o[0].hi * inv), pk2(o[1].lo * inv), pk2(o[1].hi * inv)});
            asm volatile("" : "+v"(o[2]), "+v"(o[3]));
            st16(rs, oo ^ 2048u, (v4u){pk2(o[2].lo * inv), pk2(o[2].hi * inv), pk2(o[3].lo * inv), pk2(o[3].hi * inv)});
            SCHED();
            if (__builtin_expect((h & 15) == 0 && w == 3, 0)) {
                st4s(rs, ln == 0 ? hint + 128u + (u32)h * 8u : OOB, 0, (u32)s << 16 | min(missed, 1u) << 14 | min(now - tm, 0x3FFFu));
                if (h == 0) { LST(L * 64 + s, now); LST(128 + L * 64 + s, ((int)(rdl(fl, 3) - now) >= CH_VIS - 4) | ((int)kvok << 1) | ((int)min(missed, 3u) << 2)); LST(640 + L * 64 + s, tm); LST(1728 + L * 64 + s, __builtin_amdgcn_s_memtime()); }
            }
            tm = rdl(fl, 5);
            missed = missed == SPIN_CAP ? missed : 0u;
            dirty = 0;
            if (__builtin_expect(rdl(fl, 3) - now + CH_LATE > 4000u && s + 1 < NSEG, 0)) {
                dirty = 1;
#pragma unroll 1
                for (int n = 0; n < 64; ++n) {
                    CFENCE();
                    const u32 hv = ld4(rs, fkh);
                    tm = rdl(hv, 5);
                    if (rdl(hv, 3) - now + CH_LATE <= 4000u) break;
                }
            }
        }
        LS2(L == 0 && w == 0, h);
        if (__builtin_expect(!kvok, 0) && s + 1 < NSEG) {
            st4s(rs, (ln == 0 && w == 0) ? hint + 4096u + (u32)h * 128u : OOB, 0, (u32)s + 1u);
            dirty = 1;
            wait_flags(rs, ln < 48u ? fkh : OOB, (u32)s + 2, dead);
            DMA32(dk, l16, ksrc); DMA32(dv, l16, vsrc); DMA32(dq, l16, ksrc ^ 8192u);
            const u32 now = rt32() + CH_KV;
            if ((int)(tm - now) < 0) next_slot(now);
        }
        ++s;
        } else {
            u32 ln = l16; PIN(ln);
            const u32 hv = ld4(rs, ln == 96u ? foff(F_ERR) : fkh);
            const u32 was = tm - tmp;
            tm = rdl(hv, 5); dirty = 1;
            if ((int)(tm - rdl(hv, 4) - was) > 24) tm -= rdl(hv, 4);
            *(LAS u32*)(lds + C_DUMP + (w == 0 ? 0 : 64)) = rdl(hv, 6);
            BAR();
            missed = uu(*(LAS u32*)(lds + C_DUMP)) != 0u ? SPIN_CAP : missed + 1;
            st4s(rs, missed == SPIN_CAP ? foff(F_ERR) : OOB, 0, 1u);
        }
    }
    DRAIN(); BAR();
    st4s(rs, ofl, 0, NSEG);
}

#define WO_NI 4
#define WROT 0
DEV void role_wo(const Params& p, lptr lds, int j) {
    const int tid = threadIdx.x, lane = tid & 63, w = uni(tid >> 6), g = lane >> 4, r = lane & 15;
    const int rg = j & 1, cg = j >> 1, rot = cg & WROT;
    const rsrc_t rs = mk_rsrc(p.ws, W_END);
    v8u wo[8][5];
    {
        const v4u so_ = mk_srd(p.Wo, DM * NQ * 2);
        const int one = (0x3F2FF1F0u >> (4 * (cg & 7))) & 15;
        u32 vo[3];
#pragma unroll
        for (int i = 0; i < 3; ++i) { const int n = 64 * i + lane; vo[i] = n % 6 < 5 ? (u32)((n / 6) * (DM * 2) + cg * 80 + (n % 6) * 16) : ((n / 6) & 3) == one ? (u32)((n / 6) * (DM * 2) + (cg >> 3) * 640 + 304 + rg * 16) : OOB; }
        auto fill = [&](int b) {
            const u32 dst = uu(w * 36864 + (b % 3) * 12288), src = uu((8 * w + ((b + rot) & 7)) * (DH * DM * 2));
#pragma unroll
            for (int i = 0; i < 12; ++i) DMAIN(so_, dst + i * 1024, vo[i % 3], src + (i / 3) * (32 * DM * 2));
        };
#pragma unroll 1
        for (int b = 0; b < 3; ++b) fill(b);
        const int q4 = r >> 2;
        lptr tb = lds + w * 36864 + (32 * (g >> 1) + 16 * (q4 >> 1) + 8 * ((r >> 1) & 1) + 4 * (g & 1) + (q4 & 1)) * 96 + (r & 1) * 8;
        SFOR(b, 8)
            VMW(b < 6 ? 24 : 84 - 12 * b);
#pragma unroll
            for (int t = 0; t < 5; ++t) {
                lptr q = tb + (b % 3) * 12288 + (t == 0 ? 4 : (w + t) & 3) * 16;
                wo[b][t] = tr_frag(q, q + 192, q + 64 * 96, q + 64 * 96 + 192);
                if (t < 4) asm volatile("" : "+a"(wo[b][t])); else asm volatile("" : "+v"(wo[b][t]));
            }
            CFENCE();
            if constexpr (b < 5) fill(b + 3);
        SEND
    }
    BAR();
    EV(6);
    const u32 idx = (r & 2) ? 0xEEEEu : 0x4444u;
    bool dead = false;
    const u32 pol0 = get_pol(p, rs, dead) & 1u;
    const u32 l16 = (u32)lane * 16u;
    u32 oo[8];
#pragma unroll
    for (int i = 0; i < 8; ++i) oo[i] = uu(W_ORING + ring_map((u32)(rg * 32768 + (8 * w + ((i + rot) & 7)) * 1024), pol0));
    const u32 hint = W_MRING + 4 * 32768u + pol0 * 8192u;
    u32 hvo[2];
#pragma unroll
    for (int l = 0; l < 2; ++l) hvo[l] = lane < 2 ? hint + (u32)(l ^ 1) * (MB / 4) + (u32)lane * 4u : lane == 2 ? hint + (u32)l * (MB / 4) + 128u : lane == 3 ? foff(F_ERR) : lane == 4 ? hint + (u32)l * (MB / 4) + 256u :
                 (lane >= 32 && j == 0 && w == 3) ? hint + (u32)l * (MB / 4) + (u32)lane * 128u : OOB;
    const int xo = g * 128 + (r & 7) * 16, O = 5 * cg + (r < 8 ? w : 4), q2 = 2 + (r >> 3);
    lptr xw0 = lds + (r < 8 ? 2048 + w * 4 : ((w + 1) & 3) * 512 + 4) + xo, xw1 = lds + ((w + q2) & 3) * 512 + q2 * 4 + xo;
    lptr xr = lds + (r < 8 ? w : 4) * 512 + xo;
    if (r < 8) { *(LAS u32*)xr = 0u; *(LAS u32*)(xr + 8192) = 0u; }
    const u32 mpo = (r & 4) == 0 && (r < 8 || w == 0) ? W_MRING + ring_map((u32)((O >> 1) * 256 + rg * 128 + (4 * (g & 1) + 2 * (O & 1) + (g >> 1)) * 16 + (r & 3) * 4), pol0) : OOB;
    constexpr u32 PER0 = CH_W0 + CH_MW + CH_H0 + CH_MH + 2 * CH_VIS;
    const bool lead = j == 0 && w == 3;
    u32 hso = OOB;
    MK_SRD();
    u32 t = 0, tn = 0;
    int sn[2] = {0, 0};
    u32 hav = 4 * CH_H0, wav = 4 * CH_W0, mh = 8 * (CH_MH + CH_M0), mw = 8 * (CH_MW + CH_M0), ok[2] = {0u, 0u}, dw[2] = {CH_W0 + CH_VIS + CH_MW + CH_REC, CH_W0 + CH_VIS + CH_MW + CH_REC};
    u32 zero = 0, hv = 0xFFFFu, pst = 1, pdur = 0, pmp = OOB;
    const u32 psent = PSENT;
    lptr sw = lds + 5120 + w * 4;
    bool stop = false;
    asm volatile("" : "+v"(zero), "+v"(sw), "+v"(xw0), "+v"(xw1), "+v"(xr));
    auto turn = [&](auto lc) __attribute__((always_inline)) {
        constexpr int L = decltype(lc)::value, M = L ^ 1;
        u32 now, done, rep, rd, lost, dh, dn, nt, za; v4u hw;
        const int sc = sn[L];
        auto books = [&](auto ic) __attribute__((always_inline)) {
            constexpr int i = decltype(ic)::value;
            if constexpr (i == 0) { rep = max(rdl(hv, 2), rdl(hv, 4)); PINS(rep); }
            if constexpr (i == 1) { lost = (u32)(__builtin_amdgcn_ballot_w64(hv == (u32)sn[M]) >> 32); PINS(lost); }
            if constexpr (i == 2) { lost |= (rep >> 16) ^ (u32)sn[M]; PINS(lost); }
            if constexpr (i == 3) { lost |= ok[M] ^ 2u; rd = rep & 0xFFFFu; PINS(lost); PINS(rd); }
            if constexpr (i == 4) { lost = lost == 0u ? CH_UP * 8u : 0u; PINS(lost); }
            if constexpr (i == 5) { dn = wav >> 2; PINS(dn); pdur = min(pdur, dn + 8u); PINS(pdur); }
            if constexpr (i == 6) { pdur = (done & ok[M]) ? pdur : dn; PINS(pdur); }
            if constexpr (i == 7) { wav += pdur - dn; PINS(wav); }
            if constexpr (i == 8) { dh = hav >> 2; PINS(dh); nt = min(rd, dh + 8u); PINS(nt); }
            if constexpr (i == 9) { nt = rd < 0x3FFFu ? nt : dh; PINS(nt); }
            if constexpr (i == 10) { hav += nt - dh; PINS(hav); }
            if constexpr (i == 11) { mw = max(mw - CH_DEC, CH_MW * 8u); PINS(mw); }
            if constexpr (i == 12) { mh = max(mh - CH_DEC, CH_MH * 8u); PINS(mh); }
            if constexpr (i == 13) { rd = rd >> 14 == 1u ? lost : 0u; PINS(rd); }
            if constexpr (i == 14) { mw += rd; mh += lost - rd; PINS(mw); PINS(mh); }
            if constexpr (i == 15) { ok[M] = done ? 1u : (ok[M] & 1u) << 1; PINS(ok[M]); }
            if constexpr (i == 16) { dh = (2u * hav + mh) / 8u + (sn[1] < 2 ? CH_VIS + CH_SOLO : CH_VIS); PINS(dh); }
            if constexpr (i == 17) { dn = (2u * wav + mw) / 8u + CH_VIS; PINS(dn); }
            if constexpr (i == 18) { nt = dw[L] + dh; PINS(nt); }
            if constexpr (i == 19) { dh += dn; dw[L] = dn; sn[M] += (int)done; PINS(dh); PINS(sn[M]); }
            if constexpr (i == 20) { rd = tn - t + (dh >> 1); PINS(rd); }
            if constexpr (i == 21) { rd = sn[M] + 1 < NSEG ? rd : 0u; PINS(rd); }
            if constexpr (i == 22) { nt = t + (u32)max((int)nt, (int)rd); PINS(nt); }
            if constexpr (i == 23) { hw.x = nt; hw.y = dh; PIN(hw.x); PIN(hw.y); }
            if constexpr (i == 24) { hw.z = nt + dn; hw.w = 0u; PIN(hw); }
            if constexpr (i == 25) st16(rs, hso + L * (MB / 4), hw);
        };
        if (__builtin_expect(sc + 1 < NSEG, 1)) {
            u32 vo = l16 + (L * (MB / 2) + (u32)(sc & 3) * 131072u), mp = mpo + (L * (MB / 4) + (u32)((sc + 1) & 3) * 32768u), zb, hn;
            done = uu(pst) ^ 1u;
            za = done ? pmp : OOB;
            zb = ok[L] == 1u ? OOB : mp;
            pmp = mpo + (L * (MB / 4) + (u32)(sc & 3) * 32768u);
            *(LAS u32*)(sw + L * 8192) = zero;
            f32x4 acc[5] = {};
            asm volatile("" : "+v"(acc[0]), "+v"(acc[1]), "+v"(acc[2]), "+v"(acc[3]), "+v"(acc[4]), "+v"(mp), "+v"(pmp), "+v"(vo), "+v"(za), "+v"(zb));
            PINS(done);
#define WT(k) do {} while (0)
            v4u d[8];
            {
                u32 tmp;
                asm volatile(WAIT_TXT("%1", "%2") "buffer_load_dwordx4 %0, %3, %4, %5 offen sc1\n\ts_waitcnt lgkmcnt(0)"
                             : "=&v"(d[0]), "=&s"(tmp) : "s"(uu(t - 1u)), "v"(vo), "s"(srd), "s"(oo[0]) : "vcc", "scc", "memory");
            }
            WT(0);
            SFOR(i, 7)
                asm volatile("buffer_load_dwordx4 %0, %1, %2, %3 offen sc1" : "=v"(d[i + 1]) : "v"(vo), "s"(srd), "s"(oo[i + 1]) : "memory");
                SCHED();
                static_for<3 * i, 3 * i + 3>(books);
                SCHED();
            SEND
            LD4A(hn, hvo[L], 0);
            st4s(rs, za, 0, psent);
            st4s(rs, zb, 0, psent);
            SCHED();
            WT(1);
            float f0[5], f1[5], xs; unsigned long long bad; v4u sv;
#define WSW(o_) asm volatile("v_permlane32_swap_b32 %0, %1" : "+v"(f0[o_]), "+v"(f1[o_]))
#define WAD(o_) asm volatile("v_add_f32_dpp %0, %1, %0 row_ror:8 row_mask:0xf bank_mask:0xf\n\tv_add_f32_dpp %0, %2, %3 row_ror:8 row_mask:0xf bank_mask:0xc" : "+v"(f0[o_]) : "v"(f1[o_]), "v"(f0[o_ + 1]), "v"(f1[o_ + 1]))
#define WFO(o_) do { VADD(f0[o_], acc[o_][0], acc[o_][2]); VADD(f1[o_], acc[o_][1], acc[o_][3]); } while (0)
#define WOWN(k) do { if constexpr (WO_NI + (k) < 8) SMF_V(acc[4], d[(WO_NI + (k)) & 7], wo[(WO_NI + (k)) & 7][4]); SCHED(); } while (0)
            SFOR(i, 40)
                constexpr int pc = i / 5, oc = i % 5;
                if constexpr (oc == 0) VMW(10 - pc);
                if constexpr (oc < 4) SMF_A(acc[oc], d[pc], wo[pc][oc]); else if constexpr (pc < WO_NI) SMF_V(acc[4], d[pc], wo[pc][4]);
                if constexpr (i == 0) WT(2);
                if constexpr (i == 20) WT(3);
                if constexpr (i == 35) WT(4);
                if constexpr (i < 4) books(IC<21 + i>{});
                if constexpr (i == 6) { xs = 0.f; PIN(xs); }
                if constexpr (i == 36) NOPS(1);
                if constexpr (i == 37) NOPS(2);
                SCHED();
            SEND
            WFO(0); WFO(1);
            asm volatile("v_cmp_u_f32 %0, %1, %2" : "=s"(bad) : "v"(f0[0]), "v"(f1[0]));
            WSW(0); WSW(1); WFO(2); WAD(0); WFO(3);
            *(LAS float*)(xw0 + L * 8192) = f0[0]; SCHED();
            WSW(2); WSW(3);
            if (__builtin_expect(bad != 0ull, 0)) {
                u32 mn = ~0u;
#pragma unroll
                for (int i = 0; i < 8; ++i) { u32 e = d[i].x; PIN(e); mn = min(mn, e ^ PSENT); }
                if (all_(mn != 0u)) {
#pragma unroll
                    for (int i = 0; i < 8; ++i) { v4u e = d[i]; PIN(e); mn = min(min(mn, e.y ^ PSENT), min(e.z ^ PSENT, e.w ^ PSENT)); }
                }
                *(LAS u32*)(sw + L * 8192) = all_(mn != 0u) ? 0u : 1u;
            }
            WAD(2);
            *(LAS float*)(xw1 + L * 8192) = f0[2]; SCHED();
            WOWN(0); WOWN(1); WOWN(2); WOWN(3);
            WT(5);
            f32x4 xa; float nb; u32 cw, mq;
            asm volatile("s_waitcnt lgkmcnt(0)\n\ts_barrier\n\tds_read_b128 %0, %2 offset:%4\n\tds_read_b128 %1, %3 offset:%4+5120\n\ts_nop 3"
                         : "=&v"(xa), "=&v"(sv) : "v"(xr), "v"(zero), "i"(L * 8192) : "memory");
            WT(6);
            WFO(4);
            asm volatile("s_nop 0\n\tv_permlane32_swap_b32 %1, %2\n\ts_nop 1\n\tv_add_f32_dpp %0, %2, %1 row_ror:8 row_mask:0xf bank_mask:0x3\n\ts_waitcnt lgkmcnt(1)" : "+v"(xs), "+v"(f0[4]), "+v"(f1[4]));
            asm volatile("v_add_f32 %0, %0, %4\n\tv_add_f32 %1, %5, %6\n\tv_add_f32 %0, %0, %7\n\tv_add_f32 %0, %0, %1\n\tv_cmp_u_f32 vcc, %0, %0\n\ts_nop 0\n\t"
                         "v_mov_b32_dpp %1, %0 row_shl:4 row_mask:0xf bank_mask:0xf\n\tv_cndmask_b32 %3, %8, %9, vcc\n\tv_cvt_pk_bf16_f32 %2, %0, %1"
                         : "+v"(xs), "=&v"(nb), "=&v"(cw), "=&v"(mq) : "v"(xa[0]), "v"(xa[1]), "v"(xa[2]), "v"(xa[3]), "v"(mp), "v"(OOB) : "vcc");
            st4s(rs, mq, 0, cw);
            SCHED();
            WT(7);
            now = rt32();
            books(IC<25>{});
            asm volatile("s_waitcnt vmcnt(4) lgkmcnt(0)" : "+v"(hn), "+v"(sv));
            hv = hn;
            pst = sv.x | sv.y | sv.z | sv.w;
            if (__builtin_expect(__builtin_amdgcn_ballot_w64(mq != mp && pst == 0u) != 0ull, 0)) st4s(rs, mp, 0, cw);
        } else {
            done = uu(pst) ^ 1u;
            st4s(rs, done ? pmp : OOB, 0, psent);
            static_for<0, 25>(books);
            pst = 1;
            BAR();
            hv = ld4(rs, hvo[L]);
            PIN(hv);
            now = rt32();
        }
        pdur = now - t;
        const u32 per = rdl(hv, 1), th = rdl(hv, 0);
        t = lead ? tn : (int)(th - now) < -(int)(per >> 1) ? th + per : th;
        tn = nt;
        dw[M] += max((int)(now + CH_WX - t), 0);
        stop = stop || rdl(hv, 3) != 0u;
    };
#pragma unroll 1
    for (int k = 0; k < 2 * SPIN_CAP && !stop && (sn[0] + 1 < NSEG || sn[1] + 1 < NSEG); ++k) {
        turn(IC<0>{}); turn(IC<1>{});
        if (__builtin_expect(k != 0, 1)) continue;
        if (j == 0) {
            for (int n = 0; n < SPIN_CAP && !dead && sn[0] == 0; ++n) {
                CFENCE();
                if (all_(ld4(rs, W_ORING + pol0 * 8192u + (u32)w * 16384u + (u32)lane * 128u) != PSENT)) break;
                __builtin_amdgcn_s_sleep(1);
            }
            BAR();
        }
        if (lead) {
            t = rt32() + 56u; tn = t + PER0 / 2 + CH_REC;
            st16(rs, lane < 2 ? hint + (u32)lane * (MB / 4) : OOB, (v4u){lane == 0 ? t : tn, PER0, (lane == 0 ? t : tn) + (CH_W0 + CH_VIS + CH_MW + CH_REC), 0u});
        } else {
            for (int n = 0; n < SPIN_CAP && !dead; ++n) {
                CFENCE();
                const u32 hv = ld4(rs, hvo[1]);
                t = rdl(hv, 0);
                if (rdl(hv, 1) < 400u) break;
                __builtin_amdgcn_s_sleep(1);
            }
        }
        hso = (lead && lane == 0) ? hint : OOB;
        hv = 0xFFFFu; dw[0] = dw[1] = CH_W0 + CH_VIS + CH_MW + CH_REC;
    }
    if (sn[0] + 1 < NSEG || sn[1] + 1 < NSEG) st4s(rs, tid == 0 ? foff(F_ERR) : OOB, 0, 1u);
}

__global__ void __launch_bounds__(256, 1) k_main(Args args) {
    Params p;
    (Args&)p = args;
    lptr lds = (lptr)(u32)16 - 16;
    if (blockIdx.x == 0) LST(256, rt32());
    unsigned long long pc; int len;
    asm volatile("s_getpc_b64 %0\n.Lk_main_here:\n\ts_mov_b32 %1, .Lk_end_here-.Lk_main_here" : "=s"(pc), "=s"(len));
    const v4u code = ldin16(mk_rsrc((const void*)(pc & ~15ull), (u32)max(len, 0)), (((blockIdx.x >> 3) & 31u) * 256u + threadIdx.x) * 16u, 0);
    CFENCE();
    if (threadIdx.x == 0) {
        const v4u e = ld16s(mk_rsrc(p.ws, W_END), foff(F_EP), 0);
        const unsigned long long tag = (unsigned long long)e.y << 32 | (e.x + 1u);
        u32 role = ((u32)__builtin_amdgcn_s_getreg(20 | (3 << 11)) & 7u) * 32u + ((blockIdx.x >> 3) & 31u);
#pragma unroll 1
        for (int n = 0; n < 256; ++n, role = (role + 1u) & 255u)
            if (__hip_atomic_exchange((GAS unsigned long long*)(p.ws + W_SLOT) + role * 8u, tag, __ATOMIC_RELAXED, __HIP_MEMORY_SCOPE_AGENT) != tag) break;
        lds_w16(lds, (v4u){e.x + 1u, e.y, e.z, role});
    }
    __syncthreads();
    const v4u boot = lds_r16(lds);
    p.e0 = uu(boot.x); p.e1 = uu(boot.y); p.e2 = uu(boot.z);
    const int role = uni((int)boot.w), x = role >> 5, i = role & 31;
    asm volatile("" :: "v"(code));
    __syncthreads();
    LST(8192 + blockIdx.x * 16, role);
    if (x < 2) { role_head(p, lds, i >> 4, x * 16 + (i & 15)); __builtin_amdgcn_endpgm(); }
    if (x == 2) { role_wo(p, lds, i); __builtin_amdgcn_endpgm(); }
    const int no = x == 3 ? 30 : x < 6 ? 5 : 0;
    if (x > 3 && i >= 8) { role_qkv(p, lds, (x - 4) * 24 + i - 8); __builtin_amdgcn_endpgm(); }
    const bool isout = i < no;
    const int og = x == 3 ? i / 5 : x + 2, ng = i % 5, L = og >> 2, kq = og & 3;
    const int k = i - no, w = uni(threadIdx.x >> 6);
    const int j = isout ? (L == 1 ? 0 : 44) + kq * 5 + ng : x == 3 ? 36 + k : k < 2 ? 12 + 2 * x + k : x < 6 ? 24 + x : k < 5 ? 10 + 3 * x + k : 15 + 3 * x + k;
    const rsrc_t rs = mk_rsrc(p.ws, W_END);
    if (j < NKV) {
        const int nt = j < 320 - 7 * NKV ? 8 : 7;
#pragma unroll 1
        for (int q = 0; q < 4; ++q) kv_fetch(p, j + NKV * q, uu(q * KT_BYTES));
        Filt f;
        u32 warm[3];
#pragma unroll 1
        for (int q = 0; q < nt; ++q) {
            VMW(15); BAR();
            if (q == 0) EV(2);
            kv_fetch(p, j + NKV * (q + 4), uu(((q + 4) & 7) * KT_BYTES));
            kv_tile(p, rs, lds + q * KT_BYTES, lds + (q == 0 ? 8 : q - 1) * KT_BYTES, j + NKV * q);
        }
        filt_load(p, j, f);
        VMW(10); BAR();
        boot_set(p, rs, threadIdx.x == 0 ? foff(F_KV + j) : OOB, 2u);
        EV(4);
        { bool dead = false; boot_wait(p, rs, pol_off(), 0u, dead); }
#pragma unroll
        for (int q = 0; q < 3; ++q)
            warm[q] = __builtin_amdgcn_raw_buffer_load_b32(mk_rsrc(p.ssmk, ACT), 256u * 4096u + ((u32)((q * NKV + j) * 256) + threadIdx.x) * 64u, 0, 0);
        conv_job<0>(p, rs, lds, j, f, 0);
        asm volatile("" :: "v"(warm[0]), "v"(warm[1]), "v"(warm[2]));
        if (j >= 36) filt_load<5, 0>(p, 28 + j, f);
        DRAIN(); BAR();
        st4s(rs, threadIdx.x == 0 ? foff(F_CV + j) : OOB, 0, 2u);
        if (j >= 36) {
#pragma unroll 1
            for (int g = 28 + j; g < 160; g += 8) conv_job<2>(p, rs, lds, g, f, min(g + 8, 159));
            DRAIN(); BAR();
            st4s(rs, threadIdx.x == 0 ? foff(F_CV + 124 + j) : OOB, 0, 3u);
            EV(15);
            __builtin_amdgcn_endpgm();
        }
    }
    if (!isout) { if (j < 28) role_row<0>(p, lds, (j - 20) * 4 + w); else role_row<1>(p, lds, (j - 28) * 4 + w);
        boot_set(p, rs, j == 28 && threadIdx.x == 0 ? foff(F_EP) : OOB, 0u);
        __builtin_amdgcn_endpgm();
    }
    role_out(p, lds, L, kq, ng);
}

__global__ void k_end() { asm volatile(".Lk_end_here:"); }

extern "C" void kernel_launch(void* const* d_in, const int* in_sizes, int n_in, void* d_out, int out_size, void* d_ws, size_t ws_size,
                              hipStream_t stream) {
    Args p;
    p.x = (const u16*)d_in[0]; p.mem0 = (const u16*)d_in[1]; p.Wq = (const u16*)d_in[2]; p.Wk = (const u16*)d_in[3];
    p.Wv = (const u16*)d_in[4]; p.Wo = (const u16*)d_in[5];
    p.bk = (const u16*)d_in[8]; p.bv = (const u16*)d_in[9]; p.shift = (const u16*)d_in[10]; p.ssmk = (const u16*)d_in[11]; p.ssmD = (const u16*)d_in[12];
    p.out = (u16*)d_out;
    p.ws = (unsigned char*)(((size_t)d_ws + 2 * MB - 1) & ~(size_t)(2 * MB - 1));
    (void)hipFuncSetAttribute((const void*)k_main, hipFuncAttributeMaxDynamicSharedMemorySize, LDS_BYTES);
    hipLaunchKernelGGL(k_main, dim3(256), dim3(256), LDS_BYTES, stream, p);
}
```

```cpp
#include <hip/hip_runtime.h>
#include <hip/hip_bf16.h>

typedef unsigned short u16;
typedef unsigned int u32;
typedef u32 v2u __attribute__((ext_vector_type(2)));
typedef u32 v4u __attribute__((ext_vector_type(4)));
typedef u32 v8u __attribute__((ext_vector_type(8)));
typedef float f32x2 __attribute__((ext_vector_type(2)));
typedef float f32x4 __attribute__((ext_vector_type(4)));
typedef float f32x16 __attribute__((ext_vector_type(16)));
typedef __bf16 bf16x2 __attribute__((ext_vector_type(2)));
typedef __bf16 bf16x8 __attribute__((ext_vector_type(8)));
typedef short s16x4 __attribute__((ext_vector_type(4)));
#define LAS __attribute__((address_space(3)))
#define GAS __attribute__((address_space(1)))
typedef LAS unsigned char* lptr;
#define DEV __device__ __forceinline__

constexpr int SEQ = 2048, DM = 640, NH = 32, DH = 128, NSEG = 64, NQ = NH * DH;

constexpr u32 MB = 1u << 20;
constexpr u32 W_ORING = 0;
constexpr u32 W_MRING = MB;
constexpr u32 W_CTRL = 4 * MB;
constexpr u32 ACT = SEQ * DM * 2;
constexpr u32 H_TILE = 128 * 1024;
constexpr u32 W_H2 = 8 * MB;
constexpr u32 W_KQ = 16 * MB;
constexpr u32 W_VO = W_KQ + 64 * MB;
constexpr u32 W_P = W_VO + 64 * MB;
constexpr u32 P_ITEM = 512 * 1024;
constexpr u32 W_H1 = W_P + 44 * MB, W_Y = W_H1 + 8 * MB, W_X2 = W_Y + ACT;
constexpr u32 W_KVT = W_X2 + ACT;
constexpr u32 W_END = W_KVT + 3 * MB;
constexpr u32 NREG = 12;
static_assert((size_t)W_END + 2 * MB <= 256ull * MB && W_H1 >= (NREG - 1) * 16 * MB + 2 * MB && W_H1 <= NREG * 16 * MB, "workspace");
constexpr u32 F_EP = 0, F_RG = 32, F_LN = 64, F_KV = 192,
              F_QKV = 256, F_O = F_QKV + 192, F_P = F_O + 64, F_H2 = F_P + 64, F_CV = F_H2 + 64, F_ERR = F_CV + 191, F_N = F_CV + 192;
constexpr u32 W_SLOT = 3 * MB;
DEV u32 foff(u32 idx) { return W_CTRL + (idx >> 6) * 16384u + (idx & 63u) * 128u; }
DEV u32 near_(u32 o, u32 side, u32 pol) { return o + ((side ^ (pol >> (o >> 24)) ^ (o >> 21)) & 1u) * 8192u; }
DEV u32 p_item(int s) { return W_P + (u32)s * P_ITEM; }

constexpr u32 OOB = 0x80000000u;
constexpr u32 PSENT = 0x7F817F81u;
constexpr int SPIN_CAP = 40000;
constexpr int LDS_BYTES = 160 * 1024;
#define ST_HEAD 0
#define ST_WO 0

#define EV(k) do {} while (0)
#define LST(idx, v) do {} while (0)
#define LS2(cond, idx) do {} while (0)
struct Args {
    const u16 *x, *mem0, *Wq, *Wk, *Wv, *Wo, *bk, *bv, *shift, *ssmk, *ssmD;
    u16* out;
    unsigned char* ws;
};
struct Params : Args { u32 e0, e1, e2; };

typedef __amdgpu_buffer_rsrc_t rsrc_t;
DEV rsrc_t mk_rsrc(const void* p, u32 bytes) { return __builtin_amdgcn_make_buffer_rsrc((void*)p, 0, bytes, 0x00020000); }
DEV v4u ld16s(rsrc_t r, u32 off, u32 so) { return __builtin_amdgcn_raw_buffer_load_b128(r, off, so, 16); }
DEV v2u ld8s(rsrc_t r, u32 off, u32 so) { return __builtin_amdgcn_raw_buffer_load_b64(r, off, so, 16); }
DEV v4u ldin16(rsrc_t r, u32 off, u32 so) { return __builtin_amdgcn_raw_buffer_load_b128(r, off, so, 0); }
DEV v2u ldin8(rsrc_t r, u32 off, u32 so) { return __builtin_amdgcn_raw_buffer_load_b64(r, off, so, 0); }
DEV u32 ld4(rsrc_t r, u32 off) { return __builtin_amdgcn_raw_buffer_load_b32(r, off, 0, 16); }
DEV void st16(rsrc_t r, u32 off, v4u v) { __builtin_amdgcn_raw_buffer_store_b128(v, r, off, 0, 16); }
DEV void st8s(rsrc_t r, u32 off, u32 so, v2u v) { __builtin_amdgcn_raw_buffer_store_b64(v, r, off, so, 16); }
DEV void st4s(rsrc_t r, u32 off, u32 so, u32 v) { __builtin_amdgcn_raw_buffer_store_b32(v, r, off, so, 16); }
#define DRAIN() asm volatile("s_waitcnt vmcnt(0)" ::: "memory")
#define VMW(n) asm volatile("s_waitcnt vmcnt(%0)" :: "i"(n) : "memory")
#define BAR() asm volatile("s_waitcnt lgkmcnt(0)\n\ts_barrier" ::: "memory")
#define SCHED() __builtin_amdgcn_sched_barrier(0)
#define CFENCE() asm volatile("" ::: "memory")
#define NOPS(n) asm volatile("s_nop %0" :: "i"(n))
#define PIN(x) asm volatile("" : "+v"(x))
#define PINS(x) asm volatile("" : "+s"(x))

template <int V> struct IC { static constexpr int value = V; };
template <int I, int N, class F> DEV void static_for(F&& f) { if constexpr (I < N) { f(IC<I>{}); static_for<I + 1, N>(f); } }
#define SFOR(var, n) static_for<0, n>([&](auto ic_) __attribute__((always_inline)) { constexpr int var = decltype(ic_)::value;
#define SEND });

DEV u32 pk(float a, float b) { bf16x2 v = {(__bf16)a, (__bf16)b}; return __builtin_bit_cast(u32, v); }
DEV float lo_f(u32 u) { return __builtin_bit_cast(float, u << 16); }
DEV float hi_f(u32 u) { return __builtin_bit_cast(float, u & 0xffff0000u); }
DEV f32x2 up2(u32 u) { return (f32x2){lo_f(u), hi_f(u)}; }
DEV u32 pk2(f32x2 v) { return pk(v.x, v.y); }
DEV float rbf(float a) { return (float)(__bf16)a; }
DEV v4u lds_r16(lptr p) { return *(LAS v4u*)p; }
DEV void lds_w16(lptr p, v4u v) { *(LAS v4u*)p = v; }
DEV v2u lds_tr(lptr p) { return __builtin_bit_cast(v2u, __builtin_amdgcn_ds_read_tr16_b64_v4i16((LAS s16x4*)p)); }
DEV v4u tr2(lptr p0, lptr p1) { const v2u a = lds_tr(p0), b = lds_tr(p1); return (v4u){a.x, a.y, b.x, b.y}; }
DEV v8u cat8(v4u a, v4u b) { return __builtin_shufflevector(a, b, 0, 1, 2, 3, 4, 5, 6, 7); }
DEV f32x4 mfma16(v4u a, v4u b, f32x4 c) { return __builtin_amdgcn_mfma_f32_16x16x32_bf16(__builtin_bit_cast(bf16x8, a), __builtin_bit_cast(bf16x8, b), c, 0, 0, 0); }
DEV f32x16 mfma32(v4u a, v4u b, f32x16 c) { return __builtin_amdgcn_mfma_f32_32x32x16_bf16(__builtin_bit_cast(bf16x8, a), __builtin_bit_cast(bf16x8, b), c, 0, 0, 0); }

DEV float f_(u32 u) { return __builtin_bit_cast(float, u); }
DEV u32 u_(float f) { return __builtin_bit_cast(u32, f); }
template <int CTRL> DEV float dpp(float v) { return f_((u32)__builtin_amdgcn_update_dpp(0, (int)u_(v), CTRL, 0xf, 0xf, false)); }
DEV float max16(float v) { auto r = __builtin_amdgcn_permlane16_swap(u_(v), u_(v), false, false); return fmaxf(f_(r[0]), f_(r[1])); }
DEV float max32(float v) { auto r = __builtin_amdgcn_permlane32_swap(u_(v), u_(v), false, false); return fmaxf(f_(r[0]), f_(r[1])); }
DEV float add16(float v) { auto r = __builtin_amdgcn_permlane16_swap(u_(v), u_(v), false, false); return f_(r[0]) + f_(r[1]); }
DEV float add32(float v) { auto r = __builtin_amdgcn_permlane32_swap(u_(v), u_(v), false, false); return f_(r[0]) + f_(r[1]); }
DEV float wave_sum(float v) {
    v += dpp<0xB1>(v);
    v += dpp<0x4E>(v);
    v += dpp<0x124>(v);
    v += dpp<0x128>(v);
    return add32(add16(v));
}
DEV bool all_(bool c) { return __builtin_amdgcn_ballot_w64(!c) == 0ull; }
DEV int uni(int v) { return __builtin_amdgcn_readfirstlane(v); }
DEV u32 uu(u32 v) { return (u32)__builtin_amdgcn_readfirstlane((int)v); }
DEV u32 rdl(u32 v, int l) { return (u32)__builtin_amdgcn_readlane((int)v, l); }
DEV u32 rt32() { return (u32)__builtin_amdgcn_s_memrealtime(); }

DEV void wait_flags(rsrc_t rs, u32 off, u32 want, bool& dead) {
    if (dead) return;
#pragma unroll 1
    for (int n = 0;; ++n) {
        CFENCE();
        const u32 v = ld4(rs, off);
        if (all_(off >= OOB || v >= want)) break;
        __builtin_amdgcn_s_sleep(1);
        if ((n & 63) == 63) { if (uni((int)ld4(rs, foff(F_ERR))) != 0) { dead = true; break; } }
        if (n > SPIN_CAP) { st4s(rs, foff(F_ERR), 0, 1u); dead = true; break; }
    }
    CFENCE();
}

DEV bool boot_ok(const Params& p, u32 off, v4u v, u32 want) { return all_(off >= OOB || (v.x == p.e0 && v.y == p.e1 && v.z == p.e2 && v.w >= want)); }
DEV void boot_set(const Params& p, rsrc_t rs, u32 off, u32 v) { st16(rs, off, (v4u){p.e0, p.e1, p.e2, v}); }
DEV u32 boot_wait(const Params& p, rsrc_t rs, u32 off, u32 want, bool& dead) {
    v4u v;
#pragma unroll 1
    for (int n = 0;; ++n) {
        CFENCE();
        v = ld16s(rs, off, 0);
        if (boot_ok(p, off, v, want) || dead) break;
        __builtin_amdgcn_s_sleep(1);
        if (n > SPIN_CAP) { st4s(rs, foff(F_ERR), 0, 1u); dead = true; }
    }
    CFENCE();
    return v.w;
}

DEV u32 pol_off() { const int lane = threadIdx.x & 63; return lane < 32 ? foff(F_RG + lane) : OOB; }
DEV u32 pol_of(u32 raw) {
    const int lane = threadIdx.x & 63;
    const float v = add16(__builtin_amdgcn_fmed3f((float)((int)raw - (1 << 20)), -250.f, 250.f));
    const float lo = dpp<0x121>(v), hi = dpp<0x12F>(v), ex = dpp<0x122>(v) + dpp<0x123>(v) + dpp<0x124>(v);
    const float sum = v + lo + ((lane & 15) == 11 ? 0.f : hi) + ((lane & 15) == 0 ? ex : 0.f);
    return (u32)__builtin_amdgcn_ballot_w64(sum >= 0.f) & 0xFFFFu;
}
DEV u32 get_pol(const Params& p, rsrc_t rs, bool& dead) { return pol_of(boot_wait(p, rs, pol_off(), 0u, dead)); }
DEV u32 hblk(u32 base, int ls, int h, u32 side, u32 pol) { return near_(base + (u32)(ls * NH + h) * 16384u, side, pol); }
DEV f32x2 ln_stats(const f32x2 (&x)[5]) {
    const float c = f_(uu(u_(x[0].x)));
    f32x2 d[5];
#pragma unroll
    for (int i = 0; i < 5; ++i) d[i] = x[i] - c;
    const f32x2 s2 = (d[0] + d[1]) + (d[2] + d[3]) + d[4], q = (d[0] * d[0] + d[1] * d[1]) + (d[2] * d[2] + (d[3] * d[3] + d[4] * d[4]));
    const float dm = wave_sum(s2.x + s2.y) * (1.0f / DM), mean = c + dm;
    const float var = fmaxf(wave_sum(q.x + q.y) * (1.0f / DM) - dm * dm, 0.f);
    const float sd = rbf(__builtin_amdgcn_sqrtf(rbf(rbf(var) + 1e-5f)));
    return (f32x2){rbf(mean), __builtin_amdgcn_rcpf(sd)};
}
DEV void store_h(rsrc_t rs, u32 tile, int row, const u32 (&hp)[5]) {
    const u32 lane = threadIdx.x & 63;
    const u32 sw = (u32)(row & 15) * 16u, o8 = (lane >> 5) * 16384u + (((lane & 31u) * 8u) ^ sw), hb = tile + (u32)(row & 31) * 256u;
    st8s(rs, o8, hb, (v2u){hp[0], hp[1]});
    st8s(rs, o8, hb + 32768, (v2u){hp[2], hp[3]});
    st4s(rs, (lane * 4) ^ sw, hb + 65536, hp[4]);
}

#define DMA16(ldsb, vo, so) asm volatile("s_mov_b32 m0, %0\n\ts_nop 0\n\tbuffer_load_dwordx4 %1, %2, %3 offen sc1 lds" :: "s"(ldsb), "v"(vo), "s"(srd), "s"(so) : "memory")
#define DMA16I(ldsb, lo, vo, so, go) do { u32 so_; asm volatile("s_add_u32 m0, %1, %5\n\ts_add_u32 %0, %4, %6\n\tbuffer_load_dwordx4 %2, %3, %0 offen sc1 lds" \
                         : "=&s"(so_) : "s"(ldsb), "v"(vo), "s"(srd), "s"(so), "i"(lo), "i"(go) : "memory", "scc"); } while (0)
#define DMA32(ldsb, vo, so) asm volatile("s_mov_b32 m0, %0\n\ts_nop 0\n\tbuffer_load_dwordx4 %1, %2, %3 offen sc1 lds\n\tbuffer_load_dwordx4 %1, %2, %3 offen offset:1024 sc1 lds" \
                         :: "s"(ldsb), "v"(vo), "s"(srd), "s"(so) : "memory")
#define INPOL "nt"
#define XPOL ""
#define INAUX 2
#define DMANT(sr, ldsb, vo, so) asm volatile("s_mov_b32 m0, %0\n\ts_nop 0\n\tbuffer_load_dwordx4 %1, %2, %3 offen " INPOL " lds" :: "s"(ldsb), "v"(vo), "s"(sr), "s"(so) : "memory")
#define DMAIN(sr, ldsb, vo, so) asm volatile("s_mov_b32 m0, %0\n\ts_nop 0\n\tbuffer_load_dwordx4 %1, %2, %3 offen lds" :: "s"(ldsb), "v"(vo), "s"(sr), "s"(so) : "memory")
DEV const u16* sel(bool c, const u16* a, const u16* b) { PINS(a); PINS(b); return c ? a : b; }
DEV v4u mk_srd(const void* q, u32 bytes) { return (v4u){uu((u32)(size_t)q), uu((u32)((size_t)q >> 32) & 0xffffu), bytes, 0x00020000u}; }
#define MK_SRD() const v4u srd = mk_srd(p.ws, W_END)

constexpr int CV_PAD = 40;
constexpr int CV_PL = 4416;
constexpr int CV_F = 8 * CV_PL;
constexpr int CV_KB = 80;
constexpr int CV_KV = 65 * CV_KB;
constexpr int CV_CH = CV_F + CV_KV;
static_assert(CV_CH % 16 == 0 && 4 * CV_CH <= LDS_BYTES, "conv lds");

struct Filt { v4u a[5], b[5]; };
DEV int chan(int grp, int w) { return 16 * (grp >> 2) + (grp & 3) + 4 * w; }
template <int N = 5, int AUX = INAUX> DEV void filt_load(const Params& p, int grp, Filt& f) {
    const int tid = threadIdx.x, lane = tid & 63, d = chan(grp, tid >> 6);
    const rsrc_t rf = mk_rsrc(p.ssmk, ACT);
#pragma unroll
    for (int i = 0; i < N; ++i) {
        const u32 s0 = (u32)(8 * (lane + 64 * i) - CV_PAD);
        f.a[i] = __builtin_amdgcn_raw_buffer_load_b128(rf, s0 < SEQ ? (u32)(d * SEQ + s0) * 2u : OOB, 0, AUX);
        f.b[i] = __builtin_amdgcn_raw_buffer_load_b128(rf, s0 + 8 < SEQ ? (u32)(d * SEQ + s0 + 8) * 2u : OOB, 0, AUX);
    }
}
constexpr int KT_BYTES = 18 * 1024, KT_X = 64 * 144, NKV = 44, KT_DUMP = 8 * KT_BYTES + KT_X;
DEV void kv_fetch(const Params& p, int tile, u32 dst) {
    const int lane = threadIdx.x & 63, w = uni(threadIdx.x >> 6);
    const v4u sk = mk_srd(p.bk, ACT), sv = mk_srd(p.bv, ACT), ss = mk_srd(sel(w == 0, p.bk, p.shift), w == 0 ? ACT : DM * 8);
    const u32 vo = tile < 320 ? (u32)((lane >> 3) * DM + (tile % 10) * 64 + (lane & 7) * 8) * 2u : OOB, r0 = uu((u32)((tile / 10) * 64 + 8 * w) * (DM * 2)), d = uu(dst + w * 1024);
    DMANT(sk, d + 1024, vo, r0); DMANT(sk, d + 5120, vo, r0 + 32 * DM * 2); DMANT(sv, d + 9216, vo, r0); DMANT(sv, d + 13312, vo, r0 + 32 * DM * 2);
    DMAIN(ss, uu(w == 0 ? dst : w == 1 ? dst + 17408 : KT_DUMP), w == 0 ? (tile >= 10 ? vo : OOB) : w == 1 && lane < 32 ? (u32)lane * 16u : OOB,
          uu(w == 0 ? (tile >= 10 ? r0 - 8 * DM * 2 : 0u) : (u32)(tile % 10) * 512u));
}
DEV void kv_tile(const Params& p, rsrc_t rs, lptr src, lptr xt, int tile) {
    const int tid = threadIdx.x;
    const v4u sv = lds_r16(src + 17408 + (tid & 31) * 16);
    lptr kp = src + ((tid >> 5) * 8 + 5) * 128 + (tid & 31) * 4;
    f32x2 kf[11];
#pragma unroll
    for (int i = 0; i < 11; ++i) kf[i] = up2(*(LAS u32*)(kp + i * 128));
    const f32x2 sh[4] = {{lo_f(sv.x), lo_f(sv.z)}, {hi_f(sv.x), hi_f(sv.z)}, {lo_f(sv.y), lo_f(sv.w)}, {hi_f(sv.y), hi_f(sv.w)}};
    u32 kv[8];
#pragma unroll
    for (int i = 0; i < 8; ++i) {
        const f32x2 acc = (kf[i] * sh[3] + kf[i + 1] * sh[2]) + (kf[i + 2] * sh[1] + kf[i + 3] * sh[0]);
        kv[i] = pk2(acc * up2(*(LAS u32*)(kp + 9216 + (i - 5) * 128)));
    }
    lds_w16(xt + (2 * (tid & 31)) * 144 + (tid >> 5) * 16, (v4u){__builtin_amdgcn_perm(kv[6], kv[7], 0x05040100u), __builtin_amdgcn_perm(kv[4], kv[5], 0x05040100u),
                                                             __builtin_amdgcn_perm(kv[2], kv[3], 0x05040100u), __builtin_amdgcn_perm(kv[0], kv[1], 0x05040100u)});
    lds_w16(xt + (2 * (tid & 31) + 1) * 144 + (tid >> 5) * 16, (v4u){__builtin_amdgcn_perm(kv[6], kv[7], 0x07060302u), __builtin_amdgcn_perm(kv[4], kv[5], 0x07060302u),
                                                                 __builtin_amdgcn_perm(kv[2], kv[3], 0x07060302u), __builtin_amdgcn_perm(kv[0], kv[1], 0x07060302u)});
    BAR();
#pragma unroll
    for (int i = 0; i < 2; ++i) {
        const int ch = i * 32 + (tid >> 3);
        st16(rs, W_KVT + (u32)(((tile % 10) * 64 + ch) * SEQ + (tile / 10) * 64 + (tid & 7) * 8) * 2u, lds_r16(xt + ch * 144 + (tid & 7) * 16));
    }
}
template <int MODE> DEV void conv_job(const Params& p, rsrc_t rs, lptr lds, int grp, Filt& f, int next) {
    const int tid = threadIdx.x, lane = tid & 63, w = uni(tid >> 6), bq = lane & 31, hh = lane >> 5;
    lptr F = lds + w * CV_CH;
    lptr fa = F + (bq & 7) * CV_PL + ((bq >> 3) - hh + 4) * 16;
    lptr kb = F + CV_F + CV_KB + hh * 16, kh = kb + (bq + 32) * CV_KB;
    bool dead = false;
    v4u kvr[4];
    const u32 D = p.ssmD[chan(grp, w)];
    auto kv_load = [&]() {
        if (MODE == 0) boot_wait(p, rs, lane < NKV ? foff(F_KV + lane) : OOB, 2u, dead);
#pragma unroll
        for (int i = 0; i < (MODE == 1 ? 2 : 4); ++i) kvr[i] = ld16s(rs, (u32)lane * 16u, W_KVT + (u32)(chan(grp, w) * SEQ + i * 512) * 2u);
    };
    auto windows = [&](int i) {
        if (i < 4 || lane < 16) {
            const u32 e[8] = {f.a[i].x, f.a[i].y, f.a[i].z, f.a[i].w, f.b[i].x, f.b[i].y, f.b[i].z, f.b[i].w};
            u32 o[7];
#pragma unroll
            for (int q = 0; q < 7; ++q) o[q] = __builtin_amdgcn_alignbit(e[q + 1], e[q], 16);
#pragma unroll
            for (int q = 0; q < 4; ++q) {
                lds_w16(F + (2 * q) * CV_PL + (64 * i + lane) * 16, (v4u){o[q], o[q + 1], o[q + 2], o[q + 3]});
                lds_w16(F + (2 * q + 1) * CV_PL + (64 * i + lane) * 16, (v4u){e[q + 1], e[q + 2], e[q + 3], e[q + 4]});
            }
        }
    };
    lptr yw = lds + (lane >> 4) * CV_CH + 3072 + (lane & 14) * 64 + w * 32 + (lane & 1) * 16, yr = F + 3072 + lane * 16;
    const u32 yo = W_Y + (u32)((((w & 1) * 8 + (lane >> 3)) * 8 + (w >> 1)) * DM + (grp & 3) * 160 + 4 * (grp >> 2)) * 32u + (u32)(lane & 7) * 16u;
    auto put = [&](const f32x16& ac, int q) {
        const int r = 4 * (q & 3);
        const auto x = __builtin_amdgcn_permlane32_swap(pk(ac[r], ac[r + 1]), pk(ac[r + 4], ac[r + 5]), false, false);
        const auto y = __builtin_amdgcn_permlane32_swap(pk(ac[r + 2], ac[r + 3]), pk(ac[r + 6], ac[r + 7]), false, false);
        lds_w16(yw + (q >> 1) * CV_PL, (v4u){x[0], y[0], x[1], y[1]});
    };
    if (MODE != 2) EV(8);
    if (MODE != 0) kv_load();
    if (lane < CV_KB / 16) lds_w16(F + CV_F + lane * 16, (v4u){0u, 0u, 0u, 0u});
    windows(0);
    if (MODE != 1) { windows(1); windows(2); windows(4); }
    if (MODE == 0) kv_load();
    if (MODE == 2) windows(3);
    if (MODE != 2) EV(9);
#pragma unroll
    for (int i = 0; i < (MODE == 1 ? 2 : 4); ++i) lds_w16(F + CV_F + (16 * i + (lane >> 2) + 1) * CV_KB + (lane & 3) * 16, kvr[i]);
    v4u dg[2];
#pragma unroll
    for (int ks = 0; ks < 2; ++ks)
#pragma unroll
        for (int q = 0; q < 4; ++q) { const int j = 16 * ks + 8 * hh + 7 - bq; dg[ks][q] = j == 2 * q ? D : j == 2 * q + 1 ? D << 16 : 0u; }
    f32x16 acc0 = {}, acc1 = {};
    auto first = [&]() { asm volatile("s_waitcnt vmcnt(0)\n\ts_barrier" ::: "memory"); st4s(rs, tid == 0 ? foff(F_CV + grp) : OOB, 0, 1u); EV(12); };
    if (MODE != 2) {
        EV(10);
        acc0 = mfma32(lds_r16(kb + bq * CV_KB), dg[0], acc0); acc0 = mfma32(lds_r16(kb + bq * CV_KB + 32), dg[1], acc0);
#pragma unroll
        for (int m = 0; m < 8; ++m) {
            lptr kl = kb + max(bq - m, -1) * CV_KB;
            acc0 = mfma32(lds_r16(kl), lds_r16(fa + m * 64), acc0); acc0 = mfma32(lds_r16(kl + 32), lds_r16(fa + m * 64 - 32), acc0);
        }
        put(acc0, 0);
        BAR();
        st16(rs, w < 2 ? yo : OOB, lds_r16(yr));
        EV(11);
    }
    if (MODE == 1) {
        windows(1); windows(2);
        first();
        v4u a[2][2], l[2][2];
#pragma unroll
        for (int u = 0; u < 2; ++u) { lptr kl = kb + max(bq - 8 - u, -1) * CV_KB; a[u][0] = lds_r16(fa + (8 + u) * 64); l[u][0] = lds_r16(kl); a[u][1] = lds_r16(fa + (8 + u) * 64 - 32); l[u][1] = lds_r16(kl + 32); }
#pragma unroll 1
        for (int m = 8; m < 32; m += 2) {
#pragma unroll
            for (int u = 0; u < 2; ++u) {
                lptr kl = kb + max(bq - m - u - 2, -1) * CV_KB;
                acc0 = mfma32(l[u][0], a[u][0], acc0); a[u][0] = lds_r16(fa + (m + u + 2) * 64); l[u][0] = lds_r16(kl); SCHED();
                acc0 = mfma32(l[u][1], a[u][1], acc0); a[u][1] = lds_r16(fa + (m + u + 2) * 64 - 32); l[u][1] = lds_r16(kl + 32); SCHED();
            }
        }
    } else {
        if (MODE == 0) windows(3); else filt_load<5, 0>(p, next, f);
        v4u a0 = lds_r16(fa), a1 = lds_r16(fa - 32), c0 = lds_r16(fa + 2048), c1 = lds_r16(fa + 2048 - 32);
        v4u h0 = lds_r16(kh), h1 = lds_r16(kh + 32), l0 = lds_r16(kb + bq * CV_KB), l1 = lds_r16(kb + bq * CV_KB + 32);
        acc1 = mfma32(h0, dg[0], acc1); acc1 = mfma32(h1, dg[1], acc1);
#pragma unroll 2
        for (int m = 0; m < (MODE == 0 ? 8 : 32); ++m) {
            lptr kl = kb + max(bq - m - 1, -1) * CV_KB;
            if (MODE == 0 && m == 4) first();
            acc1 = mfma32(h0, a0, acc1); h0 = lds_r16(kh - (m + 1) * CV_KB); a0 = lds_r16(fa + (m + 1) * 64); SCHED();
            acc1 = mfma32(l0, c0, acc1); c0 = lds_r16(fa + (m + 1) * 64 + 2048); l0 = lds_r16(kl); SCHED();
            acc1 = mfma32(h1, a1, acc1); h1 = lds_r16(kh - (m + 1) * CV_KB + 32); a1 = lds_r16(fa + (m + 1) * 64 - 32); SCHED();
            acc1 = mfma32(l1, c1, acc1); c1 = lds_r16(fa + (m + 1) * 64 + 2048 - 32); l1 = lds_r16(kl + 32); SCHED();
        }
        if (MODE == 0) {
#pragma unroll 2
            for (int m = 8; m < 32; ++m) {
                lptr kl = kb + max(bq - m - 1, -1) * CV_KB;
                acc1 = mfma32(h0, a0, acc1); h0 = lds_r16(kh - (m + 1) * CV_KB); SCHED();
                acc0 = mfma32(l0, a0, acc0); a0 = lds_r16(fa + (m + 1) * 64); SCHED();
                acc1 = mfma32(l0, c0, acc1); c0 = lds_r16(fa + (m + 1) * 64 + 2048); l0 = lds_r16(kl); SCHED();
                acc1 = mfma32(h1, a1, acc1); h1 = lds_r16(kh - (m + 1) * CV_KB + 32); SCHED();
                acc0 = mfma32(l1, a1, acc0); a1 = lds_r16(fa + (m + 1) * 64 - 32); SCHED();
                acc1 = mfma32(l1, c1, acc1); c1 = lds_r16(fa + (m + 1) * 64 + 2048 - 32); l1 = lds_r16(kl + 32); SCHED();
            }
        }
        BAR();
    }
    if (MODE != 2) { EV(13); put(acc0, 0); put(acc0, 2); }
    if (MODE != 1) { put(acc1, 4); put(acc1, 6); }
    BAR();
#pragma unroll
    for (int q = (MODE == 2 ? 2 : 0); q < (MODE == 1 ? 2 : 4); ++q) st16(rs, q == 0 && w < 2 ? OOB : yo + (u32)q * (2 * DM * 32), lds_r16(yr + q * CV_PL));
    BAR();
    if (MODE != 2) EV(14);
}

#define NPROBE 6
DEV int probe(rsrc_t rs, int c) {
    const int lane = threadIdx.x & 63;
    const u32 k = (u32)(c & 15) < NREG ? (u32)(c & 15) : 0u;
    const u32 off0 = k * (16 * MB) + 2 * MB - 65536 + ((u32)(c & 15) >= NREG ? (u32)(c & 3) + 1u : 0u) * 256 + (u32)(c >> 4) * 128;
    st4s(rs, lane == 0 ? off0 : OOB, 0, 0u); DRAIN();
    int d0 = 1 << 16, d1 = 1 << 16;
#pragma unroll 1
    for (int q = 0; q < NPROBE; ++q) {
        const u32 o = lane == 0 ? off0 + (u32)(q & 1) * 8192 + (u32)(q >> 1) * 18432 : OOB;
        const unsigned long long t0 = __builtin_amdgcn_s_memtime();
        st4s(rs, o, 0, 0u); DRAIN();
        const int dt = (int)(__builtin_amdgcn_s_memtime() - t0);
        if (q & 1) d1 = min(d1, dt); else d0 = min(d0, dt);
    }
    const u32 xcc = (u32)__builtin_amdgcn_s_getreg(20 | (3 << 11)) & 0xfu;
    return (d0 - d1) * ((xcc & 4u) ? -1 : 1);
}
DEV void pre_rings(const Params& p, rsrc_t rs, int j) {
    const u32 gt = (u32)(j * 256 + threadIdx.x) * 16u, n = ((gt >> 14) << 9) + ((gt & 8191u) >> 4);
    const v4u ps = {PSENT, PSENT, PSENT, PSENT};
    v4u m = ps;
    if (n < 640u) m = *(const v4u*)(p.mem0 + (2 * ((n >> 2) & 3) + (n & 1)) * DM + (n >> 4) * 16 + (n & 2) * 4);
#pragma unroll
    for (u32 i = 0; i < 8; ++i) st16(rs, W_ORING + gt + i * 131072u, ps);
#pragma unroll
    for (u32 i = 0; i < 2; ++i) st16(rs, gt < 16384u ? W_MRING + i * (MB / 4) + 131072u + gt : OOB, ps);
    if (n < 640u) {
        m = (v4u){__builtin_amdgcn_perm(m.z, m.x, 0x05040100u), __builtin_amdgcn_perm(m.z, m.x, 0x07060302u), __builtin_amdgcn_perm(m.w, m.y, 0x05040100u), __builtin_amdgcn_perm(m.w, m.y, 0x07060302u)};
#pragma unroll
        for (int i = 0; i < 4; ++i) m[i] = m[i] == PSENT ? 0x7FC17FC1u : m[i];
    }
    st16(rs, W_MRING + gt, m);
    st16(rs, W_MRING + MB / 4 + gt, m);
    static_assert(2 * (F_N - F_QKV) == 32 * 36, "clear");
    st4s(rs, threadIdx.x < 36 ? foff(F_QKV) + (u32)(j * 36 + threadIdx.x) * 128u : OOB, 0, 0u);
}

#define M32A(first, d, a, b) do { if (first) asm volatile("v_mfma_f32_32x32x16_bf16 %0, %1, %2, 0" : "=&v"(d) : "a"(a), "v"(b)); \
                                  else asm volatile("v_mfma_f32_32x32x16_bf16 %0, %1, %2, %0" : "+v"(d) : "a"(a), "v"(b)); } while (0)
#define M32B(first, d, a, b) do { if (first) asm volatile("v_mfma_f32_32x32x16_bf16 %0, %1, %2, 0" : "=&v"(d) : "v"(a), "a"(b)); \
                                  else asm volatile("v_mfma_f32_32x32x16_bf16 %0, %1, %2, %0" : "+v"(d) : "v"(a), "a"(b)); } while (0)
#define VCVT(d, a, b) asm volatile("v_cvt_pk_bf16_f32 %0, %1, %2" : "=v"(d) : "v"(a), "v"(b))
#define LD4A(d, vo, so) asm volatile("buffer_load_dword %0, %1, %2, %3 offen sc1" : "=v"(d) : "v"(vo), "s"(srd), "s"(so) : "memory")
DEV u32 wst_src(int lane, int ld) { return (u32)((lane >> 4) * ld + 8 * ((lane & 15) ^ ((lane >> 4) << 2))) * 2u; }
DEV u32 wst_rd(int lane, int w) { const int q4 = (lane >> 2) & 3; return (u32)(q4 * 256 + ((w ^ q4) * 64) + ((lane >> 4) & 1) * 32 + (lane & 3) * 8); }

constexpr int QK_A = 65536;
#define QLEAD 4
#define QFIRST 3
#define QW0 10
#define QW1 3
#define QSAMP 72
DEV void role_qkv(const Params& p, lptr lds, int c) {
    const int grp = 64 + c;
    const int tid = threadIdx.x, lane = tid & 63, w = uni(tid >> 6), hh = lane >> 5, r32 = lane & 31;
    const int m = c >> 5, h = c & 31;
    const rsrc_t rs = mk_rsrc(p.ws, W_END);
    v4u wt[40];
    Filt flt;
    bool dead = false;
    u32 pol, h2flag, fo, ol, ar[8];
    const u32 ds = uu(w * 1024), dx = ds + QK_A;
#define XRD(ks) lds_r16(lds + ar[(ks) & 7] + ((ks) >> 3) * 8192)
    const u32 t16 = (u32)tid * 16u, lnf = tid < 96 ? foff(F_LN + tid) : OOB;
    MK_SRD();
#define QDMA(i, dst, vo, so) DMA16I(dst, (i) * 4096, vo, so, ((i) >> 1) * 16384 + ((i) & 1) * 4096)
    {
        const v4u sw = mk_srd(sel(m == 0, p.Wq, sel(m == 1, p.Wk, p.Wv)), DM * NQ * 2), sx = mk_srd(p.x, ACT);
        const u32 vo = wst_src(lane, NQ) + (u32)h * (DH * 2), x8 = (u32)lane * 8u, x4 = (u32)lane * 4u;
        v2u xa[6], xb[6]; u32 xc[6];
#pragma unroll
        for (int i = 0; i < 6; ++i)
            asm volatile("buffer_load_dwordx2 %0, %3, %5, %6 offen " XPOL "\n\tbuffer_load_dwordx2 %1, %3, %5, %6 offen offset:512 " XPOL "\n\tbuffer_load_dword %2, %4, %5, %6 offen offset:1024 " XPOL
                         : "=&v"(xa[i]), "=&v"(xb[i]), "=&v"(xc[i]) : "v"(x8), "v"(x4), "s"(sx), "s"(uu((4 * c + w + 384 * i) * (DM * 2))) : "memory");
        EV(1);
        auto wfill = [&](int i0, int n) {
#pragma unroll 1
            for (int i = i0; i < i0 + n; ++i) DMAIN(sw, uu((4 * i + w) * 1024), vo, uu((16 * i + 4 * w) * (NQ * 2)));
        };
        wfill(0, QW0);
        asm volatile("s_waitcnt vmcnt(%0) ; HZCLR" :: "i"(QW0) : "memory");
        EV(2);
#pragma unroll
        for (int i = 0; i < 6; ++i) {
            wfill(QW0 + QW1 * i, QW1);
            if (i == 2) { VMW(2 * QW1 + 3); BAR(); boot_set(p, rs, tid == 0 ? foff(F_LN + c) : OOB, 1u); }
            asm volatile("" : "+v"(xa[i]), "+v"(xb[i]), "+v"(xc[i]));
            const int row = 4 * c + w + 384 * i;
            if (i < 5 || row < SEQ) {
                const f32x2 x[5] = {up2(xa[i].x), up2(xa[i].y), up2(xb[i].x), up2(xb[i].y), up2(xc[i])};
                const f32x2 st = ln_stats(x);
                u32 hp[5];
#pragma unroll
                for (int q = 0; q < 5; ++q) hp[q] = pk2(up2(pk2(x[q] - st.x)) * st.y);
                store_h(rs, W_H1 + (u32)(row >> 5) * H_TILE, row, hp);
            }
        }
        EV(3);
        constexpr int K0 = QW0 + 6 * QW1;
        static_assert(K0 * 4096 >= QK_A + 40960, "the first tile's place is free");
        v4u fv, praw;
        asm volatile("buffer_load_dwordx4 %0, %2, %4, 0 offen sc1\n\tbuffer_load_dwordx4 %1, %3, %4, 0 offen sc1" : "=&v"(praw), "=&v"(fv) : "v"(pol_off()), "v"(lnf), "s"(srd) : "memory");
        wfill(K0, 40 - K0);
        asm volatile("s_waitcnt vmcnt(%2) ; HZCLR\n\ts_barrier" : "+v"(praw), "+v"(fv) : "i"(40 - K0) : "memory");
        boot_set(p, rs, tid == 0 ? foff(F_LN + c) : OOB, 2u);
        EV(4);
        lptr tb = lds + hh * 2048 + wst_rd(lane, w);
        SFOR(k, 40)
            if constexpr (k == K0) {
                if (!boot_ok(p, lnf, fv, 1u)) boot_wait(p, rs, lnf, 1u, dead);
                if (!boot_ok(p, pol_off(), praw, 0u)) praw.w = boot_wait(p, rs, pol_off(), 0u, dead);
                BAR();
                EV(5);
                const u32 so = W_H1;
                SFOR(i, 10) QDMA(i, dx, t16, so); SEND
                pol = pol_of(praw.w);
                h2flag = tid < 8 ? near_(foff(F_H2 + lane), 1u, pol) : OOB;
                ol = (m == 2 ? (u32)(2 * w + (r32 >> 4)) * 1024u : (u32)((r32 >> 4) * 4 + w) * 1024u) + (u32)(hh * 16 + (r32 & 15)) * 16u;
                fo = near_(foff(F_QKV + m * 64 + h), 0u, pol);
#pragma unroll
                for (int j = 0; j < 8; ++j) { ar[j] = (u32)(QK_A + r32 * 256 + ((2 * j + hh) ^ (r32 & 15)) * 16); PIN(ar[j]); }
                PIN(h2flag); PIN(ol); PINS(fo); PINS(pol);
                VMW(10); BAR();
            }
            lptr q = tb + (k >> 3) * 32768 + (k & 7) * 4096; wt[k] = tr2(q, q + 1024); asm volatile("" : "+a"(wt[k]));
        SEND
    }
    EV(6);
    int n0 = -1, n1 = -QLEAD;
    f32x16 a0 = {};
    const u32 ksc = m == 1 ? 0x3e0293eeu : 0x3f800000u;
    const u32 bfo = tid < 96 ? lnf : tid - 128u < NKV ? foff(F_KV - 128 + tid) : OOB;
    v4u brk = {};
    auto run = [&](auto isv) __attribute__((always_inline)) {
      constexpr bool V = decltype(isv)::value;
      v4u x[8];
      u32 fv = 0, cvo = n1 > -QLEAD && tid == 194 ? foff(F_CV + grp) : OOB;
#pragma unroll
      for (int ks = 20; ks < 25; ++ks) x[ks & 7] = XRD(ks);
#define QS(k) do {} while (0)
#define QPK1(d, a, a_, q_) do { if constexpr (V) VCVT(d[q_], a[4 * (a_) + 8 * ((q_) >> 1) + 2 * ((q_) & 1)], a[4 * (a_) + 8 * ((q_) >> 1) + 2 * ((q_) & 1) + 1]); else VCVT(d[q_], a[4 * (a_) + (q_)], a[4 * (a_) + (q_) + 8]); } while (0)
#define QSC(a, a_) do { if constexpr (!V) { _Pragma("unroll") for (int q_ = 0; q_ < 8; ++q_) asm volatile("v_mul_f32 %0, %1, %0" : "+v"(a[4 * (a_) + (q_ & 3) + 2 * (q_ & 4)]) : "s"(ksc)); } } while (0)
#define QPK(d, a, a_) do { QPK1(d, a, a_, 0); QPK1(d, a, a_, 1); QPK1(d, a, a_, 2); QPK1(d, a, a_, 3); } while (0)
#pragma unroll 1
      for (const int end = n1 == -QLEAD ? QFIRST - QLEAD : NSEG; n1 < end; ++n1) {
        const int cur = n1 < 0 ? n0 + 1 : 64 | n1, nxt = n1 < 0 ? n0 + 2 : n0 + 1;
        u32 xs = n1 < 0 ? W_H1 + (u32)cur * H_TILE : near_(W_H2 + (u32)n1 * H_TILE, 1u, pol);
        PINS(xs);
        QS(0);
        if (w == 0 && n1 >= 0 && ((u32)__builtin_amdgcn_ballot_w64(fv <= (u32)n1) & 255u) != 0u) wait_flags(rs, h2flag, (u32)n1 + 1, dead);
        BAR();
        QS(1);
        f32x16 a1;
        u32 ss, sv, hb, oa, qfo, qfv;
        v4u pp, pq;
        SCHED();
        auto slot = [&](auto zc) __attribute__((always_inline)) {
            constexpr int z = decltype(zc)::value, ks = (z + 20) % 40;
            if constexpr (z < 20) { if constexpr (V) M32B(false, a0, x[ks & 7], wt[ks]); else M32A(false, a0, wt[ks], x[ks & 7]); }
            else if constexpr (z < 60) { if constexpr (V) M32B(z == 20, a1, x[ks & 7], wt[ks]); else M32A(z == 20, a1, wt[ks], x[ks & 7]); }
            else if constexpr (z == 60) {
                if constexpr (V) asm volatile("v_mfma_f32_32x32x16_bf16 %0, %1, %2, 0" : "+v"(a0) : "v"(x[ks & 7]), "a"(wt[ks]));
                else asm volatile("v_mfma_f32_32x32x16_bf16 %0, %1, %2, 0" : "+v"(a0) : "a"(wt[ks]), "v"(x[ks & 7]));
            } else { if constexpr (V) M32B(false, a0, x[ks & 7], wt[ks]); else M32A(false, a0, wt[ks], x[ks & 7]); }
            if constexpr (z == 18) { QS(2); asm volatile("s_waitcnt vmcnt(6) lgkmcnt(0)\n\ts_barrier" ::: "memory"); QS(3); }
            if constexpr (z < 15 || z > 20) x[(ks + 5) & 7] = XRD((ks + 5) % 40);
            if constexpr (z >= 18 && z <= 20) { x[2 * (z - 18)] = XRD(2 * (z - 18)); x[2 * (z - 18) + 1] = XRD(2 * (z - 18) + 1); }
            SCHED();
            if constexpr (z < 4) QDMA(z, dx, t16, xs);
            if constexpr (z >= 5 && z < 17 && (z & 1)) QDMA((z + 3) >> 1, dx, t16, xs);
            if constexpr ((z >= 7 && z < 15) || (z >= 47 && z < 55)) asm volatile("v_xor_b32 %0, 0x10000, %0" : "+v"(ar[(ks + 5) & 7]));
            if constexpr (z == 16) { ss = W_H1 + (u32)nxt * H_TILE; PINS(ss); }
            if constexpr (z == 17) { sv = t16 | (nxt >= NSEG ? OOB : 0u); PIN(sv); }
            if constexpr (z >= 21 && z < 41 && (z & 1)) QDMA((z - 21) >> 1, ds, sv, ss);
            if constexpr (z == 22) QSC(a0, 0);
            if constexpr (z == 24) QPK(pp, a0, 0);
            if constexpr (z == 26) QSC(a0, 1);
            if constexpr (z == 28) QPK(pq, a0, 1);
            if constexpr (z == 30) { QS(4); asm volatile("s_waitcnt vmcnt(5)\n\ts_barrier" ::: "memory"); QS(5); }
            if constexpr (z == 32 || z == 44) { hb = (m == 2 ? W_VO : W_KQ) + (u32)((z == 32 ? n0 : cur) * NH + h) * 16384u; PINS(hb); }
            if constexpr (z == 34 || z == 45) { hb = near_(hb, m == 1 ? 0u : 1u, pol); PINS(hb); }
            if constexpr (z == 36) { hb = (u32)n0 < NSEG ? hb : OOB; PINS(hb); }
            if constexpr (z == 38 || z == 46) { oa = ol + hb; PIN(oa); }
            if constexpr (z == 41) st16(rs, oa, pp);
            if constexpr (z == 43) st16(rs, oa + 512, pq);
            if constexpr (z == 54) { QS(6); asm volatile("s_waitcnt vmcnt(4)\n\ts_barrier" ::: "memory"); QS(7); }
            if constexpr (z == 56) { if (__builtin_expect(n1 == QFIRST - QLEAD - 1, 0)) { filt_load<3, 0>(p, grp, flt); brk = ld16s(rs, bfo, 0); } }
            if constexpr (z == 60) NOPS(5);
            if constexpr (z == 61) { QSC(a1, 0); QPK(pp, a1, 0); st16(rs, oa, pp); }
            if constexpr (z == 62) { QSC(a1, 1); QPK(pq, a1, 1); st16(rs, oa + 512, pq); }
            if constexpr (z == 64) { qfo = tid == 192 ? fo : tid == 193 && n1 >= 0 ? fo + 32 * 128 : cvo; PIN(qfo); }
            if constexpr (z == 66) { qfv = tid == 192 ? (u32)min(nxt, NSEG) : tid == 193 ? (u32)n1 + 1u : 2u; PIN(qfv); }
            if constexpr (z == QSAMP) LD4A(fv, h2flag, 0);
            SCHED();
        };
        if (__builtin_expect(n0 >= 0, 1)) static_for<0, 16>(slot);
        static_for<16, 80>(slot);
        QS(8);
        asm volatile("s_waitcnt vmcnt(1)\n\ts_barrier" ::: "memory");
        st4s(rs, qfo, 0, qfv);
        QS(9);
        asm volatile("s_waitcnt vmcnt(1)" : "+v"(fv) :: "memory");
        n0 = nxt; cvo = OOB;
      }
      NOPS(7);
    };
#pragma unroll 1
    for (;;) {
        if (m == 2) run(IC<1>{}); else run(IC<0>{});
        if (n1 > 0) break;
        if (!boot_ok(p, bfo, brk, 2u)) boot_wait(p, rs, bfo, 2u, dead);
        BAR();
        conv_job<1>(p, rs, lds, grp, flt, 0);
        const u32 so = uu(W_H1 + (u32)n0 * H_TILE);
        SFOR(i, 6) QDMA(i + 4, ds, t16, so); SEND
        DRAIN(); BAR();
    }
}

constexpr int OU_SEL = 131072;

DEV void role_out(const Params& p, lptr lds, int L, int kq, int ng) {
    const int tid = threadIdx.x, lane = tid & 63, w = uni(tid >> 6), hh = lane >> 5, r32 = lane & 31;
    const rsrc_t rs = mk_rsrc(p.ws, W_END);
    v4u wo[64];
    Filt flt;
    bool dead = false;
    {
        const v4u sw = mk_srd(p.Wo, DM * NQ * 2);
        const u32 vo = wst_src(lane, DM) + (ng == 4 ? 1024u : (u32)((ng >> 1) * 512 + (ng & 1) * 128 + ((lane ^ (lane >> 2)) & 8) * 16));
        auto fill = [&](int b) {
#pragma unroll 1
            for (int i = 0; i < 8; ++i) DMAIN(sw, uu((b % 5) * 32768 + (4 * i + w) * 1024), vo, uu((kq * 1024 + b * 128 + 16 * i + 4 * w) * (DM * 2)));
        };
#pragma unroll 1
        for (int b = 0; b < 5; ++b) fill(b);
        lptr tb = lds + hh * 1024 + wst_rd(lane, w);
        SFOR(b, 8)
            VMW(b < 3 ? 32 : 66 - 8 * b); BAR();
            lptr q = tb + (b % 5) * 32768;
#pragma unroll
            for (int k = 0; k < 8; ++k) { lptr a = q + (k >> 1) * 8192 + (k & 1) * 2048; wo[8 * b + k] = tr2(a, a + 4096); if (b < 7) asm volatile("" : "+a"(wo[8 * b + k])); else PIN(wo[8 * b + k]); }
            if constexpr (b < 3) { BAR(); fill(b + 5); }
            if constexpr (b == 2) filt_load(p, L == 0 ? 44 + kq * 5 + ng : 1 << 12, flt);
        SEND
    }
    EV(6);
    const u32 pol = get_pol(p, rs, dead);
    if (L == 0) {
        BAR();
        conv_job<0>(p, rs, lds, 44 + kq * 5 + ng, flt, 0);
        DRAIN(); BAR();
        st4s(rs, tid == 0 ? foff(F_CV + 44 + kq * 5 + ng) : OOB, 0, 2u);
    }
    EV(15);
    MK_SRD();
    const u32 ofl = (lane < 8 && w == 0) ? near_(foff(F_O + L * 32 + kq * 8 + lane), 0u, pol) : OOB;
    const u32 l16 = (u32)lane * 16u;
    const u32 fo = (tid == 192) ? near_(foff(F_P + L * 32 + kq * 5 + ng), 1u, pol) : OOB;
    auto osrc = [&](int s) { return uu(hblk(W_VO, L * NSEG + s, kq * 8, 0u, pol) + w * 1024); };
#define ODMA(i, dst, vo, so) DMA16I(dst, (i) * 4096, vo, so, ((i) >> 1) * 16384 + ((i) & 1) * 4096)
    const u32 pvo = (u32)(kq * 5 + ng) * 16384u + (u32)w * 2048u + l16;
    lptr tw = lds + OU_SEL + 256 + r32 * 272 + (ng == 4 ? hh * 32 + w * 4 : (w & 1) * 128 + hh * 16 + (w >> 1) * 8), tr = lds + OU_SEL + 256 + (8 * w + (lane >> 4)) * 272 + (lane & 15) * 16;
    lptr selw = lds + OU_SEL + (w == 0 ? 0 : 64);
    u32 pfo = OOB;
    u32 buf = 0;
    if (w == 0) wait_flags(rs, ofl, 1u, dead);
    BAR();
    for (int s = 0; s < NSEG; ++s) {
        u32 src = osrc(s), dst = uu(buf + w * 1024);
        lptr A = lds + buf + l16;
        asm volatile("" : "+s"(src), "+s"(dst));
#define OS(k) do {} while (0)
        OS(0);
        OS(1);
        SFOR(i, 10) ODMA(i, dst, l16, src); SEND
        OS(2);
        asm volatile("s_waitcnt vmcnt(7)\n\ts_barrier" ::: "memory");
        OS(3);
        LS2(L == 0 && w == 1, 96 + kq * 5 + ng);
        v4u o[64];
#pragma unroll
        for (int k = 0; k < 3; ++k) o[k] = lds_r16(A + k * 1024);
        st4s(rs, pfo, 0, (u32)s);
        u32 fv, pa; int rdy;
        f32x16 acc;
        SCHED();
        SFOR(k, 64)
            if constexpr (k < 56) M32A(k == 0, acc, wo[k], o[k]); else asm volatile("v_mfma_f32_32x32x16_bf16 %0, %1, %2, %0" : "+v"(acc) : "v"(wo[k]), "v"(o[k]));
            if constexpr (k == 61) rdy = *(LAS int*)(lds + OU_SEL);
            if constexpr (k < 61) o[k + 3] = lds_r16(A + (k + 3) * 1024);
            SCHED();
            if constexpr (k < 12 && (k & 1) == 0) ODMA(10 + (k >> 1), dst, l16, src);
            if constexpr (k == 7) OS(4);
            if constexpr (k == 8) OS(5);
            if constexpr (k == 23) OS(6);
            if constexpr (k == 24) OS(7);
            if constexpr (k == 43) OS(8);
            if constexpr (k == 44) OS(9);
            if constexpr (k == 59) OS(10);
            if constexpr (k == 63) OS(11);
            if constexpr (k == 7) asm volatile("s_waitcnt vmcnt(8)\n\ts_barrier" ::: "memory");
            if constexpr (k == 23) asm volatile("s_waitcnt vmcnt(5)\n\ts_barrier" ::: "memory");
            if constexpr (k == 30) { pa = pvo + near_(p_item(s), (u32)L, pol); PIN(pa); }
            if constexpr (k == 22) LD4A(fv, ofl, 0);
            if constexpr (k == 42) {
                asm volatile("s_waitcnt vmcnt(0)" : "+v"(fv) :: "memory");
                *(LAS int*)selw = (int)((u32)__builtin_amdgcn_ballot_w64(fv < (u32)s + 2) & 255u);
            }
            if constexpr (k == 43) asm volatile("s_waitcnt lgkmcnt(1)\n\ts_barrier" ::: "memory");
            SCHED();
        SEND
        const bool ready = uni(rdy) == 0;
        asm volatile("s_nop 11" : "+v"(acc));
        u32 pc[8];
#pragma unroll
        for (int i = 0; i < 8; ++i) pc[i] = pk(acc[2 * i], acc[2 * i + 1]);
        if (ng != 4) {
#pragma unroll
            for (int i = 0; i < 4; ++i) *(LAS v2u*)(tw + i * 32) = (v2u){pc[2 * i], pc[2 * i + 1]};
        } else {
#pragma unroll
            for (int i = 0; i < 8; ++i) *(LAS u32*)(tw + (i >> 1) * 64 + (i & 1) * 16) = pc[i];
        }
        BAR();
        { const v4u p0 = lds_r16(tr), p1 = lds_r16(tr + 4 * 272); st16(rs, pa, p0); st16(rs, pa + 1024, p1); }
        SCHED();
        OS(12);
        if (ready) pfo = fo;
        else {
            LD4A(fv, ofl, 0);
            asm volatile("s_waitcnt vmcnt(1)\n\ts_barrier" ::: "memory");
            st4s(rs, fo, 0, (u32)s + 1); pfo = OOB;
            OS(14);
            LS2(L == 0 && w == 1, 160 + kq * 5 + ng);
            asm volatile("s_waitcnt vmcnt(1)" : "+v"(fv) :: "memory");
            if (w == 0 && s + 1 < NSEG && ((u32)__builtin_amdgcn_ballot_w64(fv < (u32)s + 2) & 255u) != 0u) wait_flags(rs, ofl, (u32)s + 2, dead);
            BAR();
        }
        OS(13);
        buf ^= 65536u;
    }
}

template <int L> DEV void role_row(const Params& p, lptr lds, int ri) {
    const int lane = threadIdx.x & 63, w = uni(threadIdx.x >> 6);
    const rsrc_t rs = mk_rsrc(p.ws, W_END), rx = mk_rsrc(p.x, ACT);
    bool dead = false;
    const u32 pol = get_pol(p, rs, dead);
    const u32 fp = threadIdx.x < 20 ? near_(foff(F_P + L * 32 + lane), 1u, pol) : OOB, fo = threadIdx.x == 192 && L == 0 ? near_(foff(F_H2 + (ri >> 2)), 1u, pol) : OOB;
    u32 fv = 0;
    MK_SRD();
    auto yfill = [&](int b, int i) { DMA32(uu((b & 1) * 40960 + w * 10240 + i * 2048), (u32)lane * 16u, uu(W_Y + (u32)((ri >> 1) * 8 + b) * 20480u + (w & 1) * 10240 + i * 2048)); };
    auto ywait = [&](u32 v) {
        wait_flags(rs, threadIdx.x < (v == 3u ? 8u : 160u) ? foff(F_CV + (v == 3u ? 160u : 0u) + threadIdx.x) : OOB, v, dead);
    };
    v4u y8[10] = {};
    v2u xa = {}, xb = {}; u32 xc = 0;
    auto xload = [&](int s) { const u32 ro = (u32)(s * 32 + ri) * (DM * 2); xa = ldin8(rx, (u32)lane * 8u, ro); xb = ldin8(rx, (u32)lane * 8u + 512u, ro); xc = __builtin_amdgcn_raw_buffer_load_b32(rx, (u32)lane * 4u + 1024u, ro, 0); };
    if (L == 0) xload(0);
    ywait(1u);
    BAR();
    EV(15);
#pragma unroll 1
    for (int i = 0; i < 5; ++i) yfill(0, i);
    DRAIN(); BAR();
    for (int s = 0; s < NSEG; ++s) {
        u32 xi[5] = {xa.x, xa.y, xb.x, xb.y, xc};
        u32 pa = near_(p_item(s), (u32)L, pol) + (u32)ri * 256u, pc = pa + 65536u + (u32)(lane >> 4) * 81920u + (u32)(lane & 15) * 16u;
        pa += (u32)(lane >> 5) * 81920u + (u32)((lane >> 4) & 1) * 16384u + (u32)(lane & 15) * 16u;
        u32 yf = s == 1 ? foff(F_CV + min(threadIdx.x, 159u)) : s == 25 ? foff(F_CV + 160 + (lane & 7)) : OOB;
        asm volatile("" : "+v"(xi[0]), "+v"(xi[1]), "+v"(xi[2]), "+v"(xi[3]), "+v"(xi[4]), "+v"(pa), "+v"(pc), "+v"(yf));
#define RS(k) do {} while (0)
        RS(0);
        if (w == 0 && ((u32)__builtin_amdgcn_ballot_w64(fv <= (u32)s) & 0xFFFFFu) != 0u) wait_flags(rs, fp, (u32)s + 1, dead);
        BAR();
        RS(1);
        v4u d[5];
#pragma unroll
        for (int i = 0; i < 4; ++i) d[i] = ld16s(rs, pa, (i >> 1) * 32768 + (i & 1) * 163840);
        d[4] = ld16s(rs, pc, 0);
        const u32 yv = ld4(rs, yf);
        if (L == 0) xload(s + 1);
        else {
            const u32 ro = W_X2 + (u32)(s * 32 + ri) * (DM * 2);
            const v2u a = ld8s(rs, (u32)lane * 8u, ro), b = ld8s(rs, (u32)lane * 8u + 512u, ro);
            xi[0] = a.x; xi[1] = a.y; xi[2] = b.x; xi[3] = b.y; xi[4] = ld4(rs, (u32)lane * 4u + 1024u + ro);
        }
        RS(2);
        LS2(L == 0, 192 + ri);
        SCHED();
        f32x2 y[5];
        {
            if ((s & 7) == 0) {
                lptr ya = lds + ((s >> 3) & 1) * 40960 + (w >> 1) * 20480 + (w & 1) * 16, yc = ya + 4096 + (lane >> 1) * 32 + (lane & 1) * 10240;
#pragma unroll
                for (int i = 0; i < 8; ++i) y8[i] = lds_r16(ya + lane * 32 + (i >> 2) * 2048 + (i & 3) * 5120);
                y8[8] = lds_r16(yc); y8[9] = lds_r16(yc + 5120);
            }
#pragma unroll
            for (int i = 0; i < 10; ++i) {
                y[i >> 1][i & 1] = lo_f(y8[i].x);
                y8[i] = (v4u){__builtin_amdgcn_alignbit(y8[i].y, y8[i].x, 16), __builtin_amdgcn_alignbit(y8[i].z, y8[i].y, 16), __builtin_amdgcn_alignbit(y8[i].w, y8[i].z, 16), y8[i].w >> 16};
                PIN(y8[i]);
            }
            asm volatile("" : "+v"(y[0]), "+v"(y[1]), "+v"(y[2]), "+v"(y[3]), "+v"(y[4]));
        }
        f32x2 o[5];
        {
            f32x2 t[5][2];
#pragma unroll
            for (int i = 0; i < 5; ++i) {
                const auto e = __builtin_amdgcn_permlane32_swap(d[i].x, d[i].z, false, false), f = __builtin_amdgcn_permlane32_swap(d[i].y, d[i].w, false, false);
                t[i][0] = up2(e[0]) + up2(e[1]); t[i][1] = up2(f[0]) + up2(f[1]);
            }
            o[0] = t[0][0] + t[1][0]; o[1] = t[0][1] + t[1][1]; o[2] = t[2][0] + t[3][0]; o[3] = t[2][1] + t[3][1];
            const auto e = __builtin_amdgcn_permlane16_swap(u_(t[4][0].x), u_(t[4][1].x), false, false), f = __builtin_amdgcn_permlane16_swap(u_(t[4][0].y), u_(t[4][1].y), false, false);
            o[4] = (f32x2){f_(e[0]) + f_(e[1]), f_(f[0]) + f_(f[1])};
        }
        f32x2 x1[5];
#pragma unroll
        for (int i = 0; i < 5; ++i) x1[i] = up2(pk2(up2(xi[i]) + up2(pk2(o[i]))));
        asm volatile("" : "+v"(x1[0]), "+v"(x1[1]), "+v"(x1[2]), "+v"(x1[3]), "+v"(x1[4]));
        RS(3);
        if (__builtin_expect(s == 1 || s == 25, 0) && !all_(yv >= (s == 1 ? 2u : 3u))) ywait(s == 1 ? 2u : 3u);
        if ((s & 7) - 2u < 5u && s < NSEG - 8) yfill((s >> 3) + 1, (s & 7) - 2);
        SCHED();
        const f32x2 st1 = ln_stats(x1);
        u32 xp[5];
#pragma unroll
        for (int i = 0; i < 5; ++i) xp[i] = pk2(x1[i] + ((x1[i] - st1.x) * st1.y) * y[i]);
        const int row = s * 32 + ri;
        RS(4);
        if (L == 0) {
            const u32 ro = (u32)row * (DM * 2);
            f32x2 x2[5];
#pragma unroll
            for (int i = 0; i < 5; ++i) x2[i] = up2(xp[i]);
            st8s(rs, (u32)lane * 8, W_X2 + ro, (v2u){xp[0], xp[1]});
            st8s(rs, (u32)lane * 8 + 512, W_X2 + ro, (v2u){xp[2], xp[3]});
            st4s(rs, (u32)lane * 4 + 1024, W_X2 + ro, xp[4]);
            SCHED();
            const f32x2 st2 = ln_stats(x2);
            u32 hp[5];
#pragma unroll
            for (int i = 0; i < 5; ++i) hp[i] = pk2((x2[i] - st2.x) * st2.y);
            store_h(rs, near_(W_H2 + (u32)s * H_TILE, 1u, pol), row, hp);
        } else {
            u16* o_ = p.out + (size_t)row * DM;
            *(v2u*)(o_ + 4 * lane) = (v2u){xp[0], xp[1]};
            *(v2u*)(o_ + 256 + 4 * lane) = (v2u){xp[2], xp[3]};
            *(u32*)(o_ + 512 + 2 * lane) = xp[4];
        }
        RS(5);
        if (L == 0) asm volatile("" : "+v"(xa), "+v"(xb), "+v"(xc));
        LD4A(fv, fp, 0);
        if (L == 0) asm volatile("s_waitcnt vmcnt(1)\n\ts_barrier" ::: "memory");
        st4s(rs, fo, 0, (u32)s + 1);
        RS(6);
        asm volatile("s_waitcnt vmcnt(1)" : "+v"(fv) :: "memory");
        RS(7);
        if (ri == 0) LST(320 + L * 64 + s, rt32());
        LS2(L == 0, 224 + ri);
    }
}

constexpr float SCALE = 0.08838834764831845f, LOG2E = 1.4426950408889634f;
constexpr int C_K = 0, C_V = 8192, C_QX = 16384, C_MEM = 24576, C_Q = C_MEM + 10240, C_KM = C_Q + 2304, C_VM = C_KM + 2304, C_DUMP = C_VM + 2048,
              C_ZERO = C_DUMP + 1024, C_WL = C_ZERO + 128;
constexpr int NLF = 14;
static_assert(C_WL % 16 == 0 && C_WL + 4 * NLF * 2048 <= LDS_BYTES, "head lds");
#define CH_VIS 10
#define CH_H0 104
#define CH_W0 82
#define CH_MH 2
#define CH_MW 6
#define CH_REC 8
#define CH_LATE 30
#define CH_SOLO 6
#define CH_KV 12
#define CH_KV0 60
#define CH_M0 6
#define CH_DEC 2u
#define CH_UP 4
#define CH_WX 6
DEV void wait_until(u32 t) {
    unsigned long long c;
    asm volatile("1:\n\ts_memrealtime %0\n\ts_waitcnt lgkmcnt(0)\n\ts_mov_b64 vcc, %0\n\ts_sub_u32 vcc_lo, %1, vcc_lo\n\ts_cmpk_lt_u32 vcc_lo, 400\n\ts_cbranch_scc1 1b" : "=&s"(c) : "s"(uu(t - 1u)) : "vcc", "scc", "memory");
}
#define WAIT_TXT(tmp, t1) "s_memrealtime vcc\n1:\n\ts_waitcnt lgkmcnt(0)\n\ts_sub_u32 " tmp ", " t1 ", vcc_lo\n\ts_memrealtime vcc\n\ts_cmpk_lt_u32 " tmp ", 400\n\ts_cbranch_scc1 1b\n\t"
#define SMF_A(acc, a, b) asm volatile("v_smfmac_f32_16x16x64_bf16 %0, %1, %2, %3" : "+v"(acc) : "v"(a), "a"(b), "v"(idx))
#define SMF_V(acc, a, b) asm volatile("v_smfmac_f32_16x16x64_bf16 %0, %1, %2, %3" : "+v"(acc) : "v"(a), "v"(b), "v"(idx))
#define MF0(d, a, b) asm volatile("v_mfma_f32_16x16x32_bf16 %0, %1, %2, 0" : "=&v"(d) : "v"(a), "v"(b))
#define MF1(d, a, b) asm volatile("v_mfma_f32_16x16x32_bf16 %0, %1, %2, %0" : "+v"(d) : "v"(a), "v"(b))
#define MF0H(d, a, b) asm volatile("v_mfma_f32_16x16x16_bf16 %0, %1, %2, 0" : "=&v"(d) : "v"(a), "v"(b))
#define MF1H(d, a, b) asm volatile("v_mfma_f32_16x16x16_bf16 %0, %1, %2, %0" : "+v"(d) : "v"(a), "v"(b))
DEV v8u tr_frag(lptr p0, lptr p1, lptr p2, lptr p3) { return cat8(tr2(p0, p1), tr2(p2, p3)); }
constexpr int qslot(int ks) { return (ks & 1) && ks < 8 ? ks >> 1 : -1; }
constexpr int kslot(int ks) { return ks < 4 ? 4 + ks : -1; }
constexpr int vslot(int ks) { return ks >= 4 ? 4 + ks : -1; }
DEV u32 ring_map(u32 off, u32 half) { return ((off >> 13) << 14) + (half << 13) + (off & 8191u); }
#define ZRD(x, i) x = __builtin_bit_cast(f32x4, lds_r16(ZP + 16 * (i)))
#define VMAX(d, a, b) asm volatile("v_max_f32 %0, %1, %2" : "=v"(d) : "v"(a), "v"(b))
#define VMAX3(d, a, b, c_) asm volatile("v_max3_f32 %0, %1, %2, %3" : "=v"(d) : "v"(a), "v"(b), "v"(c_))
#define VADD(d, a, b) asm volatile("v_add_f32 %0, %1, %2" : "=v"(d) : "v"(a), "v"(b))
#define VEXP(d, a) asm volatile("v_exp_f32 %0, %1" : "=v"(d) : "v"(a))
#define VFMC(d, a, b) asm volatile("v_fmamk_f32 %0, %1, 0x3e0293ee, %2" : "=v"(d) : "v"(a), "v"(b))
#define VMULC(d, a) asm volatile("v_mul_f32 %0, 0x3e0293ee, %1" : "=v"(d) : "v"(a))
#define SEL(d, a, b) asm volatile("v_cndmask_b32 %0, %1, %2, %3" : "=v"(d) : "v"(a), "v"(b), "s"(himask))
#define QPERM(d, a, q) asm volatile("v_mov_b32_dpp %0, %1 quad_perm:" q " row_mask:0xf bank_mask:0xf" : "=v"(d) : "v"(a))

#define HSAMP 71
DEV void role_head(const Params& p, lptr lds, int L, int h) {
    const int tid = threadIdx.x, lane = tid & 63, w = uni(tid >> 6), g = lane >> 4, r = lane & 15;
    const rsrc_t rs = mk_rsrc(p.ws, W_END);
    v8u wq0[10], wq1[10], wk0[10], wk1[10], wv0[10], wv1[10];
    lptr WL = lds + C_WL + w * (NLF * 2048) + lane * 16;
    {
        constexpr int GS = 1056, CS = 48 * GS;
        const v4u sq = mk_srd(p.Wq, DM * NQ * 2), sk = mk_srd(p.Wk, DM * NQ * 2), sv = mk_srd(p.Wv, DM * NQ * 2);
        const u32 vo = (u32)(g * NQ + h * DH + 8 * (r ^ ((g & 2) << 2))) * 2u;
        auto fill = [&](int ch) {
#pragma unroll 1
            for (int i = 0; i < 4; ++i) {
                const u32 dst = uu((ch % 3) * CS + (4 * i + w) * GS), so = uu((ch * 64 + 16 * i + 4 * w) * (NQ * 2));
                DMAIN(sq, dst, vo, so); DMAIN(sk, dst + 16 * GS, vo, so); DMAIN(sv, dst + 32 * GS, vo, so);
            }
        };
        int vote = 0;
        if (L == 1) {
            if (w == 0) vote = probe(rs, h);
            BAR();
            EV(2);
            pre_rings(p, rs, h);
        }
        fill(0); fill(1); fill(2);
        const int q4 = r >> 2;
        lptr tb = lds + (4 * (g >> 1) + q4) * GS + (g & 1) * 512 + ((w * 64 + (r & 3) * 8) ^ ((g & 1) << 7));
        SFOR(ch, 10)
            VMW(ch == 1 || ch == 2 ? 25 : ch < 8 ? 24 : 108 - 12 * ch);
            BAR();
            if constexpr (ch == 0) boot_set(p, rs, tid == 0 && L == 1 ? foff(F_RG + h) : OOB, (u32)((1 << 20) + vote));
            auto frag = [&](int m, int t) { lptr b = tb + (ch % 3) * CS + m * 16 * GS + t * 32; return tr_frag(b, b + 256, b + 8 * GS, b + 8 * GS + 256); };
            wq0[ch] = frag(0, 0); wq1[ch] = frag(0, 1); wk0[ch] = frag(1, 0); wk1[ch] = frag(1, 1); wv0[ch] = frag(2, 0); wv1[ch] = frag(2, 1);
            asm volatile("" : "+a"(wq0[ch]), "+a"(wk0[ch]), "+v"(wv0[ch]), "+v"(wv1[ch]));
            if constexpr (qslot(ch) < 0) asm volatile("" : "+a"(wq1[ch])); else asm volatile("" : "+v"(wq1[ch]));
            if constexpr (kslot(ch) < 0) asm volatile("" : "+a"(wk1[ch])); else asm volatile("" : "+v"(wk1[ch]));
            BAR();
            if constexpr (ch < 7) fill(ch + 3);
        SEND
#pragma unroll
        for (int ch = 0; ch < 10; ++ch) {
            if (qslot(ch) >= 0) { lds_w16(WL + qslot(ch) * 2048, wq1[ch].lo); lds_w16(WL + qslot(ch) * 2048 + 1024, wq1[ch].hi); }
            if (kslot(ch) >= 0) { lds_w16(WL + kslot(ch) * 2048, wk1[ch].lo); lds_w16(WL + kslot(ch) * 2048 + 1024, wk1[ch].hi); }
            if (vslot(ch) >= 0) { lds_w16(WL + vslot(ch) * 2048, wv1[ch].lo); lds_w16(WL + vslot(ch) * 2048 + 1024, wv1[ch].hi); }
        }
        CFENCE();
    }
    const u32 idx = (r & 2) ? 0xEEEEu : 0x4444u;
    const unsigned long long himask = 0xCCCCCCCCCCCCCCCCull;
    bool dead = false;
    EV(6);
    const u32 pol = get_pol(p, rs, dead);
    const u32 pol0 = pol & 1u;
    const u32 l16 = (u32)lane * 16u;
    const u32 mring = W_MRING + (u32)L * (MB / 4) + pol0 * 8192u, hint = mring + 4 * 32768u;
    const u32 fkh = lane < 3 ? near_(foff(F_QKV + lane * 64 + L * 32 + h), 0u, pol) : lane < 6 ? hint + (u32)(lane - 3) * 4u : OOB;
    u32 tm = 0;
    MK_SRD();
    const unsigned long long kmask = __builtin_amdgcn_ballot_w64((hblk(W_KQ, L * NSEG + lane, h, 0u, pol) & 8192u) != 0u),
                             vmask = __builtin_amdgcn_ballot_w64((hblk(W_VO, L * NSEG + lane, h, 1u, pol) & 8192u) != 0u);
    u32 kb = uu(W_KQ + (u32)(L * NSEG * NH + h) * 16384u + w * 2048);
    u32 dk = uu(C_K + w * 2048), dv = dk + (C_V - C_K), dq = dk + (C_QX - C_K), dm = uu(C_MEM + w * 2048), dm2 = uu(w < 2 ? C_MEM + 8192 + w * 1024 : C_DUMP);
    asm volatile("" : "+s"(dk), "+s"(dv), "+s"(dq), "+s"(dm), "+s"(dm2));
    u32 ksrc = kb + ((u32)kmask & 1u) * 8192u, vsrc = kb + (W_VO - W_KQ) + ((u32)vmask & 1u) * 8192u;
    lptr qw = lds + C_Q + (2 * g) * 288 + (32 * w + 2 * r) * 2;
    lptr kw = qw + (C_KM - C_Q);
    lptr qr = lds + C_Q + (2 * (r >> 2) + (r & 1)) * 288 + g * 16;
    lptr LB = lds + l16, VX = lds + C_V + w * 2048 + l16;
    if (tid >= 64 && tid < 70) { const u32 z = tid == 68 ? 0x3F803F80u : tid == 69 ? PSENT : 0u; lds_w16(lds + C_ZERO + (tid - 64) * 16, (v4u){z, z, z, z}); }
    u32 zaddr = 0; PIN(zaddr);
    lptr ZP = lds + zaddr + C_ZERO;
#define LF_RD(slot, hf) lf[slot][hf] = lds_r16(WL + (slot) * 2048 + (hf) * 1024)
    BAR();
    const u32 ofl = tid == 0 ? near_(foff(F_O + L * 32 + h), 0u, pol) : OOB;
    u32 oso = (g & 1) ? OOB : W_ORING + (u32)L * (MB / 2) + ring_map((u32)((r >> 3) * 32768 + h * 1024 + w * 256 + (g >> 1) * 128 + (r & 7) * 16), pol0);
    unsigned long long vis[8];
#pragma unroll
    for (int i = 0; i < 8; ++i) vis[i] = __builtin_amdgcn_ballot_w64(16 * (i >> 2) + 4 * g + (i & 3) <= 16 * (w & 1) + r);
    u32 missed = 0, dirty = 1;
    asm volatile("" : "+v"(oso), "+v"(qw), "+v"(kw), "+v"(qr), "+v"(VX));
    asm volatile("" : "+s"(vis[0]), "+s"(vis[1]), "+s"(vis[2]), "+s"(vis[3]), "+s"(vis[4]), "+s"(vis[5]), "+s"(vis[6]), "+s"(vis[7]), "+s"(ksrc), "+s"(vsrc));
    wait_flags(rs, lane < 3 ? fkh : OOB, 1u, dead);
    EV(7);
    DMA32(dk, l16, ksrc); DMA32(dv, l16, vsrc); DMA32(dq, l16, ksrc ^ 8192u);
    EV(8);
    auto next_slot = [&](u32 now) {
        const u32 hv = ld4(rs, fkh), per = rdl(hv, 4);
        if (per < 400u) { tm = rdl(hv, 5) - per; for (int n = 0; n < 3 && (int)(tm - now) < 0; ++n) tm += per; }
    };
    if (L == 1) { next_slot(rt32() + CH_KV0); tm -= 24u; }
    for (int s = 0; s < NSEG;) {
        {
            *(LAS u32*)(lds + C_DUMP + 128 + w * 4) = tm;
            BAR();
            const v4u c = lds_r16(lds + C_DUMP + 128) - tm;
            const u32 add = uu((u32)max(max((int)c.x, (int)c.y), max((int)c.z, (int)c.w)));
            tm += add;
            if (__builtin_expect((dirty | add) != 0u, 0)) {
                wait_until(tm - 44u);
                st16(rs, oso, lds_r16(ZP + 80));
            }
        }
        u32 ms = mring + (u32)(s & 3) * 32768u + w * 2048, ms2 = mring + (u32)(s & 3) * 32768u + 16384u + (w & 1) * 1024, sv = (u32)s;
        asm volatile("" : "+s"(ms), "+s"(ms2)); PIN(sv);
        f32x4 aq0, aq1, ak0, ak1;
        v4u lf[NLF][2];
        ZRD(aq0, 0); ZRD(aq1, 1); ZRD(ak0, 2); ZRD(ak1, 3);
#pragma unroll
        for (int i = 0; i < 4; ++i) { LF_RD(i, 0); LF_RD(i, 1); }
        SCHED();
        v4u A[10];
#define HT(k) do {} while (0)
        v4u kx[8], qf[4], kmf[4], vx[2], ones, P;
        float fa, fb, fc, fd, sx[4], t[4], sm0, sm1, tm0, tm1, inv; u32 pa, pb, fl, pmm, tmp = 0; unsigned long long odd;
        f32x4 av0, av1, s0, s1, sm, oa, ob, rsum;
        v2u vm, pm, vh, vl, on[2];
        f32x2 zz[4];
#define LW(sl) cat8(lf[sl][0], lf[sl][1])
#define PQ(ks, tl) do { if constexpr (!(tl)) SMF_A(aq0, A[ks], wq0[ks]); else if constexpr (qslot(ks) < 0) SMF_A(aq1, A[ks], wq1[ks]); else { const v8u lw_ = LW(qslot(ks)); SMF_V(aq1, A[ks], lw_); } } while (0)
#define PK(ks, tl) do { if constexpr (!(tl)) SMF_A(ak0, A[ks], wk0[ks]); else if constexpr (kslot(ks) < 0) SMF_A(ak1, A[ks], wk1[ks]); else { const v8u lw_ = LW(kslot(ks)); SMF_V(ak1, A[ks], lw_); } } while (0)
#define PV(ks, tl) do { if constexpr (!(tl)) SMF_V(av0, A[ks], wv0[ks]); else if constexpr (vslot(ks) < 0) SMF_V(av1, A[ks], wv1[ks]); else { const v8u lw_ = LW(vslot(ks)); SMF_V(av1, A[ks], lw_); } } while (0)
#define KXR(i) kx[i] = lds_r16(LB + C_K + ((i) & 1) * 4096 + ((i) >> 1) * 1024)
#define XW(q_) do { *(LAS u32*)(q_) = pa; *(LAS u32*)((q_) + 288) = pb; } while (0)
        auto slot = [&](auto zc) __attribute__((always_inline)) {
            constexpr int z = decltype(zc)::value;
            constexpr int vs[] = {0, 8, 1, 10, 9, 2, 11, -1, -1, -1, -1, 4, 6, 12, -1, -1, -1, 13, 14, -1, 16, 18, 15, 3, 5, 17, 7, 19};
            if constexpr (z < 12) PQ(z >> 1, z & 1);
            else if constexpr (z < 20) PQ(6 + (z & 3), z >> 4);
            else if constexpr (z < 32) PK((z - 20) >> 1, z & 1);
            else if constexpr (z < 40) PK(6 + (z & 3), (z >> 2) & 1);
            else if constexpr (z < 42) { if constexpr (z == 40) MF0(s0, kx[0], qf[0]); else MF0(s1, kx[1], qf[0]); }
            else if constexpr (z < 48) { if constexpr (z & 1) MF1(s1, kx[z - 40], qf[(z - 40) >> 1]); else MF1(s0, kx[z - 40], qf[(z - 40) >> 1]); }
            else if constexpr (z == 55) MF0(sm, kmf[0], qf[0]);
            else if constexpr (z > 55 && z < 59) MF1(sm, kmf[z - 55], qf[z - 55]);
            else if constexpr (z == 62) MF0(rsum, ones, P);
            else if constexpr (z == 63) MF0(oa, vx[0], P);
            else if constexpr (z == 64) MF0(ob, vx[1], P);
            else if constexpr (z == 67) { const v2u ones2 = ones.lo; MF1H(rsum, ones2, pm); }
            else PV(vs[z - 48] >> 1, vs[z - 48] & 1);
#define USE(x) asm volatile("" :: "v"(x))
            if constexpr (z == 20 || z == 24 || z == 26) USE(lf[4 + ((z - 20) >> 1)][1]);
            if constexpr (z == 41) USE(qf[3]);
            if constexpr (z == 45 || z == 46) USE(kx[z - 39]);
            if constexpr (z == 53) USE(lf[9][1]);
            if constexpr (z >= 54 && z < 58) USE(kmf[z - 54]);
            if constexpr (z == 61) USE(vl);
            if constexpr (z == 64) USE(lf[10][1]);
            if constexpr (z == 69) USE(lf[11][1]);
            if constexpr (z == 72) USE(lf[12][1]);
            if constexpr (z == 74) USE(lf[13][1]);
            if constexpr (z < 8) A[2 + z] = lds_r16(LB + C_MEM + (2 + z) * 1024);
            if constexpr (z >= 15 && z < 23) LF_RD(4 + ((z - 15) >> 1), (z - 15) & 1);
            if constexpr (z >= 24 && z < 27) KXR(z - 24);
            if constexpr (z == 27) XW(qw);
            if constexpr (z >= 28 && z < 31) KXR(z - 25);
            if constexpr (z == 32) asm volatile("s_waitcnt lgkmcnt(3)\n\ts_barrier" ::: "memory");
            if constexpr (z >= 33 && z < 37) qf[z - 33] = lds_r16(qr + (z - 33) * 64);
            if constexpr (z == 37 || z == 38) LF_RD(8, z - 37);
            if constexpr (z == 39 || z == 41) KXR(6 + (z == 41));
            if constexpr (z == 44) XW(kw);
            if constexpr (z == 45 || z == 46) LF_RD(9, z - 45);
            if constexpr (z == 47) vx[0] = lds_r16(VX);
            if constexpr (z == 48) vh = *(LAS v2u*)(VX + 1032);
            if constexpr (z == 54) { vl = *(LAS v2u*)(VX + 1024); vx[1] = (v4u){vh.x, vh.y, vl.x, vl.y}; }
            if constexpr (z == 49) { USE(lf[9][0]); asm volatile("s_barrier" ::: "memory"); }
            if constexpr (z >= 50 && z < 54) kmf[z - 50] = lds_r16(qr + (C_KM - C_Q) + (z - 50) * 64);
            if constexpr (z == 59 || z == 60) LF_RD(10, z - 59);
            if constexpr (z == 61 || z == 65) LF_RD(11, z == 65);
            if constexpr (z == 66 || z == 68) LF_RD(12, z == 68);
            if constexpr (z == 69 || z == 70) LF_RD(13, z - 69);
            if constexpr (z == 9) st4s(rs, ofl, 0, sv);
            if constexpr (z == HSAMP) fl = ld4(rs, fkh);
            SCHED();
            if constexpr (z == 19) HT(2);
            if constexpr (z == 39) HT(3);
            if constexpr (z == 51) HT(4);
            if constexpr (z == 64) HT(5);
            if constexpr (z == 20) VADD(fa, aq0[0], aq0[2]);
            if constexpr (z == 21) VADD(fb, aq0[1], aq0[3]);
            if constexpr (z == 23) VADD(fc, aq1[0], aq1[2]);
            if constexpr (z == 24) VADD(fd, aq1[1], aq1[3]);
            if constexpr (z == 25) VCVT(pa, fa, fc);
            if constexpr (z == 26) VCVT(pb, fb, fd);
            if constexpr (z >= 44 && z < 48) asm volatile("v_mov_b64 %0, 0" : "=v"(zz[z - 44]));
            if constexpr (z == 47) { av0 = __builtin_shufflevector(zz[0], zz[1], 0, 1, 2, 3); av1 = __builtin_shufflevector(zz[2], zz[3], 0, 1, 2, 3); }
            if constexpr (z == 48 || z == 49) asm volatile("v_mov_b64 %0, %1" : "=v"(on[z - 48]) : "s"(0x3F803F803F803F80ull));
            if constexpr (z == 49) ones = __builtin_shufflevector(on[0], on[1], 0, 1, 2, 3);
            if constexpr (z == 40) { VADD(fa, ak0[0], ak0[2]); VADD(fb, ak0[1], ak0[3]); }
            if constexpr (z == 42) { VADD(fd, ak1[1], ak1[3]); VADD(fc, ak1[0], ak1[2]); }
            if constexpr (z == 43) { VCVT(pb, fb, fd); VCVT(pa, fa, fc); }
            if constexpr (z >= 50 && z < 54) SEL(sx[z - 50], s0[z - 50], s1[z - 50]);
            if constexpr (z >= 54 && z < 58) VEXP(t[z - 54], sx[z - 54]);
            if constexpr (z == 58) asm volatile("v_cvt_pk_bf16_f32 %0, %2, %3\n\tv_cvt_pk_bf16_f32 %1, %4, %5" : "=&v"(P.x), "=v"(P.y) : "v"(t[0]), "v"(t[1]), "v"(t[2]), "v"(t[3]));
            if constexpr (z == 59) QPERM(P.z, P.x, "[2,3,0,1]");
            if constexpr (z == 60) QPERM(P.w, P.y, "[2,3,0,1]");
            if constexpr (z == 61) { USE(sm); VMULC(sm0, sm[0]); }
            if constexpr (z == 62) { VEXP(tm0, sm0); VMULC(sm1, sm[1]); }
            if constexpr (z == 63) VEXP(tm1, sm1);
            if constexpr (z == 64) asm volatile("v_cvt_pk_bf16_f32 %0, %2, %3\n\tv_cndmask_b32 %1, %0, 0, %4" : "=&v"(pmm), "=v"(pm.x) : "v"(tm0), "v"(tm1), "s"(himask));
            if constexpr (z == 65) asm volatile("v_cndmask_b32 %0, 0, %1, %2" : "=v"(pm.y) : "v"(pmm), "s"(himask));
            if constexpr (z == 67) { SEL(oa[0], oa[0], ob[0]); SEL(oa[1], oa[1], ob[1]); }
            if constexpr (z == 68 || z == 69) SEL(oa[z - 66], oa[z - 66], ob[z - 66]);
            if constexpr (z == 70) asm volatile("v_sub_u32 %0, %1, %2" : "=v"(pa) : "v"(rsum[0]), "s"(0x2B800000u), "v"(rsum));
            if constexpr (z == 71) asm volatile("v_cmp_le_u32 %0, %1, %2" : "=s"(odd) : "s"(0x28000000u), "v"(pa));
            if constexpr (z == 72) VADD(fa, av0[0], av0[2]);
            if constexpr (z == 73) { NOPS(0); VADD(fb, av0[1], av0[3]); }
            if constexpr (z == 74) VCVT(vm.x, fa, fb);
            SCHED();
        };
        {
            asm volatile("s_mov_b32 m0, %2\n\t" WAIT_TXT("%0", "%1") "buffer_load_dwordx4 %4, %5, %6 offen sc1 lds\n\tbuffer_load_dwordx4 %4, %5, %6 offen offset:1024 sc1 lds\n\t"
                         "s_mov_b32 m0, %3\n\ts_nop 0\n\tbuffer_load_dwordx4 %4, %5, %7 offen sc1 lds\n\ts_waitcnt vmcnt(0) lgkmcnt(0)\n\ts_barrier"
                         : "=&s"(tmp) : "s"(uu(tm - 1u)), "s"(dm), "s"(dm2), "v"(l16), "s"(srd), "s"(ms), "s"(ms2) : "vcc", "scc", "memory");
        }
#pragma unroll
        for (int ks = 0; ks < 2; ++ks) A[ks] = lds_r16(LB + C_MEM + ks * 1024);
        SCHED();
        static_for<0, 76>(slot);
        u32 fail = 0;
        if (__builtin_expect(odd != 0ull, 0)) {
            u32 mn = ~0u;
#pragma unroll 5
            for (int i = 0; i < 10; ++i) { const v4u e = lds_r16(LB + C_MEM + i * 1024); mn = min(min(min(mn, e.x ^ PSENT), e.y ^ PSENT), min(e.z ^ PSENT, e.w ^ PSENT)); }
            fail = missed < SPIN_CAP && !all_(mn != 0u) ? OOB : 0u;
            float mx = fmaxf(fmaxf(fmaxf(sx[0], sx[1]), fmaxf(sx[2], sx[3])), fmaxf(sm0, sm1));
            mx = max32(max16(fmaxf(mx, dpp<0x4E>(mx))));
#pragma unroll
            for (int i = 0; i < 4; ++i) t[i] = __builtin_amdgcn_exp2f(sx[i] - mx);
            P.x = pk(t[0], t[1]); P.y = pk(t[2], t[3]); P.z = u_(dpp<0x4E>(f_(P.x))); P.w = u_(dpp<0x4E>(f_(P.y)));
            pmm = pk(__builtin_amdgcn_exp2f(sm0 - mx), __builtin_amdgcn_exp2f(sm1 - mx));
            pm = (r & 2) ? (v2u){0u, pmm} : (v2u){pmm, 0u};
            asm volatile("s_nop 1" : "+v"(P), "+v"(pm));
            MF0(rsum, ones, P); MF0(oa, vx[0], P); MF0(ob, vx[1], P);
            NOPS(7);
            { const v2u ones2 = ones.lo; MF1H(rsum, ones2, pm); }
#pragma unroll
            for (int i = 0; i < 4; ++i) SEL(oa[i], oa[i], ob[i]);
            asm volatile("s_nop 4" :: "v"(rsum));
            oso |= fail;
        }
        asm volatile("v_rcp_f32 %0, %3\n\ts_nop 4\n\tv_add_f32 %1, %4, %6\n\tv_add_f32 %2, %5, %7\n\tv_cvt_pk_bf16_f32 %1, %1, %2\n\ts_nop 0"
                     : "=&v"(inv), "=&v"(vm.y), "=&v"(fd) : "v"(rsum[0]), "v"(av1[0]), "v"(av1[1]), "v"(av1[2]), "v"(av1[3]));
        asm volatile("v_mfma_f32_16x16x16_bf16 %0, %1, %2, %0\n\ts_nop 6" : "+v"(oa) : "v"(vm), "v"(pm));
        {
            v4u oc;
            const f32x2 i2 = {inv, inv};
            const f32x2 u0 = oa.lo * i2, u1 = oa.hi * i2;
            asm volatile("v_cvt_pk_bf16_f32 %0, %4, %5\n\tv_cvt_pk_bf16_f32 %2, %4, %5\n\tv_cvt_pk_bf16_f32 %1, %6, %7\n\tv_cvt_pk_bf16_f32 %3, %6, %7\n\t"
                         "v_permlane16_swap_b32 %0, %2\n\ts_nop 0\n\tv_permlane16_swap_b32 %1, %3"
                         : "=&v"(oc.x), "=&v"(oc.y), "=&v"(oc.z), "=&v"(oc.w) : "v"(u0.x), "v"(u0.y), "v"(u1.x), "v"(u1.y));
            st16(rs, oso, oc);
        }
        SCHED();
        if (__builtin_expect(fail == 0u, 1)) {
        unsigned long long clk;
        asm volatile("s_memrealtime %0" : "=s"(clk));
        HT(6);
        v4u tq[4], tk[8], tv[2];
        f32x4 c0, c1, cm;
        u32 oo, kvo; bool kvok;
        u32 ln = l16; PIN(ln);
        lptr QX = lds + C_QX + (w & 1) * 4096 + ln, VO = lds + C_V + (w ^ 1) * 2048 + ln, VMX = lds + C_VM + w * 512 + (ln >> 1);
#pragma unroll
        for (int i = 0; i < 2; ++i) { tq[i] = lds_r16(QX + i * 1024); tk[2 * i] = lds_r16(LB + C_K + i * 1024); tk[2 * i + 1] = lds_r16(LB + C_K + 4096 + i * 1024); }
        *(LAS v2u*)VMX = vm;
        SCHED();
        SFOR(i, 12)
            if constexpr (i == 0) MF0(cm, kmf[0], tq[0]); else if constexpr (i == 1) MF0(c0, tk[0], tq[0]); else if constexpr (i == 2) MF0(c1, tk[1], tq[0]);
            else if constexpr (i % 3 == 0) MF1(cm, kmf[i / 3], tq[i / 3]); else if constexpr (i % 3 == 1) MF1(c0, tk[2 * (i / 3)], tq[i / 3]); else MF1(c1, tk[2 * (i / 3) + 1], tq[i / 3]);
            if constexpr (i == 0) { tq[2] = lds_r16(QX + 2048); tk[4] = lds_r16(LB + C_K + 2048); }
            if constexpr (i == 1) { tk[5] = lds_r16(LB + C_K + 6144); tq[3] = lds_r16(QX + 3072); }
            if constexpr (i == 2) { tk[6] = lds_r16(LB + C_K + 3072); tk[7] = lds_r16(LB + C_K + 7168); }
            if constexpr (i == 3) { tv[0] = lds_r16(VO); tv[1] = lds_r16(VO + 1024); }
            if constexpr (i == 4) vx[1] = lds_r16(VX + 1024);
            if constexpr (i == 4) { oo = ln + (ln & 0x300u) + ((vsrc ^ 8192u) + (u32)((w & 1) * 256)); PIN(oo); kb += NH * 16384; PINS(kb); }
            if constexpr (i == 5) { ksrc = kb + (((kmask >> ((s + 1) & 63)) & 1ull) ? 8192u : 0u); PINS(ksrc); }
            if constexpr (i == 6) { vsrc = kb + (((vmask >> ((s + 1) & 63)) & 1ull) ? W_VO - W_KQ + 8192u : W_VO - W_KQ); PINS(vsrc); }
            if constexpr (i == 10) { PIN(fl); kvok = ((u32)__builtin_amdgcn_ballot_w64(fl < (u32)s + 2) & 7u) == 0u; }
            if constexpr (i == 11) { kvo = l16 | (kvok ? 0u : OOB); PIN(kvo); }
            SCHED();
        SEND
        asm volatile("s_waitcnt lgkmcnt(0)\n\ts_barrier" : "+s"(clk) :: "memory");
        const v2u tvm = *(LAS v2u*)(VMX + (512 - (w & 1) * 1024));
        const v4u pv = lds_r16(ZP + 80);
        DMA32(dk, kvo, ksrc);
        SCHED(); asm volatile("" :: "v"(cm));
        float u[10], m, m2;
        {
            float NEG = -1000000000.0f * LOG2E; PIN(NEG);
            VMULC(u[8], cm[0]); VMULC(u[9], cm[1]);
#define MSK(d, a, i) asm volatile("v_cndmask_b32 %0, %1, %2, %3" : "=v"(d) : "v"(NEG), "v"(a), "s"(vis[i]))
#pragma unroll
            for (int i = 0; i < 4; ++i) MSK(u[i], c0[i], i);
#pragma unroll
            for (int i = 0; i < 4; ++i) MSK(u[4 + i], c1[i], 4 + i);
            asm volatile("v_max3_f32 %0, %2, %3, %10\n\tv_max3_f32 %1, %4, %5, %11\n\tv_max3_f32 %0, %0, %6, %7\n\tv_max3_f32 %1, %1, %8, %9\n\t"
                         "v_max_f32 %0, %0, %1\n\tv_mov_b32 %1, %0\n\ts_nop 1\n\tv_permlane16_swap_b32 %0, %1\n\tv_max_f32 %0, %0, %1\n\tv_mov_b32 %1, %0\n\ts_nop 1\n\t"
                         "v_permlane32_swap_b32 %0, %1\n\tv_max_f32 %0, %0, %1"
                         : "=&v"(m), "=&v"(m2) : "v"(u[0]), "v"(u[1]), "v"(u[2]), "v"(u[3]), "v"(u[4]), "v"(u[5]), "v"(u[6]), "v"(u[7]), "v"(u[8]), "v"(u[9]));
        }
        DMA32(dv, kvo, vsrc);
        {
            v4u px; v2u p0, p1;
#define EX2(d, a, b) asm volatile("v_sub_f32 %0, %2, %4\n\tv_sub_f32 %1, %3, %4\n\tv_exp_f32 %0, %0\n\tv_exp_f32 %1, %1" : "=&v"(d.x), "=&v"(d.y) : "v"(a), "v"(b), "v"(m))
            f32x2 ex[5];
            EX2(ex[0], u[0], u[1]); EX2(ex[1], u[2], u[3]); EX2(ex[2], u[4], u[5]); EX2(ex[3], u[6], u[7]); EX2(ex[4], u[8], u[9]);
            asm volatile("v_cvt_pk_bf16_f32 %0, %8, %9\n\tv_cvt_pk_bf16_f32 %1, %10, %11\n\tv_cvt_pk_bf16_f32 %2, %12, %13\n\tv_cvt_pk_bf16_f32 %3, %14, %15\n\t"
                         "v_mov_b32 %5, 0\n\tv_mov_b32 %6, 0\n\tv_cvt_pk_bf16_f32 %4, %16, %17\n\tv_cvt_pk_bf16_f32 %7, %16, %17"
                         : "=&v"(px.x), "=&v"(px.y), "=&v"(px.z), "=&v"(px.w), "=&v"(p0.x), "=&v"(p0.y), "=&v"(p1.x), "=&v"(p1.y)
                         : "v"(ex[0].x), "v"(ex[0].y), "v"(ex[1].x), "v"(ex[1].y), "v"(ex[2].x), "v"(ex[2].y), "v"(ex[3].x), "v"(ex[3].y), "v"(ex[4].x), "v"(ex[4].y));
            f32x4 o[4], sum;
            const v2u ones2 = ones.lo;
            const u32 now = (u32)clk;
            u32 oz;
            SCHED();
            MF0(sum, ones, px); { const u32 q = ksrc ^ 8192u; DMA32(dq, kvo, q); } SCHED();
            MF0(o[0], vx[0], px);
            MF0(o[1], vx[1], px);
            MF0(o[2], tv[0], px); oz = (oso & ~(3u << 17)) | (u32)((s + 3) & 3) << 17; PIN(oz); SCHED();
            MF0(o[3], tv[1], px); oso = (oso & ~(3u << 17)) | (u32)((s + 1) & 3) << 17; PIN(oso); SCHED();
            MF1H(sum, ones2, p0);
            MF1H(o[0], vm, p0);
            MF1H(o[1], vm, p1); st16(rs, oz, pv); SCHED();
            MF1H(o[2], tvm, p0);
            MF1H(o[3], tvm, p1);
            float inv;
            asm volatile("s_nop 2\n\tv_rcp_f32 %0, %1\n\ts_nop 0" : "=v"(inv) : "v"(sum[0]), "v"(sum));
            asm volatile("" : "+v"(o[0]), "+v"(o[1]));
            st16(rs, oo, (v4u){pk2(o[0].lo * inv), pk2(o[0].hi * inv), pk2(o[1].lo * inv), pk2(o[1].hi * inv)});
            asm volatile("" : "+v"(o[2]), "+v"(o[3]));
            st16(rs, oo ^ 2048u, (v4u){pk2(o[2].lo * inv), pk2(o[2].hi * inv), pk2(o[3].lo * inv), pk2(o[3].hi * inv)});
            SCHED();
            if (__builtin_expect((h & 15) == 0 && w == 3, 0)) {
                st4s(rs, ln == 0 ? hint + 128u + (u32)h * 8u : OOB, 0, (u32)s << 16 | min(missed, 1u) << 14 | min(now - tm, 0x3FFFu));
                if (h == 0) { LST(L * 64 + s, now); LST(128 + L * 64 + s, ((int)(rdl(fl, 3) - now) >= CH_VIS - 4) | ((int)kvok << 1) | ((int)min(missed, 3u) << 2)); LST(640 + L * 64 + s, tm); LST(1728 + L * 64 + s, __builtin_amdgcn_s_memtime()); }
            }
            tm = rdl(fl, 5);
            missed = missed == SPIN_CAP ? missed : 0u;
            dirty = 0;
            if (__builtin_expect(rdl(fl, 3) - now + CH_LATE > 4000u && s + 1 < NSEG, 0)) {
                dirty = 1;
#pragma unroll 1
                for (int n = 0; n < 64; ++n) {
                    CFENCE();
                    const u32 hv = ld4(rs, fkh);
                    tm = rdl(hv, 5);
                    if (rdl(hv, 3) - now + CH_LATE <= 4000u) break;
                }
            }
        }
        LS2(L == 0 && w == 0, h);
        if (__builtin_expect(!kvok, 0) && s + 1 < NSEG) {
            st4s(rs, (ln == 0 && w == 0) ? hint + 4096u + (u32)h * 128u : OOB, 0, (u32)s + 1u);
            dirty = 1;
            wait_flags(rs, ln < 48u ? fkh : OOB, (u32)s + 2, dead);
            DMA32(dk, l16, ksrc); DMA32(dv, l16, vsrc); DMA32(dq, l16, ksrc ^ 8192u);
            const u32 now = rt32() + CH_KV;
            if ((int)(tm - now) < 0) next_slot(now);
        }
        ++s;
        } else {
            u32 ln = l16; PIN(ln);
            oso = (ln & 256u) ? oso : oso & ~OOB;
            const u32 was = tm - tmp;
            tm = rdl(fl, 5); dirty = 1;
            if ((int)(tm - rdl(fl, 4) - was) > 24) tm -= rdl(fl, 4);
            if ((++missed & 7u) == 0u) {
                *(LAS u32*)(lds + C_DUMP + (w == 0 ? 0 : 64)) = ld4(rs, foff(F_ERR));
                BAR();
                missed = uu(*(LAS u32*)(lds + C_DUMP)) != 0u ? SPIN_CAP : missed;
                st4s(rs, missed == SPIN_CAP ? foff(F_ERR) : OOB, 0, 1u);
            }
        }
    }
    DRAIN(); BAR();
    st4s(rs, ofl, 0, NSEG);
}

#define WO_NI 4
#define WROT 0
DEV void role_wo(const Params& p, lptr lds, int j) {
    const int tid = threadIdx.x, lane = tid & 63, w = uni(tid >> 6), g = lane >> 4, r = lane & 15;
    const int rg = j & 1, cg = j >> 1, rot = cg & WROT;
    const rsrc_t rs = mk_rsrc(p.ws, W_END);
    v8u wo[8][5];
    {
        const v4u so_ = mk_srd(p.Wo, DM * NQ * 2);
        const int one = (0x3F2FF1F0u >> (4 * (cg & 7))) & 15;
        u32 vo[3];
#pragma unroll
        for (int i = 0; i < 3; ++i) { const int n = 64 * i + lane; vo[i] = n % 6 < 5 ? (u32)((n / 6) * (DM * 2) + cg * 80 + (n % 6) * 16) : ((n / 6) & 3) == one ? (u32)((n / 6) * (DM * 2) + (cg >> 3) * 640 + 304 + rg * 16) : OOB; }
        auto fill = [&](int b) {
            const u32 dst = uu(w * 36864 + (b % 3) * 12288), src = uu((8 * w + ((b + rot) & 7)) * (DH * DM * 2));
#pragma unroll
            for (int i = 0; i < 12; ++i) DMAIN(so_, dst + i * 1024, vo[i % 3], src + (i / 3) * (32 * DM * 2));
        };
#pragma unroll 1
        for (int b = 0; b < 3; ++b) fill(b);
        const int q4 = r >> 2;
        lptr tb = lds + w * 36864 + (32 * (g >> 1) + 16 * (q4 >> 1) + 8 * ((r >> 1) & 1) + 4 * (g & 1) + (q4 & 1)) * 96 + (r & 1) * 8;
        SFOR(b, 8)
            VMW(b < 6 ? 24 : 84 - 12 * b);
#pragma unroll
            for (int t = 0; t < 5; ++t) {
                lptr q = tb + (b % 3) * 12288 + (t == 0 ? 4 : (w + t) & 3) * 16;
                wo[b][t] = tr_frag(q, q + 192, q + 64 * 96, q + 64 * 96 + 192);
                if (t < 4) asm volatile("" : "+a"(wo[b][t])); else asm volatile("" : "+v"(wo[b][t]));
            }
            CFENCE();
            if constexpr (b < 5) fill(b + 3);
        SEND
    }
    BAR();
    EV(6);
    const u32 idx = (r & 2) ? 0xEEEEu : 0x4444u;
    bool dead = false;
    const u32 pol0 = get_pol(p, rs, dead) & 1u;
    const u32 l16 = (u32)lane * 16u;
    u32 oo[8];
#pragma unroll
    for (int i = 0; i < 8; ++i) oo[i] = uu(W_ORING + ring_map((u32)(rg * 32768 + (8 * w + ((i + rot) & 7)) * 1024), pol0));
    const u32 hint = W_MRING + 4 * 32768u + pol0 * 8192u;
    u32 hvo[2];
#pragma unroll
    for (int l = 0; l < 2; ++l) hvo[l] = lane < 2 ? hint + (u32)(l ^ 1) * (MB / 4) + (u32)lane * 4u : lane == 2 ? hint + (u32)l * (MB / 4) + 128u : lane == 3 ? foff(F_ERR) : lane == 4 ? hint + (u32)l * (MB / 4) + 256u :
                 (lane >= 32 && j == 0 && w == 3) ? hint + (u32)l * (MB / 4) + (u32)lane * 128u : OOB;
    const int xo = g * 128 + (r & 7) * 16, O = 5 * cg + (r < 8 ? w : 4), q2 = 2 + (r >> 3);
    lptr xw0 = lds + (r < 8 ? 2048 + w * 4 : ((w + 1) & 3) * 512 + 4) + xo, xw1 = lds + ((w + q2) & 3) * 512 + q2 * 4 + xo;
    lptr xr = lds + (r < 8 ? w : 4) * 512 + xo;
    if (r < 8) { *(LAS u32*)xr = 0u; *(LAS u32*)(xr + 8192) = 0u; }
    const u32 mpo = (r & 4) == 0 && (r < 8 || w == 0) ? W_MRING + ring_map((u32)((O >> 1) * 256 + rg * 128 + (4 * (g & 1) + 2 * (O & 1) + (g >> 1)) * 16 + (r & 3) * 4), pol0) : OOB;
    constexpr u32 PER0 = CH_W0 + CH_MW + CH_H0 + CH_MH + 2 * CH_VIS;
    const bool lead = j == 0 && w == 3;
    u32 hso = OOB;
    MK_SRD();
    u32 t = 0, tn = 0;
    int sn[2] = {0, 0};
    u32 hav = 4 * CH_H0, wav = 4 * CH_W0, mh = 8 * (CH_MH + CH_M0), mw = 8 * (CH_MW + CH_M0), ok[2] = {0u, 0u}, dw[2] = {CH_W0 + CH_VIS + CH_MW + CH_REC, CH_W0 + CH_VIS + CH_MW + CH_REC};
    u32 zero = 0, hv = 0xFFFFu, pst = 1, pdur = 0, pmp = OOB;
    const u32 psent = PSENT;
    lptr sw = lds + 5120 + w * 4;
    bool stop = false;
    asm volatile("" : "+v"(zero), "+v"(sw), "+v"(xw0), "+v"(xw1), "+v"(xr));
    auto turn = [&](auto lc) __attribute__((always_inline)) {
        constexpr int L = decltype(lc)::value, M = L ^ 1;
        u32 now, done, rep, rd, lost, dh, dn, nt, za; v4u hw;
        const int sc = sn[L];
        auto books = [&](auto ic) __attribute__((always_inline)) {
            constexpr int i = decltype(ic)::value;
            if constexpr (i == 0) { rep = max(rdl(hv, 2), rdl(hv, 4)); PINS(rep); }
            if constexpr (i == 1) { lost = (u32)(__builtin_amdgcn_ballot_w64(hv == (u32)sn[M]) >> 32); PINS(lost); }
            if constexpr (i == 2) { lost |= (rep >> 16) ^ (u32)sn[M]; PINS(lost); }
            if constexpr (i == 3) { lost |= ok[M] ^ 2u; rd = rep & 0xFFFFu; PINS(lost); PINS(rd); }
            if constexpr (i == 4) { lost = lost == 0u ? CH_UP * 8u : 0u; PINS(lost); }
            if constexpr (i == 5) { dn = wav >> 2; PINS(dn); pdur = min(pdur, dn + 8u); PINS(pdur); }
            if constexpr (i == 6) { pdur = (done & ok[M]) ? pdur : dn; PINS(pdur); }
            if constexpr (i == 7) { wav += pdur - dn; PINS(wav); }
            if constexpr (i == 8) { dh = hav >> 2; PINS(dh); nt = min(rd, dh + 8u); PINS(nt); }
            if constexpr (i == 9) { nt = rd < 0x3FFFu ? nt : dh; PINS(nt); }
            if constexpr (i == 10) { hav += nt - dh; PINS(hav); }
            if constexpr (i == 11) { mw = max(mw - CH_DEC, CH_MW * 8u); PINS(mw); }
            if constexpr (i == 12) { mh = max(mh - CH_DEC, CH_MH * 8u); PINS(mh); }
            if constexpr (i == 13) { rd = rd >> 14 == 1u ? lost : 0u; PINS(rd); }
            if constexpr (i == 14) { mw += rd; mh += lost - rd; PINS(mw); PINS(mh); }
            if constexpr (i == 15) { ok[M] = done ? 1u : (ok[M] & 1u) << 1; PINS(ok[M]); }
            if constexpr (i == 16) { dh = (2u * hav + mh) / 8u + (sn[1] < 2 ? CH_VIS + CH_SOLO : CH_VIS); PINS(dh); }
            if constexpr (i == 17) { dn = (2u * wav + mw) / 8u + CH_VIS; PINS(dn); }
            if constexpr (i == 18) { nt = dw[L] + dh; PINS(nt); }
            if constexpr (i == 19) { dh += dn; dw[L] = dn; sn[M] += (int)done; PINS(dh); PINS(sn[M]); }
            if constexpr (i == 20) { rd = tn - t + (dh >> 1); PINS(rd); }
            if constexpr (i == 21) { rd = sn[M] + 1 < NSEG ? rd : 0u; PINS(rd); }
            if constexpr (i == 22) { nt = t + (u32)max((int)nt, (int)rd); PINS(nt); }
            if constexpr (i == 23) { hw.x = nt; hw.y = dh; PIN(hw.x); PIN(hw.y); }
            if constexpr (i == 24) { hw.z = nt + dn; hw.w = 0u; PIN(hw); }
            if constexpr (i == 25) st16(rs, hso + L * (MB / 4), hw);
        };
        if (__builtin_expect(sc + 1 < NSEG, 1)) {
            u32 vo = l16 + (L * (MB / 2) + (u32)(sc & 3) * 131072u), mp = mpo + (L * (MB / 4) + (u32)((sc + 1) & 3) * 32768u), zb, hn;
            done = uu(pst) ^ 1u;
            za = done ? pmp : OOB;
            zb = ok[L] == 1u ? OOB : mp;
            pmp = mpo + (L * (MB / 4) + (u32)(sc & 3) * 32768u);
            *(LAS u32*)(sw + L * 8192) = zero;
            f32x4 acc[5] = {};
            asm volatile("" : "+v"(acc[0]), "+v"(acc[1]), "+v"(acc[2]), "+v"(acc[3]), "+v"(acc[4]), "+v"(mp), "+v"(pmp), "+v"(vo), "+v"(za), "+v"(zb));
            PINS(done);
#define WT(k) do {} while (0)
            v4u d[8];
            {
                u32 tmp;
                asm volatile(WAIT_TXT("%1", "%2") "buffer_load_dwordx4 %0, %3, %4, %5 offen sc1\n\ts_waitcnt lgkmcnt(0)"
                             : "=&v"(d[0]), "=&s"(tmp) : "s"(uu(t - 1u)), "v"(vo), "s"(srd), "s"(oo[0]) : "vcc", "scc", "memory");
            }
            WT(0);
            SFOR(i, 7)
                asm volatile("buffer_load_dwordx4 %0, %1, %2, %3 offen sc1" : "=v"(d[i + 1]) : "v"(vo), "s"(srd), "s"(oo[i + 1]) : "memory");
                SCHED();
                static_for<3 * i, 3 * i + 3>(books);
                SCHED();
            SEND
            LD4A(hn, hvo[L], 0);
            st4s(rs, za, 0, psent);
            st4s(rs, zb, 0, psent);
            SCHED();
            WT(1);
            float f0[5], f1[5], xs; unsigned long long bad; v4u sv;
#define WSW(o_) asm volatile("v_permlane32_swap_b32 %0, %1" : "+v"(f0[o_]), "+v"(f1[o_]))
#define WAD(o_) asm volatile("v_add_f32_dpp %0, %1, %0 row_ror:8 row_mask:0xf bank_mask:0xf\n\tv_add_f32_dpp %0, %2, %3 row_ror:8 row_mask:0xf bank_mask:0xc" : "+v"(f0[o_]) : "v"(f1[o_]), "v"(f0[o_ + 1]), "v"(f1[o_ + 1]))
#define WFO(o_) do { VADD(f0[o_], acc[o_][0], acc[o_][2]); VADD(f1[o_], acc[o_][1], acc[o_][3]); } while (0)
#define WOWN(k) do { if constexpr (WO_NI + (k) < 8) SMF_V(acc[4], d[(WO_NI + (k)) & 7], wo[(WO_NI + (k)) & 7][4]); SCHED(); } while (0)
            SFOR(i, 40)
                constexpr int pc = i / 5, oc = i % 5;
                if constexpr (oc == 0) VMW(10 - pc);
                if constexpr (oc < 4) SMF_A(acc[oc], d[pc], wo[pc][oc]); else if constexpr (pc < WO_NI) SMF_V(acc[4], d[pc], wo[pc][4]);
                if constexpr (i == 0) WT(2);
                if constexpr (i == 20) WT(3);
                if constexpr (i == 35) WT(4);
                if constexpr (i < 4) books(IC<21 + i>{});
                if constexpr (i == 6) { xs = 0.f; PIN(xs); }
                if constexpr (i == 36) NOPS(1);
                if constexpr (i == 37) NOPS(2);
                SCHED();
            SEND
            WFO(0); WFO(1);
            asm volatile("v_cmp_u_f32 %0, %1, %2" : "=s"(bad) : "v"(f0[0]), "v"(f1[0]));
            WSW(0); WSW(1); WFO(2); WAD(0); WFO(3);
            *(LAS float*)(xw0 + L * 8192) = f0[0]; SCHED();
            WSW(2); WSW(3);
            if (__builtin_expect(bad != 0ull, 0)) {
                u32 mn = ~0u;
#pragma unroll
                for (int i = 0; i < 8; ++i) { u32 e = d[i].x; PIN(e); mn = min(mn, e ^ PSENT); }
                if (all_(mn != 0u)) {
#pragma unroll
                    for (int i = 0; i < 8; ++i) { v4u e = d[i]; PIN(e); mn = min(min(mn, e.y ^ PSENT), min(e.z ^ PSENT, e.w ^ PSENT)); }
                }
                *(LAS u32*)(sw + L * 8192) = all_(mn != 0u) ? 0u : 1u;
            }
            WAD(2);
            *(LAS float*)(xw1 + L * 8192) = f0[2]; SCHED();
            WOWN(0); WOWN(1); WOWN(2); WOWN(3);
            WT(5);
            f32x4 xa; float nb; u32 cw, mq;
            asm volatile("s_waitcnt lgkmcnt(0)\n\ts_barrier\n\tds_read_b128 %0, %2 offset:%4\n\tds_read_b128 %1, %3 offset:%4+5120\n\ts_nop 3"
                         : "=&v"(xa), "=&v"(sv) : "v"(xr), "v"(zero), "i"(L * 8192) : "memory");
            WT(6);
            WFO(4);
            asm volatile("s_nop 0\n\tv_permlane32_swap_b32 %1, %2\n\ts_nop 1\n\tv_add_f32_dpp %0, %2, %1 row_ror:8 row_mask:0xf bank_mask:0x3\n\ts_waitcnt lgkmcnt(1)" : "+v"(xs), "+v"(f0[4]), "+v"(f1[4]));
            asm volatile("v_add_f32 %0, %0, %4\n\tv_add_f32 %1, %5, %6\n\tv_add_f32 %0, %0, %7\n\tv_add_f32 %0, %0, %1\n\tv_cmp_u_f32 vcc, %0, %0\n\ts_nop 0\n\t"
                         "v_mov_b32_dpp %1, %0 row_shl:4 row_mask:0xf bank_mask:0xf\n\tv_cndmask_b32 %3, %8, %9, vcc\n\tv_cvt_pk_bf16_f32 %2, %0, %1"
                         : "+v"(xs), "=&v"(nb), "=&v"(cw), "=&v"(mq) : "v"(xa[0]), "v"(xa[1]), "v"(xa[2]), "v"(xa[3]), "v"(mp), "v"(OOB) : "vcc");
            st4s(rs, mq, 0, cw);
            SCHED();
            WT(7);
            now = rt32();
            books(IC<25>{});
            asm volatile("s_waitcnt vmcnt(4) lgkmcnt(0)" : "+v"(hn), "+v"(sv));
            hv = hn;
            pst = sv.x | sv.y | sv.z | sv.w;
            if (__builtin_expect(__builtin_amdgcn_ballot_w64(mq != mp && pst == 0u) != 0ull, 0)) st4s(rs, mp, 0, cw);
        } else {
            done = uu(pst) ^ 1u;
            st4s(rs, done ? pmp : OOB, 0, psent);
            static_for<0, 25>(books);
            pst = 1;
            BAR();
            hv = ld4(rs, hvo[L]);
            PIN(hv);
            now = rt32();
        }
        pdur = now - t;
        const u32 per = rdl(hv, 1), th = rdl(hv, 0);
        t = lead ? tn : (int)(th - now) < -(int)(per >> 1) ? th + per : th;
        tn = nt;
        dw[M] += max((int)(now + CH_WX - t), 0);
        stop = stop || rdl(hv, 3) != 0u;
    };
#pragma unroll 1
    for (int k = 0; k < 2 * SPIN_CAP && !stop && (sn[0] + 1 < NSEG || sn[1] + 1 < NSEG); ++k) {
        turn(IC<0>{}); turn(IC<1>{});
        if (__builtin_expect(k != 0, 1)) continue;
        if (j == 0) {
            for (int n = 0; n < SPIN_CAP && !dead && sn[0] == 0; ++n) {
                CFENCE();
                if (all_(ld4(rs, W_ORING + pol0 * 8192u + (u32)w * 16384u + (u32)lane * 128u) != PSENT)) break;
                __builtin_amdgcn_s_sleep(1);
            }
            BAR();
        }
        if (lead) {
            t = rt32() + 56u; tn = t + PER0 / 2 + CH_REC;
            st16(rs, lane < 2 ? hint + (u32)lane * (MB / 4) : OOB, (v4u){lane == 0 ? t : tn, PER0, (lane == 0 ? t : tn) + (CH_W0 + CH_VIS + CH_MW + CH_REC), 0u});
        } else {
            for (int n = 0; n < SPIN_CAP && !dead; ++n) {
                CFENCE();
                const u32 hv = ld4(rs, hvo[1]);
                t = rdl(hv, 0);
                if (rdl(hv, 1) < 400u) break;
                __builtin_amdgcn_s_sleep(1);
            }
        }
        hso = (lead && lane == 0) ? hint : OOB;
        hv = 0xFFFFu; dw[0] = dw[1] = CH_W0 + CH_VIS + CH_MW + CH_REC;
    }
    if (sn[0] + 1 < NSEG || sn[1] + 1 < NSEG) st4s(rs, tid == 0 ? foff(F_ERR) : OOB, 0, 1u);
}

__global__ void __launch_bounds__(256, 1) k_main(Args args) {
    Params p;
    (Args&)p = args;
    lptr lds = (lptr)(u32)16 - 16;
    if (blockIdx.x == 0) LST(256, rt32());
    unsigned long long pc; int len;
    asm volatile("s_getpc_b64 %0\n.Lk_main_here:\n\ts_mov_b32 %1, .Lk_end_here-.Lk_main_here" : "=s"(pc), "=s"(len));
    const v4u code = ldin16(mk_rsrc((const void*)(pc & ~15ull), (u32)max(len, 0)), (((blockIdx.x >> 3) & 31u) * 256u + threadIdx.x) * 16u, 0);
    CFENCE();
    if (threadIdx.x == 0) {
        const v4u e = ld16s(mk_rsrc(p.ws, W_END), foff(F_EP), 0);
        const unsigned long long tag = (unsigned long long)e.y << 32 | (e.x + 1u);
        u32 role = ((u32)__builtin_amdgcn_s_getreg(20 | (3 << 11)) & 7u) * 32u + ((blockIdx.x >> 3) & 31u);
#pragma unroll 1
        for (int n = 0; n < 256; ++n, role = (role + 1u) & 255u)
            if (__hip_atomic_exchange((GAS unsigned long long*)(p.ws + W_SLOT) + role * 8u, tag, __ATOMIC_RELAXED, __HIP_MEMORY_SCOPE_AGENT) != tag) break;
        lds_w16(lds, (v4u){e.x + 1u, e.y, e.z, role});
    }
    __syncthreads();
    const v4u boot = lds_r16(lds);
    p.e0 = uu(boot.x); p.e1 = uu(boot.y); p.e2 = uu(boot.z);
    const int role = uni((int)boot.w), x = role >> 5, i = role & 31;
    asm volatile("" :: "v"(code));
    __syncthreads();
    LST(8192 + blockIdx.x * 16, role);
    if (x < 2) { role_head(p, lds, i >> 4, x * 16 + (i & 15)); __builtin_amdgcn_endpgm(); }
    if (x == 2) { role_wo(p, lds, i); __builtin_amdgcn_endpgm(); }
    const int no = x == 3 ? 30 : x < 6 ? 5 : 0;
    if (x > 3 && i >= 8) { role_qkv(p, lds, (x - 4) * 24 + i - 8); __builtin_amdgcn_endpgm(); }
    const bool isout = i < no;
    const int og = x == 3 ? i / 5 : x + 2, ng = i % 5, L = og >> 2, kq = og & 3;
    const int k = i - no, w = uni(threadIdx.x >> 6);
    const int j = isout ? (L == 1 ? 0 : 44) + kq * 5 + ng : x == 3 ? 36 + k : k < 2 ? 12 + 2 * x + k : x < 6 ? 24 + x : k < 5 ? 10 + 3 * x + k : 15 + 3 * x + k;
    const rsrc_t rs = mk_rsrc(p.ws, W_END);
    if (j < NKV) {
        const int nt = j < 320 - 7 * NKV ? 8 : 7;
#pragma unroll 1
        for (int q = 0; q < 4; ++q) kv_fetch(p, j + NKV * q, uu(q * KT_BYTES));
        Filt f;
        u32 warm[3];
#pragma unroll 1
        for (int q = 0; q < nt; ++q) {
            VMW(15); BAR();
            if (q == 0) EV(2);
            kv_fetch(p, j + NKV * (q + 4), uu(((q + 4) & 7) * KT_BYTES));
            kv_tile(p, rs, lds + q * KT_BYTES, lds + (q == 0 ? 8 : q - 1) * KT_BYTES, j + NKV * q);
        }
        filt_load(p, j, f);
        VMW(10); BAR();
        boot_set(p, rs, threadIdx.x == 0 ? foff(F_KV + j) : OOB, 2u);
        EV(4);
        { bool dead = false; boot_wait(p, rs, pol_off(), 0u, dead); }
#pragma unroll
        for (int q = 0; q < 3; ++q)
            warm[q] = __builtin_amdgcn_raw_buffer_load_b32(mk_rsrc(p.ssmk, ACT), 256u * 4096u + ((u32)((q * NKV + j) * 256) + threadIdx.x) * 64u, 0, 0);
        conv_job<0>(p, rs, lds, j, f, 0);
        asm volatile("" :: "v"(warm[0]), "v"(warm[1]), "v"(warm[2]));
        if (j >= 36) filt_load<5, 0>(p, 28 + j, f);
        DRAIN(); BAR();
        st4s(rs, threadIdx.x == 0 ? foff(F_CV + j) : OOB, 0, 2u);
        if (j >= 36) {
#pragma unroll 1
            for (int g = 28 + j; g < 160; g += 8) conv_job<2>(p, rs, lds, g, f, min(g + 8, 159));
            DRAIN(); BAR();
            st4s(rs, threadIdx.x == 0 ? foff(F_CV + 124 + j) : OOB, 0, 3u);
            EV(15);
            __builtin_amdgcn_endpgm();
        }
    }
    if (!isout) { if (j < 28) role_row<0>(p, lds, (j - 20) * 4 + w); else role_row<1>(p, lds, (j - 28) * 4 + w);
        boot_set(p, rs, j == 28 && threadIdx.x == 0 ? foff(F_EP) : OOB, 0u);
        __builtin_amdgcn_endpgm();
    }
    role_out(p, lds, L, kq, ng);
}

__global__ void k_end() { asm volatile(".Lk_end_here:"); }

extern "C" void kernel_launch(void* const* d_in, const int* in_sizes, int n_in, void* d_out, int out_size, void* d_ws, size_t ws_size,
                              hipStream_t stream) {
    Args p;
    p.x = (const u16*)d_in[0]; p.mem0 = (const u16*)d_in[1]; p.Wq = (const u16*)d_in[2]; p.Wk = (const u16*)d_in[3];
    p.Wv = (const u16*)d_in[4]; p.Wo = (const u16*)d_in[5];
    p.bk = (const u16*)d_in[8]; p.bv = (const u16*)d_in[9]; p.shift = (const u16*)d_in[10]; p.ssmk = (const u16*)d_in[11]; p.ssmD = (const u16*)d_in[12];
    p.out = (u16*)d_out;
    p.ws = (unsigned char*)(((size_t)d_ws + 2 * MB - 1) & ~(size_t)(2 * MB - 1));
    (void)hipFuncSetAttribute((const void*)k_main, hipFuncAttributeMaxDynamicSharedMemorySize, LDS_BYTES);
    hipLaunchKernelGGL(k_main, dim3(256), dim3(256), LDS_BYTES, stream, p);
}
```

```cpp
#include <hip/hip_runtime.h>
#include <hip/hip_bf16.h>

typedef unsigned short u16;
typedef unsigned int u32;
typedef u32 v2u __attribute__((ext_vector_type(2)));
typedef u32 v4u __attribute__((ext_vector_type(4)));
typedef u32 v8u __attribute__((ext_vector_type(8)));
typedef float f32x2 __attribute__((ext_vector_type(2)));
typedef float f32x4 __attribute__((ext_vector_type(4)));
typedef float f32x16 __attribute__((ext_vector_type(16)));
typedef __bf16 bf16x2 __attribute__((ext_vector_type(2)));
typedef __bf16 bf16x8 __attribute__((ext_vector_type(8)));
typedef short s16x4 __attribute__((ext_vector_type(4)));
#define LAS __attribute__((address_space(3)))
#define GAS __attribute__((address_space(1)))
typedef LAS unsigned char* lptr;
#define DEV __device__ __forceinline__

constexpr int SEQ = 2048, DM = 640, NH = 32, DH = 128, NSEG = 64, NQ = NH * DH;

constexpr u32 MB = 1u << 20;
constexpr u32 W_ORING = 0;
constexpr u32 W_MRING = MB;
constexpr u32 W_CTRL = 4 * MB;
constexpr u32 ACT = SEQ * DM * 2;
constexpr u32 H_TILE = 128 * 1024;
constexpr u32 W_H2 = 8 * MB;
constexpr u32 W_KQ = 16 * MB;
constexpr u32 W_VO = W_KQ + 64 * MB;
constexpr u32 W_P = W_VO + 64 * MB;
constexpr u32 P_ITEM = 512 * 1024;
constexpr u32 W_H1 = W_P + 44 * MB, W_Y = W_H1 + 8 * MB, W_X2 = W_Y + ACT;
constexpr u32 W_KVT = W_X2 + ACT;
constexpr u32 W_END = W_KVT + 3 * MB;
constexpr u32 NREG = 12;
static_assert((size_t)W_END + 2 * MB <= 256ull * MB && W_H1 >= (NREG - 1) * 16 * MB + 2 * MB && W_H1 <= NREG * 16 * MB, "workspace");
constexpr u32 F_EP = 0, F_RG = 32, F_LN = 64, F_KV = 192,
              F_QKV = 256, F_O = F_QKV + 192, F_P = F_O + 64, F_H2 = F_P + 64, F_CV = F_H2 + 64, F_ERR = F_CV + 191, F_N = F_CV + 192;
constexpr u32 W_SLOT = 3 * MB;
DEV u32 foff(u32 idx) { return W_CTRL + (idx >> 6) * 16384u + (idx & 63u) * 128u; }
DEV u32 near_(u32 o, u32 side, u32 pol) { return o + ((side ^ (pol >> (o >> 24)) ^ (o >> 21)) & 1u) * 8192u; }
DEV u32 p_item(int s) { return W_P + (u32)s * P_ITEM; }

constexpr u32 OOB = 0x80000000u;
constexpr u32 PSENT = 0x7F817F81u;
constexpr int SPIN_CAP = 40000;
constexpr int LDS_BYTES = 160 * 1024;
#define ST_HEAD 0
#define ST_WO 0

#define EV(k) do {} while (0)
#define LST(idx, v) do {} while (0)
#define LS2(cond, idx) do {} while (0)
struct Args {
    const u16 *x, *mem0, *Wq, *Wk, *Wv, *Wo, *bk, *bv, *shift, *ssmk, *ssmD;
    u16* out;
    unsigned char* ws;
};
struct Params : Args { u32 e0, e1, e2; };

typedef __amdgpu_buffer_rsrc_t rsrc_t;
DEV rsrc_t mk_rsrc(const void* p, u32 bytes) { return __builtin_amdgcn_make_buffer_rsrc((void*)p, 0, bytes, 0x00020000); }
DEV v4u ld16s(rsrc_t r, u32 off, u32 so) { return __builtin_amdgcn_raw_buffer_load_b128(r, off, so, 16); }
DEV v2u ld8s(rsrc_t r, u32 off, u32 so) { return __builtin_amdgcn_raw_buffer_load_b64(r, off, so, 16); }
DEV v4u ldin16(rsrc_t r, u32 off, u32 so) { return __builtin_amdgcn_raw_buffer_load_b128(r, off, so, 0); }
DEV v2u ldin8(rsrc_t r, u32 off, u32 so) { return __builtin_amdgcn_raw_buffer_load_b64(r, off, so, 0); }
DEV u32 ld4(rsrc_t r, u32 off) { return __builtin_amdgcn_raw_buffer_load_b32(r, off, 0, 16); }
DEV void st16(rsrc_t r, u32 off, v4u v) { __builtin_amdgcn_raw_buffer_store_b128(v, r, off, 0, 16); }
DEV void st8s(rsrc_t r, u32 off, u32 so, v2u v) { __builtin_amdgcn_raw_buffer_store_b64(v, r, off, so, 16); }
DEV void st4s(rsrc_t r, u32 off, u32 so, u32 v) { __builtin_amdgcn_raw_buffer_store_b32(v, r, off, so, 16); }
#define DRAIN() asm volatile("s_waitcnt vmcnt(0)" ::: "memory")
#define VMW(n) asm volatile("s_waitcnt vmcnt(%0)" :: "i"(n) : "memory")
#define BAR() asm volatile("s_waitcnt lgkmcnt(0)\n\ts_barrier" ::: "memory")
#define SCHED() __builtin_amdgcn_sched_barrier(0)
#define CFENCE() asm volatile("" ::: "memory")
#define NOPS(n) asm volatile("s_nop %0" :: "i"(n))
#define PIN(x) asm volatile("" : "+v"(x))
#define PINS(x) asm volatile("" : "+s"(x))

template <int V> struct IC { static constexpr int value = V; };
template <int I, int N, class F> DEV void static_for(F&& f) { if constexpr (I < N) { f(IC<I>{}); static_for<I + 1, N>(f); } }
#define SFOR(var, n) static_for<0, n>([&](auto ic_) __attribute__((always_inline)) { constexpr int var = decltype(ic_)::value;
#define SEND });

DEV u32 pk(float a, float b) { bf16x2 v = {(__bf16)a, (__bf16)b}; return __builtin_bit_cast(u32, v); }
DEV float lo_f(u32 u) { return __builtin_bit_cast(float, u << 16); }
DEV float hi_f(u32 u) { return __builtin_bit_cast(float, u & 0xffff0000u); }
DEV f32x2 up2(u32 u) { return (f32x2){lo_f(u), hi_f(u)}; }
DEV u32 pk2(f32x2 v) { return pk(v.x, v.y); }
DEV float rbf(float a) { return (float)(__bf16)a; }
DEV v4u lds_r16(lptr p) { return *(LAS v4u*)p; }
DEV void lds_w16(lptr p, v4u v) { *(LAS v4u*)p = v; }
DEV v2u lds_tr(lptr p) { return __builtin_bit_cast(v2u, __builtin_amdgcn_ds_read_tr16_b64_v4i16((LAS s16x4*)p)); }
DEV v4u tr2(lptr p0, lptr p1) { const v2u a = lds_tr(p0), b = lds_tr(p1); return (v4u){a.x, a.y, b.x, b.y}; }
DEV v8u cat8(v4u a, v4u b) { return __builtin_shufflevector(a, b, 0, 1, 2, 3, 4, 5, 6, 7); }
DEV f32x4 mfma16(v4u a, v4u b, f32x4 c) { return __builtin_amdgcn_mfma_f32_16x16x32_bf16(__builtin_bit_cast(bf16x8, a), __builtin_bit_cast(bf16x8, b), c, 0, 0, 0); }
DEV f32x16 mfma32(v4u a, v4u b, f32x16 c) { return __builtin_amdgcn_mfma_f32_32x32x16_bf16(__builtin_bit_cast(bf16x8, a), __builtin_bit_cast(bf16x8, b), c, 0, 0, 0); }

DEV float f_(u32 u) { return __builtin_bit_cast(float, u); }
DEV u32 u_(float f) { return __builtin_bit_cast(u32, f); }
template <int CTRL> DEV float dpp(float v) { return f_((u32)__builtin_amdgcn_update_dpp(0, (int)u_(v), CTRL, 0xf, 0xf, false)); }
DEV float max16(float v) { auto r = __builtin_amdgcn_permlane16_swap(u_(v), u_(v), false, false); return fmaxf(f_(r[0]), f_(r[1])); }
DEV float max32(float v) { auto r = __builtin_amdgcn_permlane32_swap(u_(v), u_(v), false, false); return fmaxf(f_(r[0]), f_(r[1])); }
DEV float add16(float v) { auto r = __builtin_amdgcn_permlane16_swap(u_(v), u_(v), false, false); return f_(r[0]) + f_(r[1]); }
DEV float add32(float v) { auto r = __builtin_amdgcn_permlane32_swap(u_(v), u_(v), false, false); return f_(r[0]) + f_(r[1]); }
DEV float wave_sum(float v) {
    v += dpp<0xB1>(v);
    v += dpp<0x4E>(v);
    v += dpp<0x124>(v);
    v += dpp<0x128>(v);
    return add32(add16(v));
}
DEV bool all_(bool c) { return __builtin_amdgcn_ballot_w64(!c) == 0ull; }
DEV int uni(int v) { return __builtin_amdgcn_readfirstlane(v); }
DEV u32 uu(u32 v) { return (u32)__builtin_amdgcn_readfirstlane((int)v); }
DEV u32 rdl(u32 v, int l) { return (u32)__builtin_amdgcn_readlane((int)v, l); }
DEV u32 rt32() { return (u32)__builtin_amdgcn_s_memrealtime(); }

DEV void wait_flags(rsrc_t rs, u32 off, u32 want, bool& dead) {
    if (dead) return;
#pragma unroll 1
    for (int n = 0;; ++n) {
        CFENCE();
        const u32 v = ld4(rs, off);
        if (all_(off >= OOB || v >= want)) break;
        __builtin_amdgcn_s_sleep(1);
        if ((n & 63) == 63) { if (uni((int)ld4(rs, foff(F_ERR))) != 0) { dead = true; break; } }
        if (n > SPIN_CAP) { st4s(rs, foff(F_ERR), 0, 1u); dead = true; break; }
    }
    CFENCE();
}

DEV bool boot_ok(const Params& p, u32 off, v4u v, u32 want) { return all_(off >= OOB || (v.x == p.e0 && v.y == p.e1 && v.z == p.e2 && v.w >= want)); }
DEV void boot_set(const Params& p, rsrc_t rs, u32 off, u32 v) { st16(rs, off, (v4u){p.e0, p.e1, p.e2, v}); }
DEV u32 boot_wait(const Params& p, rsrc_t rs, u32 off, u32 want, bool& dead) {
    v4u v;
#pragma unroll 1
    for (int n = 0;; ++n) {
        CFENCE();
        v = ld16s(rs, off, 0);
        if (boot_ok(p, off, v, want) || dead) break;
        __builtin_amdgcn_s_sleep(1);
        if (n > SPIN_CAP) { st4s(rs, foff(F_ERR), 0, 1u); dead = true; }
    }
    CFENCE();
    return v.w;
}

DEV u32 pol_off() { const int lane = threadIdx.x & 63; return lane < 32 ? foff(F_RG + lane) : OOB; }
DEV u32 pol_of(u32 raw) {
    const int lane = threadIdx.x & 63;
    const float v = add16(__builtin_amdgcn_fmed3f((float)((int)raw - (1 << 20)), -250.f, 250.f));
    const float lo = dpp<0x121>(v), hi = dpp<0x12F>(v), ex = dpp<0x122>(v) + dpp<0x123>(v) + dpp<0x124>(v);
    const float sum = v + lo + ((lane & 15) == 11 ? 0.f : hi) + ((lane & 15) == 0 ? ex : 0.f);
    return (u32)__builtin_amdgcn_ballot_w64(sum >= 0.f) & 0xFFFFu;
}
DEV u32 get_pol(const Params& p, rsrc_t rs, bool& dead) { return pol_of(boot_wait(p, rs, pol_off(), 0u, dead)); }
DEV u32 hblk(u32 base, int ls, int h, u32 side, u32 pol) { return near_(base + (u32)(ls * NH + h) * 16384u, side, pol); }
DEV f32x2 ln_stats(const f32x2 (&x)[5]) {
    const float c = f_(uu(u_(x[0].x)));
    f32x2 d[5];
#pragma unroll
    for (int i = 0; i < 5; ++i) d[i] = x[i] - c;
    const f32x2 s2 = (d[0] + d[1]) + (d[2] + d[3]) + d[4], q = (d[0] * d[0] + d[1] * d[1]) + (d[2] * d[2] + (d[3] * d[3] + d[4] * d[4]));
    const float dm = wave_sum(s2.x + s2.y) * (1.0f / DM), mean = c + dm;
    const float var = fmaxf(wave_sum(q.x + q.y) * (1.0f / DM) - dm * dm, 0.f);
    const float sd = rbf(__builtin_amdgcn_sqrtf(rbf(rbf(var) + 1e-5f)));
    return (f32x2){rbf(mean), __builtin_amdgcn_rcpf(sd)};
}
DEV void store_h(rsrc_t rs, u32 tile, int row, const u32 (&hp)[5]) {
    const u32 lane = threadIdx.x & 63;
    const u32 sw = (u32)(row & 15) * 16u, o8 = (lane >> 5) * 16384u + (((lane & 31u) * 8u) ^ sw), hb = tile + (u32)(row & 31) * 256u;
    st8s(rs, o8, hb, (v2u){hp[0], hp[1]});
    st8s(rs, o8, hb + 32768, (v2u){hp[2], hp[3]});
    st4s(rs, (lane * 4) ^ sw, hb + 65536, hp[4]);
}

#define DMA16(ldsb, vo, so) asm volatile("s_mov_b32 m0, %0\n\ts_nop 0\n\tbuffer_load_dwordx4 %1, %2, %3 offen sc1 lds" :: "s"(ldsb), "v"(vo), "s"(srd), "s"(so) : "memory")
#define DMA16I(ldsb, lo, vo, so, go) do { u32 so_; asm volatile("s_add_u32 m0, %1, %5\n\ts_add_u32 %0, %4, %6\n\tbuffer_load_dwordx4 %2, %3, %0 offen sc1 lds" \
                         : "=&s"(so_) : "s"(ldsb), "v"(vo), "s"(srd), "s"(so), "i"(lo), "i"(go) : "memory", "scc"); } while (0)
#define DMA32(ldsb, vo, so) asm volatile("s_mov_b32 m0, %0\n\ts_nop 0\n\tbuffer_load_dwordx4 %1, %2, %3 offen sc1 lds\n\tbuffer_load_dwordx4 %1, %2, %3 offen offset:1024 sc1 lds" \
                         :: "s"(ldsb), "v"(vo), "s"(srd), "s"(so) : "memory")
#define INPOL "nt"
#define XPOL ""
#define INAUX 2
#define DMANT(sr, ldsb, vo, so) asm volatile("s_mov_b32 m0, %0\n\ts_nop 0\n\tbuffer_load_dwordx4 %1, %2, %3 offen " INPOL " lds" :: "s"(ldsb), "v"(vo), "s"(sr), "s"(so) : "memory")
#define DMAIN(sr, ldsb, vo, so) asm volatile("s_mov_b32 m0, %0\n\ts_nop 0\n\tbuffer_load_dwordx4 %1, %2, %3 offen lds" :: "s"(ldsb), "v"(vo), "s"(sr), "s"(so) : "memory")
DEV const u16* sel(bool c, const u16* a, const u16* b) { PINS(a); PINS(b); return c ? a : b; }
DEV v4u mk_srd(const void* q, u32 bytes) { return (v4u){uu((u32)(size_t)q), uu((u32)((size_t)q >> 32) & 0xffffu), bytes, 0x00020000u}; }
#define MK_SRD() const v4u srd = mk_srd(p.ws, W_END)

constexpr int CV_PAD = 40;
constexpr int CV_PL = 4416;
constexpr int CV_F = 8 * CV_PL;
constexpr int CV_KB = 80;
constexpr int CV_KV = 65 * CV_KB;
constexpr int CV_CH = CV_F + CV_KV;
static_assert(CV_CH % 16 == 0 && 4 * CV_CH <= LDS_BYTES, "conv lds");

struct Filt { v4u a[5], b[5]; };
DEV int chan(int grp, int w) { return 16 * (grp >> 2) + (grp & 3) + 4 * w; }
template <int N = 5, int AUX = INAUX> DEV void filt_load(const Params& p, int grp, Filt& f) {
    const int tid = threadIdx.x, lane = tid & 63, d = chan(grp, tid >> 6);
    const rsrc_t rf = mk_rsrc(p.ssmk, ACT);
#pragma unroll
    for (int i = 0; i < N; ++i) {
        const u32 s0 = (u32)(8 * (lane + 64 * i) - CV_PAD);
        f.a[i] = __builtin_amdgcn_raw_buffer_load_b128(rf, s0 < SEQ ? (u32)(d * SEQ + s0) * 2u : OOB, 0, AUX);
        f.b[i] = __builtin_amdgcn_raw_buffer_load_b128(rf, s0 + 8 < SEQ ? (u32)(d * SEQ + s0 + 8) * 2u : OOB, 0, AUX);
    }
}
constexpr int KT_BYTES = 18 * 1024, KT_X = 64 * 144, NKV = 44, KT_DUMP = 8 * KT_BYTES + KT_X;
DEV void kv_fetch(const Params& p, int tile, u32 dst) {
    const int lane = threadIdx.x & 63, w = uni(threadIdx.x >> 6);
    const v4u sk = mk_srd(p.bk, ACT), sv = mk_srd(p.bv, ACT), ss = mk_srd(sel(w == 0, p.bk, p.shift), w == 0 ? ACT : DM * 8);
    const u32 vo = tile < 320 ? (u32)((lane >> 3) * DM + (tile % 10) * 64 + (lane & 7) * 8) * 2u : OOB, r0 = uu((u32)((tile / 10) * 64 + 8 * w) * (DM * 2)), d = uu(dst + w * 1024);
    DMANT(sk, d + 1024, vo, r0); DMANT(sk, d + 5120, vo, r0 + 32 * DM * 2); DMANT(sv, d + 9216, vo, r0); DMANT(sv, d + 13312, vo, r0 + 32 * DM * 2);
    DMAIN(ss, uu(w == 0 ? dst : w == 1 ? dst + 17408 : KT_DUMP), w == 0 ? (tile >= 10 ? vo : OOB) : w == 1 && lane < 32 ? (u32)lane * 16u : OOB,
          uu(w == 0 ? (tile >= 10 ? r0 - 8 * DM * 2 : 0u) : (u32)(tile % 10) * 512u));
}
DEV void kv_tile(const Params& p, rsrc_t rs, lptr src, lptr xt, int tile) {
    const int tid = threadIdx.x;
    const v4u sv = lds_r16(src + 17408 + (tid & 31) * 16);
    lptr kp = src + ((tid >> 5) * 8 + 5) * 128 + (tid & 31) * 4;
    f32x2 kf[11];
#pragma unroll
    for (int i = 0; i < 11; ++i) kf[i] = up2(*(LAS u32*)(kp + i * 128));
    const f32x2 sh[4] = {{lo_f(sv.x), lo_f(sv.z)}, {hi_f(sv.x), hi_f(sv.z)}, {lo_f(sv.y), lo_f(sv.w)}, {hi_f(sv.y), hi_f(sv.w)}};
    u32 kv[8];
#pragma unroll
    for (int i = 0; i < 8; ++i) {
        const f32x2 acc = (kf[i] * sh[3] + kf[i + 1] * sh[2]) + (kf[i + 2] * sh[1] + kf[i + 3] * sh[0]);
        kv[i] = pk2(acc * up2(*(LAS u32*)(kp + 9216 + (i - 5) * 128)));
    }
    lds_w16(xt + (2 * (tid & 31)) * 144 + (tid >> 5) * 16, (v4u){__builtin_amdgcn_perm(kv[6], kv[7], 0x05040100u), __builtin_amdgcn_perm(kv[4], kv[5], 0x05040100u),
                                                             __builtin_amdgcn_perm(kv[2], kv[3], 0x05040100u), __builtin_amdgcn_perm(kv[0], kv[1], 0x05040100u)});
    lds_w16(xt + (2 * (tid & 31) + 1) * 144 + (tid >> 5) * 16, (v4u){__builtin_amdgcn_perm(kv[6], kv[7], 0x07060302u), __builtin_amdgcn_perm(kv[4], kv[5], 0x07060302u),
                                                                 __builtin_amdgcn_perm(kv[2], kv[3], 0x07060302u), __builtin_amdgcn_perm(kv[0], kv[1], 0x07060302u)});
    BAR();
#pragma unroll
    for (int i = 0; i < 2; ++i) {
        const int ch = i * 32 + (tid >> 3);
        st16(rs, W_KVT + (u32)(((tile % 10) * 64 + ch) * SEQ + (tile / 10) * 64 + (tid & 7) * 8) * 2u, lds_r16(xt + ch * 144 + (tid & 7) * 16));
    }
}
template <int MODE> DEV void conv_job(const Params& p, rsrc_t rs, lptr lds, int grp, Filt& f, int next) {
    const int tid = threadIdx.x, lane = tid & 63, w = uni(tid >> 6), bq = lane & 31, hh = lane >> 5;
    lptr F = lds + w * CV_CH;
    lptr fa = F + (bq & 7) * CV_PL + ((bq >> 3) - hh + 4) * 16;
    lptr kb = F + CV_F + CV_KB + hh * 16, kh = kb + (bq + 32) * CV_KB;
    bool dead = false;
    v4u kvr[4];
    const u32 D = p.ssmD[chan(grp, w)];
    auto kv_load = [&]() {
        if (MODE == 0) boot_wait(p, rs, lane < NKV ? foff(F_KV + lane) : OOB, 2u, dead);
#pragma unroll
        for (int i = 0; i < (MODE == 1 ? 2 : 4); ++i) kvr[i] = ld16s(rs, (u32)lane * 16u, W_KVT + (u32)(chan(grp, w) * SEQ + i * 512) * 2u);
    };
    auto windows = [&](int i) {
        if (i < 4 || lane < 16) {
            const u32 e[8] = {f.a[i].x, f.a[i].y, f.a[i].z, f.a[i].w, f.b[i].x, f.b[i].y, f.b[i].z, f.b[i].w};
            u32 o[7];
#pragma unroll
            for (int q = 0; q < 7; ++q) o[q] = __builtin_amdgcn_alignbit(e[q + 1], e[q], 16);
#pragma unroll
            for (int q = 0; q < 4; ++q) {
                lds_w16(F + (2 * q) * CV_PL + (64 * i + lane) * 16, (v4u){o[q], o[q + 1], o[q + 2], o[q + 3]});
                lds_w16(F + (2 * q + 1) * CV_PL + (64 * i + lane) * 16, (v4u){e[q + 1], e[q + 2], e[q + 3], e[q + 4]});
            }
        }
    };
    lptr yw = lds + (lane >> 4) * CV_CH + 3072 + (lane & 14) * 64 + w * 32 + (lane & 1) * 16, yr = F + 3072 + lane * 16;
    const u32 yo = W_Y + (u32)((((w & 1) * 8 + (lane >> 3)) * 8 + (w >> 1)) * DM + (grp & 3) * 160 + 4 * (grp >> 2)) * 32u + (u32)(lane & 7) * 16u;
    auto put = [&](const f32x16& ac, int q) {
        const int r = 4 * (q & 3);
        const auto x = __builtin_amdgcn_permlane32_swap(pk(ac[r], ac[r + 1]), pk(ac[r + 4], ac[r + 5]), false, false);
        const auto y = __builtin_amdgcn_permlane32_swap(pk(ac[r + 2], ac[r + 3]), pk(ac[r + 6], ac[r + 7]), false, false);
        lds_w16(yw + (q >> 1) * CV_PL, (v4u){x[0], y[0], x[1], y[1]});
    };
    if (MODE != 2) EV(8);
    if (MODE != 0) kv_load();
    if (lane < CV_KB / 16) lds_w16(F + CV_F + lane * 16, (v4u){0u, 0u, 0u, 0u});
    windows(0);
    if (MODE != 1) { windows(1); windows(2); windows(4); }
    if (MODE == 0) kv_load();
    if (MODE == 2) windows(3);
    if (MODE != 2) EV(9);
#pragma unroll
    for (int i = 0; i < (MODE == 1 ? 2 : 4); ++i) lds_w16(F + CV_F + (16 * i + (lane >> 2) + 1) * CV_KB + (lane & 3) * 16, kvr[i]);
    v4u dg[2];
#pragma unroll
    for (int ks = 0; ks < 2; ++ks)
#pragma unroll
        for (int q = 0; q < 4; ++q) { const int j = 16 * ks + 8 * hh + 7 - bq; dg[ks][q] = j == 2 * q ? D : j == 2 * q + 1 ? D << 16 : 0u; }
    f32x16 acc0 = {}, acc1 = {};
    auto first = [&]() { asm volatile("s_waitcnt vmcnt(0)\n\ts_barrier" ::: "memory"); st4s(rs, tid == 0 ? foff(F_CV + grp) : OOB, 0, 1u); EV(12); };
    if (MODE != 2) {
        EV(10);
        acc0 = mfma32(lds_r16(kb + bq * CV_KB), dg[0], acc0); acc0 = mfma32(lds_r16(kb + bq * CV_KB + 32), dg[1], acc0);
#pragma unroll
        for (int m = 0; m < 8; ++m) {
            lptr kl = kb + max(bq - m, -1) * CV_KB;
            acc0 = mfma32(lds_r16(kl), lds_r16(fa + m * 64), acc0); acc0 = mfma32(lds_r16(kl + 32), lds_r16(fa + m * 64 - 32), acc0);
        }
        put(acc0, 0);
        BAR();
        st16(rs, w < 2 ? yo : OOB, lds_r16(yr));
        EV(11);
    }
    if (MODE == 1) {
        windows(1); windows(2);
        first();
        v4u a[2][2], l[2][2];
#pragma unroll
        for (int u = 0; u < 2; ++u) { lptr kl = kb + max(bq - 8 - u, -1) * CV_KB; a[u][0] = lds_r16(fa + (8 + u) * 64); l[u][0] = lds_r16(kl); a[u][1] = lds_r16(fa + (8 + u) * 64 - 32); l[u][1] = lds_r16(kl + 32); }
#pragma unroll 1
        for (int m = 8; m < 32; m += 2) {
#pragma unroll
            for (int u = 0; u < 2; ++u) {
                lptr kl = kb + max(bq - m - u - 2, -1) * CV_KB;
                acc0 = mfma32(l[u][0], a[u][0], acc0); a[u][0] = lds_r16(fa + (m + u + 2) * 64); l[u][0] = lds_r16(kl); SCHED();
                acc0 = mfma32(l[u][1], a[u][1], acc0); a[u][1] = lds_r16(fa + (m + u + 2) * 64 - 32); l[u][1] = lds_r16(kl + 32); SCHED();
            }
        }
    } else {
        if (MODE == 0) windows(3); else filt_load<5, 0>(p, next, f);
        v4u a0 = lds_r16(fa), a1 = lds_r16(fa - 32), c0 = lds_r16(fa + 2048), c1 = lds_r16(fa + 2048 - 32);
        v4u h0 = lds_r16(kh), h1 = lds_r16(kh + 32), l0 = lds_r16(kb + bq * CV_KB), l1 = lds_r16(kb + bq * CV_KB + 32);
        acc1 = mfma32(h0, dg[0], acc1); acc1 = mfma32(h1, dg[1], acc1);
#pragma unroll 2
        for (int m = 0; m < (MODE == 0 ? 8 : 32); ++m) {
            lptr kl = kb + max(bq - m - 1, -1) * CV_KB;
            if (MODE == 0 && m == 4) first();
            acc1 = mfma32(h0, a0, acc1); h0 = lds_r16(kh - (m + 1) * CV_KB); a0 = lds_r16(fa + (m + 1) * 64); SCHED();
            acc1 = mfma32(l0, c0, acc1); c0 = lds_r16(fa + (m + 1) * 64 + 2048); l0 = lds_r16(kl); SCHED();
            acc1 = mfma32(h1, a1, acc1); h1 = lds_r16(kh - (m + 1) * CV_KB + 32); a1 = lds_r16(fa + (m + 1) * 64 - 32); SCHED();
            acc1 = mfma32(l1, c1, acc1); c1 = lds_r16(fa + (m + 1) * 64 + 2048 - 32); l1 = lds_r16(kl + 32); SCHED();
        }
        if (MODE == 0) {
#pragma unroll 2
            for (int m = 8; m < 32; ++m) {
                lptr kl = kb + max(bq - m - 1, -1) * CV_KB;
                acc1 = mfma32(h0, a0, acc1); h0 = lds_r16(kh - (m + 1) * CV_KB); SCHED();
                acc0 = mfma32(l0, a0, acc0); a0 = lds_r16(fa + (m + 1) * 64); SCHED();
                acc1 = mfma32(l0, c0, acc1); c0 = lds_r16(fa + (m + 1) * 64 + 2048); l0 = lds_r16(kl); SCHED();
                acc1 = mfma32(h1, a1, acc1); h1 = lds_r16(kh - (m + 1) * CV_KB + 32); SCHED();
                acc0 = mfma32(l1, a1, acc0); a1 = lds_r16(fa + (m + 1) * 64 - 32); SCHED();
                acc1 = mfma32(l1, c1, acc1); c1 = lds_r16(fa + (m + 1) * 64 + 2048 - 32); l1 = lds_r16(kl + 32); SCHED();
            }
        }
        BAR();
    }
    if (MODE != 2) { EV(13); put(acc0, 0); put(acc0, 2); }
    if (MODE != 1) { put(acc1, 4); put(acc1, 6); }
    BAR();
#pragma unroll
    for (int q = (MODE == 2 ? 2 : 0); q < (MODE == 1 ? 2 : 4); ++q) st16(rs, q == 0 && w < 2 ? OOB : yo + (u32)q * (2 * DM * 32), lds_r16(yr + q * CV_PL));
    BAR();
    if (MODE != 2) EV(14);
}

#define NPROBE 6
DEV int probe(rsrc_t rs, int c) {
    const int lane = threadIdx.x & 63;
    const u32 k = (u32)(c & 15) < NREG ? (u32)(c & 15) : 0u;
    const u32 off0 = k * (16 * MB) + 2 * MB - 65536 + ((u32)(c & 15) >= NREG ? (u32)(c & 3) + 1u : 0u) * 256 + (u32)(c >> 4) * 128;
    st4s(rs, lane == 0 ? off0 : OOB, 0, 0u); DRAIN();
    int d0 = 1 << 16, d1 = 1 << 16;
#pragma unroll 1
    for (int q = 0; q < NPROBE; ++q) {
        const u32 o = lane == 0 ? off0 + (u32)(q & 1) * 8192 + (u32)(q >> 1) * 18432 : OOB;
        const unsigned long long t0 = __builtin_amdgcn_s_memtime();
        st4s(rs, o, 0, 0u); DRAIN();
        const int dt = (int)(__builtin_amdgcn_s_memtime() - t0);
        if (q & 1) d1 = min(d1, dt); else d0 = min(d0, dt);
    }
    const u32 xcc = (u32)__builtin_amdgcn_s_getreg(20 | (3 << 11)) & 0xfu;
    return (d0 - d1) * ((xcc & 4u) ? -1 : 1);
}
DEV void pre_rings(const Params& p, rsrc_t rs, int j) {
    const u32 gt = (u32)(j * 256 + threadIdx.x) * 16u, n = ((gt >> 14) << 9) + ((gt & 8191u) >> 4);
    const v4u ps = {PSENT, PSENT, PSENT, PSENT};
    v4u m = ps;
    if (n < 640u) m = *(const v4u*)(p.mem0 + (2 * ((n >> 2) & 3) + (n & 1)) * DM + (n >> 4) * 16 + (n & 2) * 4);
#pragma unroll
    for (u32 i = 0; i < 8; ++i) st16(rs, W_ORING + gt + i * 131072u, ps);
#pragma unroll
    for (u32 i = 0; i < 2; ++i) st16(rs, gt < 16384u ? W_MRING + i * (MB / 4) + 131072u + gt : OOB, ps);
    if (n < 640u) {
        m = (v4u){__builtin_amdgcn_perm(m.z, m.x, 0x05040100u), __builtin_amdgcn_perm(m.z, m.x, 0x07060302u), __builtin_amdgcn_perm(m.w, m.y, 0x05040100u), __builtin_amdgcn_perm(m.w, m.y, 0x07060302u)};
#pragma unroll
        for (int i = 0; i < 4; ++i) m[i] = m[i] == PSENT ? 0x7FC17FC1u : m[i];
    }
    st16(rs, W_MRING + gt, m);
    st16(rs, W_MRING + MB / 4 + gt, m);
    static_assert(2 * (F_N - F_QKV) == 32 * 36, "clear");
    st4s(rs, threadIdx.x < 36 ? foff(F_QKV) + (u32)(j * 36 + threadIdx.x) * 128u : OOB, 0, 0u);
}

#define M32A(first, d, a, b) do { if (first) asm volatile("v_mfma_f32_32x32x16_bf16 %0, %1, %2, 0" : "=&v"(d) : "a"(a), "v"(b)); \
                                  else asm volatile("v_mfma_f32_32x32x16_bf16 %0, %1, %2, %0" : "+v"(d) : "a"(a), "v"(b)); } while (0)
#define M32B(first, d, a, b) do { if (first) asm volatile("v_mfma_f32_32x32x16_bf16 %0, %1, %2, 0" : "=&v"(d) : "v"(a), "a"(b)); \
                                  else asm volatile("v_mfma_f32_32x32x16_bf16 %0, %1, %2, %0" : "+v"(d) : "v"(a), "a"(b)); } while (0)
#define VCVT(d, a, b) asm volatile("v_cvt_pk_bf16_f32 %0, %1, %2" : "=v"(d) : "v"(a), "v"(b))
#define LD4A(d, vo, so) asm volatile("buffer_load_dword %0, %1, %2, %3 offen sc1" : "=v"(d) : "v"(vo), "s"(srd), "s"(so) : "memory")
DEV u32 wst_src(int lane, int ld) { return (u32)((lane >> 4) * ld + 8 * ((lane & 15) ^ ((lane >> 4) << 2))) * 2u; }
DEV u32 wst_rd(int lane, int w) { const int q4 = (lane >> 2) & 3; return (u32)(q4 * 256 + ((w ^ q4) * 64) + ((lane >> 4) & 1) * 32 + (lane & 3) * 8); }

constexpr int QK_A = 65536;
#define QLEAD 4
#define QFIRST 3
static_assert(QFIRST < QLEAD, "a round of the lead carries the flag of the convolution");
#define QW0 10
#define QW1 3
#define QSAMP 72
DEV void role_qkv(const Params& p, lptr lds, int c) {
    const int grp = 64 + c;
    const int tid = threadIdx.x, lane = tid & 63, w = uni(tid >> 6), hh = lane >> 5, r32 = lane & 31;
    const int m = c >> 5, h = c & 31;
    const rsrc_t rs = mk_rsrc(p.ws, W_END);
    v4u wt[40];
    Filt flt;
    bool dead = false;
    u32 pol, h2flag, fo, ol, ar[8];
    const u32 ds = uu(w * 1024), dx = ds + QK_A;
#define XRD(ks) lds_r16(lds + ar[(ks) & 7] + ((ks) >> 3) * 8192)
    const u32 t16 = (u32)tid * 16u, lnf = tid < 96 ? foff(F_LN + tid) : OOB;
    MK_SRD();
#define QDMA(i, dst, vo, so) DMA16I(dst, (i) * 4096, vo, so, ((i) >> 1) * 16384 + ((i) & 1) * 4096)
    {
        const v4u sw = mk_srd(sel(m == 0, p.Wq, sel(m == 1, p.Wk, p.Wv)), DM * NQ * 2), sx = mk_srd(p.x, ACT);
        const u32 vo = wst_src(lane, NQ) + (u32)h * (DH * 2), x8 = (u32)lane * 8u, x4 = (u32)lane * 4u;
        v2u xa[6], xb[6]; u32 xc[6];
#pragma unroll
        for (int i = 0; i < 6; ++i)
            asm volatile("buffer_load_dwordx2 %0, %3, %5, %6 offen " XPOL "\n\tbuffer_load_dwordx2 %1, %3, %5, %6 offen offset:512 " XPOL "\n\tbuffer_load_dword %2, %4, %5, %6 offen offset:1024 " XPOL
                         : "=&v"(xa[i]), "=&v"(xb[i]), "=&v"(xc[i]) : "v"(x8), "v"(x4), "s"(sx), "s"(uu((4 * c + w + 384 * i) * (DM * 2))) : "memory");
        EV(1);
        auto wfill = [&](int i0, int n) {
#pragma unroll 1
            for (int i = i0; i < i0 + n; ++i) DMAIN(sw, uu((4 * i + w) * 1024), vo, uu((16 * i + 4 * w) * (NQ * 2)));
        };
        wfill(0, QW0);
        asm volatile("s_waitcnt vmcnt(%0) ; HZCLR" :: "i"(QW0) : "memory");
        EV(2);
#pragma unroll
        for (int i = 0; i < 6; ++i) {
            wfill(QW0 + QW1 * i, QW1);
            if (i == 2) { VMW(2 * QW1 + 3); BAR(); boot_set(p, rs, tid == 0 ? foff(F_LN + c) : OOB, 1u); }
            asm volatile("" : "+v"(xa[i]), "+v"(xb[i]), "+v"(xc[i]));
            const int row = 4 * c + w + 384 * i;
            if (i < 5 || row < SEQ) {
                const f32x2 x[5] = {up2(xa[i].x), up2(xa[i].y), up2(xb[i].x), up2(xb[i].y), up2(xc[i])};
                const f32x2 st = ln_stats(x);
                u32 hp[5];
#pragma unroll
                for (int q = 0; q < 5; ++q) hp[q] = pk2(up2(pk2(x[q] - st.x)) * st.y);
                store_h(rs, W_H1 + (u32)(row >> 5) * H_TILE, row, hp);
            }
        }
        EV(3);
        constexpr int K0 = QW0 + 6 * QW1;
        static_assert(K0 * 4096 >= QK_A + 40960, "the first tile's place is free");
        v4u fv, praw;
        asm volatile("buffer_load_dwordx4 %0, %2, %4, 0 offen sc1\n\tbuffer_load_dwordx4 %1, %3, %4, 0 offen sc1" : "=&v"(praw), "=&v"(fv) : "v"(pol_off()), "v"(lnf), "s"(srd) : "memory");
        wfill(K0, 40 - K0);
        asm volatile("s_waitcnt vmcnt(%2) ; HZCLR\n\ts_barrier" : "+v"(praw), "+v"(fv) : "i"(40 - K0) : "memory");
        boot_set(p, rs, tid == 0 ? foff(F_LN + c) : OOB, 2u);
        EV(4);
        lptr tb = lds + hh * 2048 + wst_rd(lane, w);
        SFOR(k, 40)
            if constexpr (k == K0) {
                if (!boot_ok(p, lnf, fv, 1u)) boot_wait(p, rs, lnf, 1u, dead);
                if (!boot_ok(p, pol_off(), praw, 0u)) praw.w = boot_wait(p, rs, pol_off(), 0u, dead);
                BAR();
                EV(5);
                const u32 so = W_H1;
                SFOR(i, 10) QDMA(i, dx, t16, so); SEND
                pol = pol_of(praw.w);
                h2flag = tid < 8 ? near_(foff(F_H2 + lane), 1u, pol) : OOB;
                ol = (m == 2 ? (u32)(2 * w + (r32 >> 4)) * 1024u : (u32)((r32 >> 4) * 4 + w) * 1024u) + (u32)(hh * 16 + (r32 & 15)) * 16u;
                fo = near_(foff(F_QKV + m * 64 + h), 0u, pol);
#pragma unroll
                for (int j = 0; j < 8; ++j) { ar[j] = (u32)(QK_A + r32 * 256 + ((2 * j + hh) ^ (r32 & 15)) * 16); PIN(ar[j]); }
                PIN(h2flag); PIN(ol); PINS(fo); PINS(pol);
                VMW(10); BAR();
            }
            lptr q = tb + (k >> 3) * 32768 + (k & 7) * 4096; wt[k] = tr2(q, q + 1024); asm volatile("" : "+a"(wt[k]));
        SEND
    }
    EV(6);
    int n0 = -1, n1 = -QLEAD;
    f32x16 a0 = {};
    const u32 ksc = m == 1 ? 0x3e0293eeu : 0x3f800000u;
    const u32 bfo = tid < 96 ? lnf : tid - 128u < NKV ? foff(F_KV - 128 + tid) : OOB;
    v4u brk = {};
    auto run = [&](auto isv) __attribute__((always_inline)) {
      constexpr bool V = decltype(isv)::value;
      v4u x[8];
      u32 fv = 0, fo0 = tid == 192 ? fo : n1 > -QLEAD && tid == 194 ? foff(F_CV + grp) : OOB, fo1 = tid == 192 ? fo : tid == 193 ? fo + 32 * 128 : OOB;
      PIN(fo0); PIN(fo1);
#pragma unroll
      for (int ks = 20; ks < 25; ++ks) x[ks & 7] = XRD(ks);
#define QS(k) do {} while (0)
#define QPK1(d, a, a_, q_) do { if constexpr (V) VCVT(d[q_], a[4 * (a_) + 8 * ((q_) >> 1) + 2 * ((q_) & 1)], a[4 * (a_) + 8 * ((q_) >> 1) + 2 * ((q_) & 1) + 1]); else VCVT(d[q_], a[4 * (a_) + (q_)], a[4 * (a_) + (q_) + 8]); } while (0)
#define QSC(a, i) asm volatile("v_mul_f32 %0, %2, %0\n\tv_mul_f32 %1, %2, %1" : "+v"(a[i]), "+v"(a[(i) + 8]) : "s"(ksc))
#define QPK(d, a, a_, q_) do { if constexpr (!V) { QSC(a, 4 * (a_) + (q_)); QSC(a, 4 * (a_) + (q_) + 1); } QPK1(d, a, a_, q_); QPK1(d, a, a_, (q_) + 1); } while (0)
#pragma unroll 1
      for (const int end = n1 == -QLEAD ? QFIRST - QLEAD : NSEG; n1 < end; ++n1) {
        const int cur = n1 < 0 ? n0 + 1 : 64 | n1, nxt = n1 < 0 ? n0 + 2 : n0 + 1;
        u32 xs = n1 < 0 ? W_H1 + (u32)cur * H_TILE : near_(W_H2 + (u32)n1 * H_TILE, 1u, pol);
        PINS(xs);
        QS(0);
        if (w == 0 && n1 >= 0 && ((u32)__builtin_amdgcn_ballot_w64(fv <= (u32)n1) & 255u) != 0u) wait_flags(rs, h2flag, (u32)n1 + 1, dead);
        BAR();
        QS(1);
        f32x16 a1;
        u32 ss, sv, hb, oa, qfo, qfv;
        v4u pp, pq;
        SCHED();
        auto slot = [&](auto zc) __attribute__((always_inline)) {
            constexpr int z = decltype(zc)::value, ks = (z + 20) % 40;
            if constexpr (z < 20) { if constexpr (V) M32B(false, a0, x[ks & 7], wt[ks]); else M32A(false, a0, wt[ks], x[ks & 7]); }
            else if constexpr (z < 60) { if constexpr (V) M32B(z == 20, a1, x[ks & 7], wt[ks]); else M32A(z == 20, a1, wt[ks], x[ks & 7]); }
            else if constexpr (z == 60) {
                if constexpr (V) asm volatile("v_mfma_f32_32x32x16_bf16 %0, %1, %2, 0" : "+v"(a0) : "v"(x[ks & 7]), "a"(wt[ks]));
                else asm volatile("v_mfma_f32_32x32x16_bf16 %0, %1, %2, 0" : "+v"(a0) : "a"(wt[ks]), "v"(x[ks & 7]));
            } else { if constexpr (V) M32B(false, a0, x[ks & 7], wt[ks]); else M32A(false, a0, wt[ks], x[ks & 7]); }
            if constexpr (z == 18) { QS(2); asm volatile("s_waitcnt vmcnt(6) lgkmcnt(0)\n\ts_barrier" ::: "memory"); QS(3); }
            if constexpr (z < 15 || z > 20) x[(ks + 5) & 7] = XRD((ks + 5) % 40);
            if constexpr (z >= 18 && z <= 20) { x[2 * (z - 18)] = XRD(2 * (z - 18)); x[2 * (z - 18) + 1] = XRD(2 * (z - 18) + 1); }
            SCHED();
            if constexpr (z < 4) QDMA(z, dx, t16, xs);
            if constexpr (z >= 5 && z < 17 && (z & 1)) QDMA((z + 3) >> 1, dx, t16, xs);
            if constexpr ((z >= 7 && z < 15) || (z >= 47 && z < 55)) asm volatile("v_xor_b32 %0, 0x10000, %0" : "+v"(ar[(ks + 5) & 7]));
            if constexpr (z == 16) { ss = W_H1 + (u32)nxt * H_TILE; PINS(ss); }
            if constexpr (z == 17) { sv = t16 | (nxt >= NSEG ? OOB : 0u); PIN(sv); }
            if constexpr (z >= 21 && z < 41 && (z & 1)) QDMA((z - 21) >> 1, ds, sv, ss);
            if constexpr (z == 22 || z == 24) QPK(pp, a0, 0, z - 22);
            if constexpr (z == 26 || z == 28) QPK(pq, a0, 1, z - 26);
            if constexpr (z == 30) { QS(4); asm volatile("s_waitcnt vmcnt(5)\n\ts_barrier" ::: "memory"); QS(5); }
            if constexpr (z == 32 || z == 44) { hb = (m == 2 ? W_VO : W_KQ) + (u32)((z == 32 ? n0 : cur) * NH + h) * 16384u; PINS(hb); }
            if constexpr (z == 34 || z == 45) { hb = near_(hb, m == 1 ? 0u : 1u, pol); PINS(hb); }
            if constexpr (z == 36) { hb = (u32)n0 < NSEG ? hb : OOB; PINS(hb); }
            if constexpr (z == 38 || z == 46) { oa = ol + hb; PIN(oa); }
            if constexpr (z == 41) st16(rs, oa, pp);
            if constexpr (z == 43) st16(rs, oa + 512, pq);
            if constexpr (z == 54) { QS(6); asm volatile("s_waitcnt vmcnt(4)\n\ts_barrier" ::: "memory"); QS(7); }
            if constexpr (z == 56) { if (__builtin_expect(n1 == QFIRST - QLEAD - 1, 0)) { filt_load<3, 0>(p, grp, flt); brk = ld16s(rs, bfo, 0); } }
            if constexpr (z == 60) NOPS(5);
            if constexpr (z == 63) st16(rs, oa, pp);
            if constexpr (z == 65) st16(rs, oa + 512, pq);
            if constexpr (z == 61 || z == 62) QPK(pp, a1, 0, 2 * (z - 61));
            if constexpr (z == 63 || z == 64) QPK(pq, a1, 1, 2 * (z - 63));
            if constexpr (z == 67) { qfo = n1 >= 0 ? fo1 : fo0; PIN(qfo); }
            if constexpr (z == 68) { qfv = tid == 193 ? (u32)n1 + 1u : (u32)min(nxt, NSEG); PIN(qfv); }
            if constexpr (z == QSAMP) LD4A(fv, h2flag, 0);
            SCHED();
        };
        if (__builtin_expect(n0 >= 0, 1)) static_for<0, 16>(slot);
        static_for<16, 80>(slot);
        QS(8);
        asm volatile("s_waitcnt vmcnt(1)\n\ts_barrier" ::: "memory");
        st4s(rs, qfo, 0, qfv);
        QS(9);
        asm volatile("s_waitcnt vmcnt(1)" : "+v"(fv) :: "memory");
        n0 = nxt;
      }
      NOPS(7);
    };
#pragma unroll 1
    for (;;) {
        if (m == 2) run(IC<1>{}); else run(IC<0>{});
        if (n1 > 0) break;
        if (!boot_ok(p, bfo, brk, 2u)) boot_wait(p, rs, bfo, 2u, dead);
        BAR();
        conv_job<1>(p, rs, lds, grp, flt, 0);
        const u32 so = uu(W_H1 + (u32)n0 * H_TILE);
        SFOR(i, 6) QDMA(i + 4, ds, t16, so); SEND
        DRAIN(); BAR();
    }
}

constexpr int OU_SEL = 131072;
#define OSAMP 46

DEV void role_out(const Params& p, lptr lds, int L, int kq, int ng) {
    const int tid = threadIdx.x, lane = tid & 63, w = uni(tid >> 6), hh = lane >> 5, r32 = lane & 31;
    const rsrc_t rs = mk_rsrc(p.ws, W_END);
    v4u wo[64];
    Filt flt;
    bool dead = false;
    {
        const v4u sw = mk_srd(p.Wo, DM * NQ * 2);
        const u32 vo = wst_src(lane, DM) + (ng == 4 ? 1024u : (u32)((ng >> 1) * 512 + (ng & 1) * 128 + ((lane ^ (lane >> 2)) & 8) * 16));
        auto fill = [&](int b) {
#pragma unroll 1
            for (int i = 0; i < 8; ++i) DMAIN(sw, uu((b % 5) * 32768 + (4 * i + w) * 1024), vo, uu((kq * 1024 + b * 128 + 16 * i + 4 * w) * (DM * 2)));
        };
#pragma unroll 1
        for (int b = 0; b < 5; ++b) fill(b);
        lptr tb = lds + hh * 1024 + wst_rd(lane, w);
        SFOR(b, 8)
            VMW(b < 3 ? 32 : 66 - 8 * b); BAR();
            lptr q = tb + (b % 5) * 32768;
#pragma unroll
            for (int k = 0; k < 8; ++k) { lptr a = q + (k >> 1) * 8192 + (k & 1) * 2048; wo[8 * b + k] = tr2(a, a + 4096); if (b < 7) asm volatile("" : "+a"(wo[8 * b + k])); else PIN(wo[8 * b + k]); }
            if constexpr (b < 3) { BAR(); fill(b + 5); }
            if constexpr (b == 2) filt_load(p, L == 0 ? 44 + kq * 5 + ng : 1 << 12, flt);
        SEND
    }
    EV(6);
    const u32 pol = get_pol(p, rs, dead);
    if (L == 0) {
        BAR();
        conv_job<0>(p, rs, lds, 44 + kq * 5 + ng, flt, 0);
        DRAIN(); BAR();
        st4s(rs, tid == 0 ? foff(F_CV + 44 + kq * 5 + ng) : OOB, 0, 2u);
    }
    EV(15);
    MK_SRD();
    const u32 ofl = (lane < 8 && w == 0) ? near_(foff(F_O + L * 32 + kq * 8 + lane), 0u, pol) : OOB;
    const u32 l16 = (u32)lane * 16u;
    const u32 fo = (tid == 192) ? near_(foff(F_P + L * 32 + kq * 5 + ng), 1u, pol) : OOB;
    auto osrc = [&](int s) { return uu(hblk(W_VO, L * NSEG + s, kq * 8, 0u, pol) + w * 1024); };
#define ODMA(i, dst, vo, so) DMA16I(dst, (i) * 4096, vo, so, ((i) >> 1) * 16384 + ((i) & 1) * 4096)
    const u32 pvo = (u32)(kq * 5 + ng) * 16384u + (u32)w * 2048u + l16;
    lptr tw = lds + OU_SEL + 256 + r32 * 272 + (ng == 4 ? hh * 32 + w * 4 : (w & 1) * 128 + hh * 16 + (w >> 1) * 8), tr = lds + OU_SEL + 256 + (8 * w + (lane >> 4)) * 272 + (lane & 15) * 16;
    lptr selw = lds + OU_SEL + (w == 0 ? 0 : 64);
    u32 pfo = OOB;
    u32 buf = 0;
    if (w == 0) wait_flags(rs, ofl, 1u, dead);
    BAR();
    for (int s = 0; s < NSEG; ++s) {
        u32 src = osrc(s), dst = uu(buf + w * 1024);
        lptr A = lds + buf + l16;
        asm volatile("" : "+s"(src), "+s"(dst));
#define OS(k) do {} while (0)
        OS(0);
        OS(1);
        SFOR(i, 10) ODMA(i, dst, l16, src); SEND
        OS(2);
        asm volatile("s_waitcnt vmcnt(7)\n\ts_barrier" ::: "memory");
        OS(3);
        LS2(L == 0 && w == 1, 96 + kq * 5 + ng);
        v4u o[64];
#pragma unroll
        for (int k = 0; k < 3; ++k) o[k] = lds_r16(A + k * 1024);
        st4s(rs, pfo, 0, (u32)s);
        u32 fv, pa; int rdy;
        f32x16 acc;
        SCHED();
        SFOR(k, 64)
            if constexpr (k < 56) M32A(k == 0, acc, wo[k], o[k]); else asm volatile("v_mfma_f32_32x32x16_bf16 %0, %1, %2, %0" : "+v"(acc) : "v"(wo[k]), "v"(o[k]));
            if constexpr (k < 61) o[k + 3] = lds_r16(A + (k + 3) * 1024);
            SCHED();
            if constexpr (k < 12 && (k & 1) == 0) ODMA(10 + (k >> 1), dst, l16, src);
            if constexpr (k == 7) OS(4);
            if constexpr (k == 8) OS(5);
            if constexpr (k == 23) OS(6);
            if constexpr (k == 24) OS(7);
            if constexpr (k == 43) OS(8);
            if constexpr (k == 44) OS(9);
            if constexpr (k == 59) OS(10);
            if constexpr (k == 63) OS(11);
            if constexpr (k == 7) asm volatile("s_waitcnt vmcnt(8)\n\ts_barrier" ::: "memory");
            if constexpr (k == 23) asm volatile("s_waitcnt vmcnt(4)\n\ts_barrier" ::: "memory");
            if constexpr (k == 30) { pa = pvo + near_(p_item(s), (u32)L, pol); PIN(pa); }
            if constexpr (k == 43) asm volatile("s_waitcnt vmcnt(0)\n\ts_barrier" ::: "memory");
            if constexpr (k == OSAMP) LD4A(fv, ofl, 0);
            if constexpr (k == 62) {
                asm volatile("s_waitcnt vmcnt(0)" : "+v"(fv) :: "memory");
                *(LAS int*)selw = (int)((u32)__builtin_amdgcn_ballot_w64(fv < (u32)s + 2) & 255u);
            }
            SCHED();
        SEND
        asm volatile("s_nop 11" : "+v"(acc));
        u32 pc[8];
#pragma unroll
        for (int i = 0; i < 8; ++i) pc[i] = pk(acc[2 * i], acc[2 * i + 1]);
        if (ng != 4) {
#pragma unroll
            for (int i = 0; i < 4; ++i) *(LAS v2u*)(tw + i * 32) = (v2u){pc[2 * i], pc[2 * i + 1]};
        } else {
#pragma unroll
            for (int i = 0; i < 8; ++i) *(LAS u32*)(tw + (i >> 1) * 64 + (i & 1) * 16) = pc[i];
        }
        BAR();
        const v4u p0 = lds_r16(tr), p1 = lds_r16(tr + 4 * 272);
        rdy = *(LAS int*)(lds + OU_SEL);
        st16(rs, pa, p0); st16(rs, pa + 1024, p1);
        const bool ready = uni(rdy) == 0;
        SCHED();
        OS(12);
        if (ready) pfo = fo;
        else {
            LD4A(fv, ofl, 0);
            asm volatile("s_waitcnt vmcnt(1)\n\ts_barrier" ::: "memory");
            st4s(rs, fo, 0, (u32)s + 1); pfo = OOB;
            OS(14);
            LS2(L == 0 && w == 1, 160 + kq * 5 + ng);
            asm volatile("s_waitcnt vmcnt(1)" : "+v"(fv) :: "memory");
            if (w == 0 && s + 1 < NSEG && ((u32)__builtin_amdgcn_ballot_w64(fv < (u32)s + 2) & 255u) != 0u) wait_flags(rs, ofl, (u32)s + 2, dead);
            BAR();
        }
        OS(13);
        buf ^= 65536u;
    }
}

template <int L> DEV void role_row(const Params& p, lptr lds, int ri) {
    const int lane = threadIdx.x & 63, w = uni(threadIdx.x >> 6);
    const rsrc_t rs = mk_rsrc(p.ws, W_END);
    bool dead = false;
    const u32 pol = get_pol(p, rs, dead);
    const u32 fp = threadIdx.x < 20 ? near_(foff(F_P + L * 32 + lane), 1u, pol) : OOB, fo = threadIdx.x == 192 && L == 0 ? near_(foff(F_H2 + (ri >> 2)), 1u, pol) : OOB;
    u32 fv = 0;
    MK_SRD();
    auto yfill = [&](int b, int i) { DMA32(uu((b & 1) * 40960 + w * 10240 + i * 2048), (u32)lane * 16u, uu(W_Y + (u32)((ri >> 1) * 8 + b) * 20480u + (w & 1) * 10240 + i * 2048)); };
    auto ywait = [&](u32 v) {
        wait_flags(rs, threadIdx.x < (v == 3u ? 8u : 160u) ? foff(F_CV + (v == 3u ? 160u : 0u) + threadIdx.x) : OOB, v, dead);
    };
    v4u y8[10] = {};
    v2u xa, xb; u32 xc;
    const v4u sx = L == 0 ? mk_srd(p.x, ACT) : mk_srd(p.ws + W_X2, ACT);
    auto xload = [&](int s) {
        asm volatile("buffer_load_dwordx2 %0, %3, %5, %6 offen sc1\n\tbuffer_load_dwordx2 %1, %3, %5, %6 offen offset:512 sc1\n\tbuffer_load_dword %2, %4, %5, %6 offen offset:1024 sc1"
                     : "=&v"(xa), "=&v"(xb), "=&v"(xc) : "v"((u32)lane * 8u), "v"((u32)lane * 4u), "s"(sx), "s"(uu((u32)(s * 32 + ri) * (DM * 2))) : "memory");
        SCHED();
    };
    ywait(1u);
    BAR();
    EV(15);
#pragma unroll 1
    for (int i = 0; i < 5; ++i) yfill(0, i);
    if (L == 1) wait_flags(rs, lane == 0 ? near_(foff(F_H2 + (ri >> 2)), 1u, pol) : OOB, 1u, dead);
    xload(0);
    asm volatile("s_waitcnt vmcnt(0)" : "+v"(xa), "+v"(xb), "+v"(xc) :: "memory");
    BAR();
    for (int s = 0; s < NSEG; ++s) {
        u32 xi[5] = {xa.x, xa.y, xb.x, xb.y, xc};
        u32 pa = near_(p_item(s), (u32)L, pol) + (u32)ri * 256u, pc = pa + 65536u + (u32)(lane >> 4) * 81920u + (u32)(lane & 15) * 16u;
        pa += (u32)(lane >> 5) * 81920u + (u32)((lane >> 4) & 1) * 16384u + (u32)(lane & 15) * 16u;
        u32 yf = s == 1 ? foff(F_CV + min(threadIdx.x, 159u)) : s == 25 ? foff(F_CV + 160 + (lane & 7)) : OOB;
        asm volatile("" : "+v"(xi[0]), "+v"(xi[1]), "+v"(xi[2]), "+v"(xi[3]), "+v"(xi[4]), "+v"(pa), "+v"(pc), "+v"(yf));
#define RS(k) do {} while (0)
        RS(0);
        if (w == 0 && ((u32)__builtin_amdgcn_ballot_w64(fv <= (u32)s) & 0xFFFFFu) != 0u) wait_flags(rs, fp, (u32)s + 1, dead);
        BAR();
        RS(1);
        v4u d[5];
        asm volatile("buffer_load_dwordx4 %0, %5, %7, 0 offen sc1\n\tbuffer_load_dwordx4 %1, %5, %7, %8 offen sc1\n\tbuffer_load_dwordx4 %2, %5, %7, %9 offen sc1\n\t"
                     "buffer_load_dwordx4 %3, %5, %7, %10 offen sc1\n\tbuffer_load_dwordx4 %4, %6, %7, 0 offen sc1"
                     : "=&v"(d[0]), "=&v"(d[1]), "=&v"(d[2]), "=&v"(d[3]), "=&v"(d[4]) : "v"(pa), "v"(pc), "s"(srd), "s"(163840), "s"(32768), "s"(196608) : "memory");
        RS(2);
        LS2(L == 0, 192 + ri);
        SCHED();
        f32x2 y[5];
        {
            if ((s & 7) == 0) {
                lptr ya = lds + ((s >> 3) & 1) * 40960 + (w >> 1) * 20480 + (w & 1) * 16, yc = ya + 4096 + (lane >> 1) * 32 + (lane & 1) * 10240;
#pragma unroll
                for (int i = 0; i < 8; ++i) y8[i] = lds_r16(ya + lane * 32 + (i >> 2) * 2048 + (i & 3) * 5120);
                y8[8] = lds_r16(yc); y8[9] = lds_r16(yc + 5120);
            }
#pragma unroll
            for (int i = 0; i < 10; ++i) {
                y[i >> 1][i & 1] = lo_f(y8[i].x);
                y8[i] = (v4u){__builtin_amdgcn_alignbit(y8[i].y, y8[i].x, 16), __builtin_amdgcn_alignbit(y8[i].z, y8[i].y, 16), __builtin_amdgcn_alignbit(y8[i].w, y8[i].z, 16), y8[i].w >> 16};
                PIN(y8[i]);
            }
            asm volatile("" : "+v"(y[0]), "+v"(y[1]), "+v"(y[2]), "+v"(y[3]), "+v"(y[4]));
        }
        u32 yv;
        {
            xload(s + 1);
            const bool yy = (s & 7) - 2u < 5u && s < NSEG - 8;
            u32 yo = yy ? (u32)lane * 16u : OOB;
            DMA32(uu(yy ? (((s >> 3) + 1) & 1) * 40960 + w * 10240 + ((s & 7) - 2) * 2048 : 81920), yo, uu(W_Y + (u32)((ri >> 1) * 8 + (s >> 3) + 1) * 20480u + (w & 1) * 10240 + ((s & 7) - 2) * 2048));
            LD4A(yv, yf, 0);
            asm volatile("s_waitcnt vmcnt(6)" : "+v"(d[0]), "+v"(d[1]), "+v"(d[2]), "+v"(d[3]), "+v"(d[4]));
        }
        f32x2 o[5];
        {
            f32x2 t[5][2];
#pragma unroll
            for (int i = 0; i < 5; ++i) {
                const auto e = __builtin_amdgcn_permlane32_swap(d[i].x, d[i].z, false, false), f = __builtin_amdgcn_permlane32_swap(d[i].y, d[i].w, false, false);
                t[i][0] = up2(e[0]) + up2(e[1]); t[i][1] = up2(f[0]) + up2(f[1]);
            }
            o[0] = t[0][0] + t[1][0]; o[1] = t[0][1] + t[1][1]; o[2] = t[2][0] + t[3][0]; o[3] = t[2][1] + t[3][1];
            const auto e = __builtin_amdgcn_permlane16_swap(u_(t[4][0].x), u_(t[4][1].x), false, false), f = __builtin_amdgcn_permlane16_swap(u_(t[4][0].y), u_(t[4][1].y), false, false);
            o[4] = (f32x2){f_(e[0]) + f_(e[1]), f_(f[0]) + f_(f[1])};
        }
        f32x2 x1[5];
#pragma unroll
        for (int i = 0; i < 5; ++i) x1[i] = up2(pk2(up2(xi[i]) + up2(pk2(o[i]))));
        asm volatile("" : "+v"(x1[0]), "+v"(x1[1]), "+v"(x1[2]), "+v"(x1[3]), "+v"(x1[4]));
        RS(3);
        const f32x2 st1 = ln_stats(x1);
        u32 xp[5];
#pragma unroll
        for (int i = 0; i < 5; ++i) xp[i] = pk2(x1[i] + ((x1[i] - st1.x) * st1.y) * y[i]);
        const int row = s * 32 + ri;
        RS(4);
        if (L == 0) {
            const u32 ro = (u32)row * (DM * 2);
            f32x2 x2[5];
#pragma unroll
            for (int i = 0; i < 5; ++i) x2[i] = up2(xp[i]);
            st8s(rs, (u32)lane * 8, W_X2 + ro, (v2u){xp[0], xp[1]});
            st8s(rs, (u32)lane * 8 + 512, W_X2 + ro, (v2u){xp[2], xp[3]});
            st4s(rs, (u32)lane * 4 + 1024, W_X2 + ro, xp[4]);
            SCHED();
            const f32x2 st2 = ln_stats(x2);
            u32 hp[5];
#pragma unroll
            for (int i = 0; i < 5; ++i) hp[i] = pk2((x2[i] - st2.x) * st2.y);
            store_h(rs, near_(W_H2 + (u32)s * H_TILE, 1u, pol), row, hp);
        } else {
            u16* o_ = p.out + (size_t)row * DM;
            *(v2u*)(o_ + 4 * lane) = (v2u){xp[0], xp[1]};
            *(v2u*)(o_ + 256 + 4 * lane) = (v2u){xp[2], xp[3]};
            *(u32*)(o_ + 512 + 2 * lane) = xp[4];
        }
        RS(5);
        LD4A(fv, fp, 0);
        if (L == 0) asm volatile("s_waitcnt vmcnt(1)\n\ts_barrier" ::: "memory");
        st4s(rs, fo, 0, (u32)s + 1);
        RS(6);
        VMW(1); SCHED();
        asm volatile("" : "+v"(fv), "+v"(yv), "+v"(xa), "+v"(xb), "+v"(xc));
        if (__builtin_expect(s == 1 || s == 25, 0) && !all_(yv >= (s == 1 ? 2u : 3u))) ywait(s == 1 ? 2u : 3u);
        RS(7);
        if (ri == 0) LST(320 + L * 64 + s, rt32());
        LS2(L == 0, 224 + ri);
    }
}

constexpr float SCALE = 0.08838834764831845f, LOG2E = 1.4426950408889634f;
constexpr int C_K = 0, C_V = 8192, C_QX = 16384, C_MEM = 24576, C_Q = C_MEM + 10240, C_KM = C_Q + 2304, C_VM = C_KM + 2304, C_DUMP = C_VM + 2048,
              C_ZERO = C_DUMP + 1024, C_WL = C_ZERO + 128;
constexpr int NLF = 14;
static_assert(C_WL % 16 == 0 && C_WL + 4 * NLF * 2048 <= LDS_BYTES, "head lds");
#define CH_VIS 10
#define CH_H0 104
#define CH_W0 82
#define CH_MH 2
#define CH_MW 6
#define CH_REC 8
#define CH_LATE 30
#define CH_SOLO 6
#define CH_KV 12
#define CH_KV0 60
#define CH_M0 6
#define CH_DEC 2u
#define CH_UP 4
#define CH_WX 6
DEV void wait_until(u32 t) {
    unsigned long long c;
    asm volatile("1:\n\ts_memrealtime %0\n\ts_waitcnt lgkmcnt(0)\n\ts_mov_b64 vcc, %0\n\ts_sub_u32 vcc_lo, %1, vcc_lo\n\ts_cmpk_lt_u32 vcc_lo, 400\n\ts_cbranch_scc1 1b" : "=&s"(c) : "s"(uu(t - 1u)) : "vcc", "scc", "memory");
}
#define WAIT_TXT(tmp, t1) "s_memrealtime vcc\n1:\n\ts_waitcnt lgkmcnt(0)\n\ts_sub_u32 " tmp ", " t1 ", vcc_lo\n\ts_memrealtime vcc\n\ts_cmpk_lt_u32 " tmp ", 400\n\ts_cbranch_scc1 1b\n\t"
#define SMF_A(acc, a, b) asm volatile("v_smfmac_f32_16x16x64_bf16 %0, %1, %2, %3" : "+v"(acc) : "v"(a), "a"(b), "v"(idx))
#define SMF_V(acc, a, b) asm volatile("v_smfmac_f32_16x16x64_bf16 %0, %1, %2, %3" : "+v"(acc) : "v"(a), "v"(b), "v"(idx))
#define MF0(d, a, b) asm volatile("v_mfma_f32_16x16x32_bf16 %0, %1, %2, 0" : "=&v"(d) : "v"(a), "v"(b))
#define MF1(d, a, b) asm volatile("v_mfma_f32_16x16x32_bf16 %0, %1, %2, %0" : "+v"(d) : "v"(a), "v"(b))
#define MF0H(d, a, b) asm volatile("v_mfma_f32_16x16x16_bf16 %0, %1, %2, 0" : "=&v"(d) : "v"(a), "v"(b))
#define MF1H(d, a, b) asm volatile("v_mfma_f32_16x16x16_bf16 %0, %1, %2, %0" : "+v"(d) : "v"(a), "v"(b))
DEV v8u tr_frag(lptr p0, lptr p1, lptr p2, lptr p3) { return cat8(tr2(p0, p1), tr2(p2, p3)); }
constexpr int qslot(int ks) { return (ks & 1) && ks < 8 ? ks >> 1 : -1; }
constexpr int kslot(int ks) { return ks < 4 ? 4 + ks : -1; }
constexpr int vslot(int ks) { return ks >= 4 ? 4 + ks : -1; }
DEV u32 ring_map(u32 off, u32 half) { return ((off >> 13) << 14) + (half << 13) + (off & 8191u); }
#define ZRD(x, i) x = __builtin_bit_cast(f32x4, lds_r16(ZP + 16 * (i)))
#define VMAX(d, a, b) asm volatile("v_max_f32 %0, %1, %2" : "=v"(d) : "v"(a), "v"(b))
#define VMAX3(d, a, b, c_) asm volatile("v_max3_f32 %0, %1, %2, %3" : "=v"(d) : "v"(a), "v"(b), "v"(c_))
#define VADD(d, a, b) asm volatile("v_add_f32 %0, %1, %2" : "=v"(d) : "v"(a), "v"(b))
#define VEXP(d, a) asm volatile("v_exp_f32 %0, %1" : "=v"(d) : "v"(a))
#define VFMC(d, a, b) asm volatile("v_fmamk_f32 %0, %1, 0x3e0293ee, %2" : "=v"(d) : "v"(a), "v"(b))
#define VMULC(d, a) asm volatile("v_mul_f32 %0, 0x3e0293ee, %1" : "=v"(d) : "v"(a))
#define SEL(d, a, b) asm volatile("v_cndmask_b32 %0, %1, %2, %3" : "=v"(d) : "v"(a), "v"(b), "s"(himask))
#define QPERM(d, a, q) asm volatile("v_mov_b32_dpp %0, %1 quad_perm:" q " row_mask:0xf bank_mask:0xf" : "=v"(d) : "v"(a))

#define HSAMP 71
DEV void role_head(const Params& p, lptr lds, int L, int h) {
    const int tid = threadIdx.x, lane = tid & 63, w = uni(tid >> 6), g = lane >> 4, r = lane & 15;
    const rsrc_t rs = mk_rsrc(p.ws, W_END);
    v8u wq0[10], wq1[10], wk0[10], wk1[10], wv0[10], wv1[10];
    lptr WL = lds + C_WL + w * (NLF * 2048) + lane * 16;
    {
        constexpr int GS = 1056, CS = 48 * GS;
        const v4u sq = mk_srd(p.Wq, DM * NQ * 2), sk = mk_srd(p.Wk, DM * NQ * 2), sv = mk_srd(p.Wv, DM * NQ * 2);
        const u32 vo = (u32)(g * NQ + h * DH + 8 * (r ^ ((g & 2) << 2))) * 2u;
        auto fill = [&](int ch) {
#pragma unroll 1
            for (int i = 0; i < 4; ++i) {
                const u32 dst = uu((ch % 3) * CS + (4 * i + w) * GS), so = uu((ch * 64 + 16 * i + 4 * w) * (NQ * 2));
                DMAIN(sq, dst, vo, so); DMAIN(sk, dst + 16 * GS, vo, so); DMAIN(sv, dst + 32 * GS, vo, so);
            }
        };
        int vote = 0;
        if (L == 1) {
            if (w == 0) vote = probe(rs, h);
            BAR();
            EV(2);
            pre_rings(p, rs, h);
        }
        fill(0); fill(1); fill(2);
        const int q4 = r >> 2;
        lptr tb = lds + (4 * (g >> 1) + q4) * GS + (g & 1) * 512 + ((w * 64 + (r & 3) * 8) ^ ((g & 1) << 7));
        SFOR(ch, 10)
            VMW(ch == 1 || ch == 2 ? 25 : ch < 8 ? 24 : 108 - 12 * ch);
            BAR();
            if constexpr (ch == 0) boot_set(p, rs, tid == 0 && L == 1 ? foff(F_RG + h) : OOB, (u32)((1 << 20) + vote));
            auto frag = [&](int m, int t) { lptr b = tb + (ch % 3) * CS + m * 16 * GS + t * 32; return tr_frag(b, b + 256, b + 8 * GS, b + 8 * GS + 256); };
            wq0[ch] = frag(0, 0); wq1[ch] = frag(0, 1); wk0[ch] = frag(1, 0); wk1[ch] = frag(1, 1); wv0[ch] = frag(2, 0); wv1[ch] = frag(2, 1);
            asm volatile("" : "+a"(wq0[ch]), "+a"(wk0[ch]), "+v"(wv0[ch]), "+v"(wv1[ch]));
            if constexpr (qslot(ch) < 0) asm volatile("" : "+a"(wq1[ch])); else asm volatile("" : "+v"(wq1[ch]));
            if constexpr (kslot(ch) < 0) asm volatile("" : "+a"(wk1[ch])); else asm volatile("" : "+v"(wk1[ch]));
            BAR();
            if constexpr (ch < 7) fill(ch + 3);
        SEND
#pragma unroll
        for (int ch = 0; ch < 10; ++ch) {
            if (qslot(ch) >= 0) { lds_w16(WL + qslot(ch) * 2048, wq1[ch].lo); lds_w16(WL + qslot(ch) * 2048 + 1024, wq1[ch].hi); }
            if (kslot(ch) >= 0) { lds_w16(WL + kslot(ch) * 2048, wk1[ch].lo); lds_w16(WL + kslot(ch) * 2048 + 1024, wk1[ch].hi); }
            if (vslot(ch) >= 0) { lds_w16(WL + vslot(ch) * 2048, wv1[ch].lo); lds_w16(WL + vslot(ch) * 2048 + 1024, wv1[ch].hi); }
        }
        CFENCE();
    }
    const u32 idx = (r & 2) ? 0xEEEEu : 0x4444u;
    const unsigned long long himask = 0xCCCCCCCCCCCCCCCCull;
    bool dead = false;
    EV(6);
    const u32 pol = get_pol(p, rs, dead);
    const u32 pol0 = pol & 1u;
    const u32 l16 = (u32)lane * 16u;
    const u32 mring = W_MRING + (u32)L * (MB / 4) + pol0 * 8192u, hint = mring + 4 * 32768u;
    const u32 fkh = lane < 3 ? near_(foff(F_QKV + lane * 64 + L * 32 + h), 0u, pol) : lane < 6 ? hint + (u32)(lane - 3) * 4u : OOB;
    u32 tm = 0;
    MK_SRD();
    const unsigned long long kmask = __builtin_amdgcn_ballot_w64((hblk(W_KQ, L * NSEG + lane, h, 0u, pol) & 8192u) != 0u),
                             vmask = __builtin_amdgcn_ballot_w64((hblk(W_VO, L * NSEG + lane, h, 1u, pol) & 8192u) != 0u);
    u32 kb = uu(W_KQ + (u32)(L * NSEG * NH + h) * 16384u + w * 2048);
    u32 dk = uu(C_K + w * 2048), dv = dk + (C_V - C_K), dq = dk + (C_QX - C_K), dm = uu(C_MEM + w * 2048), dm2 = uu(w < 2 ? C_MEM + 8192 + w * 1024 : C_DUMP);
    asm volatile("" : "+s"(dk), "+s"(dv), "+s"(dq), "+s"(dm), "+s"(dm2));
    u32 ksrc = kb + ((u32)kmask & 1u) * 8192u, vsrc = kb + (W_VO - W_KQ) + ((u32)vmask & 1u) * 8192u;
    lptr qw = lds + C_Q + (2 * g) * 288 + (32 * w + 2 * r) * 2;
    lptr kw = qw + (C_KM - C_Q);
    lptr qr = lds + C_Q + (2 * (r >> 2) + (r & 1)) * 288 + g * 16;
    lptr LB = lds + l16, VX = lds + C_V + w * 2048 + l16;
    if (tid >= 64 && tid < 70) { const u32 z = tid == 68 ? 0x3F803F80u : tid == 69 ? PSENT : 0u; lds_w16(lds + C_ZERO + (tid - 64) * 16, (v4u){z, z, z, z}); }
    u32 zaddr = 0; PIN(zaddr);
    lptr ZP = lds + zaddr + C_ZERO;
#define LF_RD(slot, hf) lf[slot][hf] = lds_r16(WL + (slot) * 2048 + (hf) * 1024)
    BAR();
    const u32 ofl = tid == 0 ? near_(foff(F_O + L * 32 + h), 0u, pol) : OOB;
    u32 oso = (g & 1) ? OOB : W_ORING + (u32)L * (MB / 2) + ring_map((u32)((r >> 3) * 32768 + h * 1024 + w * 256 + (g >> 1) * 128 + (r & 7) * 16), pol0);
    unsigned long long vis[8];
#pragma unroll
    for (int i = 0; i < 8; ++i) vis[i] = __builtin_amdgcn_ballot_w64(16 * (i >> 2) + 4 * g + (i & 3) <= 16 * (w & 1) + r);
    u32 missed = 0, dirty = 1;
    asm volatile("" : "+v"(oso), "+v"(qw), "+v"(kw), "+v"(qr), "+v"(VX));
    asm volatile("" : "+s"(vis[0]), "+s"(vis[1]), "+s"(vis[2]), "+s"(vis[3]), "+s"(vis[4]), "+s"(vis[5]), "+s"(vis[6]), "+s"(vis[7]), "+s"(ksrc), "+s"(vsrc));
    wait_flags(rs, lane < 3 ? fkh : OOB, 1u, dead);
    EV(7);
    DMA32(dk, l16, ksrc); DMA32(dv, l16, vsrc); DMA32(dq, l16, ksrc ^ 8192u);
    EV(8);
    auto next_slot = [&](u32 now) {
        const u32 hv = ld4(rs, fkh), per = rdl(hv, 4);
        if (per < 400u) { tm = rdl(hv, 5) - per; for (int n = 0; n < 3 && (int)(tm - now) < 0; ++n) tm += per; }
    };
    if (L == 1) { next_slot(rt32() + CH_KV0); tm -= 24u; }
    for (int s = 0; s < NSEG;) {
        {
            *(LAS u32*)(lds + C_DUMP + 128 + w * 4) = tm;
            BAR();
            const v4u c = lds_r16(lds + C_DUMP + 128) - tm;
            const u32 add = uu((u32)max(max((int)c.x, (int)c.y), max((int)c.z, (int)c.w)));
            tm += add;
            if (__builtin_expect((dirty | add) != 0u, 0)) {
                wait_until(tm - 44u);
                st16(rs, oso, lds_r16(ZP + 80));
            }
        }
        u32 ms = mring + (u32)(s & 3) * 32768u + w * 2048, ms2 = mring + (u32)(s & 3) * 32768u + 16384u + (w & 1) * 1024, sv = (u32)s;
        asm volatile("" : "+s"(ms), "+s"(ms2)); PIN(sv);
        f32x4 aq0, aq1, ak0, ak1;
        v4u lf[NLF][2];
        ZRD(aq0, 0); ZRD(aq1, 1); ZRD(ak0, 2); ZRD(ak1, 3);
#pragma unroll
        for (int i = 0; i < 4; ++i) { LF_RD(i, 0); LF_RD(i, 1); }
        SCHED();
        v4u A[10];
#define HT(k) do {} while (0)
        v4u kx[8], qf[4], kmf[4], vx[2], ones, P;
        float fa, fb, fc, fd, sx[4], t[4], sm0, sm1, tm0, tm1, inv; u32 pa, pb, fl, pmm, tmp = 0; unsigned long long odd;
        f32x4 av0, av1, s0, s1, sm, oa, ob, rsum;
        v2u vm, pm, vh, vl, on[2];
        f32x2 zz[4];
#define LW(sl) cat8(lf[sl][0], lf[sl][1])
#define PQ(ks, tl) do { if constexpr (!(tl)) SMF_A(aq0, A[ks], wq0[ks]); else if constexpr (qslot(ks) < 0) SMF_A(aq1, A[ks], wq1[ks]); else { const v8u lw_ = LW(qslot(ks)); SMF_V(aq1, A[ks], lw_); } } while (0)
#define PK(ks, tl) do { if constexpr (!(tl)) SMF_A(ak0, A[ks], wk0[ks]); else if constexpr (kslot(ks) < 0) SMF_A(ak1, A[ks], wk1[ks]); else { const v8u lw_ = LW(kslot(ks)); SMF_V(ak1, A[ks], lw_); } } while (0)
#define PV(ks, tl) do { if constexpr (!(tl)) SMF_V(av0, A[ks], wv0[ks]); else if constexpr (vslot(ks) < 0) SMF_V(av1, A[ks], wv1[ks]); else { const v8u lw_ = LW(vslot(ks)); SMF_V(av1, A[ks], lw_); } } while (0)
#define KXR(i) kx[i] = lds_r16(LB + C_K + ((i) & 1) * 4096 + ((i) >> 1) * 1024)
#define XW(q_) do { *(LAS u32*)(q_) = pa; *(LAS u32*)((q_) + 288) = pb; } while (0)
        auto slot = [&](auto zc) __attribute__((always_inline)) {
            constexpr int z = decltype(zc)::value;
            constexpr int vs[] = {0, 8, 1, 10, 9, 2, 11, -1, -1, -1, -1, 4, 6, 12, -1, -1, -1, 13, 14, -1, 16, 18, 15, 3, 5, 17, 7, 19};
            if constexpr (z < 12) PQ(z >> 1, z & 1);
            else if constexpr (z < 20) PQ(6 + (z & 3), z >> 4);
            else if constexpr (z < 32) PK((z - 20) >> 1, z & 1);
            else if constexpr (z < 40) PK(6 + (z & 3), (z >> 2) & 1);
            else if constexpr (z < 42) { if constexpr (z == 40) MF0(s0, kx[0], qf[0]); else MF0(s1, kx[1], qf[0]); }
            else if constexpr (z < 48) { if constexpr (z & 1) MF1(s1, kx[z - 40], qf[(z - 40) >> 1]); else MF1(s0, kx[z - 40], qf[(z - 40) >> 1]); }
            else if constexpr (z == 55) MF0(sm, kmf[0], qf[0]);
            else if constexpr (z > 55 && z < 59) MF1(sm, kmf[z - 55], qf[z - 55]);
            else if constexpr (z == 62) MF0(rsum, ones, P);
            else if constexpr (z == 63) MF0(oa, vx[0], P);
            else if constexpr (z == 64) MF0(ob, vx[1], P);
            else if constexpr (z == 67) { const v2u ones2 = ones.lo; MF1H(rsum, ones2, pm); }
            else PV(vs[z - 48] >> 1, vs[z - 48] & 1);
#define USE(x) asm volatile("" :: "v"(x))
            if constexpr (z == 20 || z == 24 || z == 26) USE(lf[4 + ((z - 20) >> 1)][1]);
            if constexpr (z == 41) USE(qf[3]);
            if constexpr (z == 45 || z == 46) USE(kx[z - 39]);
            if constexpr (z == 53) USE(lf[9][1]);
            if constexpr (z >= 54 && z < 58) USE(kmf[z - 54]);
            if constexpr (z == 61) USE(vl);
            if constexpr (z == 64) USE(lf[10][1]);
            if constexpr (z == 69) USE(lf[11][1]);
            if constexpr (z == 72) USE(lf[12][1]);
            if constexpr (z == 74) USE(lf[13][1]);
            if constexpr (z < 8) A[2 + z] = lds_r16(LB + C_MEM + (2 + z) * 1024);
            if constexpr (z >= 15 && z < 23) LF_RD(4 + ((z - 15) >> 1), (z - 15) & 1);
            if constexpr (z >= 24 && z < 27) KXR(z - 24);
            if constexpr (z == 27) XW(qw);
            if constexpr (z >= 28 && z < 31) KXR(z - 25);
            if constexpr (z == 32) asm volatile("s_waitcnt lgkmcnt(3)\n\ts_barrier" ::: "memory");
            if constexpr (z >= 33 && z < 37) qf[z - 33] = lds_r16(qr + (z - 33) * 64);
            if constexpr (z == 37 || z == 38) LF_RD(8, z - 37);
            if constexpr (z == 39 || z == 41) KXR(6 + (z == 41));
            if constexpr (z == 44) XW(kw);
            if constexpr (z == 45 || z == 46) LF_RD(9, z - 45);
            if constexpr (z == 47) vx[0] = lds_r16(VX);
            if constexpr (z == 48) vh = *(LAS v2u*)(VX + 1032);
            if constexpr (z == 54) { vl = *(LAS v2u*)(VX + 1024); vx[1] = (v4u){vh.x, vh.y, vl.x, vl.y}; }
            if constexpr (z == 49) { USE(lf[9][0]); asm volatile("s_barrier" ::: "memory"); }
            if constexpr (z >= 50 && z < 54) kmf[z - 50] = lds_r16(qr + (C_KM - C_Q) + (z - 50) * 64);
            if constexpr (z == 59 || z == 60) LF_RD(10, z - 59);
            if constexpr (z == 61 || z == 65) LF_RD(11, z == 65);
            if constexpr (z == 66 || z == 68) LF_RD(12, z == 68);
            if constexpr (z == 69 || z == 70) LF_RD(13, z - 69);
            if constexpr (z == 9) st4s(rs, ofl, 0, sv);
            if constexpr (z == HSAMP) fl = ld4(rs, fkh);
            SCHED();
            if constexpr (z == 19) HT(2);
            if constexpr (z == 39) HT(3);
            if constexpr (z == 51) HT(4);
            if constexpr (z == 64) HT(5);
            if constexpr (z == 20) VADD(fa, aq0[0], aq0[2]);
            if constexpr (z == 21) VADD(fb, aq0[1], aq0[3]);
            if constexpr (z == 23) VADD(fc, aq1[0], aq1[2]);
            if constexpr (z == 24) VADD(fd, aq1[1], aq1[3]);
            if constexpr (z == 25) VCVT(pa, fa, fc);
            if constexpr (z == 26) VCVT(pb, fb, fd);
            if constexpr (z >= 44 && z < 48) asm volatile("v_mov_b64 %0, 0" : "=v"(zz[z - 44]));
            if constexpr (z == 47) { av0 = __builtin_shufflevector(zz[0], zz[1], 0, 1, 2, 3); av1 = __builtin_shufflevector(zz[2], zz[3], 0, 1, 2, 3); }
            if constexpr (z == 48 || z == 49) asm volatile("v_mov_b64 %0, %1" : "=v"(on[z - 48]) : "s"(0x3F803F803F803F80ull));
            if constexpr (z == 49) ones = __builtin_shufflevector(on[0], on[1], 0, 1, 2, 3);
            if constexpr (z == 40) { VADD(fa, ak0[0], ak0[2]); VADD(fb, ak0[1], ak0[3]); }
            if constexpr (z == 42) { VADD(fd, ak1[1], ak1[3]); VADD(fc, ak1[0], ak1[2]); }
            if constexpr (z == 43) { VCVT(pb, fb, fd); VCVT(pa, fa, fc); }
            if constexpr (z >= 50 && z < 54) SEL(sx[z - 50], s0[z - 50], s1[z - 50]);
            if constexpr (z >= 54 && z < 58) VEXP(t[z - 54], sx[z - 54]);
            if constexpr (z == 58) asm volatile("v_cvt_pk_bf16_f32 %0, %2, %3\n\tv_cvt_pk_bf16_f32 %1, %4, %5" : "=&v"(P.x), "=v"(P.y) : "v"(t[0]), "v"(t[1]), "v"(t[2]), "v"(t[3]));
            if constexpr (z == 59) QPERM(P.z, P.x, "[2,3,0,1]");
            if constexpr (z == 60) QPERM(P.w, P.y, "[2,3,0,1]");
            if constexpr (z == 61) { USE(sm); VMULC(sm0, sm[0]); }
            if constexpr (z == 62) { VEXP(tm0, sm0); VMULC(sm1, sm[1]); }
            if constexpr (z == 63) VEXP(tm1, sm1);
            if constexpr (z == 64) asm volatile("v_cvt_pk_bf16_f32 %0, %2, %3\n\tv_cndmask_b32 %1, %0, 0, %4" : "=&v"(pmm), "=v"(pm.x) : "v"(tm0), "v"(tm1), "s"(himask));
            if constexpr (z == 65) asm volatile("v_cndmask_b32 %0, 0, %1, %2" : "=v"(pm.y) : "v"(pmm), "s"(himask));
            if constexpr (z == 67) { SEL(oa[0], oa[0], ob[0]); SEL(oa[1], oa[1], ob[1]); }
            if constexpr (z == 68 || z == 69) SEL(oa[z - 66], oa[z - 66], ob[z - 66]);
            if constexpr (z == 70) asm volatile("v_sub_u32 %0, %1, %2" : "=v"(pa) : "v"(rsum[0]), "s"(0x2B800000u), "v"(rsum));
            if constexpr (z == 71) asm volatile("v_cmp_le_u32 %0, %1, %2" : "=s"(odd) : "s"(0x28000000u), "v"(pa));
            if constexpr (z == 72) VADD(fa, av0[0], av0[2]);
            if constexpr (z == 73) { NOPS(0); VADD(fb, av0[1], av0[3]); }
            if constexpr (z == 74) VCVT(vm.x, fa, fb);
            SCHED();
        };
        {
            asm volatile("s_mov_b32 m0, %2\n\t" WAIT_TXT("%0", "%1") "buffer_load_dwordx4 %4, %5, %6 offen sc1 lds\n\tbuffer_load_dwordx4 %4, %5, %6 offen offset:1024 sc1 lds\n\t"
                         "s_mov_b32 m0, %3\n\ts_nop 0\n\tbuffer_load_dwordx4 %4, %5, %7 offen sc1 lds\n\ts_waitcnt vmcnt(0) lgkmcnt(0)\n\ts_barrier"
                         : "=&s"(tmp) : "s"(uu(tm - 1u)), "s"(dm), "s"(dm2), "v"(l16), "s"(srd), "s"(ms), "s"(ms2) : "vcc", "scc", "memory");
        }
#pragma unroll
        for (int ks = 0; ks < 2; ++ks) A[ks] = lds_r16(LB + C_MEM + ks * 1024);
        SCHED();
        static_for<0, 76>(slot);
        u32 fail = 0;
        if (__builtin_expect(odd != 0ull, 0)) {
            u32 mn = ~0u;
#pragma unroll 5
            for (int i = 0; i < 10; ++i) { const v4u e = lds_r16(LB + C_MEM + i * 1024); mn = min(min(min(mn, e.x ^ PSENT), e.y ^ PSENT), min(e.z ^ PSENT, e.w ^ PSENT)); }
            fail = missed < SPIN_CAP && !all_(mn != 0u) ? OOB : 0u;
            float mx = fmaxf(fmaxf(fmaxf(sx[0], sx[1]), fmaxf(sx[2], sx[3])), fmaxf(sm0, sm1));
            mx = max32(max16(fmaxf(mx, dpp<0x4E>(mx))));
#pragma unroll
            for (int i = 0; i < 4; ++i) t[i] = __builtin_amdgcn_exp2f(sx[i] - mx);
            P.x = pk(t[0], t[1]); P.y = pk(t[2], t[3]); P.z = u_(dpp<0x4E>(f_(P.x))); P.w = u_(dpp<0x4E>(f_(P.y)));
            pmm = pk(__builtin_amdgcn_exp2f(sm0 - mx), __builtin_amdgcn_exp2f(sm1 - mx));
            pm = (r & 2) ? (v2u){0u, pmm} : (v2u){pmm, 0u};
            asm volatile("s_nop 1" : "+v"(P), "+v"(pm));
            MF0(rsum, ones, P); MF0(oa, vx[0], P); MF0(ob, vx[1], P);
            NOPS(7);
            { const v2u ones2 = ones.lo; MF1H(rsum, ones2, pm); }
#pragma unroll
            for (int i = 0; i < 4; ++i) SEL(oa[i], oa[i], ob[i]);
            asm volatile("s_nop 4" :: "v"(rsum));
            oso |= fail;
        }
        asm volatile("v_rcp_f32 %0, %3\n\ts_nop 4\n\tv_add_f32 %1, %4, %6\n\tv_add_f32 %2, %5, %7\n\tv_cvt_pk_bf16_f32 %1, %1, %2\n\ts_nop 0"
                     : "=&v"(inv), "=&v"(vm.y), "=&v"(fd) : "v"(rsum[0]), "v"(av1[0]), "v"(av1[1]), "v"(av1[2]), "v"(av1[3]));
        asm volatile("v_mfma_f32_16x16x16_bf16 %0, %1, %2, %0\n\ts_nop 6" : "+v"(oa) : "v"(vm), "v"(pm));
        {
            v4u oc;
            const f32x2 i2 = {inv, inv};
            const f32x2 u0 = oa.lo * i2, u1 = oa.hi * i2;
            asm volatile("v_cvt_pk_bf16_f32 %0, %4, %5\n\tv_cvt_pk_bf16_f32 %2, %4, %5\n\tv_cvt_pk_bf16_f32 %1, %6, %7\n\tv_cvt_pk_bf16_f32 %3, %6, %7\n\t"
                         "v_permlane16_swap_b32 %0, %2\n\ts_nop 0\n\tv_permlane16_swap_b32 %1, %3"
                         : "=&v"(oc.x), "=&v"(oc.y), "=&v"(oc.z), "=&v"(oc.w) : "v"(u0.x), "v"(u0.y), "v"(u1.x), "v"(u1.y));
            st16(rs, oso, oc);
        }
        SCHED();
        if (__builtin_expect(fail == 0u, 1)) {
        unsigned long long clk;
        asm volatile("s_memrealtime %0" : "=s"(clk));
        HT(6);
        v4u tq[4], tk[8], tv[2];
        f32x4 c0, c1, cm;
        u32 oo, kvo; bool kvok;
        u32 ln = l16; PIN(ln);
        lptr QX = lds + C_QX + (w & 1) * 4096 + ln, VO = lds + C_V + (w ^ 1) * 2048 + ln, VMX = lds + C_VM + w * 512 + (ln >> 1);
#pragma unroll
        for (int i = 0; i < 2; ++i) { tq[i] = lds_r16(QX + i * 1024); tk[2 * i] = lds_r16(LB + C_K + i * 1024); tk[2 * i + 1] = lds_r16(LB + C_K + 4096 + i * 1024); }
        *(LAS v2u*)VMX = vm;
        SCHED();
        SFOR(i, 12)
            if constexpr (i == 0) MF0(cm, kmf[0], tq[0]); else if constexpr (i == 1) MF0(c0, tk[0], tq[0]); else if constexpr (i == 2) MF0(c1, tk[1], tq[0]);
            else if constexpr (i % 3 == 0) MF1(cm, kmf[i / 3], tq[i / 3]); else if constexpr (i % 3 == 1) MF1(c0, tk[2 * (i / 3)], tq[i / 3]); else MF1(c1, tk[2 * (i / 3) + 1], tq[i / 3]);
            if constexpr (i == 0) { tq[2] = lds_r16(QX + 2048); tk[4] = lds_r16(LB + C_K + 2048); }
            if constexpr (i == 1) { tk[5] = lds_r16(LB + C_K + 6144); tq[3] = lds_r16(QX + 3072); }
            if constexpr (i == 2) { tk[6] = lds_r16(LB + C_K + 3072); tk[7] = lds_r16(LB + C_K + 7168); }
            if constexpr (i == 3) { tv[0] = lds_r16(VO); tv[1] = lds_r16(VO + 1024); }
            if constexpr (i == 4) vx[1] = lds_r16(VX + 1024);
            if constexpr (i == 4) { oo = ln + (ln & 0x300u) + ((vsrc ^ 8192u) + (u32)((w & 1) * 256)); PIN(oo); kb += NH * 16384; PINS(kb); }
            if constexpr (i == 5) { ksrc = kb + (((kmask >> ((s + 1) & 63)) & 1ull) ? 8192u : 0u); PINS(ksrc); }
            if constexpr (i == 6) { vsrc = kb + (((vmask >> ((s + 1) & 63)) & 1ull) ? W_VO - W_KQ + 8192u : W_VO - W_KQ); PINS(vsrc); }
            if constexpr (i == 10) { PIN(fl); kvok = ((u32)__builtin_amdgcn_ballot_w64(fl < (u32)s + 2) & 7u) == 0u; }
            if constexpr (i == 11) { kvo = l16 | (kvok ? 0u : OOB); PIN(kvo); }
            SCHED();
        SEND
        asm volatile("s_waitcnt lgkmcnt(0)\n\ts_barrier" : "+s"(clk) :: "memory");
        const v2u tvm = *(LAS v2u*)(VMX + (512 - (w & 1) * 1024));
        const v4u pv = lds_r16(ZP + 80);
        DMA32(dk, kvo, ksrc);
        SCHED(); asm volatile("" :: "v"(cm));
        float u[10], m, m2;
        {
            float NEG = -1000000000.0f * LOG2E; PIN(NEG);
            VMULC(u[8], cm[0]); VMULC(u[9], cm[1]);
#define MSK(d, a, i) asm volatile("v_cndmask_b32 %0, %1, %2, %3" : "=v"(d) : "v"(NEG), "v"(a), "s"(vis[i]))
#pragma unroll
            for (int i = 0; i < 4; ++i) MSK(u[i], c0[i], i);
#pragma unroll
            for (int i = 0; i < 4; ++i) MSK(u[4 + i], c1[i], 4 + i);
            asm volatile("v_max3_f32 %0, %2, %3, %10\n\tv_max3_f32 %1, %4, %5, %11\n\tv_max3_f32 %0, %0, %6, %7\n\tv_max3_f32 %1, %1, %8, %9\n\t"
                         "v_max_f32 %0, %0, %1\n\tv_mov_b32 %1, %0\n\ts_nop 1\n\tv_permlane16_swap_b32 %0, %1\n\tv_max_f32 %0, %0, %1\n\tv_mov_b32 %1, %0\n\ts_nop 1\n\t"
                         "v_permlane32_swap_b32 %0, %1\n\tv_max_f32 %0, %0, %1"
                         : "=&v"(m), "=&v"(m2) : "v"(u[0]), "v"(u[1]), "v"(u[2]), "v"(u[3]), "v"(u[4]), "v"(u[5]), "v"(u[6]), "v"(u[7]), "v"(u[8]), "v"(u[9]));
        }
        DMA32(dv, kvo, vsrc);
        {
            v4u px; v2u p0, p1;
#define EX2(d, a, b) asm volatile("v_sub_f32 %0, %2, %4\n\tv_sub_f32 %1, %3, %4\n\tv_exp_f32 %0, %0\n\tv_exp_f32 %1, %1" : "=&v"(d.x), "=&v"(d.y) : "v"(a), "v"(b), "v"(m))
            f32x2 ex[5];
            EX2(ex[0], u[0], u[1]); EX2(ex[1], u[2], u[3]); EX2(ex[2], u[4], u[5]); EX2(ex[3], u[6], u[7]); EX2(ex[4], u[8], u[9]);
            asm volatile("v_cvt_pk_bf16_f32 %0, %8, %9\n\tv_cvt_pk_bf16_f32 %1, %10, %11\n\tv_cvt_pk_bf16_f32 %2, %12, %13\n\tv_cvt_pk_bf16_f32 %3, %14, %15\n\t"
                         "v_mov_b32 %5, 0\n\tv_mov_b32 %6, 0\n\tv_cvt_pk_bf16_f32 %4, %16, %17\n\tv_cvt_pk_bf16_f32 %7, %16, %17"
                         : "=&v"(px.x), "=&v"(px.y), "=&v"(px.z), "=&v"(px.w), "=&v"(p0.x), "=&v"(p0.y), "=&v"(p1.x), "=&v"(p1.y)
                         : "v"(ex[0].x), "v"(ex[0].y), "v"(ex[1].x), "v"(ex[1].y), "v"(ex[2].x), "v"(ex[2].y), "v"(ex[3].x), "v"(ex[3].y), "v"(ex[4].x), "v"(ex[4].y));
            f32x4 o[4], sum;
            const v2u ones2 = ones.lo;
            const u32 now = (u32)clk;
            u32 oz;
            SCHED();
            MF0(sum, ones, px); { const u32 q = ksrc ^ 8192u; DMA32(dq, kvo, q); } SCHED();
            MF0(o[0], vx[0], px);
            MF0(o[1], vx[1], px);
            MF0(o[2], tv[0], px); oz = (oso & ~(3u << 17)) | (u32)((s + 3) & 3) << 17; PIN(oz); SCHED();
            MF0(o[3], tv[1], px); oso = (oso & ~(3u << 17)) | (u32)((s + 1) & 3) << 17; PIN(oso); SCHED();
            MF1H(sum, ones2, p0);
            MF1H(o[0], vm, p0);
            MF1H(o[1], vm, p1); st16(rs, oz, pv); SCHED();
            MF1H(o[2], tvm, p0);
            MF1H(o[3], tvm, p1);
            float inv;
            asm volatile("s_nop 2\n\tv_rcp_f32 %0, %1\n\ts_nop 0" : "=v"(inv) : "v"(sum[0]), "v"(sum));
            asm volatile("" : "+v"(o[0]), "+v"(o[1]));
            st16(rs, oo, (v4u){pk2(o[0].lo * inv), pk2(o[0].hi * inv), pk2(o[1].lo * inv), pk2(o[1].hi * inv)});
            asm volatile("" : "+v"(o[2]), "+v"(o[3]));
            st16(rs, oo ^ 2048u, (v4u){pk2(o[2].lo * inv), pk2(o[2].hi * inv), pk2(o[3].lo * inv), pk2(o[3].hi * inv)});
            SCHED();
            if (__builtin_expect((h & 15) == 0 && w == 3, 0)) {
                st4s(rs, ln == 0 ? hint + 128u + (u32)h * 8u : OOB, 0, (u32)s << 16 | min(missed, 1u) << 14 | min(now - tm, 0x3FFFu));
                if (h == 0) { LST(L * 64 + s, now); LST(128 + L * 64 + s, ((int)(rdl(fl, 3) - now) >= CH_VIS - 4) | ((int)kvok << 1) | ((int)min(missed, 3u) << 2)); LST(640 + L * 64 + s, tm); LST(1728 + L * 64 + s, __builtin_amdgcn_s_memtime()); }
            }
            tm = rdl(fl, 5);
            missed = missed == SPIN_CAP ? missed : 0u;
            dirty = 0;
            if (__builtin_expect(rdl(fl, 3) - now + CH_LATE > 4000u && s + 1 < NSEG, 0)) {
                dirty = 1;
#pragma unroll 1
                for (int n = 0; n < 64; ++n) {
                    CFENCE();
                    const u32 hv = ld4(rs, fkh);
                    tm = rdl(hv, 5);
                    if (rdl(hv, 3) - now + CH_LATE <= 4000u) break;
                }
            }
        }
        LS2(L == 0 && w == 0, h);
        if (__builtin_expect(!kvok, 0) && s + 1 < NSEG) {
            st4s(rs, (ln == 0 && w == 0) ? hint + 4096u + (u32)h * 128u : OOB, 0, (u32)s + 1u);
            dirty = 1;
            wait_flags(rs, ln < 48u ? fkh : OOB, (u32)s + 2, dead);
            DMA32(dk, l16, ksrc); DMA32(dv, l16, vsrc); DMA32(dq, l16, ksrc ^ 8192u);
            const u32 now = rt32() + CH_KV;
            if ((int)(tm - now) < 0) next_slot(now);
        }
        ++s;
        } else {
            u32 ln = l16; PIN(ln);
            oso = (ln & 256u) ? oso : oso & ~OOB;
            const u32 was = tm - tmp;
            tm = rdl(fl, 5); dirty = 1;
            if ((int)(tm - rdl(fl, 4) - was) > 24) tm -= rdl(fl, 4);
            if ((++missed & 7u) == 0u) {
                *(LAS u32*)(lds + C_DUMP + (w == 0 ? 0 : 64)) = ld4(rs, foff(F_ERR));
                BAR();
                missed = uu(*(LAS u32*)(lds + C_DUMP)) != 0u ? SPIN_CAP : missed;
                st4s(rs, missed == SPIN_CAP ? foff(F_ERR) : OOB, 0, 1u);
            }
        }
    }
    DRAIN(); BAR();
    st4s(rs, ofl, 0, NSEG);
}

#define WO_NI 4
#define WROT 0
DEV void role_wo(const Params& p, lptr lds, int j) {
    const int tid = threadIdx.x, lane = tid & 63, w = uni(tid >> 6), g = lane >> 4, r = lane & 15;
    const int rg = j & 1, cg = j >> 1, rot = cg & WROT;
    const rsrc_t rs = mk_rsrc(p.ws, W_END);
    v8u wo[8][5];
    {
        const v4u so_ = mk_srd(p.Wo, DM * NQ * 2);
        const int one = (0x3F2FF1F0u >> (4 * (cg & 7))) & 15;
        u32 vo[3];
#pragma unroll
        for (int i = 0; i < 3; ++i) { const int n = 64 * i + lane; vo[i] = n % 6 < 5 ? (u32)((n / 6) * (DM * 2) + cg * 80 + (n % 6) * 16) : ((n / 6) & 3) == one ? (u32)((n / 6) * (DM * 2) + (cg >> 3) * 640 + 304 + rg * 16) : OOB; }
        auto fill = [&](int b) {
            const u32 dst = uu(w * 36864 + (b % 3) * 12288), src = uu((8 * w + ((b + rot) & 7)) * (DH * DM * 2));
#pragma unroll
            for (int i = 0; i < 12; ++i) DMAIN(so_, dst + i * 1024, vo[i % 3], src + (i / 3) * (32 * DM * 2));
        };
#pragma unroll 1
        for (int b = 0; b < 3; ++b) fill(b);
        const int q4 = r >> 2;
        lptr tb = lds + w * 36864 + (32 * (g >> 1) + 16 * (q4 >> 1) + 8 * ((r >> 1) & 1) + 4 * (g & 1) + (q4 & 1)) * 96 + (r & 1) * 8;
        SFOR(b, 8)
            VMW(b < 6 ? 24 : 84 - 12 * b);
#pragma unroll
            for (int t = 0; t < 5; ++t) {
                lptr q = tb + (b % 3) * 12288 + (t == 0 ? 4 : (w + t) & 3) * 16;
                wo[b][t] = tr_frag(q, q + 192, q + 64 * 96, q + 64 * 96 + 192);
                if (t < 4) asm volatile("" : "+a"(wo[b][t])); else asm volatile("" : "+v"(wo[b][t]));
            }
            CFENCE();
            if constexpr (b < 5) fill(b + 3);
        SEND
    }
    BAR();
    EV(6);
    const u32 idx = (r & 2) ? 0xEEEEu : 0x4444u;
    bool dead = false;
    const u32 pol0 = get_pol(p, rs, dead) & 1u;
    const u32 l16 = (u32)lane * 16u;
    u32 oo[8];
#pragma unroll
    for (int i = 0; i < 8; ++i) oo[i] = uu(W_ORING + ring_map((u32)(rg * 32768 + (8 * w + ((i + rot) & 7)) * 1024), pol0));
    const u32 hint = W_MRING + 4 * 32768u + pol0 * 8192u;
    u32 hvo[2];
#pragma unroll
    for (int l = 0; l < 2; ++l) hvo[l] = lane < 2 ? hint + (u32)(l ^ 1) * (MB / 4) + (u32)lane * 4u : lane == 2 ? hint + (u32)l * (MB / 4) + 128u : lane == 3 ? foff(F_ERR) : lane == 4 ? hint + (u32)l * (MB / 4) + 256u :
                 (lane >= 32 && j == 0 && w == 3) ? hint + (u32)l * (MB / 4) + (u32)lane * 128u : OOB;
    const int xo = g * 128 + (r & 7) * 16, O = 5 * cg + (r < 8 ? w : 4), q2 = 2 + (r >> 3);
    lptr xw0 = lds + (r < 8 ? 2048 + w * 4 : ((w + 1) & 3) * 512 + 4) + xo, xw1 = lds + ((w + q2) & 3) * 512 + q2 * 4 + xo;
    lptr xr = lds + (r < 8 ? w : 4) * 512 + xo;
    if (r < 8) { *(LAS u32*)xr = 0u; *(LAS u32*)(xr + 8192) = 0u; }
    const u32 mpo = (r & 4) == 0 && (r < 8 || w == 0) ? W_MRING + ring_map((u32)((O >> 1) * 256 + rg * 128 + (4 * (g & 1) + 2 * (O & 1) + (g >> 1)) * 16 + (r & 3) * 4), pol0) : OOB;
    constexpr u32 PER0 = CH_W0 + CH_MW + CH_H0 + CH_MH + 2 * CH_VIS;
    const bool lead = j == 0 && w == 3;
    u32 hso = OOB;
    MK_SRD();
    u32 t = 0, tn = 0;
    int sn[2] = {0, 0};
    u32 hav = 4 * CH_H0, wav = 4 * CH_W0, mh = 8 * (CH_MH + CH_M0), mw = 8 * (CH_MW + CH_M0), ok[2] = {0u, 0u}, dw[2] = {CH_W0 + CH_VIS + CH_MW + CH_REC, CH_W0 + CH_VIS + CH_MW + CH_REC};
    u32 zero = 0, hv = 0xFFFFu, pst = 1, pdur = 0, pmp = OOB;
    const u32 psent = PSENT;
    lptr sw = lds + 5120 + w * 4;
    bool stop = false;
    asm volatile("" : "+v"(zero), "+v"(sw), "+v"(xw0), "+v"(xw1), "+v"(xr));
    auto turn = [&](auto lc) __attribute__((always_inline)) {
        constexpr int L = decltype(lc)::value, M = L ^ 1;
        u32 now, done, rep, rd, lost, dh, dn, nt, za; v4u hw;
        const int sc = sn[L];
        auto books = [&](auto ic) __attribute__((always_inline)) {
            constexpr int i = decltype(ic)::value;
            if constexpr (i == 0) { rep = max(rdl(hv, 2), rdl(hv, 4)); PINS(rep); }
            if constexpr (i == 1) { lost = (u32)(__builtin_amdgcn_ballot_w64(hv == (u32)sn[M]) >> 32); PINS(lost); }
            if constexpr (i == 2) { lost |= (rep >> 16) ^ (u32)sn[M]; PINS(lost); }
            if constexpr (i == 3) { lost |= ok[M] ^ 2u; rd = rep & 0xFFFFu; PINS(lost); PINS(rd); }
            if constexpr (i == 4) { lost = lost == 0u ? CH_UP * 8u : 0u; PINS(lost); }
            if constexpr (i == 5) { dn = wav >> 2; PINS(dn); pdur = min(pdur, dn + 8u); PINS(pdur); }
            if constexpr (i == 6) { pdur = (done & ok[M]) ? pdur : dn; PINS(pdur); }
            if constexpr (i == 7) { wav += pdur - dn; PINS(wav); }
            if constexpr (i == 8) { dh = hav >> 2; PINS(dh); nt = min(rd, dh + 8u); PINS(nt); }
            if constexpr (i == 9) { nt = rd < 0x3FFFu ? nt : dh; PINS(nt); }
            if constexpr (i == 10) { hav += nt - dh; PINS(hav); }
            if constexpr (i == 11) { mw = max(mw - CH_DEC, CH_MW * 8u); PINS(mw); }
            if constexpr (i == 12) { mh = max(mh - CH_DEC, CH_MH * 8u); PINS(mh); }
            if constexpr (i == 13) { rd = rd >> 14 == 1u ? lost : 0u; PINS(rd); }
            if constexpr (i == 14) { mw += rd; mh += lost - rd; PINS(mw); PINS(mh); }
            if constexpr (i == 15) { ok[M] = done ? 1u : (ok[M] & 1u) << 1; PINS(ok[M]); }
            if constexpr (i == 16) { dh = (2u * hav + mh) / 8u + (sn[1] < 2 ? CH_VIS + CH_SOLO : CH_VIS); PINS(dh); }
            if constexpr (i == 17) { dn = (2u * wav + mw) / 8u + CH_VIS; PINS(dn); }
            if constexpr (i == 18) { nt = dw[L] + dh; PINS(nt); }
            if constexpr (i == 19) { dh += dn; dw[L] = dn; sn[M] += (int)done; PINS(dh); PINS(sn[M]); }
            if constexpr (i == 20) { rd = tn - t + (dh >> 1); PINS(rd); }
            if constexpr (i == 21) { rd = sn[M] + 1 < NSEG ? rd : 0u; PINS(rd); }
            if constexpr (i == 22) { nt = t + (u32)max((int)nt, (int)rd); PINS(nt); }
            if constexpr (i == 23) { hw.x = nt; hw.y = dh; PIN(hw.x); PIN(hw.y); }
            if constexpr (i == 24) { hw.z = nt + dn; hw.w = 0u; PIN(hw); }
            if constexpr (i == 25) st16(rs, hso + L * (MB / 4), hw);
        };
        if (__builtin_expect(sc + 1 < NSEG, 1)) {
            u32 vo = l16 + (L * (MB / 2) + (u32)(sc & 3) * 131072u), mp = mpo + (L * (MB / 4) + (u32)((sc + 1) & 3) * 32768u), zb, hn;
            done = uu(pst) ^ 1u;
            za = done ? pmp : OOB;
            zb = ok[L] == 1u ? OOB : mp;
            pmp = mpo + (L * (MB / 4) + (u32)(sc & 3) * 32768u);
            *(LAS u32*)(sw + L * 8192) = zero;
            f32x4 acc[5] = {};
            asm volatile("" : "+v"(acc[0]), "+v"(acc[1]), "+v"(acc[2]), "+v"(acc[3]), "+v"(acc[4]), "+v"(mp), "+v"(pmp), "+v"(vo), "+v"(za), "+v"(zb));
            PINS(done);
#define WT(k) do {} while (0)
            v4u d[8];
            {
                u32 tmp;
                asm volatile(WAIT_TXT("%1", "%2") "buffer_load_dwordx4 %0, %3, %4, %5 offen sc1\n\ts_waitcnt lgkmcnt(0)"
                             : "=&v"(d[0]), "=&s"(tmp) : "s"(uu(t - 1u)), "v"(vo), "s"(srd), "s"(oo[0]) : "vcc", "scc", "memory");
            }
            WT(0);
            SFOR(i, 7)
                asm volatile("buffer_load_dwordx4 %0, %1, %2, %3 offen sc1" : "=v"(d[i + 1]) : "v"(vo), "s"(srd), "s"(oo[i + 1]) : "memory");
                SCHED();
                static_for<3 * i, 3 * i + 3>(books);
                SCHED();
            SEND
            LD4A(hn, hvo[L], 0);
            st4s(rs, za, 0, psent);
            st4s(rs, zb, 0, psent);
            SCHED();
            WT(1);
            float f0[5], f1[5], xs; unsigned long long bad; v4u sv;
#define WSW(o_) asm volatile("v_permlane32_swap_b32 %0, %1" : "+v"(f0[o_]), "+v"(f1[o_]))
#define WAD(o_) asm volatile("v_add_f32_dpp %0, %1, %0 row_ror:8 row_mask:0xf bank_mask:0xf\n\tv_add_f32_dpp %0, %2, %3 row_ror:8 row_mask:0xf bank_mask:0xc" : "+v"(f0[o_]) : "v"(f1[o_]), "v"(f0[o_ + 1]), "v"(f1[o_ + 1]))
#define WFO(o_) do { VADD(f0[o_], acc[o_][0], acc[o_][2]); VADD(f1[o_], acc[o_][1], acc[o_][3]); } while (0)
#define WOWN(k) do { if constexpr (WO_NI + (k) < 8) SMF_V(acc[4], d[(WO_NI + (k)) & 7], wo[(WO_NI + (k)) & 7][4]); SCHED(); } while (0)
            SFOR(i, 40)
                constexpr int pc = i / 5, oc = i % 5;
                if constexpr (oc == 0) VMW(10 - pc);
                if constexpr (oc < 4) SMF_A(acc[oc], d[pc], wo[pc][oc]); else if constexpr (pc < WO_NI) SMF_V(acc[4], d[pc], wo[pc][4]);
                if constexpr (i == 0) WT(2);
                if constexpr (i == 20) WT(3);
                if constexpr (i == 35) WT(4);
                if constexpr (i < 4) books(IC<21 + i>{});
                if constexpr (i == 6) { xs = 0.f; PIN(xs); }
                if constexpr (i == 36) NOPS(1);
                if constexpr (i == 37) NOPS(2);
                SCHED();
            SEND
            WFO(0); WFO(1);
            asm volatile("v_cmp_u_f32 %0, %1, %2" : "=s"(bad) : "v"(f0[0]), "v"(f1[0]));
            WSW(0); WSW(1); WFO(2); WAD(0); WFO(3);
            *(LAS float*)(xw0 + L * 8192) = f0[0]; SCHED();
            WSW(2); WSW(3);
            if (__builtin_expect(bad != 0ull, 0)) {
                u32 mn = ~0u;
#pragma unroll
                for (int i = 0; i < 8; ++i) { u32 e = d[i].x; PIN(e); mn = min(mn, e ^ PSENT); }
                if (all_(mn != 0u)) {
#pragma unroll
                    for (int i = 0; i < 8; ++i) { v4u e = d[i]; PIN(e); mn = min(min(mn, e.y ^ PSENT), min(e.z ^ PSENT, e.w ^ PSENT)); }
                }
                *(LAS u32*)(sw + L * 8192) = all_(mn != 0u) ? 0u : 1u;
            }
            WAD(2);
            *(LAS float*)(xw1 + L * 8192) = f0[2]; SCHED();
            WOWN(0); WOWN(1); WOWN(2); WOWN(3);
            WT(5);
            f32x4 xa; float nb; u32 cw, mq;
            asm volatile("s_waitcnt lgkmcnt(0)\n\ts_barrier\n\tds_read_b128 %0, %2 offset:%4\n\tds_read_b128 %1, %3 offset:%4+5120\n\ts_nop 3"
                         : "=&v"(xa), "=&v"(sv) : "v"(xr), "v"(zero), "i"(L * 8192) : "memory");
            WT(6);
            WFO(4);
            asm volatile("s_nop 0\n\tv_permlane32_swap_b32 %1, %2\n\ts_nop 1\n\tv_add_f32_dpp %0, %2, %1 row_ror:8 row_mask:0xf bank_mask:0x3\n\ts_waitcnt lgkmcnt(1)" : "+v"(xs), "+v"(f0[4]), "+v"(f1[4]));
            asm volatile("v_add_f32 %0, %0, %4\n\tv_add_f32 %1, %5, %6\n\tv_add_f32 %0, %0, %7\n\tv_add_f32 %0, %0, %1\n\tv_cmp_u_f32 vcc, %0, %0\n\ts_nop 0\n\t"
                         "v_mov_b32_dpp %1, %0 row_shl:4 row_mask:0xf bank_mask:0xf\n\tv_cndmask_b32 %3, %8, %9, vcc\n\tv_cvt_pk_bf16_f32 %2, %0, %1"
                         : "+v"(xs), "=&v"(nb), "=&v"(cw), "=&v"(mq) : "v"(xa[0]), "v"(xa[1]), "v"(xa[2]), "v"(xa[3]), "v"(mp), "v"(OOB) : "vcc");
            st4s(rs, mq, 0, cw);
            SCHED();
            WT(7);
            now = rt32();
            books(IC<25>{});
            asm volatile("s_waitcnt vmcnt(4) lgkmcnt(0)" : "+v"(hn), "+v"(sv));
            hv = hn;
            pst = sv.x | sv.y | sv.z | sv.w;
            if (__builtin_expect(__builtin_amdgcn_ballot_w64(mq != mp && pst == 0u) != 0ull, 0)) st4s(rs, mp, 0, cw);
        } else {
            done = uu(pst) ^ 1u;
            st4s(rs, done ? pmp : OOB, 0, psent);
            static_for<0, 25>(books);
            pst = 1;
            BAR();
            hv = ld4(rs, hvo[L]);
            PIN(hv);
            now = rt32();
        }
        pdur = now - t;
        const u32 per = rdl(hv, 1), th = rdl(hv, 0);
        t = lead ? tn : (int)(th - now) < -(int)(per >> 1) ? th + per : th;
        tn = nt;
        dw[M] += max((int)(now + CH_WX - t), 0);
        stop = stop || rdl(hv, 3) != 0u;
    };
#pragma unroll 1
    for (int k = 0; k < 2 * SPIN_CAP && !stop && (sn[0] + 1 < NSEG || sn[1] + 1 < NSEG); ++k) {
        turn(IC<0>{}); turn(IC<1>{});
        if (__builtin_expect(k != 0, 1)) continue;
        if (j == 0) {
            for (int n = 0; n < SPIN_CAP && !dead && sn[0] == 0; ++n) {
                CFENCE();
                if (all_(ld4(rs, W_ORING + pol0 * 8192u + (u32)w * 16384u + (u32)lane * 128u) != PSENT)) break;
                __builtin_amdgcn_s_sleep(1);
            }
            BAR();
        }
        if (lead) {
            t = rt32() + 56u; tn = t + PER0 / 2 + CH_REC;
            st16(rs, lane < 2 ? hint + (u32)lane * (MB / 4) : OOB, (v4u){lane == 0 ? t : tn, PER0, (lane == 0 ? t : tn) + (CH_W0 + CH_VIS + CH_MW + CH_REC), 0u});
        } else {
            for (int n = 0; n < SPIN_CAP && !dead; ++n) {
                CFENCE();
                const u32 hv = ld4(rs, hvo[1]);
                t = rdl(hv, 0);
                if (rdl(hv, 1) < 400u) break;
                __builtin_amdgcn_s_sleep(1);
            }
        }
        hso = (lead && lane == 0) ? hint : OOB;
        hv = 0xFFFFu; dw[0] = dw[1] = CH_W0 + CH_VIS + CH_MW + CH_REC;
    }
    if (sn[0] + 1 < NSEG || sn[1] + 1 < NSEG) st4s(rs, tid == 0 ? foff(F_ERR) : OOB, 0, 1u);
}

__global__ void __launch_bounds__(256, 1) k_main(Args args) {
    Params p;
    (Args&)p = args;
    lptr lds = (lptr)(u32)16 - 16;
    if (blockIdx.x == 0) LST(256, rt32());
    unsigned long long pc; int len;
    asm volatile("s_getpc_b64 %0\n.Lk_main_here:\n\ts_mov_b32 %1, .Lk_end_here-.Lk_main_here" : "=s"(pc), "=s"(len));
    const v4u code = ldin16(mk_rsrc((const void*)(pc & ~15ull), (u32)max(len, 0)), (((blockIdx.x >> 3) & 31u) * 256u + threadIdx.x) * 16u, 0);
    CFENCE();
    if (threadIdx.x == 0) {
        const v4u e = ld16s(mk_rsrc(p.ws, W_END), foff(F_EP), 0);
        const unsigned long long tag = (unsigned long long)e.y << 32 | (e.x + 1u);
        u32 role = ((u32)__builtin_amdgcn_s_getreg(20 | (3 << 11)) & 7u) * 32u + ((blockIdx.x >> 3) & 31u);
#pragma unroll 1
        for (int n = 0; n < 256; ++n, role = (role + 1u) & 255u)
            if (__hip_atomic_exchange((GAS unsigned long long*)(p.ws + W_SLOT) + role * 8u, tag, __ATOMIC_RELAXED, __HIP_MEMORY_SCOPE_AGENT) != tag) break;
        lds_w16(lds, (v4u){e.x + 1u, e.y, e.z, role});
    }
    __syncthreads();
    const v4u boot = lds_r16(lds);
    p.e0 = uu(boot.x); p.e1 = uu(boot.y); p.e2 = uu(boot.z);
    const int role = uni((int)boot.w), x = role >> 5, i = role & 31;
    asm volatile("" :: "v"(code));
    __syncthreads();
    LST(8192 + blockIdx.x * 16, role);
    if (x < 2) { role_head(p, lds, i >> 4, x * 16 + (i & 15)); __builtin_amdgcn_endpgm(); }
    if (x == 2) { role_wo(p, lds, i); __builtin_amdgcn_endpgm(); }
    const int no = x == 3 ? 30 : x < 6 ? 5 : 0;
    if (x > 3 && i >= 8) { role_qkv(p, lds, (x - 4) * 24 + i - 8); __builtin_amdgcn_endpgm(); }
    const bool isout = i < no;
    const int og = x == 3 ? i / 5 : x + 2, ng = i % 5, L = og >> 2, kq = og & 3;
    const int k = i - no, w = uni(threadIdx.x >> 6);
    const int j = isout ? (L == 1 ? 0 : 44) + kq * 5 + ng : x == 3 ? 36 + k : k < 2 ? 12 + 2 * x + k : x < 6 ? 24 + x : k < 5 ? 10 + 3 * x + k : 15 + 3 * x + k;
    const rsrc_t rs = mk_rsrc(p.ws, W_END);
    if (j < NKV) {
        const int nt = j < 320 - 7 * NKV ? 8 : 7;
#pragma unroll 1
        for (int q = 0; q < 4; ++q) kv_fetch(p, j + NKV * q, uu(q * KT_BYTES));
        Filt f;
        u32 warm[3];
#pragma unroll 1
        for (int q = 0; q < nt; ++q) {
            VMW(15); BAR();
            if (q == 0) EV(2);
            kv_fetch(p, j + NKV * (q + 4), uu(((q + 4) & 7) * KT_BYTES));
            kv_tile(p, rs, lds + q * KT_BYTES, lds + (q == 0 ? 8 : q - 1) * KT_BYTES, j + NKV * q);
        }
        filt_load(p, j, f);
        VMW(10); BAR();
        boot_set(p, rs, threadIdx.x == 0 ? foff(F_KV + j) : OOB, 2u);
        EV(4);
        { bool dead = false; boot_wait(p, rs, pol_off(), 0u, dead); }
#pragma unroll
        for (int q = 0; q < 3; ++q)
            warm[q] = __builtin_amdgcn_raw_buffer_load_b32(mk_rsrc(p.ssmk, ACT), 256u * 4096u + ((u32)((q * NKV + j) * 256) + threadIdx.x) * 64u, 0, 0);
        conv_job<0>(p, rs, lds, j, f, 0);
        asm volatile("" :: "v"(warm[0]), "v"(warm[1]), "v"(warm[2]));
        if (j >= 36) filt_load<5, 0>(p, 28 + j, f);
        DRAIN(); BAR();
        st4s(rs, threadIdx.x == 0 ? foff(F_CV + j) : OOB, 0, 2u);
        if (j >= 36) {
#pragma unroll 1
            for (int g = 28 + j; g < 160; g += 8) conv_job<2>(p, rs, lds, g, f, min(g + 8, 159));
            DRAIN(); BAR();
            st4s(rs, threadIdx.x == 0 ? foff(F_CV + 124 + j) : OOB, 0, 3u);
            EV(15);
            __builtin_amdgcn_endpgm();
        }
    }
    if (!isout) { if (j < 28) role_row<0>(p, lds, (j - 20) * 4 + w); else role_row<1>(p, lds, (j - 28) * 4 + w);
        boot_set(p, rs, j == 28 && threadIdx.x == 0 ? foff(F_EP) : OOB, 0u);
        __builtin_amdgcn_endpgm();
    }
    role_out(p, lds, L, kq, ng);
}

__global__ void k_end() { asm volatile(".Lk_end_here:"); }

extern "C" void kernel_launch(void* const* d_in, const int* in_sizes, int n_in, void* d_out, int out_size, void* d_ws, size_t ws_size,
                              hipStream_t stream) {
    Args p;
    p.x = (const u16*)d_in[0]; p.mem0 = (const u16*)d_in[1]; p.Wq = (const u16*)d_in[2]; p.Wk = (const u16*)d_in[3];
    p.Wv = (const u16*)d_in[4]; p.Wo = (const u16*)d_in[5];
    p.bk = (const u16*)d_in[8]; p.bv = (const u16*)d_in[9]; p.shift = (const u16*)d_in[10]; p.ssmk = (const u16*)d_in[11]; p.ssmD = (const u16*)d_in[12];
    p.out = (u16*)d_out;
    p.ws = (unsigned char*)(((size_t)d_ws + 2 * MB - 1) & ~(size_t)(2 * MB - 1));
    (void)hipFuncSetAttribute((const void*)k_main, hipFuncAttributeMaxDynamicSharedMemorySize, LDS_BYTES);
    hipLaunchKernelGGL(k_main, dim3(256), dim3(256), LDS_BYTES, stream, p);
}
```

```cpp
#include <hip/hip_runtime.h>
#include <hip/hip_bf16.h>

typedef unsigned short u16;
typedef unsigned int u32;
typedef u32 v2u __attribute__((ext_vector_type(2)));
typedef u32 v4u __attribute__((ext_vector_type(4)));
typedef u32 v8u __attribute__((ext_vector_type(8)));
typedef float f32x2 __attribute__((ext_vector_type(2)));
typedef float f32x4 __attribute__((ext_vector_type(4)));
typedef float f32x16 __attribute__((ext_vector_type(16)));
typedef __bf16 bf16x2 __attribute__((ext_vector_type(2)));
typedef __bf16 bf16x8 __attribute__((ext_vector_type(8)));
typedef short s16x4 __attribute__((ext_vector_type(4)));
#define LAS __attribute__((address_space(3)))
#define GAS __attribute__((address_space(1)))
typedef LAS unsigned char* lptr;
#define DEV __device__ __forceinline__

constexpr int SEQ = 2048, DM = 640, NH = 32, DH = 128, NSEG = 64, NQ = NH * DH;

constexpr u32 MB = 1u << 20;
constexpr u32 W_ORING = 0;
constexpr u32 W_MRING = MB;
constexpr u32 W_CTRL = 4 * MB;
constexpr u32 ACT = SEQ * DM * 2;
constexpr u32 H_TILE = 128 * 1024;
constexpr u32 W_H2 = 8 * MB;
constexpr u32 W_KQ = 16 * MB;
constexpr u32 W_VO = W_KQ + 64 * MB;
constexpr u32 W_P = W_VO + 64 * MB;
constexpr u32 P_ITEM = 512 * 1024;
constexpr u32 W_H1 = W_P + 44 * MB, W_Y = W_H1 + 8 * MB, W_X2 = W_Y + ACT;
constexpr u32 W_KVT = W_X2 + ACT;
constexpr u32 W_END = W_KVT + 3 * MB;
constexpr u32 NREG = 12;
static_assert((size_t)W_END + 2 * MB <= 256ull * MB && W_H1 >= (NREG - 1) * 16 * MB + 2 * MB && W_H1 <= NREG * 16 * MB, "workspace");
constexpr u32 F_EP = 0, F_RG = 32, F_LN = 64, F_KV = 192,
              F_QKV = 256, F_O = F_QKV + 192, F_P = F_O + 64, F_H2 = F_P + 64, F_CV = F_H2 + 64, F_ERR = F_CV + 191, F_N = F_CV + 192;
constexpr u32 W_SLOT = 3 * MB;
DEV u32 foff(u32 idx) { return W_CTRL + (idx >> 6) * 16384u + (idx & 63u) * 128u; }
DEV u32 near_(u32 o, u32 side, u32 pol) { return o + ((side ^ (pol >> (o >> 24)) ^ (o >> 21)) & 1u) * 8192u; }
DEV u32 p_item(int s) { return W_P + (u32)s * P_ITEM; }

constexpr u32 OOB = 0x80000000u;
constexpr u32 PSENT = 0x7F817F81u;
constexpr int SPIN_CAP = 40000;
constexpr int LDS_BYTES = 160 * 1024;
#define ST_HEAD 0
#define ST_WO 0

#define EV(k) do {} while (0)
#define LST(idx, v) do {} while (0)
#define LS2(cond, idx) do {} while (0)
struct Args {
    const u16 *x, *mem0, *Wq, *Wk, *Wv, *Wo, *bk, *bv, *shift, *ssmk, *ssmD;
    u16* out;
    unsigned char* ws;
};
struct Params : Args { u32 e0, e1, e2; };

typedef __amdgpu_buffer_rsrc_t rsrc_t;
DEV rsrc_t mk_rsrc(const void* p, u32 bytes) { return __builtin_amdgcn_make_buffer_rsrc((void*)p, 0, bytes, 0x00020000); }
DEV v4u ld16s(rsrc_t r, u32 off, u32 so) { return __builtin_amdgcn_raw_buffer_load_b128(r, off, so, 16); }
DEV v2u ld8s(rsrc_t r, u32 off, u32 so) { return __builtin_amdgcn_raw_buffer_load_b64(r, off, so, 16); }
DEV v4u ldin16(rsrc_t r, u32 off, u32 so) { return __builtin_amdgcn_raw_buffer_load_b128(r, off, so, 0); }
DEV v2u ldin8(rsrc_t r, u32 off, u32 so) { return __builtin_amdgcn_raw_buffer_load_b64(r, off, so, 0); }
DEV u32 ld4(rsrc_t r, u32 off) { return __builtin_amdgcn_raw_buffer_load_b32(r, off, 0, 16); }
DEV void st16(rsrc_t r, u32 off, v4u v) { __builtin_amdgcn_raw_buffer_store_b128(v, r, off, 0, 16); }
DEV void st8s(rsrc_t r, u32 off, u32 so, v2u v) { __builtin_amdgcn_raw_buffer_store_b64(v, r, off, so, 16); }
DEV void st4s(rsrc_t r, u32 off, u32 so, u32 v) { __builtin_amdgcn_raw_buffer_store_b32(v, r, off, so, 16); }
#define DRAIN() asm volatile("s_waitcnt vmcnt(0)" ::: "memory")
#define VMW(n) asm volatile("s_waitcnt vmcnt(%0)" :: "i"(n) : "memory")
#define BAR() asm volatile("s_waitcnt lgkmcnt(0)\n\ts_barrier" ::: "memory")
#define SCHED() __builtin_amdgcn_sched_barrier(0)
#define CFENCE() asm volatile("" ::: "memory")
#define NOPS(n) asm volatile("s_nop %0" :: "i"(n))
#define PIN(x) asm volatile("" : "+v"(x))
#define PINS(x) asm volatile("" : "+s"(x))

template <int V> struct IC { static constexpr int value = V; };
template <int I, int N, class F> DEV void static_for(F&& f) { if constexpr (I < N) { f(IC<I>{}); static_for<I + 1, N>(f); } }
#define SFOR(var, n) static_for<0, n>([&](auto ic_) __attribute__((always_inline)) { constexpr int var = decltype(ic_)::value;
#define SEND });

DEV u32 pk(float a, float b) { bf16x2 v = {(__bf16)a, (__bf16)b}; return __builtin_bit_cast(u32, v); }
DEV float lo_f(u32 u) { return __builtin_bit_cast(float, u << 16); }
DEV float hi_f(u32 u) { return __builtin_bit_cast(float, u & 0xffff0000u); }
DEV f32x2 up2(u32 u) { return (f32x2){lo_f(u), hi_f(u)}; }
DEV u32 pk2(f32x2 v) { return pk(v.x, v.y); }
DEV float rbf(float a) { return (float)(__bf16)a; }
DEV v4u lds_r16(lptr p) { return *(LAS v4u*)p; }
DEV void lds_w16(lptr p, v4u v) { *(LAS v4u*)p = v; }
DEV v2u lds_tr(lptr p) { return __builtin_bit_cast(v2u, __builtin_amdgcn_ds_read_tr16_b64_v4i16((LAS s16x4*)p)); }
DEV v4u tr2(lptr p0, lptr p1) { const v2u a = lds_tr(p0), b = lds_tr(p1); return (v4u){a.x, a.y, b.x, b.y}; }
DEV v8u cat8(v4u a, v4u b) { return __builtin_shufflevector(a, b, 0, 1, 2, 3, 4, 5, 6, 7); }
DEV f32x4 mfma16(v4u a, v4u b, f32x4 c) { return __builtin_amdgcn_mfma_f32_16x16x32_bf16(__builtin_bit_cast(bf16x8, a), __builtin_bit_cast(bf16x8, b), c, 0, 0, 0); }
DEV f32x16 mfma32(v4u a, v4u b, f32x16 c) { return __builtin_amdgcn_mfma_f32_32x32x16_bf16(__builtin_bit_cast(bf16x8, a), __builtin_bit_cast(bf16x8, b), c, 0, 0, 0); }

DEV float f_(u32 u) { return __builtin_bit_cast(float, u); }
DEV u32 u_(float f) { return __builtin_bit_cast(u32, f); }
template <int CTRL> DEV float dpp(float v) { return f_((u32)__builtin_amdgcn_update_dpp(0, (int)u_(v), CTRL, 0xf, 0xf, false)); }
DEV float max16(float v) { auto r = __builtin_amdgcn_permlane16_swap(u_(v), u_(v), false, false); return fmaxf(f_(r[0]), f_(r[1])); }
DEV float max32(float v) { auto r = __builtin_amdgcn_permlane32_swap(u_(v), u_(v), false, false); return fmaxf(f_(r[0]), f_(r[1])); }
DEV float add16(float v) { auto r = __builtin_amdgcn_permlane16_swap(u_(v), u_(v), false, false); return f_(r[0]) + f_(r[1]); }
DEV float add32(float v) { auto r = __builtin_amdgcn_permlane32_swap(u_(v), u_(v), false, false); return f_(r[0]) + f_(r[1]); }
DEV float wave_sum(float v) {
    v += dpp<0xB1>(v);
    v += dpp<0x4E>(v);
    v += dpp<0x124>(v);
    v += dpp<0x128>(v);
    return add32(add16(v));
}
DEV bool all_(bool c) { return __builtin_amdgcn_ballot_w64(!c) == 0ull; }
DEV int uni(int v) { return __builtin_amdgcn_readfirstlane(v); }
DEV u32 uu(u32 v) { return (u32)__builtin_amdgcn_readfirstlane((int)v); }
DEV u32 rdl(u32 v, int l) { return (u32)__builtin_amdgcn_readlane((int)v, l); }
DEV u32 rt32() { return (u32)__builtin_amdgcn_s_memrealtime(); }

DEV void wait_flags(rsrc_t rs, u32 off, u32 want, bool& dead) {
    if (dead) return;
#pragma unroll 1
    for (int n = 0;; ++n) {
        CFENCE();
        const u32 v = ld4(rs, off);
        if (all_(off >= OOB || v >= want)) break;
        __builtin_amdgcn_s_sleep(1);
        if ((n & 63) == 63) { if (uni((int)ld4(rs, foff(F_ERR))) != 0) { dead = true; break; } }
        if (n > SPIN_CAP) { st4s(rs, foff(F_ERR), 0, 1u); dead = true; break; }
    }
    CFENCE();
}

DEV bool boot_ok(const Params& p, u32 off, v4u v, u32 want) { return all_(off >= OOB || (v.x == p.e0 && v.y == p.e1 && v.z == p.e2 && v.w >= want)); }
DEV void boot_set(const Params& p, rsrc_t rs, u32 off, u32 v) { st16(rs, off, (v4u){p.e0, p.e1, p.e2, v}); }
DEV u32 boot_wait(const Params& p, rsrc_t rs, u32 off, u32 want, bool& dead) {
    v4u v;
#pragma unroll 1
    for (int n = 0;; ++n) {
        CFENCE();
        v = ld16s(rs, off, 0);
        if (boot_ok(p, off, v, want) || dead) break;
        __builtin_amdgcn_s_sleep(1);
        if (n > SPIN_CAP) { st4s(rs, foff(F_ERR), 0, 1u); dead = true; }
    }
    CFENCE();
    return v.w;
}

DEV u32 pol_off() { const int lane = threadIdx.x & 63; return lane < 32 ? foff(F_RG + lane) : OOB; }
DEV u32 pol_of(u32 raw) {
    const int lane = threadIdx.x & 63;
    const float v = add16(__builtin_amdgcn_fmed3f((float)((int)raw - (1 << 20)), -250.f, 250.f));
    const float lo = dpp<0x121>(v), hi = dpp<0x12F>(v), ex = dpp<0x122>(v) + dpp<0x123>(v) + dpp<0x124>(v);
    const float sum = v + lo + ((lane & 15) == 11 ? 0.f : hi) + ((lane & 15) == 0 ? ex : 0.f);
    return (u32)__builtin_amdgcn_ballot_w64(sum >= 0.f) & 0xFFFFu;
}
DEV u32 get_pol(const Params& p, rsrc_t rs, bool& dead) { return pol_of(boot_wait(p, rs, pol_off(), 0u, dead)); }
DEV u32 hblk(u32 base, int ls, int h, u32 side, u32 pol) { return near_(base + (u32)(ls * NH + h) * 16384u, side, pol); }
DEV f32x2 ln_stats(const f32x2 (&x)[5]) {
    const float c = f_(uu(u_(x[0].x)));
    f32x2 d[5];
#pragma unroll
    for (int i = 0; i < 5; ++i) d[i] = x[i] - c;
    const f32x2 s2 = (d[0] + d[1]) + (d[2] + d[3]) + d[4], q = (d[0] * d[0] + d[1] * d[1]) + (d[2] * d[2] + (d[3] * d[3] + d[4] * d[4]));
    const float dm = wave_sum(s2.x + s2.y) * (1.0f / DM), mean = c + dm;
    const float var = fmaxf(wave_sum(q.x + q.y) * (1.0f / DM) - dm * dm, 0.f);
    const float sd = rbf(__builtin_amdgcn_sqrtf(rbf(rbf(var) + 1e-5f)));
    return (f32x2){rbf(mean), __builtin_amdgcn_rcpf(sd)};
}
DEV void store_h(rsrc_t rs, u32 tile, int row, const u32 (&hp)[5]) {
    const u32 lane = threadIdx.x & 63;
    const u32 sw = (u32)(row & 15) * 16u, o8 = (lane >> 5) * 16384u + (((lane & 31u) * 8u) ^ sw), hb = tile + (u32)(row & 31) * 256u;
    st8s(rs, o8, hb, (v2u){hp[0], hp[1]});
    st8s(rs, o8, hb + 32768, (v2u){hp[2], hp[3]});
    st4s(rs, (lane * 4) ^ sw, hb + 65536, hp[4]);
}

#define DMA16(ldsb, vo, so) asm volatile("s_mov_b32 m0, %0\n\ts_nop 0\n\tbuffer_load_dwordx4 %1, %2, %3 offen sc1 lds" :: "s"(ldsb), "v"(vo), "s"(srd), "s"(so) : "memory")
#define DMA16I(ldsb, lo, vo, so, go) do { u32 so_; asm volatile("s_add_u32 m0, %1, %5\n\ts_add_u32 %0, %4, %6\n\tbuffer_load_dwordx4 %2, %3, %0 offen sc1 lds" \
                         : "=&s"(so_) : "s"(ldsb), "v"(vo), "s"(srd), "s"(so), "i"(lo), "i"(go) : "memory", "scc"); } while (0)
#define DMA32(ldsb, vo, so) asm volatile("s_mov_b32 m0, %0\n\ts_nop 0\n\tbuffer_load_dwordx4 %1, %2, %3 offen sc1 lds\n\tbuffer_load_dwordx4 %1, %2, %3 offen offset:1024 sc1 lds" \
                         :: "s"(ldsb), "v"(vo), "s"(srd), "s"(so) : "memory")
#define INPOL "nt"
#define XPOL ""
#define INAUX 2
#define DMANT(sr, ldsb, vo, so) asm volatile("s_mov_b32 m0, %0\n\ts_nop 0\n\tbuffer_load_dwordx4 %1, %2, %3 offen " INPOL " lds" :: "s"(ldsb), "v"(vo), "s"(sr), "s"(so) : "memory")
#define DMAIN(sr, ldsb, vo, so) asm volatile("s_mov_b32 m0, %0\n\ts_nop 0\n\tbuffer_load_dwordx4 %1, %2, %3 offen lds" :: "s"(ldsb), "v"(vo), "s"(sr), "s"(so) : "memory")
DEV const u16* sel(bool c, const u16* a, const u16* b) { PINS(a); PINS(b); return c ? a : b; }
DEV v4u mk_srd(const void* q, u32 bytes) { return (v4u){uu((u32)(size_t)q), uu((u32)((size_t)q >> 32) & 0xffffu), bytes, 0x00020000u}; }
#define MK_SRD() const v4u srd = mk_srd(p.ws, W_END)

constexpr int CV_PAD = 40;
constexpr int CV_PL = 4416;
constexpr int CV_F = 8 * CV_PL;
constexpr int CV_KB = 80;
constexpr int CV_KV = 65 * CV_KB;
constexpr int CV_CH = CV_F + CV_KV;
static_assert(CV_CH % 16 == 0 && 4 * CV_CH <= LDS_BYTES, "conv lds");

struct Filt { v4u a[5], b[5]; };
DEV int chan(int grp, int w) { return 16 * (grp >> 2) + (grp & 3) + 4 * w; }
template <int N = 5, int AUX = INAUX> DEV void filt_load(const Params& p, int grp, Filt& f) {
    const int tid = threadIdx.x, lane = tid & 63, d = chan(grp, tid >> 6);
    const rsrc_t rf = mk_rsrc(p.ssmk, ACT);
#pragma unroll
    for (int i = 0; i < N; ++i) {
        const u32 s0 = (u32)(8 * (lane + 64 * i) - CV_PAD);
        f.a[i] = __builtin_amdgcn_raw_buffer_load_b128(rf, s0 < SEQ ? (u32)(d * SEQ + s0) * 2u : OOB, 0, AUX);
        f.b[i] = __builtin_amdgcn_raw_buffer_load_b128(rf, s0 + 8 < SEQ ? (u32)(d * SEQ + s0 + 8) * 2u : OOB, 0, AUX);
    }
}
constexpr int KT_BYTES = 18 * 1024, KT_X = 64 * 144, NKV = 44, KT_DUMP = 8 * KT_BYTES + KT_X;
DEV void kv_fetch(const Params& p, int tile, u32 dst) {
    const int lane = threadIdx.x & 63, w = uni(threadIdx.x >> 6);
    const v4u sk = mk_srd(p.bk, ACT), sv = mk_srd(p.bv, ACT), ss = mk_srd(sel(w == 0, p.bk, p.shift), w == 0 ? ACT : DM * 8);
    const u32 vo = tile < 320 ? (u32)((lane >> 3) * DM + (tile % 10) * 64 + (lane & 7) * 8) * 2u : OOB, r0 = uu((u32)((tile / 10) * 64 + 8 * w) * (DM * 2)), d = uu(dst + w * 1024);
    DMANT(sk, d + 1024, vo, r0); DMANT(sk, d + 5120, vo, r0 + 32 * DM * 2); DMANT(sv, d + 9216, vo, r0); DMANT(sv, d + 13312, vo, r0 + 32 * DM * 2);
    DMAIN(ss, uu(w == 0 ? dst : w == 1 ? dst + 17408 : KT_DUMP), w == 0 ? (tile >= 10 ? vo : OOB) : w == 1 && lane < 32 ? (u32)lane * 16u : OOB,
          uu(w == 0 ? (tile >= 10 ? r0 - 8 * DM * 2 : 0u) : (u32)(tile % 10) * 512u));
}
DEV void kv_tile(const Params& p, rsrc_t rs, lptr src, lptr xt, int tile) {
    const int tid = threadIdx.x;
    const v4u sv = lds_r16(src + 17408 + (tid & 31) * 16);
    lptr kp = src + ((tid >> 5) * 8 + 5) * 128 + (tid & 31) * 4;
    f32x2 kf[11];
#pragma unroll
    for (int i = 0; i < 11; ++i) kf[i] = up2(*(LAS u32*)(kp + i * 128));
    const f32x2 sh[4] = {{lo_f(sv.x), lo_f(sv.z)}, {hi_f(sv.x), hi_f(sv.z)}, {lo_f(sv.y), lo_f(sv.w)}, {hi_f(sv.y), hi_f(sv.w)}};
    u32 kv[8];
#pragma unroll
    for (int i = 0; i < 8; ++i) {
        const f32x2 acc = (kf[i] * sh[3] + kf[i + 1] * sh[2]) + (kf[i + 2] * sh[1] + kf[i + 3] * sh[0]);
        kv[i] = pk2(acc * up2(*(LAS u32*)(kp + 9216 + (i - 5) * 128)));
    }
    lds_w16(xt + (2 * (tid & 31)) * 144 + (tid >> 5) * 16, (v4u){__builtin_amdgcn_perm(kv[6], kv[7], 0x05040100u), __builtin_amdgcn_perm(kv[4], kv[5], 0x05040100u),
                                                             __builtin_amdgcn_perm(kv[2], kv[3], 0x05040100u), __builtin_amdgcn_perm(kv[0], kv[1], 0x05040100u)});
    lds_w16(xt + (2 * (tid & 31) + 1) * 144 + (tid >> 5) * 16, (v4u){__builtin_amdgcn_perm(kv[6], kv[7], 0x07060302u), __builtin_amdgcn_perm(kv[4], kv[5], 0x07060302u),
                                                                 __builtin_amdgcn_perm(kv[2], kv[3], 0x07060302u), __builtin_amdgcn_perm(kv[0], kv[1], 0x07060302u)});
    BAR();
#pragma unroll
    for (int i = 0; i < 2; ++i) {
        const int ch = i * 32 + (tid >> 3);
        st16(rs, W_KVT + (u32)(((tile % 10) * 64 + ch) * SEQ + (tile / 10) * 64 + (tid & 7) * 8) * 2u, lds_r16(xt + ch * 144 + (tid & 7) * 16));
    }
}
template <int MODE> DEV void conv_job(const Params& p, rsrc_t rs, lptr lds, int grp, Filt& f, int next) {
    const int tid = threadIdx.x, lane = tid & 63, w = uni(tid >> 6), bq = lane & 31, hh = lane >> 5;
    lptr F = lds + w * CV_CH;
    lptr fa = F + (bq & 7) * CV_PL + ((bq >> 3) - hh + 4) * 16;
    lptr kb = F + CV_F + CV_KB + hh * 16, kh = kb + (bq + 32) * CV_KB;
    bool dead = false;
    v4u kvr[4];
    const u32 D = p.ssmD[chan(grp, w)];
    auto kv_load = [&]() {
        if (MODE == 0) boot_wait(p, rs, lane < NKV ? foff(F_KV + lane) : OOB, 2u, dead);
#pragma unroll
        for (int i = 0; i < (MODE == 1 ? 2 : 4); ++i) kvr[i] = ld16s(rs, (u32)lane * 16u, W_KVT + (u32)(chan(grp, w) * SEQ + i * 512) * 2u);
    };
    auto windows = [&](int i) {
        if (i < 4 || lane < 16) {
            const u32 e[8] = {f.a[i].x, f.a[i].y, f.a[i].z, f.a[i].w, f.b[i].x, f.b[i].y, f.b[i].z, f.b[i].w};
            u32 o[7];
#pragma unroll
            for (int q = 0; q < 7; ++q) o[q] = __builtin_amdgcn_alignbit(e[q + 1], e[q], 16);
#pragma unroll
            for (int q = 0; q < 4; ++q) {
                lds_w16(F + (2 * q) * CV_PL + (64 * i + lane) * 16, (v4u){o[q], o[q + 1], o[q + 2], o[q + 3]});
                lds_w16(F + (2 * q + 1) * CV_PL + (64 * i + lane) * 16, (v4u){e[q + 1], e[q + 2], e[q + 3], e[q + 4]});
            }
        }
    };
    lptr yw = lds + (lane >> 4) * CV_CH + 3072 + (lane & 14) * 64 + w * 32 + (lane & 1) * 16, yr = F + 3072 + lane * 16;
    const u32 yo = W_Y + (u32)((((w & 1) * 8 + (lane >> 3)) * 8 + (w >> 1)) * DM + (grp & 3) * 160 + 4 * (grp >> 2)) * 32u + (u32)(lane & 7) * 16u;
    auto put = [&](const f32x16& ac, int q) {
        const int r = 4 * (q & 3);
        const auto x = __builtin_amdgcn_permlane32_swap(pk(ac[r], ac[r + 1]), pk(ac[r + 4], ac[r + 5]), false, false);
        const auto y = __builtin_amdgcn_permlane32_swap(pk(ac[r + 2], ac[r + 3]), pk(ac[r + 6], ac[r + 7]), false, false);
        lds_w16(yw + (q >> 1) * CV_PL, (v4u){x[0], y[0], x[1], y[1]});
    };
    if (MODE != 2) EV(8);
    if (MODE != 0) kv_load();
    if (lane < CV_KB / 16) lds_w16(F + CV_F + lane * 16, (v4u){0u, 0u, 0u, 0u});
    windows(0);
    if (MODE != 1) { windows(1); windows(2); windows(4); }
    if (MODE == 0) kv_load();
    if (MODE == 2) windows(3);
    if (MODE != 2) EV(9);
#pragma unroll
    for (int i = 0; i < (MODE == 1 ? 2 : 4); ++i) lds_w16(F + CV_F + (16 * i + (lane >> 2) + 1) * CV_KB + (lane & 3) * 16, kvr[i]);
    v4u dg[2];
#pragma unroll
    for (int ks = 0; ks < 2; ++ks)
#pragma unroll
        for (int q = 0; q < 4; ++q) { const int j = 16 * ks + 8 * hh + 7 - bq; dg[ks][q] = j == 2 * q ? D : j == 2 * q + 1 ? D << 16 : 0u; }
    f32x16 acc0 = {}, acc1 = {};
    auto first = [&]() { asm volatile("s_waitcnt vmcnt(0)\n\ts_barrier" ::: "memory"); st4s(rs, tid == 0 ? foff(F_CV + grp) : OOB, 0, 1u); EV(12); };
    if (MODE != 2) {
        EV(10);
        acc0 = mfma32(lds_r16(kb + bq * CV_KB), dg[0], acc0); acc0 = mfma32(lds_r16(kb + bq * CV_KB + 32), dg[1], acc0);
#pragma unroll
        for (int m = 0; m < 8; ++m) {
            lptr kl = kb + max(bq - m, -1) * CV_KB;
            acc0 = mfma32(lds_r16(kl), lds_r16(fa + m * 64), acc0); acc0 = mfma32(lds_r16(kl + 32), lds_r16(fa + m * 64 - 32), acc0);
        }
        put(acc0, 0);
        BAR();
        st16(rs, w < 2 ? yo : OOB, lds_r16(yr));
        EV(11);
    }
    if (MODE == 1) {
        windows(1); windows(2);
        first();
        v4u a[2][2], l[2][2];
#pragma unroll
        for (int u = 0; u < 2; ++u) { lptr kl = kb + max(bq - 8 - u, -1) * CV_KB; a[u][0] = lds_r16(fa + (8 + u) * 64); l[u][0] = lds_r16(kl); a[u][1] = lds_r16(fa + (8 + u) * 64 - 32); l[u][1] = lds_r16(kl + 32); }
#pragma unroll 1
        for (int m = 8; m < 32; m += 2) {
#pragma unroll
            for (int u = 0; u < 2; ++u) {
                lptr kl = kb + max(bq - m - u - 2, -1) * CV_KB;
                acc0 = mfma32(l[u][0], a[u][0], acc0); a[u][0] = lds_r16(fa + (m + u + 2) * 64); l[u][0] = lds_r16(kl); SCHED();
                acc0 = mfma32(l[u][1], a[u][1], acc0); a[u][1] = lds_r16(fa + (m + u + 2) * 64 - 32); l[u][1] = lds_r16(kl + 32); SCHED();
            }
        }
    } else {
        if (MODE == 0) windows(3); else filt_load<5, 0>(p, next, f);
        v4u a0 = lds_r16(fa), a1 = lds_r16(fa - 32), c0 = lds_r16(fa + 2048), c1 = lds_r16(fa + 2048 - 32);
        v4u h0 = lds_r16(kh), h1 = lds_r16(kh + 32), l0 = lds_r16(kb + bq * CV_KB), l1 = lds_r16(kb + bq * CV_KB + 32);
        acc1 = mfma32(h0, dg[0], acc1); acc1 = mfma32(h1, dg[1], acc1);
#pragma unroll 2
        for (int m = 0; m < (MODE == 0 ? 8 : 32); ++m) {
            lptr kl = kb + max(bq - m - 1, -1) * CV_KB;
            if (MODE == 0 && m == 4) first();
            acc1 = mfma32(h0, a0, acc1); h0 = lds_r16(kh - (m + 1) * CV_KB); a0 = lds_r16(fa + (m + 1) * 64); SCHED();
            acc1 = mfma32(l0, c0, acc1); c0 = lds_r16(fa + (m + 1) * 64 + 2048); l0 = lds_r16(kl); SCHED();
            acc1 = mfma32(h1, a1, acc1); h1 = lds_r16(kh - (m + 1) * CV_KB + 32); a1 = lds_r16(fa + (m + 1) * 64 - 32); SCHED();
            acc1 = mfma32(l1, c1, acc1); c1 = lds_r16(fa + (m + 1) * 64 + 2048 - 32); l1 = lds_r16(kl + 32); SCHED();
        }
        if (MODE == 0) {
#pragma unroll 2
            for (int m = 8; m < 32; ++m) {
                lptr kl = kb + max(bq - m - 1, -1) * CV_KB;
                acc1 = mfma32(h0, a0, acc1); h0 = lds_r16(kh - (m + 1) * CV_KB); SCHED();
                acc0 = mfma32(l0, a0, acc0); a0 = lds_r16(fa + (m + 1) * 64); SCHED();
                acc1 = mfma32(l0, c0, acc1); c0 = lds_r16(fa + (m + 1) * 64 + 2048); l0 = lds_r16(kl); SCHED();
                acc1 = mfma32(h1, a1, acc1); h1 = lds_r16(kh - (m + 1) * CV_KB + 32); SCHED();
                acc0 = mfma32(l1, a1, acc0); a1 = lds_r16(fa + (m + 1) * 64 - 32); SCHED();
                acc1 = mfma32(l1, c1, acc1); c1 = lds_r16(fa + (m + 1) * 64 + 2048 - 32); l1 = lds_r16(kl + 32); SCHED();
            }
        }
        BAR();
    }
    if (MODE != 2) { EV(13); put(acc0, 0); put(acc0, 2); }
    if (MODE != 1) { put(acc1, 4); put(acc1, 6); }
    BAR();
#pragma unroll
    for (int q = (MODE == 2 ? 2 : 0); q < (MODE == 1 ? 2 : 4); ++q) st16(rs, q == 0 && w < 2 ? OOB : yo + (u32)q * (2 * DM * 32), lds_r16(yr + q * CV_PL));
    BAR();
    if (MODE != 2) EV(14);
}

#define NPROBE 6
DEV int probe(rsrc_t rs, int c) {
    const int lane = threadIdx.x & 63;
    const u32 k = (u32)(c & 15) < NREG ? (u32)(c & 15) : 0u;
    const u32 off0 = k * (16 * MB) + 2 * MB - 65536 + ((u32)(c & 15) >= NREG ? (u32)(c & 3) + 1u : 0u) * 256 + (u32)(c >> 4) * 128;
    st4s(rs, lane == 0 ? off0 : OOB, 0, 0u); DRAIN();
    int d0 = 1 << 16, d1 = 1 << 16;
#pragma unroll 1
    for (int q = 0; q < NPROBE; ++q) {
        const u32 o = lane == 0 ? off0 + (u32)(q & 1) * 8192 + (u32)(q >> 1) * 18432 : OOB;
        const unsigned long long t0 = __builtin_amdgcn_s_memtime();
        st4s(rs, o, 0, 0u); DRAIN();
        const int dt = (int)(__builtin_amdgcn_s_memtime() - t0);
        if (q & 1) d1 = min(d1, dt); else d0 = min(d0, dt);
    }
    const u32 xcc = (u32)__builtin_amdgcn_s_getreg(20 | (3 << 11)) & 0xfu;
    return (d0 - d1) * ((xcc & 4u) ? -1 : 1);
}
DEV void pre_rings(const Params& p, rsrc_t rs, int j) {
    const u32 gt = (u32)(j * 256 + threadIdx.x) * 16u, n = ((gt >> 14) << 9) + ((gt & 8191u) >> 4);
    const v4u ps = {PSENT, PSENT, PSENT, PSENT};
    v4u m = ps;
    if (n < 640u) m = *(const v4u*)(p.mem0 + (2 * ((n >> 2) & 3) + (n & 1)) * DM + (n >> 4) * 16 + (n & 2) * 4);
#pragma unroll
    for (u32 i = 0; i < 8; ++i) st16(rs, W_ORING + gt + i * 131072u, ps);
#pragma unroll
    for (u32 i = 0; i < 2; ++i) st16(rs, gt < 16384u ? W_MRING + i * (MB / 4) + 131072u + gt : OOB, ps);
    if (n < 640u) {
        m = (v4u){__builtin_amdgcn_perm(m.z, m.x, 0x05040100u), __builtin_amdgcn_perm(m.z, m.x, 0x07060302u), __builtin_amdgcn_perm(m.w, m.y, 0x05040100u), __builtin_amdgcn_perm(m.w, m.y, 0x07060302u)};
#pragma unroll
        for (int i = 0; i < 4; ++i) m[i] = m[i] == PSENT ? 0x7FC17FC1u : m[i];
    }
    st16(rs, W_MRING + gt, m);
    st16(rs, W_MRING + MB / 4 + gt, m);
    static_assert(2 * (F_N - F_QKV) == 32 * 36, "clear");
    st4s(rs, threadIdx.x < 36 ? foff(F_QKV) + (u32)(j * 36 + threadIdx.x) * 128u : OOB, 0, 0u);
}

#define M32A(first, d, a, b) do { if (first) asm volatile("v_mfma_f32_32x32x16_bf16 %0, %1, %2, 0" : "=&v"(d) : "a"(a), "v"(b)); \
                                  else asm volatile("v_mfma_f32_32x32x16_bf16 %0, %1, %2, %0" : "+v"(d) : "a"(a), "v"(b)); } while (0)
#define M32B(first, d, a, b) do { if (first) asm volatile("v_mfma_f32_32x32x16_bf16 %0, %1, %2, 0" : "=&v"(d) : "v"(a), "a"(b)); \
                                  else asm volatile("v_mfma_f32_32x32x16_bf16 %0, %1, %2, %0" : "+v"(d) : "v"(a), "a"(b)); } while (0)
#define VCVT(d, a, b) asm volatile("v_cvt_pk_bf16_f32 %0, %1, %2" : "=v"(d) : "v"(a), "v"(b))
#define LD4A(d, vo, so) asm volatile("buffer_load_dword %0, %1, %2, %3 offen sc1" : "=v"(d) : "v"(vo), "s"(srd), "s"(so) : "memory")
DEV u32 wst_src(int lane, int ld) { return (u32)((lane >> 4) * ld + 8 * ((lane & 15) ^ ((lane >> 4) << 2))) * 2u; }
DEV u32 wst_rd(int lane, int w) { const int q4 = (lane >> 2) & 3; return (u32)(q4 * 256 + ((w ^ q4) * 64) + ((lane >> 4) & 1) * 32 + (lane & 3) * 8); }

constexpr int QK_A = 65536;
#define QLEAD 4
#define QFIRST 3
static_assert(QFIRST < QLEAD, "a round of the lead carries the flag of the convolution");
#define QW0 10
#define QW1 3
#define QSAMP 72
DEV void role_qkv(const Params& p, lptr lds, int c) {
    const int grp = 64 + c;
    const int tid = threadIdx.x, lane = tid & 63, w = uni(tid >> 6), hh = lane >> 5, r32 = lane & 31;
    const int m = c >> 5, h = c & 31;
    const rsrc_t rs = mk_rsrc(p.ws, W_END);
    v4u wt[40];
    Filt flt;
    bool dead = false;
    u32 pol, h2flag, fo, ol, ar[8];
    const u32 ds = uu(w * 1024), dx = ds + QK_A;
#define XRD(ks) lds_r16(lds + ar[(ks) & 7] + ((ks) >> 3) * 8192)
    const u32 t16 = (u32)tid * 16u, lnf = tid < 96 ? foff(F_LN + tid) : OOB;
    MK_SRD();
#define QDMA(i, dst, vo, so) DMA16I(dst, (i) * 4096, vo, so, ((i) >> 1) * 16384 + ((i) & 1) * 4096)
    {
        const v4u sw = mk_srd(sel(m == 0, p.Wq, sel(m == 1, p.Wk, p.Wv)), DM * NQ * 2), sx = mk_srd(p.x, ACT);
        const u32 vo = wst_src(lane, NQ) + (u32)h * (DH * 2), x8 = (u32)lane * 8u, x4 = (u32)lane * 4u;
        v2u xa[6], xb[6]; u32 xc[6];
#pragma unroll
        for (int i = 0; i < 6; ++i)
            asm volatile("buffer_load_dwordx2 %0, %3, %5, %6 offen " XPOL "\n\tbuffer_load_dwordx2 %1, %3, %5, %6 offen offset:512 " XPOL "\n\tbuffer_load_dword %2, %4, %5, %6 offen offset:1024 " XPOL
                         : "=&v"(xa[i]), "=&v"(xb[i]), "=&v"(xc[i]) : "v"(x8), "v"(x4), "s"(sx), "s"(uu((4 * c + w + 384 * i) * (DM * 2))) : "memory");
        EV(1);
        auto wfill = [&](int i0, int n) {
#pragma unroll 1
            for (int i = i0; i < i0 + n; ++i) DMAIN(sw, uu((4 * i + w) * 1024), vo, uu((16 * i + 4 * w) * (NQ * 2)));
        };
        wfill(0, QW0);
        asm volatile("s_waitcnt vmcnt(%0) ; HZCLR" :: "i"(QW0) : "memory");
        EV(2);
#pragma unroll
        for (int i = 0; i < 6; ++i) {
            wfill(QW0 + QW1 * i, QW1);
            if (i == 2) { VMW(2 * QW1 + 3); BAR(); boot_set(p, rs, tid == 0 ? foff(F_LN + c) : OOB, 1u); }
            asm volatile("" : "+v"(xa[i]), "+v"(xb[i]), "+v"(xc[i]));
            const int row = 4 * c + w + 384 * i;
            if (i < 5 || row < SEQ) {
                const f32x2 x[5] = {up2(xa[i].x), up2(xa[i].y), up2(xb[i].x), up2(xb[i].y), up2(xc[i])};
                const f32x2 st = ln_stats(x);
                u32 hp[5];
#pragma unroll
                for (int q = 0; q < 5; ++q) hp[q] = pk2(up2(pk2(x[q] - st.x)) * st.y);
                store_h(rs, W_H1 + (u32)(row >> 5) * H_TILE, row, hp);
            }
        }
        EV(3);
        constexpr int K0 = QW0 + 6 * QW1;
        static_assert(K0 * 4096 >= QK_A + 40960, "the first tile's place is free");
        v4u fv, praw;
        asm volatile("buffer_load_dwordx4 %0, %2, %4, 0 offen sc1\n\tbuffer_load_dwordx4 %1, %3, %4, 0 offen sc1" : "=&v"(praw), "=&v"(fv) : "v"(pol_off()), "v"(lnf), "s"(srd) : "memory");
        wfill(K0, 40 - K0);
        asm volatile("s_waitcnt vmcnt(%2) ; HZCLR\n\ts_barrier" : "+v"(praw), "+v"(fv) : "i"(40 - K0) : "memory");
        boot_set(p, rs, tid == 0 ? foff(F_LN + c) : OOB, 2u);
        EV(4);
        lptr tb = lds + hh * 2048 + wst_rd(lane, w);
        SFOR(k, 40)
            if constexpr (k == K0) {
                if (!boot_ok(p, lnf, fv, 1u)) boot_wait(p, rs, lnf, 1u, dead);
                if (!boot_ok(p, pol_off(), praw, 0u)) praw.w = boot_wait(p, rs, pol_off(), 0u, dead);
                BAR();
                EV(5);
                const u32 so = W_H1;
                SFOR(i, 10) QDMA(i, dx, t16, so); SEND
                pol = pol_of(praw.w);
                h2flag = tid < 8 ? near_(foff(F_H2 + lane), 1u, pol) : OOB;
                ol = (m == 2 ? (u32)(2 * w + (r32 >> 4)) * 1024u : (u32)((r32 >> 4) * 4 + w) * 1024u) + (u32)(hh * 16 + (r32 & 15)) * 16u;
                fo = near_(foff(F_QKV + m * 64 + h), 0u, pol);
#pragma unroll
                for (int j = 0; j < 8; ++j) { ar[j] = (u32)(QK_A + r32 * 256 + ((2 * j + hh) ^ (r32 & 15)) * 16); PIN(ar[j]); }
                PIN(h2flag); PIN(ol); PINS(fo); PINS(pol);
                VMW(10); BAR();
            }
            lptr q = tb + (k >> 3) * 32768 + (k & 7) * 4096; wt[k] = tr2(q, q + 1024); asm volatile("" : "+a"(wt[k]));
        SEND
    }
    EV(6);
    int n0 = -1, n1 = -QLEAD;
    f32x16 a0 = {};
    const u32 ksc = m == 1 ? 0x3e0293eeu : 0x3f800000u;
    const u32 bfo = tid < 96 ? lnf : tid - 128u < NKV ? foff(F_KV - 128 + tid) : OOB;
    v4u brk = {};
    auto run = [&](auto isv) __attribute__((always_inline)) {
      constexpr bool V = decltype(isv)::value;
      v4u x[8];
      u32 fv = 0, fo0 = tid == 192 ? fo : n1 > -QLEAD && tid == 194 ? foff(F_CV + grp) : OOB, fo1 = tid == 192 ? fo : tid == 193 ? fo + 32 * 128 : OOB;
      PIN(fo0); PIN(fo1);
#pragma unroll
      for (int ks = 20; ks < 25; ++ks) x[ks & 7] = XRD(ks);
#define QS(k) do {} while (0)
#define QPK1(d, a, a_, q_) do { if constexpr (V) VCVT(d[q_], a[4 * (a_) + 8 * ((q_) >> 1) + 2 * ((q_) & 1)], a[4 * (a_) + 8 * ((q_) >> 1) + 2 * ((q_) & 1) + 1]); else VCVT(d[q_], a[4 * (a_) + (q_)], a[4 * (a_) + (q_) + 8]); } while (0)
#define QSC(a, i) asm volatile("v_mul_f32 %0, %2, %0\n\tv_mul_f32 %1, %2, %1" : "+v"(a[i]), "+v"(a[(i) + 8]) : "s"(ksc))
#define QPK(d, a, a_, q_) do { if constexpr (!V) { QSC(a, 4 * (a_) + (q_)); QSC(a, 4 * (a_) + (q_) + 1); } QPK1(d, a, a_, q_); QPK1(d, a, a_, (q_) + 1); } while (0)
#pragma unroll 1
      for (const int end = n1 == -QLEAD ? QFIRST - QLEAD : NSEG; n1 < end; ++n1) {
        const int cur = n1 < 0 ? n0 + 1 : 64 | n1, nxt = n1 < 0 ? n0 + 2 : n0 + 1;
        u32 xs = n1 < 0 ? W_H1 + (u32)cur * H_TILE : near_(W_H2 + (u32)n1 * H_TILE, 1u, pol);
        PINS(xs);
        QS(0);
        if (w == 0 && n1 >= 0 && ((u32)__builtin_amdgcn_ballot_w64(fv <= (u32)n1) & 255u) != 0u) wait_flags(rs, h2flag, (u32)n1 + 1, dead);
        BAR();
        QS(1);
        f32x16 a1;
        u32 ss, sv, hb, oa, qfo, qfv;
        v4u pp, pq;
        SCHED();
        auto slot = [&](auto zc) __attribute__((always_inline)) {
            constexpr int z = decltype(zc)::value, ks = (z + 20) % 40;
            if constexpr (z < 20) { if constexpr (V) M32B(false, a0, x[ks & 7], wt[ks]); else M32A(false, a0, wt[ks], x[ks & 7]); }
            else if constexpr (z < 60) { if constexpr (V) M32B(z == 20, a1, x[ks & 7], wt[ks]); else M32A(z == 20, a1, wt[ks], x[ks & 7]); }
            else if constexpr (z == 60) {
                if constexpr (V) asm volatile("v_mfma_f32_32x32x16_bf16 %0, %1, %2, 0" : "+v"(a0) : "v"(x[ks & 7]), "a"(wt[ks]));
                else asm volatile("v_mfma_f32_32x32x16_bf16 %0, %1, %2, 0" : "+v"(a0) : "a"(wt[ks]), "v"(x[ks & 7]));
            } else { if constexpr (V) M32B(false, a0, x[ks & 7], wt[ks]); else M32A(false, a0, wt[ks], x[ks & 7]); }
            if constexpr (z == 18) { QS(2); asm volatile("s_waitcnt vmcnt(6) lgkmcnt(0)\n\ts_barrier" ::: "memory"); QS(3); }
            if constexpr (z < 15 || z > 20) x[(ks + 5) & 7] = XRD((ks + 5) % 40);
            if constexpr (z >= 18 && z <= 20) { x[2 * (z - 18)] = XRD(2 * (z - 18)); x[2 * (z - 18) + 1] = XRD(2 * (z - 18) + 1); }
            SCHED();
            if constexpr (z < 4) QDMA(z, dx, t16, xs);
            if constexpr (z >= 5 && z < 17 && (z & 1)) QDMA((z + 3) >> 1, dx, t16, xs);
            if constexpr ((z >= 7 && z < 15) || (z >= 47 && z < 55)) asm volatile("v_xor_b32 %0, 0x10000, %0" : "+v"(ar[(ks + 5) & 7]));
            if constexpr (z == 16) { ss = W_H1 + (u32)nxt * H_TILE; PINS(ss); }
            if constexpr (z == 17) { sv = t16 | (nxt >= NSEG ? OOB : 0u); PIN(sv); }
            if constexpr (z >= 21 && z < 41 && (z & 1)) QDMA((z - 21) >> 1, ds, sv, ss);
            if constexpr (z == 22 || z == 24) QPK(pp, a0, 0, z - 22);
            if constexpr (z == 26 || z == 28) QPK(pq, a0, 1, z - 26);
            if constexpr (z == 30) { QS(4); asm volatile("s_waitcnt vmcnt(5)\n\ts_barrier" ::: "memory"); QS(5); }
            if constexpr (z == 32 || z == 44) { hb = (m == 2 ? W_VO : W_KQ) + (u32)((z == 32 ? n0 : cur) * NH + h) * 16384u; PINS(hb); }
            if constexpr (z == 34 || z == 45) { hb = near_(hb, m == 1 ? 0u : 1u, pol); PINS(hb); }
            if constexpr (z == 36) { hb = (u32)n0 < NSEG ? hb : OOB; PINS(hb); }
            if constexpr (z == 38 || z == 46) { oa = ol + hb; PIN(oa); }
            if constexpr (z == 41) st16(rs, oa, pp);
            if constexpr (z == 43) st16(rs, oa + 512, pq);
            if constexpr (z == 54) { QS(6); asm volatile("s_waitcnt vmcnt(4)\n\ts_barrier" ::: "memory"); QS(7); }
            if constexpr (z == 56) { if (__builtin_expect(n1 == QFIRST - QLEAD - 1, 0)) { filt_load<3, 0>(p, grp, flt); brk = ld16s(rs, bfo, 0); } }
            if constexpr (z == 60) NOPS(5);
            if constexpr (z == 63) st16(rs, oa, pp);
            if constexpr (z == 65) st16(rs, oa + 512, pq);
            if constexpr (z == 61 || z == 62) QPK(pp, a1, 0, 2 * (z - 61));
            if constexpr (z == 63 || z == 64) QPK(pq, a1, 1, 2 * (z - 63));
            if constexpr (z == 67) { qfo = n1 >= 0 ? fo1 : fo0; PIN(qfo); }
            if constexpr (z == 68) { qfv = tid == 193 ? (u32)n1 + 1u : (u32)min(nxt, NSEG); PIN(qfv); }
            if constexpr (z == QSAMP) LD4A(fv, h2flag, 0);
            SCHED();
        };
        if (__builtin_expect(n0 >= 0, 1)) static_for<0, 16>(slot);
        static_for<16, 80>(slot);
        QS(8);
        asm volatile("s_waitcnt vmcnt(1)\n\ts_barrier" ::: "memory");
        st4s(rs, qfo, 0, qfv);
        QS(9);
        asm volatile("s_waitcnt vmcnt(1)" : "+v"(fv) :: "memory");
        n0 = nxt;
      }
      NOPS(7);
    };
#pragma unroll 1
    for (;;) {
        if (m == 2) run(IC<1>{}); else run(IC<0>{});
        if (n1 > 0) break;
        if (!boot_ok(p, bfo, brk, 2u)) boot_wait(p, rs, bfo, 2u, dead);
        BAR();
        conv_job<1>(p, rs, lds, grp, flt, 0);
        const u32 so = uu(W_H1 + (u32)n0 * H_TILE);
        SFOR(i, 6) QDMA(i + 4, ds, t16, so); SEND
        DRAIN(); BAR();
    }
}

constexpr int OU_SEL = 131072;
#define OSAMP 46

DEV void role_out(const Params& p, lptr lds, int L, int kq, int ng) {
    const int tid = threadIdx.x, lane = tid & 63, w = uni(tid >> 6), hh = lane >> 5, r32 = lane & 31;
    const rsrc_t rs = mk_rsrc(p.ws, W_END);
    v4u wo[64];
    Filt flt;
    bool dead = false;
    {
        const v4u sw = mk_srd(p.Wo, DM * NQ * 2);
        const u32 vo = wst_src(lane, DM) + (ng == 4 ? 1024u : (u32)((ng >> 1) * 512 + (ng & 1) * 128 + ((lane ^ (lane >> 2)) & 8) * 16));
        auto fill = [&](int b) {
#pragma unroll 1
            for (int i = 0; i < 8; ++i) DMAIN(sw, uu((b % 5) * 32768 + (4 * i + w) * 1024), vo, uu((kq * 1024 + b * 128 + 16 * i + 4 * w) * (DM * 2)));
        };
#pragma unroll 1
        for (int b = 0; b < 5; ++b) fill(b);
        lptr tb = lds + hh * 1024 + wst_rd(lane, w);
        SFOR(b, 8)
            VMW(b < 3 ? 32 : 66 - 8 * b); BAR();
            lptr q = tb + (b % 5) * 32768;
#pragma unroll
            for (int k = 0; k < 8; ++k) { lptr a = q + (k >> 1) * 8192 + (k & 1) * 2048; wo[8 * b + k] = tr2(a, a + 4096); if (b < 7) asm volatile("" : "+a"(wo[8 * b + k])); else PIN(wo[8 * b + k]); }
            if constexpr (b < 3) { BAR(); fill(b + 5); }
            if constexpr (b == 2) filt_load(p, L == 0 ? 44 + kq * 5 + ng : 1 << 12, flt);
        SEND
    }
    EV(6);
    const u32 pol = get_pol(p, rs, dead);
    if (L == 0) {
        BAR();
        conv_job<0>(p, rs, lds, 44 + kq * 5 + ng, flt, 0);
        DRAIN(); BAR();
        st4s(rs, tid == 0 ? foff(F_CV + 44 + kq * 5 + ng) : OOB, 0, 2u);
    }
    EV(15);
    MK_SRD();
    const u32 ofl = (lane < 8 && w == 0) ? near_(foff(F_O + L * 32 + kq * 8 + lane), 0u, pol) : OOB;
    const u32 l16 = (u32)lane * 16u;
    const u32 fo = (tid == 192) ? near_(foff(F_P + L * 32 + kq * 5 + ng), 1u, pol) : OOB;
    auto osrc = [&](int s) { return uu(hblk(W_VO, L * NSEG + s, kq * 8, 0u, pol) + w * 1024); };
#define ODMA(i, dst, vo, so) DMA16I(dst, (i) * 4096, vo, so, ((i) >> 1) * 16384 + ((i) & 1) * 4096)
    const u32 pvo = (u32)(kq * 5 + ng) * 16384u + (u32)w * 2048u + l16;
    lptr tw = lds + OU_SEL + 256 + r32 * 272 + (ng == 4 ? hh * 32 + w * 4 : (w & 1) * 128 + hh * 16 + (w >> 1) * 8), tr = lds + OU_SEL + 256 + (8 * w + (lane >> 4)) * 272 + (lane & 15) * 16;
    lptr selw = lds + OU_SEL + (w == 0 ? 0 : 64);
    u32 pfo = OOB;
    u32 buf = 0;
    if (w == 0) wait_flags(rs, ofl, 1u, dead);
    BAR();
    for (int s = 0; s < NSEG; ++s) {
        u32 src = osrc(s), dst = uu(buf + w * 1024);
        lptr A = lds + buf + l16;
        asm volatile("" : "+s"(src), "+s"(dst));
#define OS(k) do {} while (0)
        OS(0);
        OS(1);
        SFOR(i, 10) ODMA(i, dst, l16, src); SEND
        OS(2);
        asm volatile("s_waitcnt vmcnt(7)\n\ts_barrier" ::: "memory");
        OS(3);
        LS2(L == 0 && w == 1, 96 + kq * 5 + ng);
        v4u o[64];
#pragma unroll
        for (int k = 0; k < 3; ++k) o[k] = lds_r16(A + k * 1024);
        st4s(rs, pfo, 0, (u32)s);
        u32 fv, pa; int rdy;
        f32x16 acc;
        SCHED();
        SFOR(k, 64)
            if constexpr (k < 56) M32A(k == 0, acc, wo[k], o[k]); else asm volatile("v_mfma_f32_32x32x16_bf16 %0, %1, %2, %0" : "+v"(acc) : "v"(wo[k]), "v"(o[k]));
            if constexpr (k < 61) o[k + 3] = lds_r16(A + (k + 3) * 1024);
            SCHED();
            if constexpr (k < 12 && (k & 1) == 0) ODMA(10 + (k >> 1), dst, l16, src);
            if constexpr (k == 7) OS(4);
            if constexpr (k == 8) OS(5);
            if constexpr (k == 23) OS(6);
            if constexpr (k == 24) OS(7);
            if constexpr (k == 43) OS(8);
            if constexpr (k == 44) OS(9);
            if constexpr (k == 59) OS(10);
            if constexpr (k == 63) OS(11);
            if constexpr (k == 7) asm volatile("s_waitcnt vmcnt(8)\n\ts_barrier" ::: "memory");
            if constexpr (k == 23) asm volatile("s_waitcnt vmcnt(4)\n\ts_barrier" ::: "memory");
            if constexpr (k == 30) { pa = pvo + near_(p_item(s), (u32)L, pol); PIN(pa); }
            if constexpr (k == 43) asm volatile("s_waitcnt vmcnt(0)\n\ts_barrier" ::: "memory");
            if constexpr (k == OSAMP) LD4A(fv, ofl, 0);
            if constexpr (k == 62) {
                asm volatile("s_waitcnt vmcnt(0)" : "+v"(fv) :: "memory");
                *(LAS int*)selw = (int)((u32)__builtin_amdgcn_ballot_w64(fv < (u32)s + 2) & 255u);
            }
            SCHED();
        SEND
        asm volatile("s_nop 11" : "+v"(acc));
        u32 pc[8];
#pragma unroll
        for (int i = 0; i < 8; ++i) pc[i] = pk(acc[2 * i], acc[2 * i + 1]);
        if (ng != 4) {
#pragma unroll
            for (int i = 0; i < 4; ++i) *(LAS v2u*)(tw + i * 32) = (v2u){pc[2 * i], pc[2 * i + 1]};
        } else {
#pragma unroll
            for (int i = 0; i < 8; ++i) *(LAS u32*)(tw + (i >> 1) * 64 + (i & 1) * 16) = pc[i];
        }
        BAR();
        const v4u p0 = lds_r16(tr), p1 = lds_r16(tr + 4 * 272);
        rdy = *(LAS int*)(lds + OU_SEL);
        st16(rs, pa, p0); st16(rs, pa + 1024, p1);
        const bool ready = uni(rdy) == 0;
        SCHED();
        OS(12);
        if (ready) pfo = fo;
        else {
            LD4A(fv, ofl, 0);
            asm volatile("s_waitcnt vmcnt(1)\n\ts_barrier" ::: "memory");
            st4s(rs, fo, 0, (u32)s + 1); pfo = OOB;
            OS(14);
            LS2(L == 0 && w == 1, 160 + kq * 5 + ng);
            asm volatile("s_waitcnt vmcnt(1)" : "+v"(fv) :: "memory");
            if (w == 0 && s + 1 < NSEG && ((u32)__builtin_amdgcn_ballot_w64(fv < (u32)s + 2) & 255u) != 0u) wait_flags(rs, ofl, (u32)s + 2, dead);
            BAR();
        }
        OS(13);
        buf ^= 65536u;
    }
}

template <int L> DEV void role_row(const Params& p, lptr lds, int ri) {
    const int lane = threadIdx.x & 63, w = uni(threadIdx.x >> 6);
    const rsrc_t rs = mk_rsrc(p.ws, W_END);
    bool dead = false;
    const u32 pol = get_pol(p, rs, dead);
    const u32 fp = threadIdx.x < 20 ? near_(foff(F_P + L * 32 + lane), 1u, pol) : OOB, fo = threadIdx.x == 192 && L == 0 ? near_(foff(F_H2 + (ri >> 2)), 1u, pol) : OOB;
    u32 fv = 0;
    MK_SRD();
    auto yfill = [&](int b, int i) { DMA32(uu((b & 1) * 40960 + w * 10240 + i * 2048), (u32)lane * 16u, uu(W_Y + (u32)((ri >> 1) * 8 + b) * 20480u + (w & 1) * 10240 + i * 2048)); };
    auto ywait = [&](u32 v) {
        wait_flags(rs, threadIdx.x < (v == 3u ? 16u : 160u) ? foff(F_CV + (v == 3u ? 160u : 0u) + threadIdx.x) : OOB, v, dead);
    };
    v4u y8[10] = {};
    v2u xa, xb; u32 xc;
    const v4u sx = L == 0 ? mk_srd(p.x, ACT) : mk_srd(p.ws + W_X2, ACT);
    auto xload = [&](int s) {
        asm volatile("buffer_load_dwordx2 %0, %3, %5, %6 offen sc1\n\tbuffer_load_dwordx2 %1, %3, %5, %6 offen offset:512 sc1\n\tbuffer_load_dword %2, %4, %5, %6 offen offset:1024 sc1"
                     : "=&v"(xa), "=&v"(xb), "=&v"(xc) : "v"((u32)lane * 8u), "v"((u32)lane * 4u), "s"(sx), "s"(uu((u32)(s * 32 + ri) * (DM * 2))) : "memory");
        SCHED();
    };
    ywait(1u);
    BAR();
    EV(15);
#pragma unroll 1
    for (int i = 0; i < 5; ++i) yfill(0, i);
    if (L == 1) wait_flags(rs, lane == 0 ? near_(foff(F_H2 + (ri >> 2)), 1u, pol) : OOB, 1u, dead);
    xload(0);
    asm volatile("s_waitcnt vmcnt(0)" : "+v"(xa), "+v"(xb), "+v"(xc) :: "memory");
    BAR();
    for (int s = 0; s < NSEG; ++s) {
        u32 xi[5] = {xa.x, xa.y, xb.x, xb.y, xc};
        u32 pa = near_(p_item(s), (u32)L, pol) + (u32)ri * 256u, pc = pa + 65536u + (u32)(lane >> 4) * 81920u + (u32)(lane & 15) * 16u;
        pa += (u32)(lane >> 5) * 81920u + (u32)((lane >> 4) & 1) * 16384u + (u32)(lane & 15) * 16u;
        u32 yf = s == 1 ? foff(F_CV + min(threadIdx.x, 159u)) : s == 25 ? foff(F_CV + 160 + (lane & 15)) : OOB;
        asm volatile("" : "+v"(xi[0]), "+v"(xi[1]), "+v"(xi[2]), "+v"(xi[3]), "+v"(xi[4]), "+v"(pa), "+v"(pc), "+v"(yf));
#define RS(k) do {} while (0)
        RS(0);
        if (w == 0 && ((u32)__builtin_amdgcn_ballot_w64(fv <= (u32)s) & 0xFFFFFu) != 0u) wait_flags(rs, fp, (u32)s + 1, dead);
        BAR();
        RS(1);
        v4u d[5];
        asm volatile("buffer_load_dwordx4 %0, %5, %7, 0 offen sc1\n\tbuffer_load_dwordx4 %1, %5, %7, %8 offen sc1\n\tbuffer_load_dwordx4 %2, %5, %7, %9 offen sc1\n\t"
                     "buffer_load_dwordx4 %3, %5, %7, %10 offen sc1\n\tbuffer_load_dwordx4 %4, %6, %7, 0 offen sc1"
                     : "=&v"(d[0]), "=&v"(d[1]), "=&v"(d[2]), "=&v"(d[3]), "=&v"(d[4]) : "v"(pa), "v"(pc), "s"(srd), "s"(163840), "s"(32768), "s"(196608) : "memory");
        RS(2);
        LS2(L == 0, 192 + ri);
        SCHED();
        f32x2 y[5];
        {
            if ((s & 7) == 0) {
                lptr ya = lds + ((s >> 3) & 1) * 40960 + (w >> 1) * 20480 + (w & 1) * 16, yc = ya + 4096 + (lane >> 1) * 32 + (lane & 1) * 10240;
#pragma unroll
                for (int i = 0; i < 8; ++i) y8[i] = lds_r16(ya + lane * 32 + (i >> 2) * 2048 + (i & 3) * 5120);
                y8[8] = lds_r16(yc); y8[9] = lds_r16(yc + 5120);
            }
#pragma unroll
            for (int i = 0; i < 10; ++i) {
                y[i >> 1][i & 1] = lo_f(y8[i].x);
                y8[i] = (v4u){__builtin_amdgcn_alignbit(y8[i].y, y8[i].x, 16), __builtin_amdgcn_alignbit(y8[i].z, y8[i].y, 16), __builtin_amdgcn_alignbit(y8[i].w, y8[i].z, 16), y8[i].w >> 16};
                PIN(y8[i]);
            }
            asm volatile("" : "+v"(y[0]), "+v"(y[1]), "+v"(y[2]), "+v"(y[3]), "+v"(y[4]));
        }
        u32 yv;
        {
            xload(s + 1);
            const bool yy = (s & 7) - 2u < 5u && s < NSEG - 8;
            u32 yo = yy ? (u32)lane * 16u : OOB;
            DMA32(uu(yy ? (((s >> 3) + 1) & 1) * 40960 + w * 10240 + ((s & 7) - 2) * 2048 : 81920), yo, uu(W_Y + (u32)((ri >> 1) * 8 + (s >> 3) + 1) * 20480u + (w & 1) * 10240 + ((s & 7) - 2) * 2048));
            LD4A(yv, yf, 0);
            asm volatile("s_waitcnt vmcnt(6)" : "+v"(d[0]), "+v"(d[1]), "+v"(d[2]), "+v"(d[3]), "+v"(d[4]));
        }
        f32x2 o[5];
        {
            f32x2 t[5][2];
#pragma unroll
            for (int i = 0; i < 5; ++i) {
                const auto e = __builtin_amdgcn_permlane32_swap(d[i].x, d[i].z, false, false), f = __builtin_amdgcn_permlane32_swap(d[i].y, d[i].w, false, false);
                t[i][0] = up2(e[0]) + up2(e[1]); t[i][1] = up2(f[0]) + up2(f[1]);
            }
            o[0] = t[0][0] + t[1][0]; o[1] = t[0][1] + t[1][1]; o[2] = t[2][0] + t[3][0]; o[3] = t[2][1] + t[3][1];
            const auto e = __builtin_amdgcn_permlane16_swap(u_(t[4][0].x), u_(t[4][1].x), false, false), f = __builtin_amdgcn_permlane16_swap(u_(t[4][0].y), u_(t[4][1].y), false, false);
            o[4] = (f32x2){f_(e[0]) + f_(e[1]), f_(f[0]) + f_(f[1])};
        }
        f32x2 x1[5];
#pragma unroll
        for (int i = 0; i < 5; ++i) x1[i] = up2(pk2(up2(xi[i]) + up2(pk2(o[i]))));
        asm volatile("" : "+v"(x1[0]), "+v"(x1[1]), "+v"(x1[2]), "+v"(x1[3]), "+v"(x1[4]));
        RS(3);
        const f32x2 st1 = ln_stats(x1);
        u32 xp[5];
#pragma unroll
        for (int i = 0; i < 5; ++i) xp[i] = pk2(x1[i] + ((x1[i] - st1.x) * st1.y) * y[i]);
        const int row = s * 32 + ri;
        RS(4);
        if (L == 0) {
            const u32 ro = (u32)row * (DM * 2);
            f32x2 x2[5];
#pragma unroll
            for (int i = 0; i < 5; ++i) x2[i] = up2(xp[i]);
            st8s(rs, (u32)lane * 8, W_X2 + ro, (v2u){xp[0], xp[1]});
            st8s(rs, (u32)lane * 8 + 512, W_X2 + ro, (v2u){xp[2], xp[3]});
            st4s(rs, (u32)lane * 4 + 1024, W_X2 + ro, xp[4]);
            SCHED();
            const f32x2 st2 = ln_stats(x2);
            u32 hp[5];
#pragma unroll
            for (int i = 0; i < 5; ++i) hp[i] = pk2((x2[i] - st2.x) * st2.y);
            store_h(rs, near_(W_H2 + (u32)s * H_TILE, 1u, pol), row, hp);
        } else {
            u16* o_ = p.out + (size_t)row * DM;
            *(v2u*)(o_ + 4 * lane) = (v2u){xp[0], xp[1]};
            *(v2u*)(o_ + 256 + 4 * lane) = (v2u){xp[2], xp[3]};
            *(u32*)(o_ + 512 + 2 * lane) = xp[4];
        }
        RS(5);
        LD4A(fv, fp, 0);
        if (L == 0) asm volatile("s_waitcnt vmcnt(1)\n\ts_barrier" ::: "memory");
        st4s(rs, fo, 0, (u32)s + 1);
        RS(6);
        VMW(1); SCHED();
        asm volatile("" : "+v"(fv), "+v"(yv), "+v"(xa), "+v"(xb), "+v"(xc));
        if (__builtin_expect(s == 1 || s == 25, 0) && !all_(yv >= (s == 1 ? 2u : 3u))) ywait(s == 1 ? 2u : 3u);
        RS(7);
        if (ri == 0) LST(320 + L * 64 + s, rt32());
        LS2(L == 0, 224 + ri);
    }
}

constexpr float SCALE = 0.08838834764831845f, LOG2E = 1.4426950408889634f;
constexpr int C_K = 0, C_V = 8192, C_QX = 16384, C_MEM = 24576, C_Q = C_MEM + 10240, C_KM = C_Q + 2304, C_VM = C_KM + 2304, C_DUMP = C_VM + 2048,
              C_ZERO = C_DUMP + 1024, C_WL = C_ZERO + 128;
constexpr int NLF = 14;
constexpr int C_MASK = C_WL + 4 * NLF * 2048;
static_assert(C_WL % 16 == 0 && C_MASK + 3072 <= LDS_BYTES, "head lds");
#define CH_VIS 10
#define CH_H0 104
#define CH_W0 82
#define CH_MH 2
#define CH_MW 6
#define CH_REC 8
#define CH_LATE 30
#define CH_SOLO 6
#define CH_KV 12
#define CH_KV0 60
#define CH_M0 6
#define CH_DEC 2u
#define CH_UP 4
#define CH_WX 6
DEV void wait_until(u32 t) {
    unsigned long long c;
    asm volatile("1:\n\ts_memrealtime %0\n\ts_waitcnt lgkmcnt(0)\n\ts_mov_b64 vcc, %0\n\ts_sub_u32 vcc_lo, %1, vcc_lo\n\ts_cmpk_lt_u32 vcc_lo, 400\n\ts_cbranch_scc1 1b" : "=&s"(c) : "s"(uu(t - 1u)) : "vcc", "scc", "memory");
}
#define WAIT_TXT(tmp, t1) "s_memrealtime vcc\n1:\n\ts_waitcnt lgkmcnt(0)\n\ts_sub_u32 " tmp ", " t1 ", vcc_lo\n\ts_memrealtime vcc\n\ts_cmpk_lt_u32 " tmp ", 400\n\ts_cbranch_scc1 1b\n\t"
#define SMF_A(acc, a, b) asm volatile("v_smfmac_f32_16x16x64_bf16 %0, %1, %2, %3" : "+v"(acc) : "v"(a), "a"(b), "v"(idx))
#define SMF_V(acc, a, b) asm volatile("v_smfmac_f32_16x16x64_bf16 %0, %1, %2, %3" : "+v"(acc) : "v"(a), "v"(b), "v"(idx))
#define MF0(d, a, b) asm volatile("v_mfma_f32_16x16x32_bf16 %0, %1, %2, 0" : "=&v"(d) : "v"(a), "v"(b))
#define MF1(d, a, b) asm volatile("v_mfma_f32_16x16x32_bf16 %0, %1, %2, %0" : "+v"(d) : "v"(a), "v"(b))
#define MF0H(d, a, b) asm volatile("v_mfma_f32_16x16x16_bf16 %0, %1, %2, 0" : "=&v"(d) : "v"(a), "v"(b))
#define MF1H(d, a, b) asm volatile("v_mfma_f32_16x16x16_bf16 %0, %1, %2, %0" : "+v"(d) : "v"(a), "v"(b))
DEV v8u tr_frag(lptr p0, lptr p1, lptr p2, lptr p3) { return cat8(tr2(p0, p1), tr2(p2, p3)); }
constexpr int qslot(int ks) { return (ks & 1) && ks < 8 ? ks >> 1 : -1; }
constexpr int kslot(int ks) { return ks < 4 ? 4 + ks : -1; }
constexpr int vslot(int ks) { return ks >= 4 ? 4 + ks : -1; }
DEV u32 ring_map(u32 off, u32 half) { return ((off >> 13) << 14) + (half << 13) + (off & 8191u); }
#define ZRD(x, i) x = __builtin_bit_cast(f32x4, lds_r16(ZP + 16 * (i)))
#define VMAX(d, a, b) asm volatile("v_max_f32 %0, %1, %2" : "=v"(d) : "v"(a), "v"(b))
#define VMAX3(d, a, b, c_) asm volatile("v_max3_f32 %0, %1, %2, %3" : "=v"(d) : "v"(a), "v"(b), "v"(c_))
#define VADD(d, a, b) asm volatile("v_add_f32 %0, %1, %2" : "=v"(d) : "v"(a), "v"(b))
#define VEXP(d, a) asm volatile("v_exp_f32 %0, %1" : "=v"(d) : "v"(a))
#define VFMC(d, a, b) asm volatile("v_fmamk_f32 %0, %1, 0x3e0293ee, %2" : "=v"(d) : "v"(a), "v"(b))
#define VMULC(d, a) asm volatile("v_mul_f32 %0, 0x3e0293ee, %1" : "=v"(d) : "v"(a))
#define SEL(d, a, b) asm volatile("v_cndmask_b32 %0, %1, %2, %3" : "=v"(d) : "v"(a), "v"(b), "s"(himask))
#define QPERM(d, a, q) asm volatile("v_mov_b32_dpp %0, %1 quad_perm:" q " row_mask:0xf bank_mask:0xf" : "=v"(d) : "v"(a))

#define SUMRANGE 0x28000000u
#define HSAMP 71
DEV void role_head(const Params& p, lptr lds, int L, int h) {
    const int tid = threadIdx.x, lane = tid & 63, w = uni(tid >> 6), g = lane >> 4, r = lane & 15;
    const rsrc_t rs = mk_rsrc(p.ws, W_END);
    v8u wq0[10], wq1[10], wk0[10], wk1[10], wv0[10], wv1[10];
    lptr WL = lds + C_WL + w * (NLF * 2048) + lane * 16;
    {
        constexpr int GS = 1056, CS = 48 * GS;
        const v4u sq = mk_srd(p.Wq, DM * NQ * 2), sk = mk_srd(p.Wk, DM * NQ * 2), sv = mk_srd(p.Wv, DM * NQ * 2);
        const u32 vo = (u32)(g * NQ + h * DH + 8 * (r ^ ((g & 2) << 2))) * 2u;
        auto fill = [&](int ch) {
#pragma unroll 1
            for (int i = 0; i < 4; ++i) {
                const u32 dst = uu((ch % 3) * CS + (4 * i + w) * GS), so = uu((ch * 64 + 16 * i + 4 * w) * (NQ * 2));
                DMAIN(sq, dst, vo, so); DMAIN(sk, dst + 16 * GS, vo, so); DMAIN(sv, dst + 32 * GS, vo, so);
            }
        };
        int vote = 0;
        if (L == 1) {
            if (w == 0) vote = probe(rs, h);
            BAR();
            EV(2);
            pre_rings(p, rs, h);
        }
        fill(0); fill(1); fill(2);
        const int q4 = r >> 2;
        lptr tb = lds + (4 * (g >> 1) + q4) * GS + (g & 1) * 512 + ((w * 64 + (r & 3) * 8) ^ ((g & 1) << 7));
        SFOR(ch, 10)
            VMW(ch == 1 || ch == 2 ? 25 : ch < 8 ? 24 : 108 - 12 * ch);
            BAR();
            if constexpr (ch == 0) boot_set(p, rs, tid == 0 && L == 1 ? foff(F_RG + h) : OOB, (u32)((1 << 20) + vote));
            auto frag = [&](int m, int t) { lptr b = tb + (ch % 3) * CS + m * 16 * GS + t * 32; return tr_frag(b, b + 256, b + 8 * GS, b + 8 * GS + 256); };
            wq0[ch] = frag(0, 0); wq1[ch] = frag(0, 1); wk0[ch] = frag(1, 0); wk1[ch] = frag(1, 1); wv0[ch] = frag(2, 0); wv1[ch] = frag(2, 1);
            asm volatile("" : "+a"(wq0[ch]), "+a"(wk0[ch]), "+v"(wv0[ch]), "+v"(wv1[ch]));
            if constexpr (qslot(ch) < 0) asm volatile("" : "+a"(wq1[ch])); else asm volatile("" : "+v"(wq1[ch]));
            if constexpr (kslot(ch) < 0) asm volatile("" : "+a"(wk1[ch])); else asm volatile("" : "+v"(wk1[ch]));
            BAR();
            if constexpr (ch < 7) fill(ch + 3);
        SEND
#pragma unroll
        for (int ch = 0; ch < 10; ++ch) {
            if (qslot(ch) >= 0) { lds_w16(WL + qslot(ch) * 2048, wq1[ch].lo); lds_w16(WL + qslot(ch) * 2048 + 1024, wq1[ch].hi); }
            if (kslot(ch) >= 0) { lds_w16(WL + kslot(ch) * 2048, wk1[ch].lo); lds_w16(WL + kslot(ch) * 2048 + 1024, wk1[ch].hi); }
            if (vslot(ch) >= 0) { lds_w16(WL + vslot(ch) * 2048, wv1[ch].lo); lds_w16(WL + vslot(ch) * 2048 + 1024, wv1[ch].hi); }
        }
        CFENCE();
    }
    const u32 idx = (r & 2) ? 0xEEEEu : 0x4444u;
    const unsigned long long himask = 0xCCCCCCCCCCCCCCCCull;
    bool dead = false;
    EV(6);
    const u32 pol = get_pol(p, rs, dead);
    const u32 pol0 = pol & 1u;
    const u32 l16 = (u32)lane * 16u;
    const u32 mring = W_MRING + (u32)L * (MB / 4) + pol0 * 8192u, hint = mring + 4 * 32768u;
    const u32 fkh = lane < 3 ? near_(foff(F_QKV + lane * 64 + L * 32 + h), 0u, pol) : lane < 6 ? hint + (u32)(lane - 3) * 4u : OOB;
    u32 tm = 0;
    MK_SRD();
    const unsigned long long kmask = __builtin_amdgcn_ballot_w64((hblk(W_KQ, L * NSEG + lane, h, 0u, pol) & 8192u) != 0u),
                             vmask = __builtin_amdgcn_ballot_w64((hblk(W_VO, L * NSEG + lane, h, 1u, pol) & 8192u) != 0u);
    u32 kb = uu(W_KQ + (u32)(L * NSEG * NH + h) * 16384u + w * 2048);
    u32 dk = uu(C_K + w * 2048), dv = dk + (C_V - C_K), dq = dk + (C_QX - C_K), dm = uu(C_MEM + w * 2048), dm2 = uu(w < 2 ? C_MEM + 8192 + w * 1024 : C_DUMP);
    asm volatile("" : "+s"(dk), "+s"(dv), "+s"(dq), "+s"(dm), "+s"(dm2));
    u32 ksrc = kb + ((u32)kmask & 1u) * 8192u, vsrc = kb + (W_VO - W_KQ) + ((u32)vmask & 1u) * 8192u;
    lptr qw = lds + C_Q + (2 * g) * 288 + (32 * w + 2 * r) * 2;
    lptr kw = qw + (C_KM - C_Q);
    lptr qr = lds + C_Q + (2 * (r >> 2) + (r & 1)) * 288 + g * 16;
    lptr LB = lds + l16, VX = lds + C_V + w * 2048 + l16;
    if (tid >= 64 && tid < 70) { const u32 z = tid == 68 ? 0x3F803F80u : tid == 69 ? PSENT : 0u; lds_w16(lds + C_ZERO + (tid - 64) * 16, (v4u){z, z, z, z}); }
    if (w < 3) {
        const u32 ng = u_(-1000000000.0f * LOG2E);
        v4u mk;
#pragma unroll
        for (int i = 0; i < 4; ++i) mk[i] = w == 0 || (w == 1 && 4 * g + i > r) ? ng : 0u;
        lds_w16(lds + C_MASK + w * 1024 + l16, mk);
    }
    u32 zaddr = 0; PIN(zaddr);
    lptr ZP = lds + zaddr + C_ZERO;
#define LF_RD(slot, hf) lf[slot][hf] = lds_r16(WL + (slot) * 2048 + (hf) * 1024)
    BAR();
    const u32 ofl = tid == 192 ? near_(foff(F_O + L * 32 + h), 0u, pol) : tid == 193 && (h & 15) == 0 ? hint + 128u + (u32)h * 8u : OOB;
    u32 oso = (g & 1) ? OOB : W_ORING + (u32)L * (MB / 2) + ring_map((u32)((r >> 3) * 32768 + h * 1024 + w * 256 + (g >> 1) * 128 + (r & 7) * 16), pol0);
    u32 missed = 0, dirty = 1, rep = PSENT;
    asm volatile("" : "+v"(oso), "+v"(qw), "+v"(kw), "+v"(qr), "+v"(VX));
    asm volatile("" : "+s"(ksrc), "+s"(vsrc));
    wait_flags(rs, lane < 3 ? fkh : OOB, 1u, dead);
    EV(7);
    DMA32(dv, l16, vsrc); DMA32(dk, l16, ksrc);
    EV(8);
    auto next_slot = [&](u32 now) {
        const u32 hv = ld4(rs, fkh), per = rdl(hv, 4);
        if (per < 400u) { tm = rdl(hv, 5) - per; for (int n = 0; n < 3 && (int)(tm - now) < 0; ++n) tm += per; }
    };
    if (L == 1) { next_slot(rt32() + CH_KV0); tm -= 24u; }
    for (int s = 0; s < NSEG;) {
        {
            *(LAS u32*)(lds + C_DUMP + 128 + w * 4) = tm;
            VMW(5); BAR();
            st4s(rs, ofl, 0, (l16 & 16u) ? rep : (u32)s);
            const v4u c = lds_r16(lds + C_DUMP + 128) - tm;
            const u32 add = uu((u32)max(max((int)c.x, (int)c.y), max((int)c.z, (int)c.w)));
            tm += add;
            if (__builtin_expect((dirty | add) != 0u, 0)) {
                wait_until(tm - 44u);
                st16(rs, oso, lds_r16(ZP + 80));
            }
        }
        u32 ms = mring + (u32)(s & 3) * 32768u + w * 2048, ms2 = mring + (u32)(s & 3) * 32768u + 16384u + (w & 1) * 1024;
        u32 l3 = w < 2 ? l16 : OOB;
        asm volatile("" : "+s"(ms), "+s"(ms2)); PIN(l3);
        f32x4 aq0, aq1, ak0, ak1;
        v4u lf[NLF][2];
        ZRD(aq0, 0); ZRD(aq1, 1); ZRD(ak0, 2); ZRD(ak1, 3);
#pragma unroll
        for (int i = 0; i < 4; ++i) { LF_RD(i, 0); LF_RD(i, 1); }
        SCHED();
        v4u A[10];
#define HT(k) do {} while (0)
        v4u kx[8], qf[4], kmf[4], vx[2], ones, P;
        float fa, fb, fc, fd, sx[4], t[4], sm0, sm1, tm0, tm1, inv; u32 pa, pb, fl, pmm, tmp = 0; unsigned long long odd;
        f32x4 av0, av1, s0, s1, sm, oa, ob, rsum;
        v2u vm, pm, vh, vl, on[2];
        f32x2 zz[4];
#define LW(sl) cat8(lf[sl][0], lf[sl][1])
#define PQ(ks, tl) do { if constexpr (!(tl)) SMF_A(aq0, A[ks], wq0[ks]); else if constexpr (qslot(ks) < 0) SMF_A(aq1, A[ks], wq1[ks]); else { const v8u lw_ = LW(qslot(ks)); SMF_V(aq1, A[ks], lw_); } } while (0)
#define PK(ks, tl) do { if constexpr (!(tl)) SMF_A(ak0, A[ks], wk0[ks]); else if constexpr (kslot(ks) < 0) SMF_A(ak1, A[ks], wk1[ks]); else { const v8u lw_ = LW(kslot(ks)); SMF_V(ak1, A[ks], lw_); } } while (0)
#define PV(ks, tl) do { if constexpr (!(tl)) SMF_V(av0, A[ks], wv0[ks]); else if constexpr (vslot(ks) < 0) SMF_V(av1, A[ks], wv1[ks]); else { const v8u lw_ = LW(vslot(ks)); SMF_V(av1, A[ks], lw_); } } while (0)
#define KXR(i) kx[i] = lds_r16(LB + C_K + ((i) & 1) * 4096 + ((i) >> 1) * 1024)
#define XW(q_) do { *(LAS u32*)(q_) = pa; *(LAS u32*)((q_) + 288) = pb; } while (0)
        auto slot = [&](auto zc) __attribute__((always_inline)) {
            constexpr int z = decltype(zc)::value;
            constexpr int vs[] = {0, 8, 1, 10, 9, 2, 11, -1, -1, -1, -1, 4, 6, 12, -1, -1, -1, 13, 14, -1, 16, 18, 15, 3, 5, 17, 7, 19};
            if constexpr (z < 12) PQ(z >> 1, z & 1);
            else if constexpr (z < 20) PQ(6 + (z & 3), z >> 4);
            else if constexpr (z < 32) PK((z - 20) >> 1, z & 1);
            else if constexpr (z < 40) PK(6 + (z & 3), (z >> 2) & 1);
            else if constexpr (z < 42) { if constexpr (z == 40) MF0(s0, kx[0], qf[0]); else MF0(s1, kx[1], qf[0]); }
            else if constexpr (z < 48) { if constexpr (z & 1) MF1(s1, kx[z - 40], qf[(z - 40) >> 1]); else MF1(s0, kx[z - 40], qf[(z - 40) >> 1]); }
            else if constexpr (z == 55) MF0(sm, kmf[0], qf[0]);
            else if constexpr (z > 55 && z < 59) MF1(sm, kmf[z - 55], qf[z - 55]);
            else if constexpr (z == 62) MF0(rsum, ones, P);
            else if constexpr (z == 63) MF0(oa, vx[0], P);
            else if constexpr (z == 64) MF0(ob, vx[1], P);
            else if constexpr (z == 67) { const v2u ones2 = ones.lo; MF1H(rsum, ones2, pm); }
            else PV(vs[z - 48] >> 1, vs[z - 48] & 1);
#define USE(x) asm volatile("" :: "v"(x))
            if constexpr (z == 20 || z == 24 || z == 26) USE(lf[4 + ((z - 20) >> 1)][1]);
            if constexpr (z == 41) USE(qf[3]);
            if constexpr (z == 45 || z == 46) USE(kx[z - 39]);
            if constexpr (z == 53) USE(lf[9][1]);
            if constexpr (z >= 54 && z < 58) USE(kmf[z - 54]);
            if constexpr (z == 61) USE(vl);
            if constexpr (z == 64) USE(lf[10][1]);
            if constexpr (z == 69) USE(lf[11][1]);
            if constexpr (z == 72) USE(lf[12][1]);
            if constexpr (z == 74) USE(lf[13][1]);
            if constexpr (z < 8) A[2 + z] = lds_r16(LB + C_MEM + (2 + z) * 1024);
            if constexpr (z >= 15 && z < 23) LF_RD(4 + ((z - 15) >> 1), (z - 15) & 1);
            if constexpr (z >= 24 && z < 27) KXR(z - 24);
            if constexpr (z == 27) XW(qw);
            if constexpr (z >= 28 && z < 31) KXR(z - 25);
            if constexpr (z == 32) asm volatile("s_waitcnt lgkmcnt(3)\n\ts_barrier" ::: "memory");
            if constexpr (z >= 33 && z < 37) qf[z - 33] = lds_r16(qr + (z - 33) * 64);
            if constexpr (z == 37 || z == 38) LF_RD(8, z - 37);
            if constexpr (z == 39 || z == 41) KXR(6 + (z == 41));
            if constexpr (z == 44) XW(kw);
            if constexpr (z == 45 || z == 46) LF_RD(9, z - 45);
            if constexpr (z == 47) vx[0] = lds_r16(VX);
            if constexpr (z == 48) { VMW(0); vh = *(LAS v2u*)(VX + 1032); }
            if constexpr (z == 54) { vl = *(LAS v2u*)(VX + 1024); vx[1] = (v4u){vh.x, vh.y, vl.x, vl.y}; }
            if constexpr (z == 49) { USE(lf[9][0]); asm volatile("s_barrier" ::: "memory"); }
            if constexpr (z >= 50 && z < 54) kmf[z - 50] = lds_r16(qr + (C_KM - C_Q) + (z - 50) * 64);
            if constexpr (z == 59 || z == 60) LF_RD(10, z - 59);
            if constexpr (z == 61 || z == 65) LF_RD(11, z == 65);
            if constexpr (z == 66 || z == 68) LF_RD(12, z == 68);
            if constexpr (z == 69 || z == 70) LF_RD(13, z - 69);
            if constexpr (z == HSAMP) fl = ld4(rs, fkh);
            SCHED();
            if constexpr (z == 19) HT(2);
            if constexpr (z == 39) HT(3);
            if constexpr (z == 51) HT(4);
            if constexpr (z == 64) HT(5);
            if constexpr (z == 20) VADD(fa, aq0[0], aq0[2]);
            if constexpr (z == 21) VADD(fb, aq0[1], aq0[3]);
            if constexpr (z == 23) VADD(fc, aq1[0], aq1[2]);
            if constexpr (z == 24) VADD(fd, aq1[1], aq1[3]);
            if constexpr (z == 25) VCVT(pa, fa, fc);
            if constexpr (z == 26) VCVT(pb, fb, fd);
            if constexpr (z >= 44 && z < 48) asm volatile("v_mov_b64 %0, 0" : "=v"(zz[z - 44]));
            if constexpr (z == 47) { av0 = __builtin_shufflevector(zz[0], zz[1], 0, 1, 2, 3); av1 = __builtin_shufflevector(zz[2], zz[3], 0, 1, 2, 3); }
            if constexpr (z == 48 || z == 49) asm volatile("v_mov_b64 %0, %1" : "=v"(on[z - 48]) : "s"(0x3F803F803F803F80ull));
            if constexpr (z == 49) ones = __builtin_shufflevector(on[0], on[1], 0, 1, 2, 3);
            if constexpr (z == 40) { VADD(fa, ak0[0], ak0[2]); VADD(fb, ak0[1], ak0[3]); }
            if constexpr (z == 42) { VADD(fd, ak1[1], ak1[3]); VADD(fc, ak1[0], ak1[2]); }
            if constexpr (z == 43) { VCVT(pb, fb, fd); VCVT(pa, fa, fc); }
            if constexpr (z >= 50 && z < 54) SEL(sx[z - 50], s0[z - 50], s1[z - 50]);
            if constexpr (z >= 54 && z < 58) VEXP(t[z - 54], sx[z - 54]);
            if constexpr (z == 58) asm volatile("v_cvt_pk_bf16_f32 %0, %2, %3\n\tv_cvt_pk_bf16_f32 %1, %4, %5" : "=&v"(P.x), "=v"(P.y) : "v"(t[0]), "v"(t[1]), "v"(t[2]), "v"(t[3]));
            if constexpr (z == 59) QPERM(P.z, P.x, "[2,3,0,1]");
            if constexpr (z == 60) QPERM(P.w, P.y, "[2,3,0,1]");
            if constexpr (z == 61) { USE(sm); VMULC(sm0, sm[0]); }
            if constexpr (z == 62) { VEXP(tm0, sm0); VMULC(sm1, sm[1]); }
            if constexpr (z == 63) VEXP(tm1, sm1);
            if constexpr (z == 64) asm volatile("v_cvt_pk_bf16_f32 %0, %2, %3\n\tv_cndmask_b32 %1, %0, 0, %4" : "=&v"(pmm), "=v"(pm.x) : "v"(tm0), "v"(tm1), "s"(himask));
            if constexpr (z == 65) asm volatile("v_cndmask_b32 %0, 0, %1, %2" : "=v"(pm.y) : "v"(pmm), "s"(himask));
            if constexpr (z == 67) { SEL(oa[0], oa[0], ob[0]); SEL(oa[1], oa[1], ob[1]); }
            if constexpr (z == 68 || z == 69) SEL(oa[z - 66], oa[z - 66], ob[z - 66]);
            if constexpr (z == 70) asm volatile("v_sub_u32 %0, %1, %2" : "=v"(pa) : "v"(rsum[0]), "s"(0x2B800000u), "v"(rsum));
            if constexpr (z == 71) asm volatile("v_cmp_le_u32 %0, %1, %2" : "=s"(odd) : "s"(SUMRANGE), "v"(pa));
            if constexpr (z == 72) VADD(fa, av0[0], av0[2]);
            if constexpr (z == 73) { NOPS(0); VADD(fb, av0[1], av0[3]); }
            if constexpr (z == 74) VCVT(vm.x, fa, fb);
            SCHED();
        };
        asm volatile("s_mov_b32 m0, %2\n\t" WAIT_TXT("%0", "%1") "buffer_load_dwordx4 %4, %5, %6 offen sc1 lds\n\tbuffer_load_dwordx4 %4, %5, %6 offen offset:1024 sc1 lds\n\t"
                     "s_mov_b32 m0, %3\n\ts_nop 0\n\tbuffer_load_dwordx4 %8, %5, %7 offen sc1 lds\n\ts_sleep 3\n\ts_mov_b32 m0, %9\n\ts_nop 0\n\t"
                     "buffer_load_dwordx4 %4, %5, %10 offen sc1 lds\n\tbuffer_load_dwordx4 %4, %5, %10 offen offset:1024 sc1 lds\n\ts_waitcnt vmcnt(2) lgkmcnt(0)\n\ts_barrier"
                     : "=&s"(tmp) : "s"(uu(tm - 1u)), "s"(dm), "s"(dm2), "v"(l16), "s"(srd), "s"(ms), "s"(ms2), "v"(l3), "s"(dq), "s"(ksrc ^ 8192u) : "vcc", "scc", "memory");
#pragma unroll
        for (int ks = 0; ks < 2; ++ks) A[ks] = lds_r16(LB + C_MEM + ks * 1024);
        SCHED();
        static_for<0, 76>(slot);
        u32 fail = 0;
        if (__builtin_expect(odd != 0ull, 0)) {
            u32 mn = ~0u;
#pragma unroll 5
            for (int i = 0; i < 10; ++i) { const v4u e = lds_r16(LB + C_MEM + i * 1024); mn = min(min(min(mn, e.x ^ PSENT), e.y ^ PSENT), min(e.z ^ PSENT, e.w ^ PSENT)); }
            fail = missed < SPIN_CAP && !all_(mn != 0u) ? OOB : 0u;
            float mx = fmaxf(fmaxf(fmaxf(sx[0], sx[1]), fmaxf(sx[2], sx[3])), fmaxf(sm0, sm1));
            mx = max32(max16(fmaxf(mx, dpp<0x4E>(mx))));
#pragma unroll
            for (int i = 0; i < 4; ++i) t[i] = __builtin_amdgcn_exp2f(sx[i] - mx);
            P.x = pk(t[0], t[1]); P.y = pk(t[2], t[3]); P.z = u_(dpp<0x4E>(f_(P.x))); P.w = u_(dpp<0x4E>(f_(P.y)));
            pmm = pk(__builtin_amdgcn_exp2f(sm0 - mx), __builtin_amdgcn_exp2f(sm1 - mx));
            pm = (r & 2) ? (v2u){0u, pmm} : (v2u){pmm, 0u};
            asm volatile("s_nop 1" : "+v"(P), "+v"(pm));
            MF0(rsum, ones, P); MF0(oa, vx[0], P); MF0(ob, vx[1], P);
            NOPS(7);
            { const v2u ones2 = ones.lo; MF1H(rsum, ones2, pm); }
#pragma unroll
            for (int i = 0; i < 4; ++i) SEL(oa[i], oa[i], ob[i]);
            asm volatile("s_nop 4" :: "v"(rsum));
            oso |= fail;
        }
        asm volatile("v_rcp_f32 %0, %3\n\ts_nop 4\n\tv_add_f32 %1, %4, %6\n\tv_add_f32 %2, %5, %7\n\tv_cvt_pk_bf16_f32 %1, %1, %2\n\ts_nop 0"
                     : "=&v"(inv), "=&v"(vm.y), "=&v"(fd) : "v"(rsum[0]), "v"(av1[0]), "v"(av1[1]), "v"(av1[2]), "v"(av1[3]));
        asm volatile("v_mfma_f32_16x16x16_bf16 %0, %1, %2, %0\n\ts_nop 6" : "+v"(oa) : "v"(vm), "v"(pm));
        {
            v4u oc;
            const f32x2 i2 = {inv, inv};
            const f32x2 u0 = oa.lo * i2, u1 = oa.hi * i2;
            asm volatile("v_cvt_pk_bf16_f32 %0, %4, %5\n\tv_cvt_pk_bf16_f32 %2, %4, %5\n\tv_cvt_pk_bf16_f32 %1, %6, %7\n\tv_cvt_pk_bf16_f32 %3, %6, %7\n\t"
                         "v_permlane16_swap_b32 %0, %2\n\ts_nop 0\n\tv_permlane16_swap_b32 %1, %3"
                         : "=&v"(oc.x), "=&v"(oc.y), "=&v"(oc.z), "=&v"(oc.w) : "v"(u0.x), "v"(u0.y), "v"(u1.x), "v"(u1.y));
            st16(rs, oso, oc);
        }
        SCHED();
        if (__builtin_expect(fail == 0u, 1)) {
        unsigned long long clk;
        asm volatile("s_memrealtime %0" : "=s"(clk));
        HT(6);
        v4u tq[4], tk[8], tv[2], px, pv;
        f32x4 c0, c1, cm, o[4], sum;
        float ex[10], cs0, cs1;
        v2u p0, p1, tvm;
        u32 oo, kvo, oz; bool kvok;
        u32 ln = l16; PIN(ln);
        lptr QX = lds + C_QX + (w & 1) * 4096 + ln, VO = lds + C_V + (w ^ 1) * 2048 + ln, VMX = lds + C_VM + w * 512 + (ln >> 1), MK = lds + C_MASK + (w & 1) * 1024 + ln;
#define TKR(i) tk[i] = lds_r16(LB + C_K + ((i) >> 2) * 4096 + ((i) & 3) * 1024)
#pragma unroll
        for (int i = 0; i < 4; ++i) tq[i] = lds_r16(QX + i * 1024);
        c0 = __builtin_bit_cast(f32x4, lds_r16(MK + 1024));
        TKR(0);
        *(LAS v2u*)VMX = vm;
        SCHED();
        SFOR(i, 12)
            if constexpr (i == 0) MF0(cm, kmf[0], tq[0]); else if constexpr (i < 4) MF1(cm, kmf[i], tq[i]);
            else if constexpr (i < 8) MF1(c0, tk[i - 4], tq[i - 4]); else MF1(c1, tk[i - 4], tq[i - 8]);
            if constexpr (i < 3) TKR(i + 1);
            if constexpr (i == 2) c1 = __builtin_bit_cast(f32x4, lds_r16(MK));
            if constexpr (i >= 3 && i < 7) TKR(i + 1);
            if constexpr (i == 7) { vx[1] = lds_r16(VX + 1024); tv[0] = lds_r16(VO); }
            if constexpr (i == 8) tv[1] = lds_r16(VO + 1024);
            SCHED();
            if constexpr (i == 0) { oo = ln + (ln & 0x300u) + ((vsrc ^ 8192u) + (u32)((w & 1) * 256)); PIN(oo); kb += NH * 16384; PINS(kb); }
            if constexpr (i == 1) { ksrc = kb + (((kmask >> ((s + 1) & 63)) & 1ull) ? 8192u : 0u); PINS(ksrc); }
            if constexpr (i == 3) { vsrc = kb + (((vmask >> ((s + 1) & 63)) & 1ull) ? W_VO - W_KQ + 8192u : W_VO - W_KQ); PINS(vsrc); }
            if constexpr (i == 6) { USE(cm); VMULC(cs0, cm[0]); }
            if constexpr (i == 8) VMULC(cs1, cm[1]);
            if constexpr (i == 9) VEXP(ex[8], cs0);
            if constexpr (i == 10) { VEXP(ex[9], cs1); VEXP(ex[0], c0[0]); }
            if constexpr (i == 11) { VEXP(ex[1], c0[1]); VEXP(ex[2], c0[2]); VEXP(ex[3], c0[3]); }
            SCHED();
        SEND
        asm volatile("v_cvt_pk_bf16_f32 %0, %6, %7\n\tv_mov_b32 %1, 0\n\tv_mov_b32 %2, 0\n\tv_cvt_pk_bf16_f32 %3, %6, %7\n\tv_cvt_pk_bf16_f32 %4, %8, %9\n\tv_cvt_pk_bf16_f32 %5, %10, %11"
                     : "=&v"(p0.x), "=&v"(p0.y), "=&v"(p1.x), "=&v"(p1.y), "=&v"(px.x), "=&v"(px.y) : "v"(ex[8]), "v"(ex[9]), "v"(ex[0]), "v"(ex[1]), "v"(ex[2]), "v"(ex[3]));
        asm volatile("s_waitcnt lgkmcnt(0)\n\ts_barrier" : "+s"(clk) :: "memory");
        tvm = *(LAS v2u*)(VMX + (512 - (w & 1) * 1024));
        pv = lds_r16(ZP + 80);
        SCHED();
#pragma unroll
        for (int i = 0; i < 4; ++i) VEXP(ex[4 + i], c1[i]);
        asm volatile("v_cvt_pk_bf16_f32 %0, %2, %3\n\tv_cvt_pk_bf16_f32 %1, %4, %5\n\ts_nop 1" : "=&v"(px.z), "=&v"(px.w) : "v"(ex[4]), "v"(ex[5]), "v"(ex[6]), "v"(ex[7]));
        {
            const v2u ones2 = ones.lo;
            const u32 now = (u32)clk;
            auto pvm = [&](auto first) __attribute__((always_inline)) {
                constexpr bool F = decltype(first)::value;
                SCHED();
                MF0(sum, ones, px); if constexpr (F) { oz = (oso & ~(3u << 17)) | (u32)((s + 3) & 3) << 17; PIN(oz); } SCHED();
                MF0(o[0], vx[0], px); if constexpr (F) { oso = (oso & ~(3u << 17)) | (u32)((s + 1) & 3) << 17; PIN(oso); } SCHED();
                MF0(o[1], vx[1], px); NOPS(1);
                MF0(o[2], tv[0], px); NOPS(0);
                MF1H(sum, ones2, p0); NOPS(1);
                MF0(o[3], tv[1], px); NOPS(1);
                MF1H(o[0], vm, p0);
                MF1H(o[1], vm, p1);
                MF1H(o[2], tvm, p0);
                asm volatile("v_sub_u32 %0, %2, %3\n\tv_rcp_f32 %1, %2" : "=&v"(pa), "=&v"(inv) : "v"(sum[0]), "s"(0x2B800000u), "v"(sum));
                MF1H(o[3], tvm, p1);
                SCHED();
            };
            pvm(IC<1>{});
            if (__builtin_expect(__builtin_amdgcn_ballot_w64(pa >= SUMRANGE) != 0ull, 0)) {
                const f32x4 m0 = __builtin_bit_cast(f32x4, lds_r16(MK + 1024)), m1 = __builtin_bit_cast(f32x4, lds_r16(MK));
                float mx = fmaxf(cs0, cs1);
#pragma unroll
                for (int i = 0; i < 4; ++i) { ex[i] = m0[i] == 0.f ? c0[i] : m0[i]; ex[4 + i] = m1[i] == 0.f ? c1[i] : m1[i]; mx = fmaxf(mx, fmaxf(ex[i], ex[4 + i])); }
                mx = max32(max16(mx));
#pragma unroll
                for (int i = 0; i < 4; ++i) px[i] = pk(__builtin_amdgcn_exp2f(ex[2 * i] - mx), __builtin_amdgcn_exp2f(ex[2 * i + 1] - mx));
                p0.x = p1.y = pk(__builtin_amdgcn_exp2f(cs0 - mx), __builtin_amdgcn_exp2f(cs1 - mx));
                asm volatile("s_nop 7" : "+v"(px), "+v"(p0), "+v"(p1));
                pvm(IC<0>{});
                NOPS(3);
            }
            asm volatile("s_nop 1" : "+v"(o[0]), "+v"(o[1]));
            st16(rs, oo, (v4u){pk2(o[0].lo * inv), pk2(o[0].hi * inv), pk2(o[1].lo * inv), pk2(o[1].hi * inv)});
            asm volatile("" : "+v"(o[2]), "+v"(o[3]));
            st16(rs, oo ^ 2048u, (v4u){pk2(o[2].lo * inv), pk2(o[2].hi * inv), pk2(o[3].lo * inv), pk2(o[3].hi * inv)});
            SCHED();
            PIN(fl); kvok = ((u32)__builtin_amdgcn_ballot_w64(fl < (u32)s + 2) & 7u) == 0u;
            kvo = l16 | (kvok ? 0u : OOB);
            DMA32(dv, kvo, vsrc); DMA32(dk, kvo, ksrc);
            st16(rs, oz, pv);
            SCHED();
            rep = (u32)s << 16 | min(missed, 1u) << 14 | min(now - tm, 0x3FFFu);
            if (h == 0 && w == 3) { LST(L * 64 + s, now); LST(128 + L * 64 + s, ((int)(rdl(fl, 3) - now) >= CH_VIS - 4) | ((int)kvok << 1) | ((int)min(missed, 3u) << 2)); LST(640 + L * 64 + s, tm); LST(1728 + L * 64 + s, __builtin_amdgcn_s_memtime()); }
            tm = rdl(fl, 5);
            missed = missed == SPIN_CAP ? missed : 0u;
            dirty = 0;
            if (__builtin_expect(rdl(fl, 3) - now + CH_LATE > 4000u && s + 1 < NSEG, 0)) {
                dirty = 1;
#pragma unroll 1
                for (int n = 0; n < 64; ++n) {
                    CFENCE();
                    const u32 hv = ld4(rs, fkh);
                    tm = rdl(hv, 5);
                    if (rdl(hv, 3) - now + CH_LATE <= 4000u) break;
                }
            }
        }
        LS2(L == 0 && w == 0, h);
        if (__builtin_expect(!kvok, 0) && s + 1 < NSEG) {
            st4s(rs, (ln == 0 && w == 0) ? hint + 4096u + (u32)h * 128u : OOB, 0, (u32)s + 1u);
            dirty = 1;
            wait_flags(rs, ln < 48u ? fkh : OOB, (u32)s + 2, dead);
            DMA32(dv, l16, vsrc); DMA32(dk, l16, ksrc);
            const u32 now = rt32() + CH_KV;
            if ((int)(tm - now) < 0) next_slot(now);
        }
        ++s;
        } else {
            u32 ln = l16; PIN(ln);
            oso = (ln & 256u) ? oso : oso & ~OOB;
            const u32 was = tm - tmp;
            tm = rdl(fl, 5); dirty = 1;
            if ((int)(tm - rdl(fl, 4) - was) > 24) tm -= rdl(fl, 4);
            if ((++missed & 7u) == 0u) {
                *(LAS u32*)(lds + C_DUMP + (w == 0 ? 0 : 64)) = ld4(rs, foff(F_ERR));
                BAR();
                missed = uu(*(LAS u32*)(lds + C_DUMP)) != 0u ? SPIN_CAP : missed;
                st4s(rs, missed == SPIN_CAP ? foff(F_ERR) : OOB, 0, 1u);
            }
        }
    }
    DRAIN(); BAR();
    st4s(rs, ofl, 0, (l16 & 16u) ? rep : NSEG);
}

#define WO_NI 4
#define WROT 0
DEV void role_wo(const Params& p, lptr lds, int j) {
    const int tid = threadIdx.x, lane = tid & 63, w = uni(tid >> 6), g = lane >> 4, r = lane & 15;
    const int rg = j & 1, cg = j >> 1, rot = cg & WROT;
    const rsrc_t rs = mk_rsrc(p.ws, W_END);
    v8u wo[8][5];
    {
        const v4u so_ = mk_srd(p.Wo, DM * NQ * 2);
        const int one = (0x3F2FF1F0u >> (4 * (cg & 7))) & 15;
        u32 vo[3];
#pragma unroll
        for (int i = 0; i < 3; ++i) { const int n = 64 * i + lane; vo[i] = n % 6 < 5 ? (u32)((n / 6) * (DM * 2) + cg * 80 + (n % 6) * 16) : ((n / 6) & 3) == one ? (u32)((n / 6) * (DM * 2) + (cg >> 3) * 640 + 304 + rg * 16) : OOB; }
        auto fill = [&](int b) {
            const u32 dst = uu(w * 36864 + (b % 3) * 12288), src = uu((8 * w + ((b + rot) & 7)) * (DH * DM * 2));
#pragma unroll
            for (int i = 0; i < 12; ++i) DMAIN(so_, dst + i * 1024, vo[i % 3], src + (i / 3) * (32 * DM * 2));
        };
#pragma unroll 1
        for (int b = 0; b < 3; ++b) fill(b);
        const int q4 = r >> 2;
        lptr tb = lds + w * 36864 + (32 * (g >> 1) + 16 * (q4 >> 1) + 8 * ((r >> 1) & 1) + 4 * (g & 1) + (q4 & 1)) * 96 + (r & 1) * 8;
        SFOR(b, 8)
            VMW(b < 6 ? 24 : 84 - 12 * b);
#pragma unroll
            for (int t = 0; t < 5; ++t) {
                lptr q = tb + (b % 3) * 12288 + (t == 0 ? 4 : (w + t) & 3) * 16;
                wo[b][t] = tr_frag(q, q + 192, q + 64 * 96, q + 64 * 96 + 192);
                if (t < 4) asm volatile("" : "+a"(wo[b][t])); else asm volatile("" : "+v"(wo[b][t]));
            }
            CFENCE();
            if constexpr (b < 5) fill(b + 3);
        SEND
    }
    BAR();
    EV(6);
    const u32 idx = (r & 2) ? 0xEEEEu : 0x4444u;
    bool dead = false;
    const u32 pol0 = get_pol(p, rs, dead) & 1u;
    const u32 l16 = (u32)lane * 16u;
    u32 oo[8];
#pragma unroll
    for (int i = 0; i < 8; ++i) oo[i] = uu(W_ORING + ring_map((u32)(rg * 32768 + (8 * w + ((i + rot) & 7)) * 1024), pol0));
    const u32 hint = W_MRING + 4 * 32768u + pol0 * 8192u;
    u32 hvo[2];
#pragma unroll
    for (int l = 0; l < 2; ++l) hvo[l] = lane < 2 ? hint + (u32)(l ^ 1) * (MB / 4) + (u32)lane * 4u : lane == 2 ? hint + (u32)l * (MB / 4) + 128u : lane == 3 ? foff(F_ERR) : lane == 4 ? hint + (u32)l * (MB / 4) + 256u :
                 (lane >= 32 && j == 0 && w == 3) ? hint + (u32)l * (MB / 4) + (u32)lane * 128u : OOB;
    const int xo = g * 128 + (r & 7) * 16, O = 5 * cg + (r < 8 ? w : 4), q2 = 2 + (r >> 3);
    lptr xw0 = lds + (r < 8 ? 2048 + w * 4 : ((w + 1) & 3) * 512 + 4) + xo, xw1 = lds + ((w + q2) & 3) * 512 + q2 * 4 + xo;
    lptr xr = lds + (r < 8 ? w : 4) * 512 + xo;
    if (r < 8) { *(LAS u32*)xr = 0u; *(LAS u32*)(xr + 8192) = 0u; }
    const u32 mpo = (r & 4) == 0 && (r < 8 || w == 0) ? W_MRING + ring_map((u32)((O >> 1) * 256 + rg * 128 + (4 * (g & 1) + 2 * (O & 1) + (g >> 1)) * 16 + (r & 3) * 4), pol0) : OOB;
    constexpr u32 PER0 = CH_W0 + CH_MW + CH_H0 + CH_MH + 2 * CH_VIS;
    const bool lead = j == 0 && w == 3;
    u32 hso = OOB;
    MK_SRD();
    u32 t = 0, tn = 0;
    int sn[2] = {0, 0};
    u32 hav = 4 * CH_H0, wav = 4 * CH_W0, mh = 8 * (CH_MH + CH_M0), mw = 8 * (CH_MW + CH_M0), ok[2] = {0u, 0u}, dw[2] = {CH_W0 + CH_VIS + CH_MW + CH_REC, CH_W0 + CH_VIS + CH_MW + CH_REC};
    u32 zero = 0, hv = 0xFFFFu, pst = 1, pdur = 0, pmp = OOB;
    const u32 psent = PSENT;
    lptr sw = lds + 5120 + w * 4;
    bool stop = false;
    asm volatile("" : "+v"(zero), "+v"(sw), "+v"(xw0), "+v"(xw1), "+v"(xr));
    auto turn = [&](auto lc) __attribute__((always_inline)) {
        constexpr int L = decltype(lc)::value, M = L ^ 1;
        u32 now, done, rep, rd, lost, dh, dn, nt, za; v4u hw;
        const int sc = sn[L];
        auto books = [&](auto ic) __attribute__((always_inline)) {
            constexpr int i = decltype(ic)::value;
            if constexpr (i == 0) { rep = max(rdl(hv, 2), rdl(hv, 4)); PINS(rep); }
            if constexpr (i == 1) { lost = (u32)(__builtin_amdgcn_ballot_w64(hv == (u32)sn[M]) >> 32); PINS(lost); }
            if constexpr (i == 2) { lost |= (rep >> 16) ^ (u32)sn[M]; PINS(lost); }
            if constexpr (i == 3) { lost |= ok[M] ^ 2u; rd = rep & 0xFFFFu; PINS(lost); PINS(rd); }
            if constexpr (i == 4) { lost = lost == 0u ? CH_UP * 8u : 0u; PINS(lost); }
            if constexpr (i == 5) { dn = wav >> 2; PINS(dn); pdur = min(pdur, dn + 8u); PINS(pdur); }
            if constexpr (i == 6) { pdur = (done & ok[M]) ? pdur : dn; PINS(pdur); }
            if constexpr (i == 7) { wav += pdur - dn; PINS(wav); }
            if constexpr (i == 8) { dh = hav >> 2; PINS(dh); nt = min(rd, dh + 8u); PINS(nt); }
            if constexpr (i == 9) { nt = rd < 0x3FFFu ? nt : dh; PINS(nt); }
            if constexpr (i == 10) { hav += nt - dh; PINS(hav); }
            if constexpr (i == 11) { mw = max(mw - CH_DEC, CH_MW * 8u); PINS(mw); }
            if constexpr (i == 12) { mh = max(mh - CH_DEC, CH_MH * 8u); PINS(mh); }
            if constexpr (i == 13) { rd = rd >> 14 == 1u ? lost : 0u; PINS(rd); }
            if constexpr (i == 14) { mw += rd; mh += lost - rd; PINS(mw); PINS(mh); }
            if constexpr (i == 15) { ok[M] = done ? 1u : (ok[M] & 1u) << 1; PINS(ok[M]); }
            if constexpr (i == 16) { dh = (2u * hav + mh) / 8u + (sn[1] < 2 ? CH_VIS + CH_SOLO : CH_VIS); PINS(dh); }
            if constexpr (i == 17) { dn = (2u * wav + mw) / 8u + CH_VIS; PINS(dn); }
            if constexpr (i == 18) { nt = dw[L] + dh; PINS(nt); }
            if constexpr (i == 19) { dh += dn; dw[L] = dn; sn[M] += (int)done; PINS(dh); PINS(sn[M]); }
            if constexpr (i == 20) { rd = tn - t + (dh >> 1); PINS(rd); }
            if constexpr (i == 21) { rd = sn[M] + 1 < NSEG ? rd : 0u; PINS(rd); }
            if constexpr (i == 22) { nt = t + (u32)max((int)nt, (int)rd); PINS(nt); }
            if constexpr (i == 23) { hw.x = nt; hw.y = dh; PIN(hw.x); PIN(hw.y); }
            if constexpr (i == 24) { hw.z = nt + dn; hw.w = 0u; PIN(hw); }
            if constexpr (i == 25) st16(rs, hso + L * (MB / 4), hw);
        };
        if (__builtin_expect(sc + 1 < NSEG, 1)) {
            u32 vo = l16 + (L * (MB / 2) + (u32)(sc & 3) * 131072u), mp = mpo + (L * (MB / 4) + (u32)((sc + 1) & 3) * 32768u), zb, hn;
            done = uu(pst) ^ 1u;
            za = done ? pmp : OOB;
            zb = ok[L] == 1u ? OOB : mp;
            pmp = mpo + (L * (MB / 4) + (u32)(sc & 3) * 32768u);
            *(LAS u32*)(sw + L * 8192) = zero;
            f32x4 acc[5] = {};
            asm volatile("" : "+v"(acc[0]), "+v"(acc[1]), "+v"(acc[2]), "+v"(acc[3]), "+v"(acc[4]), "+v"(mp), "+v"(pmp), "+v"(vo), "+v"(za), "+v"(zb));
            PINS(done);
#define WT(k) do {} while (0)
            v4u d[8];
            {
                u32 tmp;
                asm volatile(WAIT_TXT("%1", "%2") "buffer_load_dwordx4 %0, %3, %4, %5 offen sc1\n\ts_waitcnt lgkmcnt(0)"
                             : "=&v"(d[0]), "=&s"(tmp) : "s"(uu(t - 1u)), "v"(vo), "s"(srd), "s"(oo[0]) : "vcc", "scc", "memory");
            }
            WT(0);
            SFOR(i, 7)
                asm volatile("buffer_load_dwordx4 %0, %1, %2, %3 offen sc1" : "=v"(d[i + 1]) : "v"(vo), "s"(srd), "s"(oo[i + 1]) : "memory");
                SCHED();
                static_for<3 * i, 3 * i + 3>(books);
                SCHED();
            SEND
            LD4A(hn, hvo[L], 0);
            st4s(rs, za, 0, psent);
            st4s(rs, zb, 0, psent);
            SCHED();
            WT(1);
            float f0[5], f1[5], xs; unsigned long long bad; v4u sv;
#define WSW(o_) asm volatile("v_permlane32_swap_b32 %0, %1" : "+v"(f0[o_]), "+v"(f1[o_]))
#define WAD(o_) asm volatile("v_add_f32_dpp %0, %1, %0 row_ror:8 row_mask:0xf bank_mask:0xf\n\tv_add_f32_dpp %0, %2, %3 row_ror:8 row_mask:0xf bank_mask:0xc" : "+v"(f0[o_]) : "v"(f1[o_]), "v"(f0[o_ + 1]), "v"(f1[o_ + 1]))
#define WFO(o_) do { VADD(f0[o_], acc[o_][0], acc[o_][2]); VADD(f1[o_], acc[o_][1], acc[o_][3]); } while (0)
#define WOWN(k) do { if constexpr (WO_NI + (k) < 8) SMF_V(acc[4], d[(WO_NI + (k)) & 7], wo[(WO_NI + (k)) & 7][4]); SCHED(); } while (0)
            SFOR(i, 40)
                constexpr int pc = i / 5, oc = i % 5;
                if constexpr (oc == 0) VMW(10 - pc);
                if constexpr (oc < 4) SMF_A(acc[oc], d[pc], wo[pc][oc]); else if constexpr (pc < WO_NI) SMF_V(acc[4], d[pc], wo[pc][4]);
                if constexpr (i == 0) WT(2);
                if constexpr (i == 20) WT(3);
                if constexpr (i == 35) WT(4);
                if constexpr (i < 4) books(IC<21 + i>{});
                if constexpr (i == 6) { xs = 0.f; PIN(xs); }
                if constexpr (i == 36) NOPS(1);
                if constexpr (i == 37) NOPS(2);
                SCHED();
            SEND
            WFO(0); WFO(1);
            asm volatile("v_cmp_u_f32 %0, %1, %2" : "=s"(bad) : "v"(f0[0]), "v"(f1[0]));
            WSW(0); WSW(1); WFO(2); WAD(0); WFO(3);
            *(LAS float*)(xw0 + L * 8192) = f0[0]; SCHED();
            WSW(2); WSW(3);
            if (__builtin_expect(bad != 0ull, 0)) {
                u32 mn = ~0u;
#pragma unroll
                for (int i = 0; i < 8; ++i) { u32 e = d[i].x; PIN(e); mn = min(mn, e ^ PSENT); }
                if (all_(mn != 0u)) {
#pragma unroll
                    for (int i = 0; i < 8; ++i) { v4u e = d[i]; PIN(e); mn = min(min(mn, e.y ^ PSENT), min(e.z ^ PSENT, e.w ^ PSENT)); }
                }
                *(LAS u32*)(sw + L * 8192) = all_(mn != 0u) ? 0u : 1u;
            }
            WAD(2);
            *(LAS float*)(xw1 + L * 8192) = f0[2]; SCHED();
            WOWN(0); WOWN(1); WOWN(2); WOWN(3);
            WT(5);
            f32x4 xa; float nb; u32 cw, mq;
            asm volatile("s_waitcnt lgkmcnt(0)\n\ts_barrier\n\tds_read_b128 %0, %2 offset:%4\n\tds_read_b128 %1, %3 offset:%4+5120\n\ts_nop 3"
                         : "=&v"(xa), "=&v"(sv) : "v"(xr), "v"(zero), "i"(L * 8192) : "memory");
            WT(6);
            WFO(4);
            asm volatile("s_nop 0\n\tv_permlane32_swap_b32 %1, %2\n\ts_nop 1\n\tv_add_f32_dpp %0, %2, %1 row_ror:8 row_mask:0xf bank_mask:0x3\n\ts_waitcnt lgkmcnt(1)" : "+v"(xs), "+v"(f0[4]), "+v"(f1[4]));
            asm volatile("v_add_f32 %0, %0, %4\n\tv_add_f32 %1, %5, %6\n\tv_add_f32 %0, %0, %7\n\tv_add_f32 %0, %0, %1\n\tv_cmp_u_f32 vcc, %0, %0\n\ts_nop 0\n\t"
                         "v_mov_b32_dpp %1, %0 row_shl:4 row_mask:0xf bank_mask:0xf\n\tv_cndmask_b32 %3, %8, %9, vcc\n\tv_cvt_pk_bf16_f32 %2, %0, %1"
                         : "+v"(xs), "=&v"(nb), "=&v"(cw), "=&v"(mq) : "v"(xa[0]), "v"(xa[1]), "v"(xa[2]), "v"(xa[3]), "v"(mp), "v"(OOB) : "vcc");
            st4s(rs, mq, 0, cw);
            SCHED();
            WT(7);
            now = rt32();
            books(IC<25>{});
            asm volatile("s_waitcnt vmcnt(4) lgkmcnt(0)" : "+v"(hn), "+v"(sv));
            hv = hn;
            pst = sv.x | sv.y | sv.z | sv.w;
            if (__builtin_expect(__builtin_amdgcn_ballot_w64(mq != mp && pst == 0u) != 0ull, 0)) st4s(rs, mp, 0, cw);
        } else {
            done = uu(pst) ^ 1u;
            st4s(rs, done ? pmp : OOB, 0, psent);
            static_for<0, 25>(books);
            pst = 1;
            BAR();
            hv = ld4(rs, hvo[L]);
            PIN(hv);
            now = rt32();
        }
        pdur = now - t;
        const u32 per = rdl(hv, 1), th = rdl(hv, 0);
        t = lead ? tn : (int)(th - now) < -(int)(per >> 1) ? th + per : th;
        tn = nt;
        dw[M] += max((int)(now + CH_WX - t), 0);
        stop = stop || rdl(hv, 3) != 0u;
    };
#pragma unroll 1
    for (int k = 0; k < 2 * SPIN_CAP && !stop && (sn[0] + 1 < NSEG || sn[1] + 1 < NSEG); ++k) {
        turn(IC<0>{}); turn(IC<1>{});
        if (__builtin_expect(k != 0, 1)) continue;
        if (j == 0) {
            for (int n = 0; n < SPIN_CAP && !dead && sn[0] == 0; ++n) {
                CFENCE();
                if (all_(ld4(rs, W_ORING + pol0 * 8192u + (u32)w * 16384u + (u32)lane * 128u) != PSENT)) break;
                __builtin_amdgcn_s_sleep(1);
            }
            BAR();
        }
        if (lead) {
            t = rt32() + 56u; tn = t + PER0 / 2 + CH_REC;
            st16(rs, lane < 2 ? hint + (u32)lane * (MB / 4) : OOB, (v4u){lane == 0 ? t : tn, PER0, (lane == 0 ? t : tn) + (CH_W0 + CH_VIS + CH_MW + CH_REC), 0u});
        } else {
            for (int n = 0; n < SPIN_CAP && !dead; ++n) {
                CFENCE();
                const u32 hv = ld4(rs, hvo[1]);
                t = rdl(hv, 0);
                if (rdl(hv, 1) < 400u) break;
                __builtin_amdgcn_s_sleep(1);
            }
        }
        hso = (lead && lane == 0) ? hint : OOB;
        hv = 0xFFFFu; dw[0] = dw[1] = CH_W0 + CH_VIS + CH_MW + CH_REC;
    }
    if (sn[0] + 1 < NSEG || sn[1] + 1 < NSEG) st4s(rs, tid == 0 ? foff(F_ERR) : OOB, 0, 1u);
}

__global__ void __launch_bounds__(256, 1) k_main(Args args) {
    Params p;
    (Args&)p = args;
    lptr lds = (lptr)(u32)16 - 16;
    if (blockIdx.x == 0) LST(256, rt32());
    unsigned long long pc; int len;
    asm volatile("s_getpc_b64 %0\n.Lk_main_here:\n\ts_mov_b32 %1, .Lk_end_here-.Lk_main_here" : "=s"(pc), "=s"(len));
    const v4u code = ldin16(mk_rsrc((const void*)(pc & ~15ull), (u32)max(len, 0)), (((blockIdx.x >> 3) & 31u) * 256u + threadIdx.x) * 16u, 0);
    CFENCE();
    if (threadIdx.x == 0) {
        const v4u e = ld16s(mk_rsrc(p.ws, W_END), foff(F_EP), 0);
        const unsigned long long tag = (unsigned long long)e.y << 32 | (e.x + 1u);
        u32 role = ((u32)__builtin_amdgcn_s_getreg(20 | (3 << 11)) & 7u) * 32u + ((blockIdx.x >> 3) & 31u);
#pragma unroll 1
        for (int n = 0; n < 256; ++n, role = (role + 1u) & 255u)
            if (__hip_atomic_exchange((GAS unsigned long long*)(p.ws + W_SLOT) + role * 8u, tag, __ATOMIC_RELAXED, __HIP_MEMORY_SCOPE_AGENT) != tag) break;
        lds_w16(lds, (v4u){e.x + 1u, e.y, e.z, role});
    }
    __syncthreads();
    const v4u boot = lds_r16(lds);
    p.e0 = uu(boot.x); p.e1 = uu(boot.y); p.e2 = uu(boot.z);
    const int role = uni((int)boot.w), x = role >> 5, i = role & 31;
    asm volatile("" :: "v"(code));
    __syncthreads();
    LST(8192 + blockIdx.x * 16, role);
    if (x < 2) { role_head(p, lds, i >> 4, x * 16 + (i & 15)); __builtin_amdgcn_endpgm(); }
    if (x == 2) { role_wo(p, lds, i); __builtin_amdgcn_endpgm(); }
    const int no = x == 3 ? 30 : x < 6 ? 5 : 0;
    if (x > 3 && i >= 8) { role_qkv(p, lds, (x - 4) * 24 + i - 8); __builtin_amdgcn_endpgm(); }
    const bool isout = i < no;
    const int og = x == 3 ? i / 5 : x + 2, ng = i % 5, L = og >> 2, kq = og & 3;
    const int k = i - no, w = uni(threadIdx.x >> 6);
    const int j = isout ? (L == 1 ? 0 : 44) + kq * 5 + ng : x == 3 ? 36 + k : k < 2 ? 12 + 2 * x + k : x < 6 ? 24 + x : k < 5 ? 10 + 3 * x + k : 15 + 3 * x + k;
    const rsrc_t rs = mk_rsrc(p.ws, W_END);
    if (j < NKV) {
        const int nt = j < 320 - 7 * NKV ? 8 : 7;
#pragma unroll 1
        for (int q = 0; q < 4; ++q) kv_fetch(p, j + NKV * q, uu(q * KT_BYTES));
        Filt f;
        u32 warm[3];
#pragma unroll 1
        for (int q = 0; q < nt; ++q) {
            VMW(15); BAR();
            if (q == 0) EV(2);
            kv_fetch(p, j + NKV * (q + 4), uu(((q + 4) & 7) * KT_BYTES));
            kv_tile(p, rs, lds + q * KT_BYTES, lds + (q == 0 ? 8 : q - 1) * KT_BYTES, j + NKV * q);
        }
        filt_load(p, j, f);
        VMW(10); BAR();
        boot_set(p, rs, threadIdx.x == 0 ? foff(F_KV + j) : OOB, 2u);
        EV(4);
        { bool dead = false; boot_wait(p, rs, pol_off(), 0u, dead); }
#pragma unroll
        for (int q = 0; q < 3; ++q)
            warm[q] = __builtin_amdgcn_raw_buffer_load_b32(mk_rsrc(p.ssmk, ACT), 256u * 4096u + ((u32)((q * NKV + j) * 256) + threadIdx.x) * 64u, 0, 0);
        conv_job<0>(p, rs, lds, j, f, 0);
        asm volatile("" :: "v"(warm[0]), "v"(warm[1]), "v"(warm[2]));
        const int g0 = j < 36 ? 108 + j : 28 + j, g1 = j < 36 ? 160 : 136;
        if (j >= 28) filt_load<5, 0>(p, g0, f);
        DRAIN(); BAR();
        st4s(rs, threadIdx.x == 0 ? foff(F_CV + j) : OOB, 0, 2u);
        if (j >= 28) {
#pragma unroll 1
            for (int g = g0; g < g1; g += 8) conv_job<2>(p, rs, lds, g, f, min(g + 8, 159));
            DRAIN(); BAR();
            st4s(rs, threadIdx.x == 0 ? foff(F_CV + 132 + j) : OOB, 0, 3u);
            if (j >= 36) { EV(15); __builtin_amdgcn_endpgm(); }
        }
    }
    if (!isout) { if (j < 28) role_row<0>(p, lds, (j - 20) * 4 + w); else role_row<1>(p, lds, (j - 28) * 4 + w);
        boot_set(p, rs, j == 28 && threadIdx.x == 0 ? foff(F_EP) : OOB, 0u);
        __builtin_amdgcn_endpgm();
    }
    role_out(p, lds, L, kq, ng);
}

__global__ void k_end() { asm volatile(".Lk_end_here:"); }

extern "C" void kernel_launch(void* const* d_in, const int* in_sizes, int n_in, void* d_out, int out_size, void* d_ws, size_t ws_size,
                              hipStream_t stream) {
    Args p;
    p.x = (const u16*)d_in[0]; p.mem0 = (const u16*)d_in[1]; p.Wq = (const u16*)d_in[2]; p.Wk = (const u16*)d_in[3];
    p.Wv = (const u16*)d_in[4]; p.Wo = (const u16*)d_in[5];
    p.bk = (const u16*)d_in[8]; p.bv = (const u16*)d_in[9]; p.shift = (const u16*)d_in[10]; p.ssmk = (const u16*)d_in[11]; p.ssmD = (const u16*)d_in[12];
    p.out = (u16*)d_out;
    p.ws = (unsigned char*)(((size_t)d_ws + 2 * MB - 1) & ~(size_t)(2 * MB - 1));
    (void)hipFuncSetAttribute((const void*)k_main, hipFuncAttributeMaxDynamicSharedMemorySize, LDS_BYTES);
    hipLaunchKernelGGL(k_main, dim3(256), dim3(256), LDS_BYTES, stream, p);
}
```

```cpp
#include <hip/hip_runtime.h>
#include <hip/hip_bf16.h>

typedef unsigned short u16;
typedef unsigned int u32;
typedef u32 v2u __attribute__((ext_vector_type(2)));
typedef u32 v4u __attribute__((ext_vector_type(4)));
typedef u32 v8u __attribute__((ext_vector_type(8)));
typedef float f32x2 __attribute__((ext_vector_type(2)));
typedef float f32x4 __attribute__((ext_vector_type(4)));
typedef float f32x16 __attribute__((ext_vector_type(16)));
typedef __bf16 bf16x2 __attribute__((ext_vector_type(2)));
typedef __bf16 bf16x8 __attribute__((ext_vector_type(8)));
typedef short s16x4 __attribute__((ext_vector_type(4)));
#define LAS __attribute__((address_space(3)))
#define GAS __attribute__((address_space(1)))
typedef LAS unsigned char* lptr;
#define DEV __device__ __forceinline__

constexpr int SEQ = 2048, DM = 640, NH = 32, DH = 128, NSEG = 64, NQ = NH * DH;

constexpr u32 MB = 1u << 20;
constexpr u32 W_ORING = 0;
constexpr u32 W_MRING = MB;
constexpr u32 W_CTRL = 4 * MB;
constexpr u32 ACT = SEQ * DM * 2;
constexpr u32 H_TILE = 128 * 1024;
constexpr u32 W_H2 = 8 * MB;
constexpr u32 W_KQ = 16 * MB;
constexpr u32 W_VO = W_KQ + 64 * MB;
constexpr u32 W_P = W_VO + 64 * MB;
constexpr u32 P_ITEM = 512 * 1024;
constexpr u32 W_H1 = W_P + 44 * MB, W_Y = W_H1 + 8 * MB, W_X2 = W_Y + ACT;
constexpr u32 W_KVT = W_X2 + ACT;
constexpr u32 W_END = W_KVT + 3 * MB;
constexpr u32 NREG = 12;
static_assert((size_t)W_END + 2 * MB <= 256ull * MB && W_H1 >= (NREG - 1) * 16 * MB + 2 * MB && W_H1 <= NREG * 16 * MB, "workspace");
constexpr u32 F_EP = 0, F_RG = 32, F_LN = 64, F_KV = 192,
              F_QKV = 256, F_O = F_QKV + 192, F_P = F_O + 64, F_H2 = F_P + 64, F_CV = F_H2 + 64, F_ERR = F_CV + 191, F_N = F_CV + 192;
constexpr u32 W_SLOT = 3 * MB;
DEV u32 foff(u32 idx) { return W_CTRL + (idx >> 6) * 16384u + (idx & 63u) * 128u; }
DEV u32 near_(u32 o, u32 side, u32 pol) { return o + ((side ^ (pol >> (o >> 24)) ^ (o >> 21)) & 1u) * 8192u; }
DEV u32 p_item(int s) { return W_P + (u32)s * P_ITEM; }

constexpr u32 OOB = 0x80000000u;
constexpr u32 PSENT = 0x7F817F81u;
constexpr int SPIN_CAP = 40000;
constexpr int LDS_BYTES = 160 * 1024;
#define ST_KV 0
#define ST_OUT 0

#define EV(k) do {} while (0)
#define LST(idx, v) do {} while (0)
#define LS2(cond, idx) do {} while (0)
struct Args {
    const u16 *x, *mem0, *Wq, *Wk, *Wv, *Wo, *bk, *bv, *shift, *ssmk, *ssmD;
    u16* out;
    unsigned char* ws;
};
struct Params : Args { u32 e0, e1, e2; };

typedef __amdgpu_buffer_rsrc_t rsrc_t;
DEV rsrc_t mk_rsrc(const void* p, u32 bytes) { return __builtin_amdgcn_make_buffer_rsrc((void*)p, 0, bytes, 0x00020000); }
DEV v4u ld16s(rsrc_t r, u32 off, u32 so) { return __builtin_amdgcn_raw_buffer_load_b128(r, off, so, 16); }
DEV v2u ld8s(rsrc_t r, u32 off, u32 so) { return __builtin_amdgcn_raw_buffer_load_b64(r, off, so, 16); }
DEV v4u ldin16(rsrc_t r, u32 off, u32 so) { return __builtin_amdgcn_raw_buffer_load_b128(r, off, so, 0); }
DEV v2u ldin8(rsrc_t r, u32 off, u32 so) { return __builtin_amdgcn_raw_buffer_load_b64(r, off, so, 0); }
DEV u32 ld4(rsrc_t r, u32 off) { return __builtin_amdgcn_raw_buffer_load_b32(r, off, 0, 16); }
DEV void st16(rsrc_t r, u32 off, v4u v) { __builtin_amdgcn_raw_buffer_store_b128(v, r, off, 0, 16); }
DEV void st8s(rsrc_t r, u32 off, u32 so, v2u v) { __builtin_amdgcn_raw_buffer_store_b64(v, r, off, so, 16); }
DEV void st4s(rsrc_t r, u32 off, u32 so, u32 v) { __builtin_amdgcn_raw_buffer_store_b32(v, r, off, so, 16); }
#define DRAIN() asm volatile("s_waitcnt vmcnt(0)" ::: "memory")
#define VMW(n) asm volatile("s_waitcnt vmcnt(%0)" :: "i"(n) : "memory")
#define BAR() asm volatile("s_waitcnt lgkmcnt(0)\n\ts_barrier" ::: "memory")
#define SCHED() __builtin_amdgcn_sched_barrier(0)
#define CFENCE() asm volatile("" ::: "memory")
#define NOPS(n) asm volatile("s_nop %0" :: "i"(n))
#define PIN(x) asm volatile("" : "+v"(x))
#define PINS(x) asm volatile("" : "+s"(x))

template <int V> struct IC { static constexpr int value = V; };
template <int I, int N, class F> DEV void static_for(F&& f) { if constexpr (I < N) { f(IC<I>{}); static_for<I + 1, N>(f); } }
#define SFOR(var, n) static_for<0, n>([&](auto ic_) __attribute__((always_inline)) { constexpr int var = decltype(ic_)::value;
#define SEND });

DEV u32 pk(float a, float b) { bf16x2 v = {(__bf16)a, (__bf16)b}; return __builtin_bit_cast(u32, v); }
DEV float lo_f(u32 u) { return __builtin_bit_cast(float, u << 16); }
DEV float hi_f(u32 u) { return __builtin_bit_cast(float, u & 0xffff0000u); }
DEV f32x2 up2(u32 u) { return (f32x2){lo_f(u), hi_f(u)}; }
DEV u32 pk2(f32x2 v) { return pk(v.x, v.y); }
DEV f32x2 rb2(f32x2 a) { f32x2 r; asm("v_cvt_pk_bf16_f32 %0, 0, %1" : "=v"(r.x) : "v"(a.x)); asm("v_cvt_pk_bf16_f32 %0, 0, %1" : "=v"(r.y) : "v"(a.y)); return r; }
DEV float rbf(float a) { return (float)(__bf16)a; }
DEV v4u lds_r16(lptr p) { return *(LAS v4u*)p; }
DEV void lds_w16(lptr p, v4u v) { *(LAS v4u*)p = v; }
DEV v2u lds_tr(lptr p) { return __builtin_bit_cast(v2u, __builtin_amdgcn_ds_read_tr16_b64_v4i16((LAS s16x4*)p)); }
DEV v4u tr2(lptr p0, lptr p1) { const v2u a = lds_tr(p0), b = lds_tr(p1); return (v4u){a.x, a.y, b.x, b.y}; }
DEV v8u cat8(v4u a, v4u b) { return __builtin_shufflevector(a, b, 0, 1, 2, 3, 4, 5, 6, 7); }
DEV f32x4 mfma16(v4u a, v4u b, f32x4 c) { return __builtin_amdgcn_mfma_f32_16x16x32_bf16(__builtin_bit_cast(bf16x8, a), __builtin_bit_cast(bf16x8, b), c, 0, 0, 0); }
DEV f32x16 mfma32(v4u a, v4u b, f32x16 c) { return __builtin_amdgcn_mfma_f32_32x32x16_bf16(__builtin_bit_cast(bf16x8, a), __builtin_bit_cast(bf16x8, b), c, 0, 0, 0); }

DEV float f_(u32 u) { return __builtin_bit_cast(float, u); }
DEV u32 u_(float f) { return __builtin_bit_cast(u32, f); }
template <int CTRL> DEV float dpp(float v) { return f_((u32)__builtin_amdgcn_update_dpp(0, (int)u_(v), CTRL, 0xf, 0xf, false)); }
DEV float max16(float v) { auto r = __builtin_amdgcn_permlane16_swap(u_(v), u_(v), false, false); return fmaxf(f_(r[0]), f_(r[1])); }
DEV float max32(float v) { auto r = __builtin_amdgcn_permlane32_swap(u_(v), u_(v), false, false); return fmaxf(f_(r[0]), f_(r[1])); }
DEV float add16(float v) { auto r = __builtin_amdgcn_permlane16_swap(u_(v), u_(v), false, false); return f_(r[0]) + f_(r[1]); }
DEV float add32(float v) { auto r = __builtin_amdgcn_permlane32_swap(u_(v), u_(v), false, false); return f_(r[0]) + f_(r[1]); }
DEV float wave_sum(float v) {
    v += dpp<0xB1>(v);
    v += dpp<0x4E>(v);
    v += dpp<0x124>(v);
    v += dpp<0x128>(v);
    return add32(add16(v));
}
DEV bool all_(bool c) { return __builtin_amdgcn_ballot_w64(!c) == 0ull; }
DEV int uni(int v) { return __builtin_amdgcn_readfirstlane(v); }
DEV u32 uu(u32 v) { return (u32)__builtin_amdgcn_readfirstlane((int)v); }
DEV u32 rdl(u32 v, int l) { return (u32)__builtin_amdgcn_readlane((int)v, l); }
DEV u32 rt32() { return (u32)__builtin_amdgcn_s_memrealtime(); }

DEV void wait_flags(rsrc_t rs, u32 off, u32 want, bool& dead) {
    if (dead) return;
#pragma unroll 1
    for (int n = 0;; ++n) {
        CFENCE();
        const u32 v = ld4(rs, off);
        if (all_(off >= OOB || v >= want)) break;
        __builtin_amdgcn_s_sleep(1);
        if ((n & 63) == 63) { if (uni((int)ld4(rs, foff(F_ERR))) != 0) { dead = true; break; } }
        if (n > SPIN_CAP) { st4s(rs, foff(F_ERR), 0, 1u); dead = true; break; }
    }
    CFENCE();
}

DEV bool boot_ok(const Params& p, u32 off, v4u v, u32 want) { return all_(off >= OOB || (v.x == p.e0 && v.y == p.e1 && v.z == p.e2 && v.w >= want)); }
DEV void boot_set(const Params& p, rsrc_t rs, u32 off, u32 v) { st16(rs, off, (v4u){p.e0, p.e1, p.e2, v}); }
DEV u32 boot_wait(const Params& p, rsrc_t rs, u32 off, u32 want, bool& dead) {
    v4u v;
#pragma unroll 1
    for (int n = 0;; ++n) {
        CFENCE();
        v = ld16s(rs, off, 0);
        if (boot_ok(p, off, v, want) || dead) break;
        __builtin_amdgcn_s_sleep(1);
        if (n > SPIN_CAP) { st4s(rs, foff(F_ERR), 0, 1u); dead = true; }
    }
    CFENCE();
    return v.w;
}

DEV u32 pol_off() { const int lane = threadIdx.x & 63; return lane < 32 ? foff(F_RG + lane) : OOB; }
DEV u32 pol_of(u32 raw) {
    const int lane = threadIdx.x & 63;
    const float v = add16(__builtin_amdgcn_fmed3f((float)((int)raw - (1 << 20)), -250.f, 250.f));
    const float lo = dpp<0x121>(v), hi = dpp<0x12F>(v), ex = dpp<0x122>(v) + dpp<0x123>(v) + dpp<0x124>(v);
    const float sum = v + lo + ((lane & 15) == 11 ? 0.f : hi) + ((lane & 15) == 0 ? ex : 0.f);
    return (u32)__builtin_amdgcn_ballot_w64(sum >= 0.f) & 0xFFFFu;
}
DEV u32 get_pol(const Params& p, rsrc_t rs, bool& dead) { return pol_of(boot_wait(p, rs, pol_off(), 0u, dead)); }
DEV u32 hblk(u32 base, int ls, int h, u32 side, u32 pol) { return near_(base + (u32)(ls * NH + h) * 16384u, side, pol); }
DEV f32x2 ln_stats(const f32x2 (&x)[5]) {
    const float c = f_(uu(u_(x[0].x)));
    f32x2 d[5];
#pragma unroll
    for (int i = 0; i < 5; ++i) d[i] = x[i] - c;
    const f32x2 s2 = (d[0] + d[1]) + (d[2] + d[3]) + d[4], q = (d[0] * d[0] + d[1] * d[1]) + (d[2] * d[2] + (d[3] * d[3] + d[4] * d[4]));
    const float dm = wave_sum(s2.x + s2.y) * (1.0f / DM), mean = c + dm;
    const float var = fmaxf(wave_sum(q.x + q.y) * (1.0f / DM) - dm * dm, 0.f);
    const float sd = rbf(__builtin_amdgcn_sqrtf(rbf(rbf(var) + 1e-5f)));
    return (f32x2){rbf(mean), __builtin_amdgcn_rcpf(sd)};
}
DEV void store_h(rsrc_t rs, u32 tile, int row, const u32 (&hp)[5]) {
    const u32 lane = threadIdx.x & 63;
    const u32 sw = (u32)(row & 15) * 16u, o8 = (lane >> 5) * 16384u + (((lane & 31u) * 8u) ^ sw), hb = tile + (u32)(row & 31) * 256u;
    st8s(rs, o8, hb, (v2u){hp[0], hp[1]});
    st8s(rs, o8, hb + 32768, (v2u){hp[2], hp[3]});
    st4s(rs, (lane * 4) ^ sw, hb + 65536, hp[4]);
}

#define DMA16(ldsb, vo, so) asm volatile("s_mov_b32 m0, %0\n\ts_nop 0\n\tbuffer_load_dwordx4 %1, %2, %3 offen sc1 lds" :: "s"(ldsb), "v"(vo), "s"(srd), "s"(so) : "memory")
#define DMA16I(ldsb, lo, vo, so, go) do { u32 so_; asm volatile("s_add_u32 m0, %1, %5\n\ts_add_u32 %0, %4, %6\n\tbuffer_load_dwordx4 %2, %3, %0 offen sc1 lds" \
                         : "=&s"(so_) : "s"(ldsb), "v"(vo), "s"(srd), "s"(so), "i"(lo), "i"(go) : "memory", "scc"); } while (0)
#define DMA32(ldsb, vo, so) asm volatile("s_mov_b32 m0, %0\n\ts_nop 0\n\tbuffer_load_dwordx4 %1, %2, %3 offen sc1 lds\n\tbuffer_load_dwordx4 %1, %2, %3 offen offset:1024 sc1 lds" \
                         :: "s"(ldsb), "v"(vo), "s"(srd), "s"(so) : "memory")
#define INPOL "nt"
#define XPOL ""
#define INAUX 2
#define DMANT(sr, ldsb, vo, so) asm volatile("s_mov_b32 m0, %0\n\ts_nop 0\n\tbuffer_load_dwordx4 %1, %2, %3 offen " INPOL " lds" :: "s"(ldsb), "v"(vo), "s"(sr), "s"(so) : "memory")
#define DMAIN(sr, ldsb, vo, so) asm volatile("s_mov_b32 m0, %0\n\ts_nop 0\n\tbuffer_load_dwordx4 %1, %2, %3 offen lds" :: "s"(ldsb), "v"(vo), "s"(sr), "s"(so) : "memory")
DEV const u16* sel(bool c, const u16* a, const u16* b) { PINS(a); PINS(b); return c ? a : b; }
DEV v4u mk_srd(const void* q, u32 bytes) { return (v4u){uu((u32)(size_t)q), uu((u32)((size_t)q >> 32) & 0xffffu), bytes, 0x00020000u}; }
#define MK_SRD() const v4u srd = mk_srd(p.ws, W_END)

constexpr int CV_PAD = 40;
constexpr int CV_PL = 4416;
constexpr int CV_F = 8 * CV_PL;
constexpr int CV_KB = 80;
constexpr int CV_KV = 65 * CV_KB;
constexpr int CV_CH = CV_F + CV_KV;
static_assert(CV_CH % 16 == 0 && 4 * CV_CH <= LDS_BYTES, "conv lds");

struct Filt { v4u a[5], b[5]; };
DEV int chan(int grp, int w) { return 16 * (grp >> 2) + (grp & 3) + 4 * w; }
template <int N = 5, int AUX = INAUX> DEV void filt_load(const Params& p, int grp, Filt& f) {
    const int tid = threadIdx.x, lane = tid & 63, d = chan(grp, tid >> 6);
    const rsrc_t rf = mk_rsrc(p.ssmk, ACT);
#pragma unroll
    for (int i = 0; i < N; ++i) {
        const u32 s0 = (u32)(8 * (lane + 64 * i) - CV_PAD);
        f.a[i] = __builtin_amdgcn_raw_buffer_load_b128(rf, s0 < SEQ ? (u32)(d * SEQ + s0) * 2u : OOB, 0, AUX);
        f.b[i] = __builtin_amdgcn_raw_buffer_load_b128(rf, s0 + 8 < SEQ ? (u32)(d * SEQ + s0 + 8) * 2u : OOB, 0, AUX);
    }
}
constexpr int KT_BYTES = 18 * 1024, KT_X = 64 * 144, NKV = 44, KT_DUMP = 8 * KT_BYTES + KT_X;
DEV void kv_fetch(const Params& p, int tile, u32 dst) {
    const int lane = threadIdx.x & 63, w = uni(threadIdx.x >> 6);
    const v4u sk = mk_srd(p.bk, ACT), sv = mk_srd(p.bv, ACT), ss = mk_srd(sel(w == 0, p.bk, p.shift), w == 0 ? ACT : DM * 8);
    const u32 vo = tile < 320 ? (u32)((lane >> 3) * DM + (tile % 10) * 64 + (lane & 7) * 8) * 2u : OOB, r0 = uu((u32)((tile / 10) * 64 + 8 * w) * (DM * 2)), d = uu(dst + w * 1024);
    DMANT(sk, d + 1024, vo, r0); DMANT(sk, d + 5120, vo, r0 + 32 * DM * 2); DMANT(sv, d + 9216, vo, r0); DMANT(sv, d + 13312, vo, r0 + 32 * DM * 2);
    DMAIN(ss, uu(w == 0 ? dst : w == 1 ? dst + 17408 : KT_DUMP), w == 0 ? (tile >= 10 ? vo : OOB) : w == 1 && lane < 32 ? (u32)lane * 16u : OOB,
          uu(w == 0 ? (tile >= 10 ? r0 - 8 * DM * 2 : 0u) : (u32)(tile % 10) * 512u));
}
DEV void kv_tile(const Params& p, rsrc_t rs, lptr src, lptr xt, int tile) {
    const int tid = threadIdx.x;
    const v4u sv = lds_r16(src + 17408 + (tid & 31) * 16);
    lptr kp = src + ((tid >> 5) * 8 + 5) * 128 + (tid & 31) * 4;
    f32x2 kf[11];
#pragma unroll
    for (int i = 0; i < 11; ++i) kf[i] = up2(*(LAS u32*)(kp + i * 128));
    const f32x2 sh[4] = {{lo_f(sv.x), lo_f(sv.z)}, {hi_f(sv.x), hi_f(sv.z)}, {lo_f(sv.y), lo_f(sv.w)}, {hi_f(sv.y), hi_f(sv.w)}};
    u32 kv[8];
#pragma unroll
    for (int i = 0; i < 8; ++i) {
        const f32x2 acc = rb2(rb2(rb2(rb2(kf[i] * sh[3]) + rb2(kf[i + 1] * sh[2])) + rb2(kf[i + 2] * sh[1])) + rb2(kf[i + 3] * sh[0]));
        kv[i] = pk2(acc * up2(*(LAS u32*)(kp + 9216 + (i - 5) * 128)));
    }
    lds_w16(xt + (2 * (tid & 31)) * 144 + (tid >> 5) * 16, (v4u){__builtin_amdgcn_perm(kv[6], kv[7], 0x05040100u), __builtin_amdgcn_perm(kv[4], kv[5], 0x05040100u),
                                                             __builtin_amdgcn_perm(kv[2], kv[3], 0x05040100u), __builtin_amdgcn_perm(kv[0], kv[1], 0x05040100u)});
    lds_w16(xt + (2 * (tid & 31) + 1) * 144 + (tid >> 5) * 16, (v4u){__builtin_amdgcn_perm(kv[6], kv[7], 0x07060302u), __builtin_amdgcn_perm(kv[4], kv[5], 0x07060302u),
                                                                 __builtin_amdgcn_perm(kv[2], kv[3], 0x07060302u), __builtin_amdgcn_perm(kv[0], kv[1], 0x07060302u)});
    BAR();
#pragma unroll
    for (int i = 0; i < 2; ++i) {
        const int ch = i * 32 + (tid >> 3);
        st16(rs, W_KVT + (u32)(((tile % 10) * 64 + ch) * SEQ + (tile / 10) * 64 + (tid & 7) * 8) * 2u, lds_r16(xt + ch * 144 + (tid & 7) * 16));
    }
}
template <int MODE> DEV void conv_job(const Params& p, rsrc_t rs, lptr lds, int grp, Filt& f, int next) {
    const int tid = threadIdx.x, lane = tid & 63, w = uni(tid >> 6), bq = lane & 31, hh = lane >> 5;
    lptr F = lds + w * CV_CH;
    lptr fa = F + (bq & 7) * CV_PL + ((bq >> 3) - hh + 4) * 16;
    lptr kb = F + CV_F + CV_KB + hh * 16, kh = kb + (bq + 32) * CV_KB;
    bool dead = false;
    v4u kvr[4];
    const u32 D = p.ssmD[chan(grp, w)];
    auto kv_load = [&]() {
        if (MODE == 0) boot_wait(p, rs, lane < NKV ? foff(F_KV + lane) : OOB, 2u, dead);
#pragma unroll
        for (int i = 0; i < (MODE == 1 ? 2 : 4); ++i) kvr[i] = ld16s(rs, (u32)lane * 16u, W_KVT + (u32)(chan(grp, w) * SEQ + i * 512) * 2u);
    };
    auto windows = [&](int i) {
        if (i < 4 || lane < 16) {
            const u32 e[8] = {f.a[i].x, f.a[i].y, f.a[i].z, f.a[i].w, f.b[i].x, f.b[i].y, f.b[i].z, f.b[i].w};
            u32 o[7];
#pragma unroll
            for (int q = 0; q < 7; ++q) o[q] = __builtin_amdgcn_alignbit(e[q + 1], e[q], 16);
#pragma unroll
            for (int q = 0; q < 4; ++q) {
                lds_w16(F + (2 * q) * CV_PL + (64 * i + lane) * 16, (v4u){o[q], o[q + 1], o[q + 2], o[q + 3]});
                lds_w16(F + (2 * q + 1) * CV_PL + (64 * i + lane) * 16, (v4u){e[q + 1], e[q + 2], e[q + 3], e[q + 4]});
            }
        }
    };
    lptr yw = lds + (lane >> 4) * CV_CH + 3072 + (lane & 14) * 64 + w * 32 + (lane & 1) * 16, yr = F + 3072 + lane * 16;
    const u32 yo = W_Y + (u32)((((w & 1) * 8 + (lane >> 3)) * 8 + (w >> 1)) * DM + (grp & 3) * 160 + 4 * (grp >> 2)) * 32u + (u32)(lane & 7) * 16u;
    v4u dg[2];
    auto skip = [&](lptr k) { const f32x16 z = {}; return mfma32(lds_r16(k + 32), dg[1], mfma32(lds_r16(k), dg[0], z)); };
    auto pack = [&](const f32x16& ac, const f32x16& sk, int q) {
        const int r = 4 * (q & 3);
        u32 v[4];
#pragma unroll
        for (int i = 0; i < 4; ++i) v[i] = pk2(rb2((f32x2){ac[r + 2 * i], ac[r + 2 * i + 1]}) + rb2((f32x2){sk[r + 2 * i], sk[r + 2 * i + 1]}));
        const auto x = __builtin_amdgcn_permlane32_swap(v[0], v[2], false, false), y = __builtin_amdgcn_permlane32_swap(v[1], v[3], false, false);
        return (v4u){x[0], y[0], x[1], y[1]};
    };
    if (MODE != 2) EV(8);
    if (MODE != 0) kv_load();
    if (lane < CV_KB / 16) lds_w16(F + CV_F + lane * 16, (v4u){0u, 0u, 0u, 0u});
    windows(0);
    if (MODE != 1) { windows(1); windows(2); windows(4); }
    if (MODE == 0) kv_load();
    if (MODE == 2) windows(3);
    if (MODE != 2) EV(9);
#pragma unroll
    for (int i = 0; i < (MODE == 1 ? 2 : 4); ++i) lds_w16(F + CV_F + (16 * i + (lane >> 2) + 1) * CV_KB + (lane & 3) * 16, kvr[i]);
#pragma unroll
    for (int ks = 0; ks < 2; ++ks)
#pragma unroll
        for (int q = 0; q < 4; ++q) { const int j = 16 * ks + 8 * hh + 7 - bq; dg[ks][q] = j == 2 * q ? D : j == 2 * q + 1 ? D << 16 : 0u; }
    f32x16 acc0 = {}, acc1 = {};
    auto first = [&]() { asm volatile("s_waitcnt vmcnt(0)\n\ts_barrier" ::: "memory"); st4s(rs, tid == 0 ? foff(F_CV + grp) : OOB, 0, 1u); EV(12); };
    if (MODE != 2) {
        EV(10);
        v4u fr[2][4];
        auto rd = [&](int m) { lptr kl = kb + max(bq - m, -1) * CV_KB; fr[m & 1][0] = lds_r16(kl); fr[m & 1][1] = lds_r16(fa + m * 64); fr[m & 1][2] = lds_r16(kl + 32); fr[m & 1][3] = lds_r16(fa + m * 64 - 32); };
        rd(0); rd(1);
        const f32x16 sk = skip(kb + bq * CV_KB);
#pragma unroll
        for (int m = 0; m < 8; ++m) {
            acc0 = mfma32(fr[m & 1][0], fr[m & 1][1], acc0); acc0 = mfma32(fr[m & 1][2], fr[m & 1][3], acc0);
            if (m < 6) rd(m + 2);
            SCHED();
        }
        lds_w16(yw, pack(acc0, sk, 0));
        BAR();
        st16(rs, w < 2 ? yo : OOB, lds_r16(yr));
        EV(11);
    }
    if (MODE == 1) {
        windows(1); windows(2);
        first();
        v4u a[2][2], l[2][2];
#pragma unroll
        for (int u = 0; u < 2; ++u) { lptr kl = kb + max(bq - 8 - u, -1) * CV_KB; a[u][0] = lds_r16(fa + (8 + u) * 64); l[u][0] = lds_r16(kl); a[u][1] = lds_r16(fa + (8 + u) * 64 - 32); l[u][1] = lds_r16(kl + 32); }
#pragma unroll 1
        for (int m = 8; m < 32; m += 2) {
#pragma unroll
            for (int u = 0; u < 2; ++u) {
                lptr kl = kb + max(bq - m - u - 2, -1) * CV_KB;
                acc0 = mfma32(l[u][0], a[u][0], acc0); a[u][0] = lds_r16(fa + (m + u + 2) * 64); l[u][0] = lds_r16(kl); SCHED();
                acc0 = mfma32(l[u][1], a[u][1], acc0); a[u][1] = lds_r16(fa + (m + u + 2) * 64 - 32); l[u][1] = lds_r16(kl + 32); SCHED();
            }
        }
    } else {
        if (MODE == 0) windows(3); else filt_load<5, 0>(p, next, f);
        v4u a0 = lds_r16(fa), a1 = lds_r16(fa - 32), c0 = lds_r16(fa + 2048), c1 = lds_r16(fa + 2048 - 32);
        v4u h0 = lds_r16(kh), h1 = lds_r16(kh + 32), l0 = lds_r16(kb + bq * CV_KB), l1 = lds_r16(kb + bq * CV_KB + 32);
#pragma unroll 2
        for (int m = 0; m < (MODE == 0 ? 8 : 32); ++m) {
            lptr kl = kb + max(bq - m - 1, -1) * CV_KB;
            if (MODE == 0 && m == 4) first();
            acc1 = mfma32(h0, a0, acc1); h0 = lds_r16(kh - (m + 1) * CV_KB); a0 = lds_r16(fa + (m + 1) * 64); SCHED();
            acc1 = mfma32(l0, c0, acc1); c0 = lds_r16(fa + (m + 1) * 64 + 2048); l0 = lds_r16(kl); SCHED();
            acc1 = mfma32(h1, a1, acc1); h1 = lds_r16(kh - (m + 1) * CV_KB + 32); a1 = lds_r16(fa + (m + 1) * 64 - 32); SCHED();
            acc1 = mfma32(l1, c1, acc1); c1 = lds_r16(fa + (m + 1) * 64 + 2048 - 32); l1 = lds_r16(kl + 32); SCHED();
        }
        if (MODE == 0) {
#pragma unroll 2
            for (int m = 8; m < 32; ++m) {
                lptr kl = kb + max(bq - m - 1, -1) * CV_KB;
                acc1 = mfma32(h0, a0, acc1); h0 = lds_r16(kh - (m + 1) * CV_KB); SCHED();
                acc0 = mfma32(l0, a0, acc0); a0 = lds_r16(fa + (m + 1) * 64); SCHED();
                acc1 = mfma32(l0, c0, acc1); c0 = lds_r16(fa + (m + 1) * 64 + 2048); l0 = lds_r16(kl); SCHED();
                acc1 = mfma32(h1, a1, acc1); h1 = lds_r16(kh - (m + 1) * CV_KB + 32); SCHED();
                acc0 = mfma32(l1, a1, acc0); a1 = lds_r16(fa + (m + 1) * 64 - 32); SCHED();
                acc1 = mfma32(l1, c1, acc1); c1 = lds_r16(fa + (m + 1) * 64 + 2048 - 32); l1 = lds_r16(kl + 32); SCHED();
            }
        }
    }
    if (MODE != 2) EV(13);
    v4u yy[4];
    if (MODE != 2) { const f32x16 sk = skip(kb + bq * CV_KB); yy[0] = pack(acc0, sk, 0); yy[1] = pack(acc0, sk, 2); }
    if (MODE != 1) { const f32x16 sk = skip(kh); yy[2] = pack(acc1, sk, 0); yy[3] = pack(acc1, sk, 2); BAR(); }
#pragma unroll
    for (int q = (MODE == 2 ? 2 : 0); q < (MODE == 1 ? 2 : 4); ++q) lds_w16(yw + q * CV_PL, yy[q]);
    BAR();
#pragma unroll
    for (int q = (MODE == 2 ? 2 : 0); q < (MODE == 1 ? 2 : 4); ++q) st16(rs, q == 0 && w < 2 ? OOB : yo + (u32)q * (2 * DM * 32), lds_r16(yr + q * CV_PL));
    BAR();
    if (MODE != 2) EV(14);
}

#define NPROBE 6
DEV int probe(rsrc_t rs, int c) {
    const int lane = threadIdx.x & 63;
    const u32 k = (u32)(c & 15) < NREG ? (u32)(c & 15) : 0u;
    const u32 off0 = k * (16 * MB) + 2 * MB - 65536 + ((u32)(c & 15) >= NREG ? (u32)(c & 3) + 1u : 0u) * 256 + (u32)(c >> 4) * 128;
    st4s(rs, lane == 0 ? off0 : OOB, 0, 0u); DRAIN();
    int d0 = 1 << 16, d1 = 1 << 16;
#pragma unroll 1
    for (int q = 0; q < NPROBE; ++q) {
        const u32 o = lane == 0 ? off0 + (u32)(q & 1) * 8192 + (u32)(q >> 1) * 18432 : OOB;
        const unsigned long long t0 = __builtin_amdgcn_s_memtime();
        st4s(rs, o, 0, 0u); DRAIN();
        const int dt = (int)(__builtin_amdgcn_s_memtime() - t0);
        if (q & 1) d1 = min(d1, dt); else d0 = min(d0, dt);
    }
    const u32 xcc = (u32)__builtin_amdgcn_s_getreg(20 | (3 << 11)) & 0xfu;
    return (d0 - d1) * ((xcc & 4u) ? -1 : 1);
}
DEV void pre_rings(const Params& p, rsrc_t rs, int j) {
    const u32 gt = (u32)(j * 256 + threadIdx.x) * 16u, n = ((gt >> 14) << 9) + ((gt & 8191u) >> 4);
    const v4u ps = {PSENT, PSENT, PSENT, PSENT};
    v4u m = ps;
    if (n < 640u) m = *(const v4u*)(p.mem0 + (2 * ((n >> 2) & 3) + (n & 1)) * DM + (n >> 4) * 16 + (n & 2) * 4);
#pragma unroll
    for (u32 i = 0; i < 8; ++i) st16(rs, W_ORING + gt + i * 131072u, ps);
#pragma unroll
    for (u32 i = 0; i < 2; ++i) st16(rs, gt < 16384u ? W_MRING + i * (MB / 4) + 131072u + gt : OOB, ps);
    if (n < 640u) {
        m = (v4u){__builtin_amdgcn_perm(m.z, m.x, 0x05040100u), __builtin_amdgcn_perm(m.z, m.x, 0x07060302u), __builtin_amdgcn_perm(m.w, m.y, 0x05040100u), __builtin_amdgcn_perm(m.w, m.y, 0x07060302u)};
#pragma unroll
        for (int i = 0; i < 4; ++i) m[i] = m[i] == PSENT ? 0x7FC17FC1u : m[i];
    }
    st16(rs, W_MRING + gt, m);
    st16(rs, W_MRING + MB / 4 + gt, m);
    static_assert(2 * (F_N - F_QKV) == 32 * 36, "clear");
    st4s(rs, threadIdx.x < 36 ? foff(F_QKV) + (u32)(j * 36 + threadIdx.x) * 128u : OOB, 0, 0u);
}

#define M32A(first, d, a, b) do { if (first) asm volatile("v_mfma_f32_32x32x16_bf16 %0, %1, %2, 0" : "=&v"(d) : "a"(a), "v"(b)); \
                                  else asm volatile("v_mfma_f32_32x32x16_bf16 %0, %1, %2, %0" : "+v"(d) : "a"(a), "v"(b)); } while (0)
#define M32B(first, d, a, b) do { if (first) asm volatile("v_mfma_f32_32x32x16_bf16 %0, %1, %2, 0" : "=&v"(d) : "v"(a), "a"(b)); \
                                  else asm volatile("v_mfma_f32_32x32x16_bf16 %0, %1, %2, %0" : "+v"(d) : "v"(a), "a"(b)); } while (0)
#define VCVT(d, a, b) asm volatile("v_cvt_pk_bf16_f32 %0, %1, %2" : "=v"(d) : "v"(a), "v"(b))
#define LD4A(d, vo, so) asm volatile("buffer_load_dword %0, %1, %2, %3 offen sc1" : "=v"(d) : "v"(vo), "s"(srd), "s"(so) : "memory")
DEV u32 wst_src(int lane, int ld) { return (u32)((lane >> 4) * ld + 8 * ((lane & 15) ^ ((lane >> 4) << 2))) * 2u; }
DEV u32 wst_rd(int lane, int w) { const int q4 = (lane >> 2) & 3; return (u32)(q4 * 256 + ((w ^ q4) * 64) + ((lane >> 4) & 1) * 32 + (lane & 3) * 8); }

constexpr int QK_A = 65536;
#define QLEAD 4
#define QFIRST 3
static_assert(QFIRST < QLEAD, "a round of the lead carries the flag of the convolution");
#define QW0 10
#define QW1 3
#define QSAMP 72
DEV void role_qkv(const Params& p, lptr lds, int c) {
    const int grp = 64 + c;
    const int tid = threadIdx.x, lane = tid & 63, w = uni(tid >> 6), hh = lane >> 5, r32 = lane & 31;
    const int m = c >> 5, h = c & 31;
    const rsrc_t rs = mk_rsrc(p.ws, W_END);
    v4u wt[40];
    Filt flt;
    bool dead = false;
    u32 pol, h2flag, fo, ol, ar[8];
    const u32 ds = uu(w * 1024), dx = ds + QK_A;
#define XRD(ks) lds_r16(lds + ar[(ks) & 7] + ((ks) >> 3) * 8192)
    const u32 t16 = (u32)tid * 16u, lnf = tid < 96 ? foff(F_LN + tid) : OOB;
    MK_SRD();
#define QDMA(i, dst, vo, so) DMA16I(dst, (i) * 4096, vo, so, ((i) >> 1) * 16384 + ((i) & 1) * 4096)
    {
        const v4u sw = mk_srd(sel(m == 0, p.Wq, sel(m == 1, p.Wk, p.Wv)), DM * NQ * 2), sx = mk_srd(p.x, ACT);
        const u32 vo = wst_src(lane, NQ) + (u32)h * (DH * 2), x8 = (u32)lane * 8u, x4 = (u32)lane * 4u;
        v2u xa[6], xb[6]; u32 xc[6];
#pragma unroll
        for (int i = 0; i < 6; ++i)
            asm volatile("buffer_load_dwordx2 %0, %3, %5, %6 offen " XPOL "\n\tbuffer_load_dwordx2 %1, %3, %5, %6 offen offset:512 " XPOL "\n\tbuffer_load_dword %2, %4, %5, %6 offen offset:1024 " XPOL
                         : "=&v"(xa[i]), "=&v"(xb[i]), "=&v"(xc[i]) : "v"(x8), "v"(x4), "s"(sx), "s"(uu((4 * c + w + 384 * i) * (DM * 2))) : "memory");
        EV(1);
        auto wfill = [&](int i0, int n) {
#pragma unroll 1
            for (int i = i0; i < i0 + n; ++i) DMAIN(sw, uu((4 * i + w) * 1024), vo, uu((16 * i + 4 * w) * (NQ * 2)));
        };
        wfill(0, QW0);
        asm volatile("s_waitcnt vmcnt(%0) ; HZCLR" :: "i"(QW0) : "memory");
        EV(2);
#pragma unroll
        for (int i = 0; i < 6; ++i) {
            wfill(QW0 + QW1 * i, QW1);
            if (i == 2) { VMW(2 * QW1 + 3); BAR(); boot_set(p, rs, tid == 0 ? foff(F_LN + c) : OOB, 1u); }
            asm volatile("" : "+v"(xa[i]), "+v"(xb[i]), "+v"(xc[i]));
            const int row = 4 * c + w + 384 * i;
            if (i < 5 || row < SEQ) {
                const f32x2 x[5] = {up2(xa[i].x), up2(xa[i].y), up2(xb[i].x), up2(xb[i].y), up2(xc[i])};
                const f32x2 st = ln_stats(x);
                u32 hp[5];
#pragma unroll
                for (int q = 0; q < 5; ++q) hp[q] = pk2(up2(pk2(x[q] - st.x)) * st.y);
                store_h(rs, W_H1 + (u32)(row >> 5) * H_TILE, row, hp);
            }
        }
        EV(3);
        constexpr int K0 = QW0 + 6 * QW1;
        static_assert(K0 * 4096 >= QK_A + 40960, "the first tile's place is free");
        v4u fv, praw;
        asm volatile("buffer_load_dwordx4 %0, %2, %4, 0 offen sc1\n\tbuffer_load_dwordx4 %1, %3, %4, 0 offen sc1" : "=&v"(praw), "=&v"(fv) : "v"(pol_off()), "v"(lnf), "s"(srd) : "memory");
        wfill(K0, 40 - K0);
        asm volatile("s_waitcnt vmcnt(%2) ; HZCLR\n\ts_barrier" : "+v"(praw), "+v"(fv) : "i"(40 - K0) : "memory");
        boot_set(p, rs, tid == 0 ? foff(F_LN + c) : OOB, 2u);
        EV(4);
        lptr tb = lds + hh * 2048 + wst_rd(lane, w);
        SFOR(k, 40)
            if constexpr (k == K0) {
                if (!boot_ok(p, lnf, fv, 1u)) boot_wait(p, rs, lnf, 1u, dead);
                if (!boot_ok(p, pol_off(), praw, 0u)) praw.w = boot_wait(p, rs, pol_off(), 0u, dead);
                BAR();
                EV(5);
                const u32 so = W_H1;
                SFOR(i, 10) QDMA(i, dx, t16, so); SEND
                pol = pol_of(praw.w);
                h2flag = tid < 8 ? near_(foff(F_H2 + lane), 1u, pol) : OOB;
                ol = (m == 2 ? (u32)(2 * w + (r32 >> 4)) * 1024u : (u32)((r32 >> 4) * 4 + w) * 1024u) + (u32)(hh * 16 + (r32 & 15)) * 16u;
                fo = near_(foff(F_QKV + m * 64 + h), 0u, pol);
#pragma unroll
                for (int j = 0; j < 8; ++j) { ar[j] = (u32)(QK_A + r32 * 256 + ((2 * j + hh) ^ (r32 & 15)) * 16); PIN(ar[j]); }
                PIN(h2flag); PIN(ol); PINS(fo); PINS(pol);
                VMW(10); BAR();
            }
            lptr q = tb + (k >> 3) * 32768 + (k & 7) * 4096; wt[k] = tr2(q, q + 1024); asm volatile("" : "+a"(wt[k]));
        SEND
    }
    EV(6);
    int n0 = -1, n1 = -QLEAD;
    f32x16 a0 = {};
    const u32 ksc = m == 1 ? 0x3e0293eeu : 0x3f800000u;
    const u32 bfo = tid < 96 ? lnf : tid - 128u < NKV ? foff(F_KV - 128 + tid) : OOB;
    v4u brk = {};
    auto run = [&](auto isv) __attribute__((always_inline)) {
      constexpr bool V = decltype(isv)::value;
      v4u x[8];
      u32 fv = 0, fo0 = tid == 192 ? fo : n1 > -QLEAD && tid == 194 ? foff(F_CV + grp) : OOB, fo1 = tid == 192 ? fo : tid == 193 ? fo + 32 * 128 : OOB;
      PIN(fo0); PIN(fo1);
#pragma unroll
      for (int ks = 20; ks < 25; ++ks) x[ks & 7] = XRD(ks);
#define QS(k) do {} while (0)
#define QPK1(d, a, a_, q_) do { if constexpr (V) VCVT(d[q_], a[4 * (a_) + 8 * ((q_) >> 1) + 2 * ((q_) & 1)], a[4 * (a_) + 8 * ((q_) >> 1) + 2 * ((q_) & 1) + 1]); else VCVT(d[q_], a[4 * (a_) + (q_)], a[4 * (a_) + (q_) + 8]); } while (0)
#define QSC(a, i) asm volatile("v_mul_f32 %0, %2, %0\n\tv_mul_f32 %1, %2, %1" : "+v"(a[i]), "+v"(a[(i) + 8]) : "s"(ksc))
#define QPK(d, a, a_, q_) do { if constexpr (!V) { QSC(a, 4 * (a_) + (q_)); QSC(a, 4 * (a_) + (q_) + 1); } QPK1(d, a, a_, q_); QPK1(d, a, a_, (q_) + 1); } while (0)
#pragma unroll 1
      for (const int end = n1 == -QLEAD ? QFIRST - QLEAD : NSEG; n1 < end; ++n1) {
        const int cur = n1 < 0 ? n0 + 1 : 64 | n1, nxt = n1 < 0 ? n0 + 2 : n0 + 1;
        u32 xs = n1 < 0 ? W_H1 + (u32)cur * H_TILE : near_(W_H2 + (u32)n1 * H_TILE, 1u, pol);
        PINS(xs);
        QS(0);
        if (w == 0 && n1 >= 0 && ((u32)__builtin_amdgcn_ballot_w64(fv <= (u32)n1) & 255u) != 0u) wait_flags(rs, h2flag, (u32)n1 + 1, dead);
        BAR();
        QS(1);
        f32x16 a1;
        u32 ss, sv, hb, oa, qfo, qfv;
        v4u pp, pq;
        SCHED();
        auto slot = [&](auto zc) __attribute__((always_inline)) {
            constexpr int z = decltype(zc)::value, ks = (z + 20) % 40;
            if constexpr (z < 20) { if constexpr (V) M32B(false, a0, x[ks & 7], wt[ks]); else M32A(false, a0, wt[ks], x[ks & 7]); }
            else if constexpr (z < 60) { if constexpr (V) M32B(z == 20, a1, x[ks & 7], wt[ks]); else M32A(z == 20, a1, wt[ks], x[ks & 7]); }
            else if constexpr (z == 60) {
                if constexpr (V) asm volatile("v_mfma_f32_32x32x16_bf16 %0, %1, %2, 0" : "+v"(a0) : "v"(x[ks & 7]), "a"(wt[ks]));
                else asm volatile("v_mfma_f32_32x32x16_bf16 %0, %1, %2, 0" : "+v"(a0) : "a"(wt[ks]), "v"(x[ks & 7]));
            } else { if constexpr (V) M32B(false, a0, x[ks & 7], wt[ks]); else M32A(false, a0, wt[ks], x[ks & 7]); }
            if constexpr (z == 18) { QS(2); asm volatile("s_waitcnt vmcnt(6) lgkmcnt(0)\n\ts_barrier" ::: "memory"); QS(3); }
            if constexpr (z < 15 || z > 20) x[(ks + 5) & 7] = XRD((ks + 5) % 40);
            if constexpr (z >= 18 && z <= 20) { x[2 * (z - 18)] = XRD(2 * (z - 18)); x[2 * (z - 18) + 1] = XRD(2 * (z - 18) + 1); }
            SCHED();
            if constexpr (z < 4) QDMA(z, dx, t16, xs);
            if constexpr (z >= 5 && z < 17 && (z & 1)) QDMA((z + 3) >> 1, dx, t16, xs);
            if constexpr ((z >= 7 && z < 15) || (z >= 47 && z < 55)) asm volatile("v_xor_b32 %0, 0x10000, %0" : "+v"(ar[(ks + 5) & 7]));
            if constexpr (z == 16) { ss = W_H1 + (u32)nxt * H_TILE; PINS(ss); }
            if constexpr (z == 17) { sv = t16 | (nxt >= NSEG ? OOB : 0u); PIN(sv); }
            if constexpr (z >= 21 && z < 41 && (z & 1)) QDMA((z - 21) >> 1, ds, sv, ss);
            if constexpr (z == 22 || z == 24) QPK(pp, a0, 0, z - 22);
            if constexpr (z == 26 || z == 28) QPK(pq, a0, 1, z - 26);
            if constexpr (z == 30) { QS(4); asm volatile("s_waitcnt vmcnt(5)\n\ts_barrier" ::: "memory"); QS(5); }
            if constexpr (z == 32 || z == 44) { hb = (m == 2 ? W_VO : W_KQ) + (u32)((z == 32 ? n0 : cur) * NH + h) * 16384u; PINS(hb); }
            if constexpr (z == 34 || z == 45) { hb = near_(hb, m == 1 ? 0u : 1u, pol); PINS(hb); }
            if constexpr (z == 36) { hb = (u32)n0 < NSEG ? hb : OOB; PINS(hb); }
            if constexpr (z == 38 || z == 46) { oa = ol + hb; PIN(oa); }
            if constexpr (z == 41) st16(rs, oa, pp);
            if constexpr (z == 43) st16(rs, oa + 512, pq);
            if constexpr (z == 54) { QS(6); asm volatile("s_waitcnt vmcnt(4)\n\ts_barrier" ::: "memory"); QS(7); }
            if constexpr (z == 56) { if (__builtin_expect(n1 == QFIRST - QLEAD - 1, 0)) { filt_load<3, 0>(p, grp, flt); brk = ld16s(rs, bfo, 0); } }
            if constexpr (z == 60) NOPS(5);
            if constexpr (z == 63) st16(rs, oa, pp);
            if constexpr (z == 65) st16(rs, oa + 512, pq);
            if constexpr (z == 61 || z == 62) QPK(pp, a1, 0, 2 * (z - 61));
            if constexpr (z == 63 || z == 64) QPK(pq, a1, 1, 2 * (z - 63));
            if constexpr (z == 67) { qfo = n1 >= 0 ? fo1 : fo0; PIN(qfo); }
            if constexpr (z == 68) { qfv = tid == 193 ? (u32)n1 + 1u : (u32)min(nxt, NSEG); PIN(qfv); }
            if constexpr (z == QSAMP) LD4A(fv, h2flag, 0);
            SCHED();
        };
        if (__builtin_expect(n0 >= 0, 1)) static_for<0, 16>(slot);
        static_for<16, 80>(slot);
        QS(8);
        asm volatile("s_waitcnt vmcnt(1)\n\ts_barrier" ::: "memory");
        st4s(rs, qfo, 0, qfv);
        QS(9);
        asm volatile("s_waitcnt vmcnt(1)" : "+v"(fv) :: "memory");
        n0 = nxt;
      }
      NOPS(7);
    };
#pragma unroll 1
    for (;;) {
        if (m == 2) run(IC<1>{}); else run(IC<0>{});
        if (n1 > 0) break;
        if (!boot_ok(p, bfo, brk, 2u)) boot_wait(p, rs, bfo, 2u, dead);
        BAR();
        conv_job<1>(p, rs, lds, grp, flt, 0);
        const u32 so = uu(W_H1 + (u32)n0 * H_TILE);
        SFOR(i, 6) QDMA(i + 4, ds, t16, so); SEND
        DRAIN(); BAR();
    }
}

constexpr int OU_SEL = 131072;
#define OSAMP 46

DEV void role_out(const Params& p, lptr lds, int L, int kq, int ng) {
    const int tid = threadIdx.x, lane = tid & 63, w = uni(tid >> 6), hh = lane >> 5, r32 = lane & 31;
    const rsrc_t rs = mk_rsrc(p.ws, W_END);
    v4u wo[64];
    Filt flt;
    bool dead = false;
    {
        const v4u sw = mk_srd(p.Wo, DM * NQ * 2);
        const u32 vo = wst_src(lane, DM) + (ng == 4 ? 1024u : (u32)((ng >> 1) * 512 + (ng & 1) * 128 + ((lane ^ (lane >> 2)) & 8) * 16));
        auto fill = [&](int b) {
#pragma unroll 1
            for (int i = 0; i < 8; ++i) DMAIN(sw, uu((b % 5) * 32768 + (4 * i + w) * 1024), vo, uu((kq * 1024 + b * 128 + 16 * i + 4 * w) * (DM * 2)));
        };
#pragma unroll 1
        for (int b = 0; b < 5; ++b) fill(b);
        lptr tb = lds + hh * 1024 + wst_rd(lane, w);
        SFOR(b, 8)
            VMW(b < 3 ? 32 : 66 - 8 * b); BAR();
            lptr q = tb + (b % 5) * 32768;
#pragma unroll
            for (int k = 0; k < 8; ++k) { lptr a = q + (k >> 1) * 8192 + (k & 1) * 2048; wo[8 * b + k] = tr2(a, a + 4096); if (b < 7) asm volatile("" : "+a"(wo[8 * b + k])); else PIN(wo[8 * b + k]); }
            if constexpr (b < 3) { BAR(); fill(b + 5); }
            if constexpr (b == 2) filt_load(p, L == 0 ? 44 + kq * 5 + ng : 1 << 12, flt);
        SEND
    }
    EV(6);
    const u32 pol = get_pol(p, rs, dead);
    if (L == 0) {
        BAR();
        conv_job<0>(p, rs, lds, 44 + kq * 5 + ng, flt, 0);
        DRAIN(); BAR();
        st4s(rs, tid == 0 ? foff(F_CV + 44 + kq * 5 + ng) : OOB, 0, 2u);
    }
    EV(15);
    MK_SRD();
    const u32 ofl = (lane < 8 && w == 0) ? near_(foff(F_O + L * 32 + kq * 8 + lane), 0u, pol) : OOB;
    const u32 l16 = (u32)lane * 16u;
    const u32 fo = (tid == 192) ? near_(foff(F_P + L * 32 + kq * 5 + ng), 1u, pol) : OOB;
    auto osrc = [&](int s) { return uu(hblk(W_VO, L * NSEG + s, kq * 8, 0u, pol) + w * 1024); };
#define ODMA(i, dst, vo, so) DMA16I(dst, (i) * 4096, vo, so, ((i) >> 1) * 16384 + ((i) & 1) * 4096)
    const u32 pvo = (u32)(kq * 5 + ng) * 16384u + (u32)w * 2048u + l16;
    lptr tw = lds + OU_SEL + 256 + r32 * 272 + (ng == 4 ? hh * 32 + w * 4 : (w & 1) * 128 + hh * 16 + (w >> 1) * 8), tr = lds + OU_SEL + 256 + (8 * w + (lane >> 4)) * 272 + (lane & 15) * 16;
    lptr selw = lds + OU_SEL + (w == 0 ? 0 : 64);
    u32 pfo = OOB;
    u32 buf = 0;
    if (w == 0) wait_flags(rs, ofl, 1u, dead);
    BAR();
    for (int s = 0; s < NSEG; ++s) {
        u32 src = osrc(s), dst = uu(buf + w * 1024);
        lptr A = lds + buf + l16;
        asm volatile("" : "+s"(src), "+s"(dst));
#define OS(k) do {} while (0)
        OS(0);
        OS(1);
        SFOR(i, 10) ODMA(i, dst, l16, src); SEND
        OS(2);
        asm volatile("s_waitcnt vmcnt(7)\n\ts_barrier" ::: "memory");
        OS(3);
        LS2(L == 0 && w == 1, 96 + kq * 5 + ng);
        v4u o[64];
#pragma unroll
        for (int k = 0; k < 3; ++k) o[k] = lds_r16(A + k * 1024);
        st4s(rs, pfo, 0, (u32)s);
        u32 fv, pa; int rdy;
        f32x16 acc;
        SCHED();
        SFOR(k, 64)
            if constexpr (k < 56) M32A(k == 0, acc, wo[k], o[k]); else asm volatile("v_mfma_f32_32x32x16_bf16 %0, %1, %2, %0" : "+v"(acc) : "v"(wo[k]), "v"(o[k]));
            if constexpr (k < 61) o[k + 3] = lds_r16(A + (k + 3) * 1024);
            SCHED();
            if constexpr (k < 12 && (k & 1) == 0) ODMA(10 + (k >> 1), dst, l16, src);
            if constexpr (k == 7) OS(4);
            if constexpr (k == 8) OS(5);
            if constexpr (k == 23) OS(6);
            if constexpr (k == 24) OS(7);
            if constexpr (k == 43) OS(8);
            if constexpr (k == 44) OS(9);
            if constexpr (k == 59) OS(10);
            if constexpr (k == 63) OS(11);
            if constexpr (k == 7) asm volatile("s_waitcnt vmcnt(8)\n\ts_barrier" ::: "memory");
            if constexpr (k == 23) asm volatile("s_waitcnt vmcnt(4)\n\ts_barrier" ::: "memory");
            if constexpr (k == 30) { pa = pvo + near_(p_item(s), (u32)L, pol); PIN(pa); }
            if constexpr (k == 43) asm volatile("s_waitcnt vmcnt(0)\n\ts_barrier" ::: "memory");
            if constexpr (k == OSAMP) LD4A(fv, ofl, 0);
            if constexpr (k == 62) {
                asm volatile("s_waitcnt vmcnt(0)" : "+v"(fv) :: "memory");
                *(LAS int*)selw = (int)((u32)__builtin_amdgcn_ballot_w64(fv < (u32)s + 2) & 255u);
            }
            SCHED();
        SEND
        asm volatile("s_nop 11" : "+v"(acc));
        u32 pc[8];
#pragma unroll
        for (int i = 0; i < 8; ++i) pc[i] = pk(acc[2 * i], acc[2 * i + 1]);
        if (ng != 4) {
#pragma unroll
            for (int i = 0; i < 4; ++i) *(LAS v2u*)(tw + i * 32) = (v2u){pc[2 * i], pc[2 * i + 1]};
        } else {
#pragma unroll
            for (int i = 0; i < 8; ++i) *(LAS u32*)(tw + (i >> 1) * 64 + (i & 1) * 16) = pc[i];
        }
        BAR();
        const v4u p0 = lds_r16(tr), p1 = lds_r16(tr + 4 * 272);
        rdy = *(LAS int*)(lds + OU_SEL);
        st16(rs, pa, p0); st16(rs, pa + 1024, p1);
        const bool ready = uni(rdy) == 0;
        SCHED();
        OS(12);
        if (ready) pfo = fo;
        else {
            LD4A(fv, ofl, 0);
            asm volatile("s_waitcnt vmcnt(1)\n\ts_barrier" ::: "memory");
            st4s(rs, fo, 0, (u32)s + 1); pfo = OOB;
            OS(14);
            LS2(L == 0 && w == 1, 160 + kq * 5 + ng);
            asm volatile("s_waitcnt vmcnt(1)" : "+v"(fv) :: "memory");
            if (w == 0 && s + 1 < NSEG && ((u32)__builtin_amdgcn_ballot_w64(fv < (u32)s + 2) & 255u) != 0u) wait_flags(rs, ofl, (u32)s + 2, dead);
            BAR();
        }
        OS(13);
        buf ^= 65536u;
    }
}

template <int L> DEV void role_row(const Params& p, lptr lds, int ri) {
    const int lane = threadIdx.x & 63, w = uni(threadIdx.x >> 6);
    const rsrc_t rs = mk_rsrc(p.ws, W_END);
    bool dead = false;
    const u32 pol = get_pol(p, rs, dead);
    const u32 fp = threadIdx.x < 20 ? near_(foff(F_P + L * 32 + lane), 1u, pol) : OOB, fo = threadIdx.x == 192 && L == 0 ? near_(foff(F_H2 + (ri >> 2)), 1u, pol) : OOB;
    u32 fv = 0;
    MK_SRD();
    auto yfill = [&](int b, int i) { DMA32(uu((b & 1) * 40960 + w * 10240 + i * 2048), (u32)lane * 16u, uu(W_Y + (u32)((ri >> 1) * 8 + b) * 20480u + (w & 1) * 10240 + i * 2048)); };
    auto ywait = [&](u32 v) {
        wait_flags(rs, threadIdx.x < (v == 3u ? 16u : 160u) ? foff(F_CV + (v == 3u ? 160u : 0u) + threadIdx.x) : OOB, v, dead);
    };
    v4u y8[10] = {};
    v2u xa, xb; u32 xc;
    const v4u sx = L == 0 ? mk_srd(p.x, ACT) : mk_srd(p.ws + W_X2, ACT);
    auto xload = [&](int s) {
        asm volatile("buffer_load_dwordx2 %0, %3, %5, %6 offen sc1\n\tbuffer_load_dwordx2 %1, %3, %5, %6 offen offset:512 sc1\n\tbuffer_load_dword %2, %4, %5, %6 offen offset:1024 sc1"
                     : "=&v"(xa), "=&v"(xb), "=&v"(xc) : "v"((u32)lane * 8u), "v"((u32)lane * 4u), "s"(sx), "s"(uu((u32)(s * 32 + ri) * (DM * 2))) : "memory");
        SCHED();
    };
    ywait(1u);
    BAR();
    EV(15);
#pragma unroll 1
    for (int i = 0; i < 5; ++i) yfill(0, i);
    if (L == 1) wait_flags(rs, lane == 0 ? near_(foff(F_H2 + (ri >> 2)), 1u, pol) : OOB, 1u, dead);
    xload(0);
    asm volatile("s_waitcnt vmcnt(0)" : "+v"(xa), "+v"(xb), "+v"(xc) :: "memory");
    BAR();
    for (int s = 0; s < NSEG; ++s) {
        u32 xi[5] = {xa.x, xa.y, xb.x, xb.y, xc};
        u32 pa = near_(p_item(s), (u32)L, pol) + (u32)ri * 256u, pc = pa + 65536u + (u32)(lane >> 4) * 81920u + (u32)(lane & 15) * 16u;
        pa += (u32)(lane >> 5) * 81920u + (u32)((lane >> 4) & 1) * 16384u + (u32)(lane & 15) * 16u;
        u32 yf = s == 1 ? foff(F_CV + min(threadIdx.x, 159u)) : s == 25 ? foff(F_CV + 160 + (lane & 15)) : OOB;
        asm volatile("" : "+v"(xi[0]), "+v"(xi[1]), "+v"(xi[2]), "+v"(xi[3]), "+v"(xi[4]), "+v"(pa), "+v"(pc), "+v"(yf));
#define RS(k) do {} while (0)
        RS(0);
        if (w == 0 && ((u32)__builtin_amdgcn_ballot_w64(fv <= (u32)s) & 0xFFFFFu) != 0u) wait_flags(rs, fp, (u32)s + 1, dead);
        BAR();
        RS(1);
        v4u d[5];
        asm volatile("buffer_load_dwordx4 %0, %5, %7, 0 offen sc1\n\tbuffer_load_dwordx4 %1, %5, %7, %8 offen sc1\n\tbuffer_load_dwordx4 %2, %5, %7, %9 offen sc1\n\t"
                     "buffer_load_dwordx4 %3, %5, %7, %10 offen sc1\n\tbuffer_load_dwordx4 %4, %6, %7, 0 offen sc1"
                     : "=&v"(d[0]), "=&v"(d[1]), "=&v"(d[2]), "=&v"(d[3]), "=&v"(d[4]) : "v"(pa), "v"(pc), "s"(srd), "s"(163840), "s"(32768), "s"(196608) : "memory");
        RS(2);
        LS2(L == 0, 192 + ri);
        SCHED();
        f32x2 y[5];
        {
            if ((s & 7) == 0) {
                lptr ya = lds + ((s >> 3) & 1) * 40960 + (w >> 1) * 20480 + (w & 1) * 16, yc = ya + 4096 + (lane >> 1) * 32 + (lane & 1) * 10240;
#pragma unroll
                for (int i = 0; i < 8; ++i) y8[i] = lds_r16(ya + lane * 32 + (i >> 2) * 2048 + (i & 3) * 5120);
                y8[8] = lds_r16(yc); y8[9] = lds_r16(yc + 5120);
            }
#pragma unroll
            for (int i = 0; i < 10; ++i) {
                y[i >> 1][i & 1] = lo_f(y8[i].x);
                y8[i] = (v4u){__builtin_amdgcn_alignbit(y8[i].y, y8[i].x, 16), __builtin_amdgcn_alignbit(y8[i].z, y8[i].y, 16), __builtin_amdgcn_alignbit(y8[i].w, y8[i].z, 16), y8[i].w >> 16};
                PIN(y8[i]);
            }
            asm volatile("" : "+v"(y[0]), "+v"(y[1]), "+v"(y[2]), "+v"(y[3]), "+v"(y[4]));
        }
        u32 yv;
        {
            xload(s + 1);
            const bool yy = (s & 7) - 2u < 5u && s < NSEG - 8;
            u32 yo = yy ? (u32)lane * 16u : OOB;
            DMA32(uu(yy ? (((s >> 3) + 1) & 1) * 40960 + w * 10240 + ((s & 7) - 2) * 2048 : 81920), yo, uu(W_Y + (u32)((ri >> 1) * 8 + (s >> 3) + 1) * 20480u + (w & 1) * 10240 + ((s & 7) - 2) * 2048));
            LD4A(yv, yf, 0);
            asm volatile("s_waitcnt vmcnt(6)" : "+v"(d[0]), "+v"(d[1]), "+v"(d[2]), "+v"(d[3]), "+v"(d[4]));
        }
        f32x2 o[5];
        {
            f32x2 t[5][2];
#pragma unroll
            for (int i = 0; i < 5; ++i) {
                const auto e = __builtin_amdgcn_permlane32_swap(d[i].x, d[i].z, false, false), f = __builtin_amdgcn_permlane32_swap(d[i].y, d[i].w, false, false);
                t[i][0] = up2(e[0]) + up2(e[1]); t[i][1] = up2(f[0]) + up2(f[1]);
            }
            o[0] = t[0][0] + t[1][0]; o[1] = t[0][1] + t[1][1]; o[2] = t[2][0] + t[3][0]; o[3] = t[2][1] + t[3][1];
            const auto e = __builtin_amdgcn_permlane16_swap(u_(t[4][0].x), u_(t[4][1].x), false, false), f = __builtin_amdgcn_permlane16_swap(u_(t[4][0].y), u_(t[4][1].y), false, false);
            o[4] = (f32x2){f_(e[0]) + f_(e[1]), f_(f[0]) + f_(f[1])};
        }
        f32x2 x1[5];
#pragma unroll
        for (int i = 0; i < 5; ++i) x1[i] = up2(pk2(up2(xi[i]) + up2(pk2(o[i]))));
        asm volatile("" : "+v"(x1[0]), "+v"(x1[1]), "+v"(x1[2]), "+v"(x1[3]), "+v"(x1[4]));
        RS(3);
        const f32x2 st1 = ln_stats(x1);
        u32 xp[5];
#pragma unroll
        for (int i = 0; i < 5; ++i) xp[i] = pk2(x1[i] + ((x1[i] - st1.x) * st1.y) * y[i]);
        const int row = s * 32 + ri;
        RS(4);
        if (L == 0) {
            const u32 ro = (u32)row * (DM * 2);
            f32x2 x2[5];
#pragma unroll
            for (int i = 0; i < 5; ++i) x2[i] = up2(xp[i]);
            st8s(rs, (u32)lane * 8, W_X2 + ro, (v2u){xp[0], xp[1]});
            st8s(rs, (u32)lane * 8 + 512, W_X2 + ro, (v2u){xp[2], xp[3]});
            st4s(rs, (u32)lane * 4 + 1024, W_X2 + ro, xp[4]);
            SCHED();
            const f32x2 st2 = ln_stats(x2);
            u32 hp[5];
#pragma unroll
            for (int i = 0; i < 5; ++i) hp[i] = pk2((x2[i] - st2.x) * st2.y);
            store_h(rs, near_(W_H2 + (u32)s * H_TILE, 1u, pol), row, hp);
        } else {
            u16* o_ = p.out + (size_t)row * DM;
            *(v2u*)(o_ + 4 * lane) = (v2u){xp[0], xp[1]};
            *(v2u*)(o_ + 256 + 4 * lane) = (v2u){xp[2], xp[3]};
            *(u32*)(o_ + 512 + 2 * lane) = xp[4];
        }
        RS(5);
        LD4A(fv, fp, 0);
        if (L == 0) asm volatile("s_waitcnt vmcnt(1)\n\ts_barrier" ::: "memory");
        st4s(rs, fo, 0, (u32)s + 1);
        RS(6);
        VMW(1); SCHED();
        asm volatile("" : "+v"(fv), "+v"(yv), "+v"(xa), "+v"(xb), "+v"(xc));
        if (__builtin_expect(s == 1 || s == 25, 0) && !all_(yv >= (s == 1 ? 2u : 3u))) ywait(s == 1 ? 2u : 3u);
        RS(7);
        if (ri == 0) LST(320 + L * 64 + s, rt32());
        LS2(L == 0, 224 + ri);
    }
}

constexpr float SCALE = 0.08838834764831845f, LOG2E = 1.4426950408889634f;
constexpr int C_K = 0, C_V = 8192, C_QX = 16384, C_MEM = 24576, C_Q = C_MEM + 10240, C_KM = C_Q + 2304, C_VM = C_KM + 2304, C_DUMP = C_VM + 2048,
              C_ZERO = C_DUMP + 1024, C_WL = C_ZERO + 128;
constexpr int NLF = 14;
constexpr int C_MASK = C_WL + 4 * NLF * 2048;
static_assert(C_WL % 16 == 0 && C_MASK + 3072 <= LDS_BYTES, "head lds");
#define CH_VIS 10
#define CH_H0 104
#define CH_W0 82
#define CH_MH 2
#define CH_MW 6
#define CH_REC 8
#define CH_LATE 30
#define CH_SOLO 6
#define CH_KV 12
#define CH_KV0 60
#define CH_M0 6
#define CH_DEC 2u
#define CH_UP 4
#define CH_WX 6
DEV void wait_until(u32 t) {
    unsigned long long c;
    asm volatile("1:\n\ts_memrealtime %0\n\ts_waitcnt lgkmcnt(0)\n\ts_mov_b64 vcc, %0\n\ts_sub_u32 vcc_lo, %1, vcc_lo\n\ts_cmpk_lt_u32 vcc_lo, 400\n\ts_cbranch_scc1 1b" : "=&s"(c) : "s"(uu(t - 1u)) : "vcc", "scc", "memory");
}
#define WAIT_TXT(tmp, t1) "s_memrealtime vcc\n1:\n\ts_waitcnt lgkmcnt(0)\n\ts_sub_u32 " tmp ", " t1 ", vcc_lo\n\ts_memrealtime vcc\n\ts_cmpk_lt_u32 " tmp ", 400\n\ts_cbranch_scc1 1b\n\t"
#define SMF_A(acc, a, b) asm volatile("v_smfmac_f32_16x16x64_bf16 %0, %1, %2, %3" : "+v"(acc) : "v"(a), "a"(b), "v"(idx))
#define SMF_V(acc, a, b) asm volatile("v_smfmac_f32_16x16x64_bf16 %0, %1, %2, %3" : "+v"(acc) : "v"(a), "v"(b), "v"(idx))
#define MF0(d, a, b) asm volatile("v_mfma_f32_16x16x32_bf16 %0, %1, %2, 0" : "=&v"(d) : "v"(a), "v"(b))
#define MF1(d, a, b) asm volatile("v_mfma_f32_16x16x32_bf16 %0, %1, %2, %0" : "+v"(d) : "v"(a), "v"(b))
#define MF0H(d, a, b) asm volatile("v_mfma_f32_16x16x16_bf16 %0, %1, %2, 0" : "=&v"(d) : "v"(a), "v"(b))
#define MF1H(d, a, b) asm volatile("v_mfma_f32_16x16x16_bf16 %0, %1, %2, %0" : "+v"(d) : "v"(a), "v"(b))
DEV v8u tr_frag(lptr p0, lptr p1, lptr p2, lptr p3) { return cat8(tr2(p0, p1), tr2(p2, p3)); }
constexpr int qslot(int ks) { return (ks & 1) && ks < 8 ? ks >> 1 : -1; }
constexpr int kslot(int ks) { return ks < 4 ? 4 + ks : -1; }
constexpr int vslot(int ks) { return ks >= 4 ? 4 + ks : -1; }
DEV u32 ring_map(u32 off, u32 half) { return ((off >> 13) << 14) + (half << 13) + (off & 8191u); }
#define ZRD(x, i) x = __builtin_bit_cast(f32x4, lds_r16(ZP + 16 * (i)))
#define VMAX(d, a, b) asm volatile("v_max_f32 %0, %1, %2" : "=v"(d) : "v"(a), "v"(b))
#define VMAX3(d, a, b, c_) asm volatile("v_max3_f32 %0, %1, %2, %3" : "=v"(d) : "v"(a), "v"(b), "v"(c_))
#define VADD(d, a, b) asm volatile("v_add_f32 %0, %1, %2" : "=v"(d) : "v"(a), "v"(b))
#define VEXP(d, a) asm volatile("v_exp_f32 %0, %1" : "=v"(d) : "v"(a))
#define VFMC(d, a, b) asm volatile("v_fmamk_f32 %0, %1, 0x3e0293ee, %2" : "=v"(d) : "v"(a), "v"(b))
#define VMULC(d, a) asm volatile("v_mul_f32 %0, 0x3e0293ee, %1" : "=v"(d) : "v"(a))
#define SEL(d, a, b) asm volatile("v_cndmask_b32 %0, %1, %2, %3" : "=v"(d) : "v"(a), "v"(b), "s"(himask))
#define QPERM(d, a, q) asm volatile("v_mov_b32_dpp %0, %1 quad_perm:" q " row_mask:0xf bank_mask:0xf" : "=v"(d) : "v"(a))

#define SUMRANGE 0x28000000u
#define HSAMP 71
DEV void role_head(const Params& p, lptr lds, int L, int h) {
    const int tid = threadIdx.x, lane = tid & 63, w = uni(tid >> 6), g = lane >> 4, r = lane & 15;
    const rsrc_t rs = mk_rsrc(p.ws, W_END);
    v8u wq0[10], wq1[10], wk0[10], wk1[10], wv0[10], wv1[10];
    lptr WL = lds + C_WL + w * (NLF * 2048) + lane * 16;
    {
        constexpr int GS = 1056, CS = 48 * GS;
        const v4u sq = mk_srd(p.Wq, DM * NQ * 2), sk = mk_srd(p.Wk, DM * NQ * 2), sv = mk_srd(p.Wv, DM * NQ * 2);
        const u32 vo = (u32)(g * NQ + h * DH + 8 * (r ^ ((g & 2) << 2))) * 2u;
        auto fill = [&](int ch) {
#pragma unroll 1
            for (int i = 0; i < 4; ++i) {
                const u32 dst = uu((ch % 3) * CS + (4 * i + w) * GS), so = uu((ch * 64 + 16 * i + 4 * w) * (NQ * 2));
                DMAIN(sq, dst, vo, so); DMAIN(sk, dst + 16 * GS, vo, so); DMAIN(sv, dst + 32 * GS, vo, so);
            }
        };
        int vote = 0;
        if (L == 1) {
            if (w == 0) vote = probe(rs, h);
            BAR();
            EV(2);
            pre_rings(p, rs, h);
        }
        fill(0); fill(1); fill(2);
        const int q4 = r >> 2;
        lptr tb = lds + (4 * (g >> 1) + q4) * GS + (g & 1) * 512 + ((w * 64 + (r & 3) * 8) ^ ((g & 1) << 7));
        SFOR(ch, 10)
            VMW(ch == 1 || ch == 2 ? 25 : ch < 8 ? 24 : 108 - 12 * ch);
            BAR();
            if constexpr (ch == 0) boot_set(p, rs, tid == 0 && L == 1 ? foff(F_RG + h) : OOB, (u32)((1 << 20) + vote));
            auto frag = [&](int m, int t) { lptr b = tb + (ch % 3) * CS + m * 16 * GS + t * 32; return tr_frag(b, b + 256, b + 8 * GS, b + 8 * GS + 256); };
            wq0[ch] = frag(0, 0); wq1[ch] = frag(0, 1); wk0[ch] = frag(1, 0); wk1[ch] = frag(1, 1); wv0[ch] = frag(2, 0); wv1[ch] = frag(2, 1);
            asm volatile("" : "+a"(wq0[ch]), "+a"(wk0[ch]), "+v"(wv0[ch]), "+v"(wv1[ch]));
            if constexpr (qslot(ch) < 0) asm volatile("" : "+a"(wq1[ch])); else asm volatile("" : "+v"(wq1[ch]));
            if constexpr (kslot(ch) < 0) asm volatile("" : "+a"(wk1[ch])); else asm volatile("" : "+v"(wk1[ch]));
            BAR();
            if constexpr (ch < 7) fill(ch + 3);
        SEND
#pragma unroll
        for (int ch = 0; ch < 10; ++ch) {
            if (qslot(ch) >= 0) { lds_w16(WL + qslot(ch) * 2048, wq1[ch].lo); lds_w16(WL + qslot(ch) * 2048 + 1024, wq1[ch].hi); }
            if (kslot(ch) >= 0) { lds_w16(WL + kslot(ch) * 2048, wk1[ch].lo); lds_w16(WL + kslot(ch) * 2048 + 1024, wk1[ch].hi); }
            if (vslot(ch) >= 0) { lds_w16(WL + vslot(ch) * 2048, wv1[ch].lo); lds_w16(WL + vslot(ch) * 2048 + 1024, wv1[ch].hi); }
        }
        CFENCE();
    }
    const u32 idx = (r & 2) ? 0xEEEEu : 0x4444u;
    const unsigned long long himask = 0xCCCCCCCCCCCCCCCCull;
    bool dead = false;
    EV(6);
    const u32 pol = get_pol(p, rs, dead);
    const u32 pol0 = pol & 1u;
    const u32 l16 = (u32)lane * 16u;
    const u32 mring = W_MRING + (u32)L * (MB / 4) + pol0 * 8192u, hint = mring + 4 * 32768u;
    const u32 fkh = lane < 3 ? near_(foff(F_QKV + lane * 64 + L * 32 + h), 0u, pol) : lane < 6 ? hint + (u32)(lane - 3) * 4u : OOB;
    u32 tm = 0;
    MK_SRD();
    const unsigned long long kmask = __builtin_amdgcn_ballot_w64((hblk(W_KQ, L * NSEG + lane, h, 0u, pol) & 8192u) != 0u),
                             vmask = __builtin_amdgcn_ballot_w64((hblk(W_VO, L * NSEG + lane, h, 1u, pol) & 8192u) != 0u);
    u32 kb = uu(W_KQ + (u32)(L * NSEG * NH + h) * 16384u + w * 2048);
    u32 dk = uu(C_K + w * 2048), dv = dk + (C_V - C_K), dq = dk + (C_QX - C_K), dm = uu(C_MEM + w * 2048), dm2 = uu(w < 2 ? C_MEM + 8192 + w * 1024 : C_DUMP);
    asm volatile("" : "+s"(dk), "+s"(dv), "+s"(dq), "+s"(dm), "+s"(dm2));
    u32 ksrc = kb + ((u32)kmask & 1u) * 8192u, vsrc = kb + (W_VO - W_KQ) + ((u32)vmask & 1u) * 8192u;
    lptr qw = lds + C_Q + (2 * g) * 288 + (32 * w + 2 * r) * 2;
    lptr kw = qw + (C_KM - C_Q);
    lptr qr = lds + C_Q + (2 * (r >> 2) + (r & 1)) * 288 + g * 16;
    lptr LB = lds + l16, VX = lds + C_V + w * 2048 + l16;
    if (tid >= 64 && tid < 70) { const u32 z = tid == 68 ? 0x3F803F80u : tid == 69 ? PSENT : 0u; lds_w16(lds + C_ZERO + (tid - 64) * 16, (v4u){z, z, z, z}); }
    if (w < 3) {
        const u32 ng = u_(-1000000000.0f * LOG2E);
        v4u mk;
#pragma unroll
        for (int i = 0; i < 4; ++i) mk[i] = w == 0 || (w == 1 && 4 * g + i > r) ? ng : 0u;
        lds_w16(lds + C_MASK + w * 1024 + l16, mk);
    }
    u32 zaddr = 0; PIN(zaddr);
    lptr ZP = lds + zaddr + C_ZERO;
#define LF_RD(slot, hf) lf[slot][hf] = lds_r16(WL + (slot) * 2048 + (hf) * 1024)
    BAR();
    const u32 ofl = tid == 192 ? near_(foff(F_O + L * 32 + h), 0u, pol) : tid == 193 && (h & 15) == 0 ? hint + 128u + (u32)h * 8u : OOB;
    u32 oso = (g & 1) ? OOB : W_ORING + (u32)L * (MB / 2) + ring_map((u32)((r >> 3) * 32768 + h * 1024 + w * 256 + (g >> 1) * 128 + (r & 7) * 16), pol0);
    u32 missed = 0, dirty = 1, rep = PSENT;
    asm volatile("" : "+v"(oso), "+v"(qw), "+v"(kw), "+v"(qr), "+v"(VX));
    asm volatile("" : "+s"(ksrc), "+s"(vsrc));
    wait_flags(rs, lane < 3 ? fkh : OOB, 1u, dead);
    EV(7);
    DMA32(dv, l16, vsrc); DMA32(dk, l16, ksrc);
    EV(8);
    auto next_slot = [&](u32 now) {
        const u32 hv = ld4(rs, fkh), per = rdl(hv, 4);
        if (per < 400u) { tm = rdl(hv, 5) - per; for (int n = 0; n < 3 && (int)(tm - now) < 0; ++n) tm += per; }
    };
    if (L == 1) { next_slot(rt32() + CH_KV0); tm -= 24u; }
    for (int s = 0; s < NSEG;) {
        {
            *(LAS u32*)(lds + C_DUMP + 128 + w * 4) = tm;
            VMW(5); BAR();
            st4s(rs, ofl, 0, (l16 & 16u) ? rep : (u32)s);
            const v4u c = lds_r16(lds + C_DUMP + 128) - tm;
            const u32 add = uu((u32)max(max((int)c.x, (int)c.y), max((int)c.z, (int)c.w)));
            tm += add;
            if (__builtin_expect((dirty | add) != 0u, 0)) {
                wait_until(tm - 44u);
                st16(rs, oso, lds_r16(ZP + 80));
            }
        }
        u32 ms = mring + (u32)(s & 3) * 32768u + w * 2048, ms2 = mring + (u32)(s & 3) * 32768u + 16384u + (w & 1) * 1024;
        u32 l3 = w < 2 ? l16 : OOB;
        asm volatile("" : "+s"(ms), "+s"(ms2)); PIN(l3);
        f32x4 aq0, aq1, ak0, ak1;
        v4u lf[NLF][2];
        ZRD(aq0, 0); ZRD(aq1, 1); ZRD(ak0, 2); ZRD(ak1, 3);
#pragma unroll
        for (int i = 0; i < 4; ++i) { LF_RD(i, 0); LF_RD(i, 1); }
        SCHED();
        v4u A[10];
#define HT(k) do {} while (0)
        v4u kx[8], qf[4], kmf[4], vx[2], ones, P;
        float fa, fb, fc, fd, sx[4], t[4], sm0, sm1, tm0, tm1, inv; u32 pa, pb, fl, pmm, tmp = 0; unsigned long long odd;
        f32x4 av0, av1, s0, s1, sm, oa, ob, rsum;
        v2u vm, pm, vh, vl, on[2];
        f32x2 zz[4];
#define LW(sl) cat8(lf[sl][0], lf[sl][1])
#define PQ(ks, tl) do { if constexpr (!(tl)) SMF_A(aq0, A[ks], wq0[ks]); else if constexpr (qslot(ks) < 0) SMF_A(aq1, A[ks], wq1[ks]); else { const v8u lw_ = LW(qslot(ks)); SMF_V(aq1, A[ks], lw_); } } while (0)
#define PK(ks, tl) do { if constexpr (!(tl)) SMF_A(ak0, A[ks], wk0[ks]); else if constexpr (kslot(ks) < 0) SMF_A(ak1, A[ks], wk1[ks]); else { const v8u lw_ = LW(kslot(ks)); SMF_V(ak1, A[ks], lw_); } } while (0)
#define PV(ks, tl) do { if constexpr (!(tl)) SMF_V(av0, A[ks], wv0[ks]); else if constexpr (vslot(ks) < 0) SMF_V(av1, A[ks], wv1[ks]); else { const v8u lw_ = LW(vslot(ks)); SMF_V(av1, A[ks], lw_); } } while (0)
#define KXR(i) kx[i] = lds_r16(LB + C_K + ((i) & 1) * 4096 + ((i) >> 1) * 1024)
#define XW(q_) do { *(LAS u32*)(q_) = pa; *(LAS u32*)((q_) + 288) = pb; } while (0)
        auto slot = [&](auto zc) __attribute__((always_inline)) {
            constexpr int z = decltype(zc)::value;
            constexpr int vs[] = {0, 8, 1, 10, 9, 2, 11, -1, -1, -1, -1, 4, 6, 12, -1, -1, -1, 13, 14, -1, 16, 18, 15, 3, 5, 17, 7, 19};
            if constexpr (z < 12) PQ(z >> 1, z & 1);
            else if constexpr (z < 20) PQ(6 + (z & 3), z >> 4);
            else if constexpr (z < 32) PK((z - 20) >> 1, z & 1);
            else if constexpr (z < 40) PK(6 + (z & 3), (z >> 2) & 1);
            else if constexpr (z < 42) { if constexpr (z == 40) MF0(s0, kx[0], qf[0]); else MF0(s1, kx[1], qf[0]); }
            else if constexpr (z < 48) { if constexpr (z & 1) MF1(s1, kx[z - 40], qf[(z - 40) >> 1]); else MF1(s0, kx[z - 40], qf[(z - 40) >> 1]); }
            else if constexpr (z == 55) MF0(sm, kmf[0], qf[0]);
            else if constexpr (z > 55 && z < 59) MF1(sm, kmf[z - 55], qf[z - 55]);
            else if constexpr (z == 62) MF0(rsum, ones, P);
            else if constexpr (z == 63) MF0(oa, vx[0], P);
            else if constexpr (z == 64) MF0(ob, vx[1], P);
            else if constexpr (z == 67) { const v2u ones2 = ones.lo; MF1H(rsum, ones2, pm); }
            else PV(vs[z - 48] >> 1, vs[z - 48] & 1);
#define USE(x) asm volatile("" :: "v"(x))
            if constexpr (z == 20 || z == 24 || z == 26) USE(lf[4 + ((z - 20) >> 1)][1]);
            if constexpr (z == 41) USE(qf[3]);
            if constexpr (z == 45 || z == 46) USE(kx[z - 39]);
            if constexpr (z == 53) USE(lf[9][1]);
            if constexpr (z >= 54 && z < 58) USE(kmf[z - 54]);
            if constexpr (z == 61) USE(vl);
            if constexpr (z == 64) USE(lf[10][1]);
            if constexpr (z == 69) USE(lf[11][1]);
            if constexpr (z == 72) USE(lf[12][1]);
            if constexpr (z == 74) USE(lf[13][1]);
            if constexpr (z < 8) A[2 + z] = lds_r16(LB + C_MEM + (2 + z) * 1024);
            if constexpr (z >= 15 && z < 23) LF_RD(4 + ((z - 15) >> 1), (z - 15) & 1);
            if constexpr (z >= 24 && z < 27) KXR(z - 24);
            if constexpr (z == 27) XW(qw);
            if constexpr (z >= 28 && z < 31) KXR(z - 25);
            if constexpr (z == 32) asm volatile("s_waitcnt lgkmcnt(3)\n\ts_barrier" ::: "memory");
            if constexpr (z >= 33 && z < 37) qf[z - 33] = lds_r16(qr + (z - 33) * 64);
            if constexpr (z == 37 || z == 38) LF_RD(8, z - 37);
            if constexpr (z == 39 || z == 41) KXR(6 + (z == 41));
            if constexpr (z == 44) XW(kw);
            if constexpr (z == 45 || z == 46) LF_RD(9, z - 45);
            if constexpr (z == 47) vx[0] = lds_r16(VX);
            if constexpr (z == 48) { VMW(0); vh = *(LAS v2u*)(VX + 1032); }
            if constexpr (z == 54) { vl = *(LAS v2u*)(VX + 1024); vx[1] = (v4u){vh.x, vh.y, vl.x, vl.y}; }
            if constexpr (z == 49) { USE(lf[9][0]); asm volatile("s_barrier" ::: "memory"); }
            if constexpr (z >= 50 && z < 54) kmf[z - 50] = lds_r16(qr + (C_KM - C_Q) + (z - 50) * 64);
            if constexpr (z == 59 || z == 60) LF_RD(10, z - 59);
            if constexpr (z == 61 || z == 65) LF_RD(11, z == 65);
            if constexpr (z == 66 || z == 68) LF_RD(12, z == 68);
            if constexpr (z == 69 || z == 70) LF_RD(13, z - 69);
            if constexpr (z == HSAMP) fl = ld4(rs, fkh);
            SCHED();
            if constexpr (z == 19) HT(2);
            if constexpr (z == 39) HT(3);
            if constexpr (z == 51) HT(4);
            if constexpr (z == 64) HT(5);
            if constexpr (z == 20) VADD(fa, aq0[0], aq0[2]);
            if constexpr (z == 21) VADD(fb, aq0[1], aq0[3]);
            if constexpr (z == 23) VADD(fc, aq1[0], aq1[2]);
            if constexpr (z == 24) VADD(fd, aq1[1], aq1[3]);
            if constexpr (z == 25) VCVT(pa, fa, fc);
            if constexpr (z == 26) VCVT(pb, fb, fd);
            if constexpr (z >= 44 && z < 48) asm volatile("v_mov_b64 %0, 0" : "=v"(zz[z - 44]));
            if constexpr (z == 47) { av0 = __builtin_shufflevector(zz[0], zz[1], 0, 1, 2, 3); av1 = __builtin_shufflevector(zz[2], zz[3], 0, 1, 2, 3); }
            if constexpr (z == 48 || z == 49) asm volatile("v_mov_b64 %0, %1" : "=v"(on[z - 48]) : "s"(0x3F803F803F803F80ull));
            if constexpr (z == 49) ones = __builtin_shufflevector(on[0], on[1], 0, 1, 2, 3);
            if constexpr (z == 40) { VADD(fa, ak0[0], ak0[2]); VADD(fb, ak0[1], ak0[3]); }
            if constexpr (z == 42) { VADD(fd, ak1[1], ak1[3]); VADD(fc, ak1[0], ak1[2]); }
            if constexpr (z == 43) { VCVT(pb, fb, fd); VCVT(pa, fa, fc); }
            if constexpr (z >= 50 && z < 54) SEL(sx[z - 50], s0[z - 50], s1[z - 50]);
            if constexpr (z >= 54 && z < 58) VEXP(t[z - 54], sx[z - 54]);
            if constexpr (z == 58) asm volatile("v_cvt_pk_bf16_f32 %0, %2, %3\n\tv_cvt_pk_bf16_f32 %1, %4, %5" : "=&v"(P.x), "=v"(P.y) : "v"(t[0]), "v"(t[1]), "v"(t[2]), "v"(t[3]));
            if constexpr (z == 59) QPERM(P.z, P.x, "[2,3,0,1]");
            if constexpr (z == 60) QPERM(P.w, P.y, "[2,3,0,1]");
            if constexpr (z == 61) { USE(sm); VMULC(sm0, sm[0]); }
            if constexpr (z == 62) { VEXP(tm0, sm0); VMULC(sm1, sm[1]); }
            if constexpr (z == 63) VEXP(tm1, sm1);
            if constexpr (z == 64) asm volatile("v_cvt_pk_bf16_f32 %0, %2, %3\n\tv_cndmask_b32 %1, %0, 0, %4" : "=&v"(pmm), "=v"(pm.x) : "v"(tm0), "v"(tm1), "s"(himask));
            if constexpr (z == 65) asm volatile("v_cndmask_b32 %0, 0, %1, %2" : "=v"(pm.y) : "v"(pmm), "s"(himask));
            if constexpr (z == 67) { SEL(oa[0], oa[0], ob[0]); SEL(oa[1], oa[1], ob[1]); }
            if constexpr (z == 68 || z == 69) SEL(oa[z - 66], oa[z - 66], ob[z - 66]);
            if constexpr (z == 70) asm volatile("v_sub_u32 %0, %1, %2" : "=v"(pa) : "v"(rsum[0]), "s"(0x2B800000u), "v"(rsum));
            if constexpr (z == 71) asm volatile("v_cmp_le_u32 %0, %1, %2" : "=s"(odd) : "s"(SUMRANGE), "v"(pa));
            if constexpr (z == 72) VADD(fa, av0[0], av0[2]);
            if constexpr (z == 73) { NOPS(0); VADD(fb, av0[1], av0[3]); }
            if constexpr (z == 74) VCVT(vm.x, fa, fb);
            SCHED();
        };
        asm volatile("s_mov_b32 m0, %2\n\t" WAIT_TXT("%0", "%1") "buffer_load_dwordx4 %4, %5, %6 offen sc1 lds\n\tbuffer_load_dwordx4 %4, %5, %6 offen offset:1024 sc1 lds\n\t"
                     "s_mov_b32 m0, %3\n\ts_nop 0\n\tbuffer_load_dwordx4 %8, %5, %7 offen sc1 lds\n\ts_sleep 3\n\ts_mov_b32 m0, %9\n\ts_nop 0\n\t"
                     "buffer_load_dwordx4 %4, %5, %10 offen sc1 lds\n\tbuffer_load_dwordx4 %4, %5, %10 offen offset:1024 sc1 lds\n\ts_waitcnt vmcnt(2) lgkmcnt(0)\n\ts_barrier"
                     : "=&s"(tmp) : "s"(uu(tm - 1u)), "s"(dm), "s"(dm2), "v"(l16), "s"(srd), "s"(ms), "s"(ms2), "v"(l3), "s"(dq), "s"(ksrc ^ 8192u) : "vcc", "scc", "memory");
#pragma unroll
        for (int ks = 0; ks < 2; ++ks) A[ks] = lds_r16(LB + C_MEM + ks * 1024);
        SCHED();
        static_for<0, 76>(slot);
        u32 fail = 0;
        if (__builtin_expect(odd != 0ull, 0)) {
            u32 mn = ~0u;
#pragma unroll 5
            for (int i = 0; i < 10; ++i) { const v4u e = lds_r16(LB + C_MEM + i * 1024); mn = min(min(min(mn, e.x ^ PSENT), e.y ^ PSENT), min(e.z ^ PSENT, e.w ^ PSENT)); }
            fail = missed < SPIN_CAP && !all_(mn != 0u) ? OOB : 0u;
            float mx = fmaxf(fmaxf(fmaxf(sx[0], sx[1]), fmaxf(sx[2], sx[3])), fmaxf(sm0, sm1));
            mx = max32(max16(fmaxf(mx, dpp<0x4E>(mx))));
#pragma unroll
            for (int i = 0; i < 4; ++i) t[i] = __builtin_amdgcn_exp2f(sx[i] - mx);
            P.x = pk(t[0], t[1]); P.y = pk(t[2], t[3]); P.z = u_(dpp<0x4E>(f_(P.x))); P.w = u_(dpp<0x4E>(f_(P.y)));
            pmm = pk(__builtin_amdgcn_exp2f(sm0 - mx), __builtin_amdgcn_exp2f(sm1 - mx));
            pm = (r & 2) ? (v2u){0u, pmm} : (v2u){pmm, 0u};
            asm volatile("s_nop 1" : "+v"(P), "+v"(pm));
            MF0(rsum, ones, P); MF0(oa, vx[0], P); MF0(ob, vx[1], P);
            NOPS(7);
            { const v2u ones2 = ones.lo; MF1H(rsum, ones2, pm); }
#pragma unroll
            for (int i = 0; i < 4; ++i) SEL(oa[i], oa[i], ob[i]);
            asm volatile("s_nop 4" :: "v"(rsum));
            oso |= fail;
        }
        asm volatile("v_rcp_f32 %0, %3\n\ts_nop 4\n\tv_add_f32 %1, %4, %6\n\tv_add_f32 %2, %5, %7\n\tv_cvt_pk_bf16_f32 %1, %1, %2\n\ts_nop 0"
                     : "=&v"(inv), "=&v"(vm.y), "=&v"(fd) : "v"(rsum[0]), "v"(av1[0]), "v"(av1[1]), "v"(av1[2]), "v"(av1[3]));
        asm volatile("v_mfma_f32_16x16x16_bf16 %0, %1, %2, %0\n\ts_nop 6" : "+v"(oa) : "v"(vm), "v"(pm));
        {
            v4u oc;
            const f32x2 i2 = {inv, inv};
            const f32x2 u0 = oa.lo * i2, u1 = oa.hi * i2;
            asm volatile("v_cvt_pk_bf16_f32 %0, %4, %5\n\tv_cvt_pk_bf16_f32 %2, %4, %5\n\tv_cvt_pk_bf16_f32 %1, %6, %7\n\tv_cvt_pk_bf16_f32 %3, %6, %7\n\t"
                         "v_permlane16_swap_b32 %0, %2\n\ts_nop 0\n\tv_permlane16_swap_b32 %1, %3"
                         : "=&v"(oc.x), "=&v"(oc.y), "=&v"(oc.z), "=&v"(oc.w) : "v"(u0.x), "v"(u0.y), "v"(u1.x), "v"(u1.y));
            st16(rs, oso, oc);
        }
        SCHED();
        if (__builtin_expect(fail == 0u, 1)) {
        unsigned long long clk;
        asm volatile("s_memrealtime %0" : "=s"(clk));
        HT(6);
        v4u tq[4], tk[8], tv[2], px, pv;
        f32x4 c0, c1, cm, o[4], sum;
        float ex[10], cs0, cs1;
        v2u p0, p1, tvm;
        u32 oo, kvo, oz; bool kvok;
        u32 ln = l16; PIN(ln);
        lptr QX = lds + C_QX + (w & 1) * 4096 + ln, VO = lds + C_V + (w ^ 1) * 2048 + ln, VMX = lds + C_VM + w * 512 + (ln >> 1), MK = lds + C_MASK + (w & 1) * 1024 + ln;
#define TKR(i) tk[i] = lds_r16(LB + C_K + ((i) >> 2) * 4096 + ((i) & 3) * 1024)
#pragma unroll
        for (int i = 0; i < 4; ++i) tq[i] = lds_r16(QX + i * 1024);
        c0 = __builtin_bit_cast(f32x4, lds_r16(MK + 1024));
        TKR(0);
        *(LAS v2u*)VMX = vm;
        SCHED();
        SFOR(i, 12)
            if constexpr (i == 0) MF0(cm, kmf[0], tq[0]); else if constexpr (i < 4) MF1(cm, kmf[i], tq[i]);
            else if constexpr (i < 8) MF1(c0, tk[i - 4], tq[i - 4]); else MF1(c1, tk[i - 4], tq[i - 8]);
            if constexpr (i < 3) TKR(i + 1);
            if constexpr (i == 2) c1 = __builtin_bit_cast(f32x4, lds_r16(MK));
            if constexpr (i >= 3 && i < 7) TKR(i + 1);
            if constexpr (i == 7) { vx[1] = lds_r16(VX + 1024); tv[0] = lds_r16(VO); }
            if constexpr (i == 8) tv[1] = lds_r16(VO + 1024);
            SCHED();
            if constexpr (i == 0) { oo = ln + (ln & 0x300u) + ((vsrc ^ 8192u) + (u32)((w & 1) * 256)); PIN(oo); kb += NH * 16384; PINS(kb); }
            if constexpr (i == 1) { ksrc = kb + (((kmask >> ((s + 1) & 63)) & 1ull) ? 8192u : 0u); PINS(ksrc); }
            if constexpr (i == 3) { vsrc = kb + (((vmask >> ((s + 1) & 63)) & 1ull) ? W_VO - W_KQ + 8192u : W_VO - W_KQ); PINS(vsrc); }
            if constexpr (i == 6) { USE(cm); VMULC(cs0, cm[0]); }
            if constexpr (i == 8) VMULC(cs1, cm[1]);
            if constexpr (i == 9) VEXP(ex[8], cs0);
            if constexpr (i == 10) { VEXP(ex[9], cs1); VEXP(ex[0], c0[0]); }
            if constexpr (i == 11) { VEXP(ex[1], c0[1]); VEXP(ex[2], c0[2]); VEXP(ex[3], c0[3]); }
            SCHED();
        SEND
        asm volatile("v_cvt_pk_bf16_f32 %0, %6, %7\n\tv_mov_b32 %1, 0\n\tv_mov_b32 %2, 0\n\tv_cvt_pk_bf16_f32 %3, %6, %7\n\tv_cvt_pk_bf16_f32 %4, %8, %9\n\tv_cvt_pk_bf16_f32 %5, %10, %11"
                     : "=&v"(p0.x), "=&v"(p0.y), "=&v"(p1.x), "=&v"(p1.y), "=&v"(px.x), "=&v"(px.y) : "v"(ex[8]), "v"(ex[9]), "v"(ex[0]), "v"(ex[1]), "v"(ex[2]), "v"(ex[3]));
        asm volatile("s_waitcnt lgkmcnt(0)\n\ts_barrier" : "+s"(clk) :: "memory");
        tvm = *(LAS v2u*)(VMX + (512 - (w & 1) * 1024));
        pv = lds_r16(ZP + 80);
        SCHED();
#pragma unroll
        for (int i = 0; i < 4; ++i) VEXP(ex[4 + i], c1[i]);
        asm volatile("v_cvt_pk_bf16_f32 %0, %2, %3\n\tv_cvt_pk_bf16_f32 %1, %4, %5\n\ts_nop 1" : "=&v"(px.z), "=&v"(px.w) : "v"(ex[4]), "v"(ex[5]), "v"(ex[6]), "v"(ex[7]));
        {
            const v2u ones2 = ones.lo;
            const u32 now = (u32)clk;
            auto pvm = [&](auto first) __attribute__((always_inline)) {
                constexpr bool F = decltype(first)::value;
                SCHED();
                MF0(sum, ones, px); if constexpr (F) { oz = (oso & ~(3u << 17)) | (u32)((s + 3) & 3) << 17; PIN(oz); } SCHED();
                MF0(o[0], vx[0], px); if constexpr (F) { oso = (oso & ~(3u << 17)) | (u32)((s + 1) & 3) << 17; PIN(oso); } SCHED();
                MF0(o[1], vx[1], px); NOPS(1);
                MF0(o[2], tv[0], px); NOPS(0);
                MF1H(sum, ones2, p0); NOPS(1);
                MF0(o[3], tv[1], px); NOPS(1);
                MF1H(o[0], vm, p0);
                MF1H(o[1], vm, p1);
                MF1H(o[2], tvm, p0);
                asm volatile("v_sub_u32 %0, %2, %3\n\tv_rcp_f32 %1, %2" : "=&v"(pa), "=&v"(inv) : "v"(sum[0]), "s"(0x2B800000u), "v"(sum));
                MF1H(o[3], tvm, p1);
                SCHED();
            };
            pvm(IC<1>{});
            if (__builtin_expect(__builtin_amdgcn_ballot_w64(pa >= SUMRANGE) != 0ull, 0)) {
                const f32x4 m0 = __builtin_bit_cast(f32x4, lds_r16(MK + 1024)), m1 = __builtin_bit_cast(f32x4, lds_r16(MK));
                float mx = fmaxf(cs0, cs1);
#pragma unroll
                for (int i = 0; i < 4; ++i) { ex[i] = m0[i] == 0.f ? c0[i] : m0[i]; ex[4 + i] = m1[i] == 0.f ? c1[i] : m1[i]; mx = fmaxf(mx, fmaxf(ex[i], ex[4 + i])); }
                mx = max32(max16(mx));
#pragma unroll
                for (int i = 0; i < 4; ++i) px[i] = pk(__builtin_amdgcn_exp2f(ex[2 * i] - mx), __builtin_amdgcn_exp2f(ex[2 * i + 1] - mx));
                p0.x = p1.y = pk(__builtin_amdgcn_exp2f(cs0 - mx), __builtin_amdgcn_exp2f(cs1 - mx));
                asm volatile("s_nop 7" : "+v"(px), "+v"(p0), "+v"(p1));
                pvm(IC<0>{});
                NOPS(3);
            }
            asm volatile("s_nop 1" : "+v"(o[0]), "+v"(o[1]));
            st16(rs, oo, (v4u){pk2(o[0].lo * inv), pk2(o[0].hi * inv), pk2(o[1].lo * inv), pk2(o[1].hi * inv)});
            asm volatile("" : "+v"(o[2]), "+v"(o[3]));
            st16(rs, oo ^ 2048u, (v4u){pk2(o[2].lo * inv), pk2(o[2].hi * inv), pk2(o[3].lo * inv), pk2(o[3].hi * inv)});
            SCHED();
            PIN(fl); kvok = ((u32)__builtin_amdgcn_ballot_w64(fl < (u32)s + 2) & 7u) == 0u;
            kvo = l16 | (kvok ? 0u : OOB);
            DMA32(dv, kvo, vsrc); DMA32(dk, kvo, ksrc);
            st16(rs, oz, pv);
            SCHED();
            rep = (u32)s << 16 | min(missed, 1u) << 14 | min(now - tm, 0x3FFFu);
            if (h == 0 && w == 3) { LST(L * 64 + s, now); LST(128 + L * 64 + s, ((int)(rdl(fl, 3) - now) >= CH_VIS - 4) | ((int)kvok << 1) | ((int)min(missed, 3u) << 2)); LST(640 + L * 64 + s, tm); LST(1728 + L * 64 + s, __builtin_amdgcn_s_memtime()); }
            tm = rdl(fl, 5);
            missed = missed == SPIN_CAP ? missed : 0u;
            dirty = 0;
            if (__builtin_expect(rdl(fl, 3) - now + CH_LATE > 4000u && s + 1 < NSEG, 0)) {
                dirty = 1;
#pragma unroll 1
                for (int n = 0; n < 64; ++n) {
                    CFENCE();
                    const u32 hv = ld4(rs, fkh);
                    tm = rdl(hv, 5);
                    if (rdl(hv, 3) - now + CH_LATE <= 4000u) break;
                }
            }
        }
        LS2(L == 0 && w == 0, h);
        if (__builtin_expect(!kvok, 0) && s + 1 < NSEG) {
            st4s(rs, (ln == 0 && w == 0) ? hint + 4096u + (u32)h * 128u : OOB, 0, (u32)s + 1u);
            dirty = 1;
            wait_flags(rs, ln < 48u ? fkh : OOB, (u32)s + 2, dead);
            DMA32(dv, l16, vsrc); DMA32(dk, l16, ksrc);
            const u32 now = rt32() + CH_KV;
            if ((int)(tm - now) < 0) next_slot(now);
        }
        ++s;
        } else {
            u32 ln = l16; PIN(ln);
            oso = (ln & 256u) ? oso : oso & ~OOB;
            const u32 was = tm - tmp;
            tm = rdl(fl, 5); dirty = 1;
            if ((int)(tm - rdl(fl, 4) - was) > 24) tm -= rdl(fl, 4);
            if ((++missed & 7u) == 0u) {
                *(LAS u32*)(lds + C_DUMP + (w == 0 ? 0 : 64)) = ld4(rs, foff(F_ERR));
                BAR();
                missed = uu(*(LAS u32*)(lds + C_DUMP)) != 0u ? SPIN_CAP : missed;
                st4s(rs, missed == SPIN_CAP ? foff(F_ERR) : OOB, 0, 1u);
            }
        }
    }
    DRAIN(); BAR();
    st4s(rs, ofl, 0, (l16 & 16u) ? rep : NSEG);
}

#define WO_NI 4
#define WROT 0
DEV void role_wo(const Params& p, lptr lds, int j) {
    const int tid = threadIdx.x, lane = tid & 63, w = uni(tid >> 6), g = lane >> 4, r = lane & 15;
    const int rg = j & 1, cg = j >> 1, rot = cg & WROT;
    const rsrc_t rs = mk_rsrc(p.ws, W_END);
    v8u wo[8][5];
    {
        const v4u so_ = mk_srd(p.Wo, DM * NQ * 2);
        const int one = (0x3F2FF1F0u >> (4 * (cg & 7))) & 15;
        u32 vo[3];
#pragma unroll
        for (int i = 0; i < 3; ++i) { const int n = 64 * i + lane; vo[i] = n % 6 < 5 ? (u32)((n / 6) * (DM * 2) + cg * 80 + (n % 6) * 16) : ((n / 6) & 3) == one ? (u32)((n / 6) * (DM * 2) + (cg >> 3) * 640 + 304 + rg * 16) : OOB; }
        auto fill = [&](int b) {
            const u32 dst = uu(w * 36864 + (b % 3) * 12288), src = uu((8 * w + ((b + rot) & 7)) * (DH * DM * 2));
#pragma unroll
            for (int i = 0; i < 12; ++i) DMAIN(so_, dst + i * 1024, vo[i % 3], src + (i / 3) * (32 * DM * 2));
        };
#pragma unroll 1
        for (int b = 0; b < 3; ++b) fill(b);
        const int q4 = r >> 2;
        lptr tb = lds + w * 36864 + (32 * (g >> 1) + 16 * (q4 >> 1) + 8 * ((r >> 1) & 1) + 4 * (g & 1) + (q4 & 1)) * 96 + (r & 1) * 8;
        SFOR(b, 8)
            VMW(b < 6 ? 24 : 84 - 12 * b);
#pragma unroll
            for (int t = 0; t < 5; ++t) {
                lptr q = tb + (b % 3) * 12288 + (t == 0 ? 4 : (w + t) & 3) * 16;
                wo[b][t] = tr_frag(q, q + 192, q + 64 * 96, q + 64 * 96 + 192);
                if (t < 4) asm volatile("" : "+a"(wo[b][t])); else asm volatile("" : "+v"(wo[b][t]));
            }
            CFENCE();
            if constexpr (b < 5) fill(b + 3);
        SEND
    }
    BAR();
    EV(6);
    const u32 idx = (r & 2) ? 0xEEEEu : 0x4444u;
    bool dead = false;
    const u32 pol0 = get_pol(p, rs, dead) & 1u;
    const u32 l16 = (u32)lane * 16u;
    u32 oo[8];
#pragma unroll
    for (int i = 0; i < 8; ++i) oo[i] = uu(W_ORING + ring_map((u32)(rg * 32768 + (8 * w + ((i + rot) & 7)) * 1024), pol0));
    const u32 hint = W_MRING + 4 * 32768u + pol0 * 8192u;
    u32 hvo[2];
#pragma unroll
    for (int l = 0; l < 2; ++l) hvo[l] = lane < 2 ? hint + (u32)(l ^ 1) * (MB / 4) + (u32)lane * 4u : lane == 2 ? hint + (u32)l * (MB / 4) + 128u : lane == 3 ? foff(F_ERR) : lane == 4 ? hint + (u32)l * (MB / 4) + 256u :
                 (lane >= 32 && j == 0 && w == 3) ? hint + (u32)l * (MB / 4) + (u32)lane * 128u : OOB;
    const int xo = g * 128 + (r & 7) * 16, O = 5 * cg + (r < 8 ? w : 4), q2 = 2 + (r >> 3);
    lptr xw0 = lds + (r < 8 ? 2048 + w * 4 : ((w + 1) & 3) * 512 + 4) + xo, xw1 = lds + ((w + q2) & 3) * 512 + q2 * 4 + xo;
    lptr xr = lds + (r < 8 ? w : 4) * 512 + xo;
    if (r < 8) { *(LAS u32*)xr = 0u; *(LAS u32*)(xr + 8192) = 0u; }
    const u32 mpo = (r & 4) == 0 && (r < 8 || w == 0) ? W_MRING + ring_map((u32)((O >> 1) * 256 + rg * 128 + (4 * (g & 1) + 2 * (O & 1) + (g >> 1)) * 16 + (r & 3) * 4), pol0) : OOB;
    constexpr u32 PER0 = CH_W0 + CH_MW + CH_H0 + CH_MH + 2 * CH_VIS;
    const bool lead = j == 0 && w == 3;
    u32 hso = OOB;
    MK_SRD();
    u32 t = 0, tn = 0;
    int sn[2] = {0, 0};
    u32 hav = 4 * CH_H0, wav = 4 * CH_W0, mh = 8 * (CH_MH + CH_M0), mw = 8 * (CH_MW + CH_M0), ok[2] = {0u, 0u}, dw[2] = {CH_W0 + CH_VIS + CH_MW + CH_REC, CH_W0 + CH_VIS + CH_MW + CH_REC};
    u32 zero = 0, hv = 0xFFFFu, pst = 1, pdur = 0, pmp = OOB;
    const u32 psent = PSENT;
    lptr sw = lds + 5120 + w * 4;
    bool stop = false;
    asm volatile("" : "+v"(zero), "+v"(sw), "+v"(xw0), "+v"(xw1), "+v"(xr));
    auto turn = [&](auto lc) __attribute__((always_inline)) {
        constexpr int L = decltype(lc)::value, M = L ^ 1;
        u32 now, done, rep, rd, lost, dh, dn, nt, za; v4u hw;
        const int sc = sn[L];
        auto books = [&](auto ic) __attribute__((always_inline)) {
            constexpr int i = decltype(ic)::value;
            if constexpr (i == 0) { rep = max(rdl(hv, 2), rdl(hv, 4)); PINS(rep); }
            if constexpr (i == 1) { lost = (u32)(__builtin_amdgcn_ballot_w64(hv == (u32)sn[M]) >> 32); PINS(lost); }
            if constexpr (i == 2) { lost |= (rep >> 16) ^ (u32)sn[M]; PINS(lost); }
            if constexpr (i == 3) { lost |= ok[M] ^ 2u; rd = rep & 0xFFFFu; PINS(lost); PINS(rd); }
            if constexpr (i == 4) { lost = lost == 0u ? CH_UP * 8u : 0u; PINS(lost); }
            if constexpr (i == 5) { dn = wav >> 2; PINS(dn); pdur = min(pdur, dn + 8u); PINS(pdur); }
            if constexpr (i == 6) { pdur = (done & ok[M]) ? pdur : dn; PINS(pdur); }
            if constexpr (i == 7) { wav += pdur - dn; PINS(wav); }
            if constexpr (i == 8) { dh = hav >> 2; PINS(dh); nt = min(rd, dh + 8u); PINS(nt); }
            if constexpr (i == 9) { nt = rd < 0x3FFFu ? nt : dh; PINS(nt); }
            if constexpr (i == 10) { hav += nt - dh; PINS(hav); }
            if constexpr (i == 11) { mw = max(mw - CH_DEC, CH_MW * 8u); PINS(mw); }
            if constexpr (i == 12) { mh = max(mh - CH_DEC, CH_MH * 8u); PINS(mh); }
            if constexpr (i == 13) { rd = rd >> 14 == 1u ? lost : 0u; PINS(rd); }
            if constexpr (i == 14) { mw += rd; mh += lost - rd; PINS(mw); PINS(mh); }
            if constexpr (i == 15) { ok[M] = done ? 1u : (ok[M] & 1u) << 1; PINS(ok[M]); }
            if constexpr (i == 16) { dh = (2u * hav + mh) / 8u + (sn[1] < 2 ? CH_VIS + CH_SOLO : CH_VIS); PINS(dh); }
            if constexpr (i == 17) { dn = (2u * wav + mw) / 8u + CH_VIS; PINS(dn); }
            if constexpr (i == 18) { nt = dw[L] + dh; PINS(nt); }
            if constexpr (i == 19) { dh += dn; dw[L] = dn; sn[M] += (int)done; PINS(dh); PINS(sn[M]); }
            if constexpr (i == 20) { rd = tn - t + (dh >> 1); PINS(rd); }
            if constexpr (i == 21) { rd = sn[M] + 1 < NSEG ? rd : 0u; PINS(rd); }
            if constexpr (i == 22) { nt = t + (u32)max((int)nt, (int)rd); PINS(nt); }
            if constexpr (i == 23) { hw.x = nt; hw.y = dh; PIN(hw.x); PIN(hw.y); }
            if constexpr (i == 24) { hw.z = nt + dn; hw.w = 0u; PIN(hw); }
            if constexpr (i == 25) st16(rs, hso + L * (MB / 4), hw);
        };
        if (__builtin_expect(sc + 1 < NSEG, 1)) {
            u32 vo = l16 + (L * (MB / 2) + (u32)(sc & 3) * 131072u), mp = mpo + (L * (MB / 4) + (u32)((sc + 1) & 3) * 32768u), zb, hn;
            done = uu(pst) ^ 1u;
            za = done ? pmp : OOB;
            zb = ok[L] == 1u ? OOB : mp;
            pmp = mpo + (L * (MB / 4) + (u32)(sc & 3) * 32768u);
            *(LAS u32*)(sw + L * 8192) = zero;
            f32x4 acc[5] = {};
            asm volatile("" : "+v"(acc[0]), "+v"(acc[1]), "+v"(acc[2]), "+v"(acc[3]), "+v"(acc[4]), "+v"(mp), "+v"(pmp), "+v"(vo), "+v"(za), "+v"(zb));
            PINS(done);
#define WT(k) do {} while (0)
            v4u d[8];
            {
                u32 tmp;
                asm volatile(WAIT_TXT("%1", "%2") "buffer_load_dwordx4 %0, %3, %4, %5 offen sc1\n\ts_waitcnt lgkmcnt(0)"
                             : "=&v"(d[0]), "=&s"(tmp) : "s"(uu(t - 1u)), "v"(vo), "s"(srd), "s"(oo[0]) : "vcc", "scc", "memory");
            }
            WT(0);
            SFOR(i, 7)
                asm volatile("buffer_load_dwordx4 %0, %1, %2, %3 offen sc1" : "=v"(d[i + 1]) : "v"(vo), "s"(srd), "s"(oo[i + 1]) : "memory");
                SCHED();
                static_for<3 * i, 3 * i + 3>(books);
                SCHED();
            SEND
            LD4A(hn, hvo[L], 0);
            st4s(rs, za, 0, psent);
            st4s(rs, zb, 0, psent);
            SCHED();
            WT(1);
            float f0[5], f1[5], xs; unsigned long long bad; v4u sv;
#define WSW(o_) asm volatile("v_permlane32_swap_b32 %0, %1" : "+v"(f0[o_]), "+v"(f1[o_]))
#define WAD(o_) asm volatile("v_add_f32_dpp %0, %1, %0 row_ror:8 row_mask:0xf bank_mask:0xf\n\tv_add_f32_dpp %0, %2, %3 row_ror:8 row_mask:0xf bank_mask:0xc" : "+v"(f0[o_]) : "v"(f1[o_]), "v"(f0[o_ + 1]), "v"(f1[o_ + 1]))
#define WFO(o_) do { VADD(f0[o_], acc[o_][0], acc[o_][2]); VADD(f1[o_], acc[o_][1], acc[o_][3]); } while (0)
#define WOWN(k) do { if constexpr (WO_NI + (k) < 8) SMF_V(acc[4], d[(WO_NI + (k)) & 7], wo[(WO_NI + (k)) & 7][4]); SCHED(); } while (0)
            SFOR(i, 40)
                constexpr int pc = i / 5, oc = i % 5;
                if constexpr (oc == 0) VMW(10 - pc);
                if constexpr (oc < 4) SMF_A(acc[oc], d[pc], wo[pc][oc]); else if constexpr (pc < WO_NI) SMF_V(acc[4], d[pc], wo[pc][4]);
                if constexpr (i == 0) WT(2);
                if constexpr (i == 20) WT(3);
                if constexpr (i == 35) WT(4);
                if constexpr (i < 4) books(IC<21 + i>{});
                if constexpr (i == 6) { xs = 0.f; PIN(xs); }
                if constexpr (i == 36) NOPS(1);
                if constexpr (i == 37) NOPS(2);
                SCHED();
            SEND
            WFO(0); WFO(1);
            asm volatile("v_cmp_u_f32 %0, %1, %2" : "=s"(bad) : "v"(f0[0]), "v"(f1[0]));
            WSW(0); WSW(1); WFO(2); WAD(0); WFO(3);
            *(LAS float*)(xw0 + L * 8192) = f0[0]; SCHED();
            WSW(2); WSW(3);
            if (__builtin_expect(bad != 0ull, 0)) {
                u32 mn = ~0u;
#pragma unroll
                for (int i = 0; i < 8; ++i) { u32 e = d[i].x; PIN(e); mn = min(mn, e ^ PSENT); }
                if (all_(mn != 0u)) {
#pragma unroll
                    for (int i = 0; i < 8; ++i) { v4u e = d[i]; PIN(e); mn = min(min(mn, e.y ^ PSENT), min(e.z ^ PSENT, e.w ^ PSENT)); }
                }
                *(LAS u32*)(sw + L * 8192) = all_(mn != 0u) ? 0u : 1u;
            }
            WAD(2);
            *(LAS float*)(xw1 + L * 8192) = f0[2]; SCHED();
            WOWN(0); WOWN(1); WOWN(2); WOWN(3);
            WT(5);
            f32x4 xa; float nb; u32 cw, mq;
            asm volatile("s_waitcnt lgkmcnt(0)\n\ts_barrier\n\tds_read_b128 %0, %2 offset:%4\n\tds_read_b128 %1, %3 offset:%4+5120\n\ts_nop 3"
                         : "=&v"(xa), "=&v"(sv) : "v"(xr), "v"(zero), "i"(L * 8192) : "memory");
            WT(6);
            WFO(4);
            asm volatile("s_nop 0\n\tv_permlane32_swap_b32 %1, %2\n\ts_nop 1\n\tv_add_f32_dpp %0, %2, %1 row_ror:8 row_mask:0xf bank_mask:0x3\n\ts_waitcnt lgkmcnt(1)" : "+v"(xs), "+v"(f0[4]), "+v"(f1[4]));
            asm volatile("v_add_f32 %0, %0, %4\n\tv_add_f32 %1, %5, %6\n\tv_add_f32 %0, %0, %7\n\tv_add_f32 %0, %0, %1\n\tv_cmp_u_f32 vcc, %0, %0\n\ts_nop 0\n\t"
                         "v_mov_b32_dpp %1, %0 row_shl:4 row_mask:0xf bank_mask:0xf\n\tv_cndmask_b32 %3, %8, %9, vcc\n\tv_cvt_pk_bf16_f32 %2, %0, %1"
                         : "+v"(xs), "=&v"(nb), "=&v"(cw), "=&v"(mq) : "v"(xa[0]), "v"(xa[1]), "v"(xa[2]), "v"(xa[3]), "v"(mp), "v"(OOB) : "vcc");
            st4s(rs, mq, 0, cw);
            SCHED();
            WT(7);
            now = rt32();
            books(IC<25>{});
            asm volatile("s_waitcnt vmcnt(4) lgkmcnt(0)" : "+v"(hn), "+v"(sv));
            hv = hn;
            pst = sv.x | sv.y | sv.z | sv.w;
            if (__builtin_expect(__builtin_amdgcn_ballot_w64(mq != mp && pst == 0u) != 0ull, 0)) st4s(rs, mp, 0, cw);
        } else {
            done = uu(pst) ^ 1u;
            st4s(rs, done ? pmp : OOB, 0, psent);
            static_for<0, 25>(books);
            pst = 1;
            BAR();
            hv = ld4(rs, hvo[L]);
            PIN(hv);
            now = rt32();
        }
        pdur = now - t;
        const u32 per = rdl(hv, 1), th = rdl(hv, 0);
        t = lead ? tn : (int)(th - now) < -(int)(per >> 1) ? th + per : th;
        tn = nt;
        dw[M] += max((int)(now + CH_WX - t), 0);
        stop = stop || rdl(hv, 3) != 0u;
    };
#pragma unroll 1
    for (int k = 0; k < 2 * SPIN_CAP && !stop && (sn[0] + 1 < NSEG || sn[1] + 1 < NSEG); ++k) {
        turn(IC<0>{}); turn(IC<1>{});
        if (__builtin_expect(k != 0, 1)) continue;
        if (j == 0) {
            for (int n = 0; n < SPIN_CAP && !dead && sn[0] == 0; ++n) {
                CFENCE();
                if (all_(ld4(rs, W_ORING + pol0 * 8192u + (u32)w * 16384u + (u32)lane * 128u) != PSENT)) break;
                __builtin_amdgcn_s_sleep(1);
            }
            BAR();
        }
        if (lead) {
            t = rt32() + 56u; tn = t + PER0 / 2 + CH_REC;
            st16(rs, lane < 2 ? hint + (u32)lane * (MB / 4) : OOB, (v4u){lane == 0 ? t : tn, PER0, (lane == 0 ? t : tn) + (CH_W0 + CH_VIS + CH_MW + CH_REC), 0u});
        } else {
            for (int n = 0; n < SPIN_CAP && !dead; ++n) {
                CFENCE();
                const u32 hv = ld4(rs, hvo[1]);
                t = rdl(hv, 0);
                if (rdl(hv, 1) < 400u) break;
                __builtin_amdgcn_s_sleep(1);
            }
        }
        hso = (lead && lane == 0) ? hint : OOB;
        hv = 0xFFFFu; dw[0] = dw[1] = CH_W0 + CH_VIS + CH_MW + CH_REC;
    }
    if (sn[0] + 1 < NSEG || sn[1] + 1 < NSEG) st4s(rs, tid == 0 ? foff(F_ERR) : OOB, 0, 1u);
}

__global__ void __launch_bounds__(256, 1) k_main(Args args) {
    Params p;
    (Args&)p = args;
    lptr lds = (lptr)(u32)16 - 16;
    if (blockIdx.x == 0) LST(256, rt32());
    unsigned long long pc; int len;
    asm volatile("s_getpc_b64 %0\n.Lk_main_here:\n\ts_mov_b32 %1, .Lk_end_here-.Lk_main_here" : "=s"(pc), "=s"(len));
    const v4u code = ldin16(mk_rsrc((const void*)(pc & ~15ull), (u32)max(len, 0)), (((blockIdx.x >> 3) & 31u) * 256u + threadIdx.x) * 16u, 0);
    CFENCE();
    if (threadIdx.x == 0) {
        const v4u e = ld16s(mk_rsrc(p.ws, W_END), foff(F_EP), 0);
        const unsigned long long tag = (unsigned long long)e.y << 32 | (e.x + 1u);
        u32 role = ((u32)__builtin_amdgcn_s_getreg(20 | (3 << 11)) & 7u) * 32u + ((blockIdx.x >> 3) & 31u);
#pragma unroll 1
        for (int n = 0; n < 256; ++n, role = (role + 1u) & 255u)
            if (__hip_atomic_exchange((GAS unsigned long long*)(p.ws + W_SLOT) + role * 8u, tag, __ATOMIC_RELAXED, __HIP_MEMORY_SCOPE_AGENT) != tag) break;
        lds_w16(lds, (v4u){e.x + 1u, e.y, e.z, role});
    }
    __syncthreads();
    const v4u boot = lds_r16(lds);
    p.e0 = uu(boot.x); p.e1 = uu(boot.y); p.e2 = uu(boot.z);
    const int role = uni((int)boot.w), x = role >> 5, i = role & 31;
    asm volatile("" :: "v"(code));
    __syncthreads();
    LST(8192 + blockIdx.x * 16, role);
    if (x < 2) { role_head(p, lds, i >> 4, x * 16 + (i & 15)); __builtin_amdgcn_endpgm(); }
    if (x == 2) { role_wo(p, lds, i); __builtin_amdgcn_endpgm(); }
    const int no = x == 3 ? 30 : x < 6 ? 5 : 0;
    if (x > 3 && i >= 8) { role_qkv(p, lds, (x - 4) * 24 + i - 8); __builtin_amdgcn_endpgm(); }
    const bool isout = i < no;
    const int og = x == 3 ? i / 5 : x + 2, ng = i % 5, L = og >> 2, kq = og & 3;
    const int k = i - no, w = uni(threadIdx.x >> 6);
    const int j = isout ? (L == 1 ? 0 : 44) + kq * 5 + ng : x == 3 ? 36 + k : k < 2 ? 12 + 2 * x + k : x < 6 ? 24 + x : k < 5 ? 10 + 3 * x + k : 15 + 3 * x + k;
    const rsrc_t rs = mk_rsrc(p.ws, W_END);
    wait_until(rt32() + (j < NKV ? ST_KV : ST_OUT));
    if (j < NKV) {
        const int nt = j < 320 - 7 * NKV ? 8 : 7;
#pragma unroll 1
        for (int q = 0; q < 4; ++q) kv_fetch(p, j + NKV * q, uu(q * KT_BYTES));
        Filt f;
        u32 warm[3];
#pragma unroll 1
        for (int q = 0; q < nt; ++q) {
            VMW(15); BAR();
            if (q == 0) EV(2);
            kv_fetch(p, j + NKV * (q + 4), uu(((q + 4) & 7) * KT_BYTES));
            kv_tile(p, rs, lds + q * KT_BYTES, lds + (q == 0 ? 8 : q - 1) * KT_BYTES, j + NKV * q);
        }
        filt_load(p, j, f);
        VMW(10); BAR();
        boot_set(p, rs, threadIdx.x == 0 ? foff(F_KV + j) : OOB, 2u);
        EV(4);
        { bool dead = false; boot_wait(p, rs, pol_off(), 0u, dead); }
#pragma unroll
        for (int q = 0; q < 3; ++q)
            warm[q] = __builtin_amdgcn_raw_buffer_load_b32(mk_rsrc(p.ssmk, ACT), 256u * 4096u + ((u32)((q * NKV + j) * 256) + threadIdx.x) * 64u, 0, 0);
        conv_job<0>(p, rs, lds, j, f, 0);
        asm volatile("" :: "v"(warm[0]), "v"(warm[1]), "v"(warm[2]));
        const int g0 = j < 36 ? 108 + j : 28 + j, g1 = j < 36 ? 160 : 136;
        if (j >= 28) filt_load<5, 0>(p, g0, f);
        DRAIN(); BAR();
        st4s(rs, threadIdx.x == 0 ? foff(F_CV + j) : OOB, 0, 2u);
        if (j >= 28) {
#pragma unroll 1
            for (int g = g0; g < g1; g += 8) conv_job<2>(p, rs, lds, g, f, min(g + 8, 159));
            DRAIN(); BAR();
            st4s(rs, threadIdx.x == 0 ? foff(F_CV + 132 + j) : OOB, 0, 3u);
            if (j >= 36) { EV(15); __builtin_amdgcn_endpgm(); }
        }
    }
    if (!isout) { if (j < 28) role_row<0>(p, lds, (j - 20) * 4 + w); else role_row<1>(p, lds, (j - 28) * 4 + w);
        boot_set(p, rs, j == 28 && threadIdx.x == 0 ? foff(F_EP) : OOB, 0u);
        __builtin_amdgcn_endpgm();
    }
    role_out(p, lds, L, kq, ng);
}

__global__ void k_end() { asm volatile(".Lk_end_here:"); }

extern "C" void kernel_launch(void* const* d_in, const int* in_sizes, int n_in, void* d_out, int out_size, void* d_ws, size_t ws_size,
                              hipStream_t stream) {
    Args p;
    p.x = (const u16*)d_in[0]; p.mem0 = (const u16*)d_in[1]; p.Wq = (const u16*)d_in[2]; p.Wk = (const u16*)d_in[3];
    p.Wv = (const u16*)d_in[4]; p.Wo = (const u16*)d_in[5];
    p.bk = (const u16*)d_in[8]; p.bv = (const u16*)d_in[9]; p.shift = (const u16*)d_in[10]; p.ssmk = (const u16*)d_in[11]; p.ssmD = (const u16*)d_in[12];
    p.out = (u16*)d_out;
    p.ws = (unsigned char*)(((size_t)d_ws + 2 * MB - 1) & ~(size_t)(2 * MB - 1));
    (void)hipFuncSetAttribute((const void*)k_main, hipFuncAttributeMaxDynamicSharedMemorySize, LDS_BYTES);
    hipLaunchKernelGGL(k_main, dim3(256), dim3(256), LDS_BYTES, stream, p);
}
```

```cpp
#include <hip/hip_runtime.h>
#include <hip/hip_bf16.h>

typedef unsigned short u16;
typedef unsigned int u32;
typedef u32 v2u __attribute__((ext_vector_type(2)));
typedef u32 v4u __attribute__((ext_vector_type(4)));
typedef u32 v8u __attribute__((ext_vector_type(8)));
typedef float f32x2 __attribute__((ext_vector_type(2)));
typedef float f32x4 __attribute__((ext_vector_type(4)));
typedef float f32x16 __attribute__((ext_vector_type(16)));
typedef __bf16 bf16x2 __attribute__((ext_vector_type(2)));
typedef __bf16 bf16x8 __attribute__((ext_vector_type(8)));
typedef short s16x4 __attribute__((ext_vector_type(4)));
#define LAS __attribute__((address_space(3)))
#define GAS __attribute__((address_space(1)))
typedef LAS unsigned char* lptr;
#define DEV __device__ __forceinline__

constexpr int SEQ = 2048, DM = 640, NH = 32, DH = 128, NSEG = 64, NQ = NH * DH;

constexpr u32 MB = 1u << 20;
constexpr u32 W_ORING = 0;
constexpr u32 W_MRING = MB;
constexpr u32 W_CTRL = 4 * MB;
constexpr u32 ACT = SEQ * DM * 2;
constexpr u32 H_TILE = 128 * 1024;
constexpr u32 W_H2 = 8 * MB;
constexpr u32 W_KQ = 16 * MB;
constexpr u32 W_VO = W_KQ + 64 * MB;
constexpr u32 W_P = W_VO + 64 * MB;
constexpr u32 P_ITEM = 512 * 1024;
constexpr u32 W_H1 = W_P + 44 * MB, W_Y = W_H1 + 8 * MB, W_X2 = W_Y + ACT;
constexpr u32 W_KVT = W_X2 + ACT;
constexpr u32 W_END = W_KVT + 3 * MB;
constexpr u32 NREG = 12;
static_assert((size_t)W_END + 2 * MB <= 256ull * MB && W_H1 >= (NREG - 1) * 16 * MB + 2 * MB && W_H1 <= NREG * 16 * MB, "workspace");
constexpr u32 F_EP = 0, F_RG = 32, F_LN = 64, F_KV = 192,
              F_QKV = 256, F_O = F_QKV + 192, F_P = F_O + 64, F_H2 = F_P + 64, F_CV = F_H2 + 64, F_ERR = F_CV + 191, F_N = F_CV + 192;
constexpr u32 W_SLOT = 3 * MB;
DEV u32 foff(u32 idx) { return W_CTRL + (idx >> 6) * 16384u + (idx & 63u) * 128u; }
DEV u32 near_(u32 o, u32 side, u32 pol) { return o + ((side ^ (pol >> (o >> 24)) ^ (o >> 21)) & 1u) * 8192u; }
DEV u32 p_item(int s) { return W_P + (u32)s * P_ITEM; }

constexpr u32 OOB = 0x80000000u;
constexpr u32 PSENT = 0x7F817F81u;
constexpr int SPIN_CAP = 40000;
constexpr int LDS_BYTES = 160 * 1024;
#define ST_KV 0
#define ST_OUT 0

#define EV(k) do {} while (0)
#define LST(idx, v) do {} while (0)
#define LS2(cond, idx) do {} while (0)
struct Args {
    const u16 *x, *mem0, *Wq, *Wk, *Wv, *Wo, *bk, *bv, *shift, *ssmk, *ssmD;
    u16* out;
    unsigned char* ws;
};
struct Params : Args { u32 e0, e1, e2; };

typedef __amdgpu_buffer_rsrc_t rsrc_t;
DEV rsrc_t mk_rsrc(const void* p, u32 bytes) { return __builtin_amdgcn_make_buffer_rsrc((void*)p, 0, bytes, 0x00020000); }
DEV v4u ld16s(rsrc_t r, u32 off, u32 so) { return __builtin_amdgcn_raw_buffer_load_b128(r, off, so, 16); }
DEV v2u ld8s(rsrc_t r, u32 off, u32 so) { return __builtin_amdgcn_raw_buffer_load_b64(r, off, so, 16); }
DEV v4u ldin16(rsrc_t r, u32 off, u32 so) { return __builtin_amdgcn_raw_buffer_load_b128(r, off, so, 0); }
DEV v2u ldin8(rsrc_t r, u32 off, u32 so) { return __builtin_amdgcn_raw_buffer_load_b64(r, off, so, 0); }
DEV u32 ld4(rsrc_t r, u32 off) { return __builtin_amdgcn_raw_buffer_load_b32(r, off, 0, 16); }
DEV void st16(rsrc_t r, u32 off, v4u v) { __builtin_amdgcn_raw_buffer_store_b128(v, r, off, 0, 16); }
DEV void st8s(rsrc_t r, u32 off, u32 so, v2u v) { __builtin_amdgcn_raw_buffer_store_b64(v, r, off, so, 16); }
DEV void st4s(rsrc_t r, u32 off, u32 so, u32 v) { __builtin_amdgcn_raw_buffer_store_b32(v, r, off, so, 16); }
#define DRAIN() asm volatile("s_waitcnt vmcnt(0)" ::: "memory")
#define VMW(n) asm volatile("s_waitcnt vmcnt(%0)" :: "i"(n) : "memory")
#define BAR() asm volatile("s_waitcnt lgkmcnt(0)\n\ts_barrier" ::: "memory")
#define SCHED() __builtin_amdgcn_sched_barrier(0)
#define CFENCE() asm volatile("" ::: "memory")
#define NOPS(n) asm volatile("s_nop %0" :: "i"(n))
#define PIN(x) asm volatile("" : "+v"(x))
#define PINS(x) asm volatile("" : "+s"(x))

template <int V> struct IC { static constexpr int value = V; };
template <int I, int N, class F> DEV void static_for(F&& f) { if constexpr (I < N) { f(IC<I>{}); static_for<I + 1, N>(f); } }
#define SFOR(var, n) static_for<0, n>([&](auto ic_) __attribute__((always_inline)) { constexpr int var = decltype(ic_)::value;
#define SEND });

DEV u32 pk(float a, float b) { bf16x2 v = {(__bf16)a, (__bf16)b}; return __builtin_bit_cast(u32, v); }
DEV float lo_f(u32 u) { return __builtin_bit_cast(float, u << 16); }
DEV float hi_f(u32 u) { return __builtin_bit_cast(float, u & 0xffff0000u); }
DEV f32x2 up2(u32 u) { return (f32x2){lo_f(u), hi_f(u)}; }
DEV u32 pk2(f32x2 v) { return pk(v.x, v.y); }
DEV f32x2 rb2(f32x2 a) { f32x2 r; asm("v_cvt_pk_bf16_f32 %0, 0, %1" : "=v"(r.x) : "v"(a.x)); asm("v_cvt_pk_bf16_f32 %0, 0, %1" : "=v"(r.y) : "v"(a.y)); return r; }
DEV float rbf(float a) { return (float)(__bf16)a; }
DEV v4u lds_r16(lptr p) { return *(LAS v4u*)p; }
DEV void lds_w16(lptr p, v4u v) { *(LAS v4u*)p = v; }
DEV v2u lds_tr(lptr p) { return __builtin_bit_cast(v2u, __builtin_amdgcn_ds_read_tr16_b64_v4i16((LAS s16x4*)p)); }
DEV v4u tr2(lptr p0, lptr p1) { const v2u a = lds_tr(p0), b = lds_tr(p1); return (v4u){a.x, a.y, b.x, b.y}; }
DEV v8u cat8(v4u a, v4u b) { return __builtin_shufflevector(a, b, 0, 1, 2, 3, 4, 5, 6, 7); }
DEV f32x4 mfma16(v4u a, v4u b, f32x4 c) { return __builtin_amdgcn_mfma_f32_16x16x32_bf16(__builtin_bit_cast(bf16x8, a), __builtin_bit_cast(bf16x8, b), c, 0, 0, 0); }
DEV f32x16 mfma32(v4u a, v4u b, f32x16 c) { return __builtin_amdgcn_mfma_f32_32x32x16_bf16(__builtin_bit_cast(bf16x8, a), __builtin_bit_cast(bf16x8, b), c, 0, 0, 0); }

DEV float f_(u32 u) { return __builtin_bit_cast(float, u); }
DEV u32 u_(float f) { return __builtin_bit_cast(u32, f); }
template <int CTRL> DEV float dpp(float v) { return f_((u32)__builtin_amdgcn_update_dpp(0, (int)u_(v), CTRL, 0xf, 0xf, false)); }
DEV float max16(float v) { auto r = __builtin_amdgcn_permlane16_swap(u_(v), u_(v), false, false); return fmaxf(f_(r[0]), f_(r[1])); }
DEV float max32(float v) { auto r = __builtin_amdgcn_permlane32_swap(u_(v), u_(v), false, false); return fmaxf(f_(r[0]), f_(r[1])); }
DEV float add16(float v) { auto r = __builtin_amdgcn_permlane16_swap(u_(v), u_(v), false, false); return f_(r[0]) + f_(r[1]); }
DEV float add32(float v) { auto r = __builtin_amdgcn_permlane32_swap(u_(v), u_(v), false, false); return f_(r[0]) + f_(r[1]); }
DEV float wave_sum(float v) {
    v += dpp<0xB1>(v);
    v += dpp<0x4E>(v);
    v += dpp<0x124>(v);
    v += dpp<0x128>(v);
    return add32(add16(v));
}
DEV bool all_(bool c) { return __builtin_amdgcn_ballot_w64(!c) == 0ull; }
DEV int uni(int v) { return __builtin_amdgcn_readfirstlane(v); }
DEV u32 uu(u32 v) { return (u32)__builtin_amdgcn_readfirstlane((int)v); }
DEV u32 rdl(u32 v, int l) { return (u32)__builtin_amdgcn_readlane((int)v, l); }
DEV u32 rt32() { return (u32)__builtin_amdgcn_s_memrealtime(); }

DEV void wait_flags(rsrc_t rs, u32 off, u32 want, bool& dead) {
    if (dead) return;
#pragma unroll 1
    for (int n = 0;; ++n) {
        CFENCE();
        const u32 v = ld4(rs, off);
        if (all_(off >= OOB || v >= want)) break;
        __builtin_amdgcn_s_sleep(1);
        if ((n & 63) == 63) { if (uni((int)ld4(rs, foff(F_ERR))) != 0) { dead = true; break; } }
        if (n > SPIN_CAP) { st4s(rs, foff(F_ERR), 0, 1u); dead = true; break; }
    }
    CFENCE();
}

DEV bool boot_ok(const Params& p, u32 off, v4u v, u32 want) { return all_(off >= OOB || (v.x == p.e0 && v.y == p.e1 && v.z == p.e2 && v.w >= want)); }
DEV void boot_set(const Params& p, rsrc_t rs, u32 off, u32 v) { st16(rs, off, (v4u){p.e0, p.e1, p.e2, v}); }
DEV u32 boot_wait(const Params& p, rsrc_t rs, u32 off, u32 want, bool& dead) {
    v4u v;
#pragma unroll 1
    for (int n = 0;; ++n) {
        CFENCE();
        v = ld16s(rs, off, 0);
        if (boot_ok(p, off, v, want) || dead) break;
        __builtin_amdgcn_s_sleep(1);
        if (n > SPIN_CAP) { st4s(rs, foff(F_ERR), 0, 1u); dead = true; }
    }
    CFENCE();
    return v.w;
}

DEV u32 pol_off() { const int lane = threadIdx.x & 63; return lane < 32 ? foff(F_RG + lane) : OOB; }
DEV u32 pol_of(u32 raw) {
    const int lane = threadIdx.x & 63;
    const float v = add16(__builtin_amdgcn_fmed3f((float)((int)raw - (1 << 20)), -250.f, 250.f));
    const float lo = dpp<0x121>(v), hi = dpp<0x12F>(v), ex = dpp<0x122>(v) + dpp<0x123>(v) + dpp<0x124>(v);
    const float sum = v + lo + ((lane & 15) == 11 ? 0.f : hi) + ((lane & 15) == 0 ? ex : 0.f);
    return (u32)__builtin_amdgcn_ballot_w64(sum >= 0.f) & 0xFFFFu;
}
DEV u32 get_pol(const Params& p, rsrc_t rs, bool& dead) { return pol_of(boot_wait(p, rs, pol_off(), 0u, dead)); }
DEV u32 hblk(u32 base, int ls, int h, u32 side, u32 pol) { return near_(base + (u32)(ls * NH + h) * 16384u, side, pol); }
DEV f32x2 ln_stats(const f32x2 (&x)[5]) {
    const float c = f_(uu(u_(x[0].x)));
    f32x2 d[5];
#pragma unroll
    for (int i = 0; i < 5; ++i) d[i] = x[i] - c;
    const f32x2 s2 = (d[0] + d[1]) + (d[2] + d[3]) + d[4], q = (d[0] * d[0] + d[1] * d[1]) + (d[2] * d[2] + (d[3] * d[3] + d[4] * d[4]));
    const float dm = wave_sum(s2.x + s2.y) * (1.0f / DM), mean = c + dm;
    const float var = fmaxf(wave_sum(q.x + q.y) * (1.0f / DM) - dm * dm, 0.f);
    const float sd = rbf(__builtin_amdgcn_sqrtf(rbf(rbf(var) + 1e-5f)));
    return (f32x2){rbf(mean), __builtin_amdgcn_rcpf(sd)};
}
DEV void store_h(rsrc_t rs, u32 tile, int row, const u32 (&hp)[5]) {
    const u32 lane = threadIdx.x & 63;
    const u32 sw = (u32)(row & 15) * 16u, o8 = (lane >> 5) * 16384u + (((lane & 31u) * 8u) ^ sw), hb = tile + (u32)(row & 31) * 256u;
    st8s(rs, o8, hb, (v2u){hp[0], hp[1]});
    st8s(rs, o8, hb + 32768, (v2u){hp[2], hp[3]});
    st4s(rs, (lane * 4) ^ sw, hb + 65536, hp[4]);
}

#define DMA16(ldsb, vo, so) asm volatile("s_mov_b32 m0, %0\n\ts_nop 0\n\tbuffer_load_dwordx4 %1, %2, %3 offen sc1 lds" :: "s"(ldsb), "v"(vo), "s"(srd), "s"(so) : "memory")
#define DMA16I(ldsb, lo, vo, so, go) do { u32 so_; asm volatile("s_add_u32 m0, %1, %5\n\ts_add_u32 %0, %4, %6\n\tbuffer_load_dwordx4 %2, %3, %0 offen sc1 lds" \
                         : "=&s"(so_) : "s"(ldsb), "v"(vo), "s"(srd), "s"(so), "i"(lo), "i"(go) : "memory", "scc"); } while (0)
#define DMA32(ldsb, vo, so) asm volatile("s_mov_b32 m0, %0\n\ts_nop 0\n\tbuffer_load_dwordx4 %1, %2, %3 offen sc1 lds\n\tbuffer_load_dwordx4 %1, %2, %3 offen offset:1024 sc1 lds" \
                         :: "s"(ldsb), "v"(vo), "s"(srd), "s"(so) : "memory")
#define INPOL "nt"
#define XPOL ""
#define INAUX 2
#define DMANT(sr, ldsb, vo, so) asm volatile("s_mov_b32 m0, %0\n\ts_nop 0\n\tbuffer_load_dwordx4 %1, %2, %3 offen " INPOL " lds" :: "s"(ldsb), "v"(vo), "s"(sr), "s"(so) : "memory")
#define DMAIN(sr, ldsb, vo, so) asm volatile("s_mov_b32 m0, %0\n\ts_nop 0\n\tbuffer_load_dwordx4 %1, %2, %3 offen lds" :: "s"(ldsb), "v"(vo), "s"(sr), "s"(so) : "memory")
DEV const u16* sel(bool c, const u16* a, const u16* b) { PINS(a); PINS(b); return c ? a : b; }
DEV v4u mk_srd(const void* q, u32 bytes) { return (v4u){uu((u32)(size_t)q), uu((u32)((size_t)q >> 32) & 0xffffu), bytes, 0x00020000u}; }
#define MK_SRD() const v4u srd = mk_srd(p.ws, W_END)

constexpr int CV_PAD = 40;
constexpr int CV_PL = 4416;
constexpr int CV_F = 8 * CV_PL;
constexpr int CV_KB = 80;
constexpr int CV_KV = 65 * CV_KB;
constexpr int CV_CH = CV_F + CV_KV;
static_assert(CV_CH % 16 == 0 && 4 * CV_CH <= LDS_BYTES, "conv lds");

struct Filt { v4u a[5], b[5]; };
DEV int chan(int grp, int w) { return 16 * (grp >> 2) + (grp & 3) + 4 * w; }
template <int N = 5, int AUX = INAUX> DEV void filt_load(const Params& p, int grp, Filt& f) {
    const int tid = threadIdx.x, lane = tid & 63, d = chan(grp, tid >> 6);
    const rsrc_t rf = mk_rsrc(p.ssmk, ACT);
#pragma unroll
    for (int i = 0; i < N; ++i) {
        const u32 s0 = (u32)(8 * (lane + 64 * i) - CV_PAD);
        f.a[i] = __builtin_amdgcn_raw_buffer_load_b128(rf, s0 < SEQ ? (u32)(d * SEQ + s0) * 2u : OOB, 0, AUX);
        f.b[i] = __builtin_amdgcn_raw_buffer_load_b128(rf, s0 + 8 < SEQ ? (u32)(d * SEQ + s0 + 8) * 2u : OOB, 0, AUX);
    }
}
constexpr int KT_BYTES = 18 * 1024, KT_X = 64 * 144, NKV = 44, KT_DUMP = 8 * KT_BYTES + KT_X;
DEV void kv_fetch(const Params& p, int tile, u32 dst) {
    const int lane = threadIdx.x & 63, w = uni(threadIdx.x >> 6);
    const v4u sk = mk_srd(p.bk, ACT), sv = mk_srd(p.bv, ACT), ss = mk_srd(sel(w == 0, p.bk, p.shift), w == 0 ? ACT : DM * 8);
    const u32 vo = tile < 320 ? (u32)((lane >> 3) * DM + (tile % 10) * 64 + (lane & 7) * 8) * 2u : OOB, r0 = uu((u32)((tile / 10) * 64 + 8 * w) * (DM * 2)), d = uu(dst + w * 1024);
    DMANT(sk, d + 1024, vo, r0); DMANT(sk, d + 5120, vo, r0 + 32 * DM * 2); DMANT(sv, d + 9216, vo, r0); DMANT(sv, d + 13312, vo, r0 + 32 * DM * 2);
    DMAIN(ss, uu(w == 0 ? dst : w == 1 ? dst + 17408 : KT_DUMP), w == 0 ? (tile >= 10 ? vo : OOB) : w == 1 && lane < 32 ? (u32)lane * 16u : OOB,
          uu(w == 0 ? (tile >= 10 ? r0 - 8 * DM * 2 : 0u) : (u32)(tile % 10) * 512u));
}
DEV void kv_tile(const Params& p, rsrc_t rs, lptr src, lptr xt, int tile) {
    const int tid = threadIdx.x;
    const v4u sv = lds_r16(src + 17408 + (tid & 31) * 16);
    lptr kp = src + ((tid >> 5) * 8 + 5) * 128 + (tid & 31) * 4;
    f32x2 kf[11];
#pragma unroll
    for (int i = 0; i < 11; ++i) kf[i] = up2(*(LAS u32*)(kp + i * 128));
    const f32x2 sh[4] = {{lo_f(sv.x), lo_f(sv.z)}, {hi_f(sv.x), hi_f(sv.z)}, {lo_f(sv.y), lo_f(sv.w)}, {hi_f(sv.y), hi_f(sv.w)}};
    u32 kv[8];
#pragma unroll
    for (int i = 0; i < 8; ++i) {
        const f32x2 acc = rb2(rb2(rb2(rb2(kf[i] * sh[3]) + rb2(kf[i + 1] * sh[2])) + rb2(kf[i + 2] * sh[1])) + rb2(kf[i + 3] * sh[0]));
        kv[i] = pk2(acc * up2(*(LAS u32*)(kp + 9216 + (i - 5) * 128)));
    }
    lds_w16(xt + (2 * (tid & 31)) * 144 + (tid >> 5) * 16, (v4u){__builtin_amdgcn_perm(kv[6], kv[7], 0x05040100u), __builtin_amdgcn_perm(kv[4], kv[5], 0x05040100u),
                                                             __builtin_amdgcn_perm(kv[2], kv[3], 0x05040100u), __builtin_amdgcn_perm(kv[0], kv[1], 0x05040100u)});
    lds_w16(xt + (2 * (tid & 31) + 1) * 144 + (tid >> 5) * 16, (v4u){__builtin_amdgcn_perm(kv[6], kv[7], 0x07060302u), __builtin_amdgcn_perm(kv[4], kv[5], 0x07060302u),
                                                                 __builtin_amdgcn_perm(kv[2], kv[3], 0x07060302u), __builtin_amdgcn_perm(kv[0], kv[1], 0x07060302u)});
    BAR();
#pragma unroll
    for (int i = 0; i < 2; ++i) {
        const int ch = i * 32 + (tid >> 3);
        st16(rs, W_KVT + (u32)(((tile % 10) * 64 + ch) * SEQ + (tile / 10) * 64 + (tid & 7) * 8) * 2u, lds_r16(xt + ch * 144 + (tid & 7) * 16));
    }
}
template <int MODE> DEV void conv_job(const Params& p, rsrc_t rs, lptr lds, int grp, Filt& f, int next, u32 fo2 = OOB, u32 fv2 = 0) {
    const int tid = threadIdx.x, lane = tid & 63, w = uni(tid >> 6), bq = lane & 31, hh = lane >> 5;
    lptr F = lds + w * CV_CH;
    lptr fa = F + (bq & 7) * CV_PL + ((bq >> 3) - hh + 4) * 16;
    lptr kb = F + CV_F + CV_KB + hh * 16, kh = kb + (bq + 32) * CV_KB;
    constexpr bool LO = MODE & 1, EARLY = MODE == 0 || MODE == 3;
    bool dead = false;
    v4u kvr[4];
    const u32 D = p.ssmD[chan(grp, w)];
    auto kv_load = [&]() {
        if (EARLY) boot_wait(p, rs, lane < NKV ? foff(F_KV + lane) : OOB, 2u, dead);
#pragma unroll
        for (int i = 0; i < (LO ? 2 : 4); ++i) kvr[i] = ld16s(rs, (u32)lane * 16u, W_KVT + (u32)(chan(grp, w) * SEQ + i * 512) * 2u);
    };
    auto windows = [&](int i) {
        if (i < 4 || lane < 16) {
            const u32 e[8] = {f.a[i].x, f.a[i].y, f.a[i].z, f.a[i].w, f.b[i].x, f.b[i].y, f.b[i].z, f.b[i].w};
            u32 o[7];
#pragma unroll
            for (int q = 0; q < 7; ++q) o[q] = __builtin_amdgcn_alignbit(e[q + 1], e[q], 16);
#pragma unroll
            for (int q = 0; q < 4; ++q) {
                lds_w16(F + (2 * q) * CV_PL + (64 * i + lane) * 16, (v4u){o[q], o[q + 1], o[q + 2], o[q + 3]});
                lds_w16(F + (2 * q + 1) * CV_PL + (64 * i + lane) * 16, (v4u){e[q + 1], e[q + 2], e[q + 3], e[q + 4]});
            }
        }
    };
    lptr yw = lds + (lane >> 4) * CV_CH + 3072 + (lane & 14) * 64 + w * 32 + (lane & 1) * 16, yr = F + 3072 + lane * 16;
    const u32 yo = W_Y + (u32)((((w & 1) * 8 + (lane >> 3)) * 8 + (w >> 1)) * DM + (grp & 3) * 160 + 4 * (grp >> 2)) * 32u + (u32)(lane & 7) * 16u;
    v4u dg[2];
    auto skip = [&](lptr k) { const f32x16 z = {}; return mfma32(lds_r16(k + 32), dg[1], mfma32(lds_r16(k), dg[0], z)); };
    auto pack = [&](const f32x16& ac, const f32x16& sk, int q) {
        const int r = 4 * (q & 3);
        u32 v[4];
#pragma unroll
        for (int i = 0; i < 4; ++i) v[i] = pk2(rb2((f32x2){ac[r + 2 * i], ac[r + 2 * i + 1]}) + rb2((f32x2){sk[r + 2 * i], sk[r + 2 * i + 1]}));
        const auto x = __builtin_amdgcn_permlane32_swap(v[0], v[2], false, false), y = __builtin_amdgcn_permlane32_swap(v[1], v[3], false, false);
        return (v4u){x[0], y[0], x[1], y[1]};
    };
    if (MODE != 2) EV(8);
    if (!EARLY) kv_load();
    if (lane < CV_KB / 16) lds_w16(F + CV_F + lane * 16, (v4u){0u, 0u, 0u, 0u});
    windows(0);
    if (MODE != 1) { windows(1); windows(2); }
    if (!LO) windows(4);
    if (EARLY) kv_load();
    if (MODE == 2) windows(3);
    if (MODE != 2) EV(9);
#pragma unroll
    for (int i = 0; i < (LO ? 2 : 4); ++i) lds_w16(F + CV_F + (16 * i + (lane >> 2) + 1) * CV_KB + (lane & 3) * 16, kvr[i]);
#pragma unroll
    for (int ks = 0; ks < 2; ++ks)
#pragma unroll
        for (int q = 0; q < 4; ++q) { const int j = 16 * ks + 8 * hh + 7 - bq; dg[ks][q] = j == 2 * q ? D : j == 2 * q + 1 ? D << 16 : 0u; }
    f32x16 acc0 = {}, acc1 = {};
    auto first = [&]() { asm volatile("s_waitcnt vmcnt(0)\n\ts_barrier" ::: "memory"); st4s(rs, tid == 0 ? foff(F_CV + grp) : fo2, 0, tid == 0 ? 1u : fv2); EV(12); };
    if (MODE != 2) {
        EV(10);
        v4u fr[2][4];
        auto rd = [&](int m) { lptr kl = kb + max(bq - m, -1) * CV_KB; fr[m & 1][0] = lds_r16(kl); fr[m & 1][1] = lds_r16(fa + m * 64); fr[m & 1][2] = lds_r16(kl + 32); fr[m & 1][3] = lds_r16(fa + m * 64 - 32); };
        rd(0); rd(1);
        const f32x16 sk = skip(kb + bq * CV_KB);
#pragma unroll
        for (int m = 0; m < 8; ++m) {
            acc0 = mfma32(fr[m & 1][0], fr[m & 1][1], acc0); acc0 = mfma32(fr[m & 1][2], fr[m & 1][3], acc0);
            if (m < 6) rd(m + 2);
            SCHED();
        }
        lds_w16(yw, pack(acc0, sk, 0));
        BAR();
        st16(rs, w < 2 ? yo : OOB, lds_r16(yr));
        EV(11);
    }
    if (LO) {
        if (MODE == 1) { windows(1); windows(2); first(); }
        v4u a[2][2], l[2][2];
#pragma unroll
        for (int u = 0; u < 2; ++u) { lptr kl = kb + max(bq - 8 - u, -1) * CV_KB; a[u][0] = lds_r16(fa + (8 + u) * 64); l[u][0] = lds_r16(kl); a[u][1] = lds_r16(fa + (8 + u) * 64 - 32); l[u][1] = lds_r16(kl + 32); }
#pragma unroll 1
        for (int m = 8; m < 32; m += 2) {
            if (MODE == 3 && m == 20) first();
#pragma unroll
            for (int u = 0; u < 2; ++u) {
                lptr kl = kb + max(bq - m - u - 2, -1) * CV_KB;
                acc0 = mfma32(l[u][0], a[u][0], acc0); a[u][0] = lds_r16(fa + (m + u + 2) * 64); l[u][0] = lds_r16(kl); SCHED();
                acc0 = mfma32(l[u][1], a[u][1], acc0); a[u][1] = lds_r16(fa + (m + u + 2) * 64 - 32); l[u][1] = lds_r16(kl + 32); SCHED();
            }
        }
    } else {
        if (MODE == 0) windows(3); else filt_load<5, 0>(p, next, f);
        v4u a0 = lds_r16(fa), a1 = lds_r16(fa - 32), c0 = lds_r16(fa + 2048), c1 = lds_r16(fa + 2048 - 32);
        v4u h0 = lds_r16(kh), h1 = lds_r16(kh + 32), l0 = lds_r16(kb + bq * CV_KB), l1 = lds_r16(kb + bq * CV_KB + 32);
#pragma unroll 2
        for (int m = 0; m < (MODE == 0 ? 8 : 32); ++m) {
            lptr kl = kb + max(bq - m - 1, -1) * CV_KB;
            if (MODE == 0 && m == 4) first();
            acc1 = mfma32(h0, a0, acc1); h0 = lds_r16(kh - (m + 1) * CV_KB); a0 = lds_r16(fa + (m + 1) * 64); SCHED();
            acc1 = mfma32(l0, c0, acc1); c0 = lds_r16(fa + (m + 1) * 64 + 2048); l0 = lds_r16(kl); SCHED();
            acc1 = mfma32(h1, a1, acc1); h1 = lds_r16(kh - (m + 1) * CV_KB + 32); a1 = lds_r16(fa + (m + 1) * 64 - 32); SCHED();
            acc1 = mfma32(l1, c1, acc1); c1 = lds_r16(fa + (m + 1) * 64 + 2048 - 32); l1 = lds_r16(kl + 32); SCHED();
        }
        if (MODE == 0) {
#pragma unroll 2
            for (int m = 8; m < 32; ++m) {
                lptr kl = kb + max(bq - m - 1, -1) * CV_KB;
                acc1 = mfma32(h0, a0, acc1); h0 = lds_r16(kh - (m + 1) * CV_KB); SCHED();
                acc0 = mfma32(l0, a0, acc0); a0 = lds_r16(fa + (m + 1) * 64); SCHED();
                acc1 = mfma32(l0, c0, acc1); c0 = lds_r16(fa + (m + 1) * 64 + 2048); l0 = lds_r16(kl); SCHED();
                acc1 = mfma32(h1, a1, acc1); h1 = lds_r16(kh - (m + 1) * CV_KB + 32); SCHED();
                acc0 = mfma32(l1, a1, acc0); a1 = lds_r16(fa + (m + 1) * 64 - 32); SCHED();
                acc1 = mfma32(l1, c1, acc1); c1 = lds_r16(fa + (m + 1) * 64 + 2048 - 32); l1 = lds_r16(kl + 32); SCHED();
            }
        }
    }
    if (MODE != 2) EV(13);
    v4u yy[4];
    if (MODE != 2) { const f32x16 sk = skip(kb + bq * CV_KB); yy[0] = pack(acc0, sk, 0); yy[1] = pack(acc0, sk, 2); }
    if (!LO) { const f32x16 sk = skip(kh); yy[2] = pack(acc1, sk, 0); yy[3] = pack(acc1, sk, 2); BAR(); }
#pragma unroll
    for (int q = (MODE == 2 ? 2 : 0); q < (LO ? 2 : 4); ++q) lds_w16(yw + q * CV_PL, yy[q]);
    BAR();
#pragma unroll
    for (int q = (MODE == 2 ? 2 : 0); q < (LO ? 2 : 4); ++q) st16(rs, q == 0 && w < 2 ? OOB : yo + (u32)q * (2 * DM * 32), lds_r16(yr + q * CV_PL));
    BAR();
    if (MODE != 2) EV(14);
}

#define NPROBE 6
DEV int probe(rsrc_t rs, int c) {
    const int lane = threadIdx.x & 63;
    const u32 k = (u32)(c & 15) < NREG ? (u32)(c & 15) : 0u;
    const u32 off0 = k * (16 * MB) + 2 * MB - 65536 + ((u32)(c & 15) >= NREG ? (u32)(c & 3) + 1u : 0u) * 256 + (u32)(c >> 4) * 128;
    st4s(rs, lane == 0 ? off0 : OOB, 0, 0u); DRAIN();
    int d0 = 1 << 16, d1 = 1 << 16;
#pragma unroll 1
    for (int q = 0; q < NPROBE; ++q) {
        const u32 o = lane == 0 ? off0 + (u32)(q & 1) * 8192 + (u32)(q >> 1) * 18432 : OOB;
        const unsigned long long t0 = __builtin_amdgcn_s_memtime();
        st4s(rs, o, 0, 0u); DRAIN();
        const int dt = (int)(__builtin_amdgcn_s_memtime() - t0);
        if (q & 1) d1 = min(d1, dt); else d0 = min(d0, dt);
    }
    const u32 xcc = (u32)__builtin_amdgcn_s_getreg(20 | (3 << 11)) & 0xfu;
    return (d0 - d1) * ((xcc & 4u) ? -1 : 1);
}
DEV void pre_rings(const Params& p, rsrc_t rs, int j) {
    const u32 gt = (u32)(j * 256 + threadIdx.x) * 16u, n = ((gt >> 14) << 9) + ((gt & 8191u) >> 4);
    const v4u ps = {PSENT, PSENT, PSENT, PSENT};
    v4u m = ps;
    if (n < 640u) m = *(const v4u*)(p.mem0 + (2 * ((n >> 2) & 3) + (n & 1)) * DM + (n >> 4) * 16 + (n & 2) * 4);
#pragma unroll
    for (u32 i = 0; i < 8; ++i) st16(rs, W_ORING + gt + i * 131072u, ps);
#pragma unroll
    for (u32 i = 0; i < 2; ++i) st16(rs, gt < 16384u ? W_MRING + i * (MB / 4) + 131072u + gt : OOB, ps);
    if (n < 640u) {
        m = (v4u){__builtin_amdgcn_perm(m.z, m.x, 0x05040100u), __builtin_amdgcn_perm(m.z, m.x, 0x07060302u), __builtin_amdgcn_perm(m.w, m.y, 0x05040100u), __builtin_amdgcn_perm(m.w, m.y, 0x07060302u)};
#pragma unroll
        for (int i = 0; i < 4; ++i) m[i] = m[i] == PSENT ? 0x7FC17FC1u : m[i];
    }
    st16(rs, W_MRING + gt, m);
    st16(rs, W_MRING + MB / 4 + gt, m);
    static_assert(2 * (F_N - F_QKV) == 32 * 36, "clear");
    st4s(rs, threadIdx.x < 36 ? foff(F_QKV) + (u32)(j * 36 + threadIdx.x) * 128u : OOB, 0, 0u);
}

#define M32A(first, d, a, b) do { if (first) asm volatile("v_mfma_f32_32x32x16_bf16 %0, %1, %2, 0" : "=&v"(d) : "a"(a), "v"(b)); \
                                  else asm volatile("v_mfma_f32_32x32x16_bf16 %0, %1, %2, %0" : "+v"(d) : "a"(a), "v"(b)); } while (0)
#define M32B(first, d, a, b) do { if (first) asm volatile("v_mfma_f32_32x32x16_bf16 %0, %1, %2, 0" : "=&v"(d) : "v"(a), "a"(b)); \
                                  else asm volatile("v_mfma_f32_32x32x16_bf16 %0, %1, %2, %0" : "+v"(d) : "v"(a), "a"(b)); } while (0)
#define VCVT(d, a, b) asm volatile("v_cvt_pk_bf16_f32 %0, %1, %2" : "=v"(d) : "v"(a), "v"(b))
#define LD4A(d, vo, so) asm volatile("buffer_load_dword %0, %1, %2, %3 offen sc1" : "=v"(d) : "v"(vo), "s"(srd), "s"(so) : "memory")
DEV u32 wst_src(int lane, int ld) { return (u32)((lane >> 4) * ld + 8 * ((lane & 15) ^ ((lane >> 4) << 2))) * 2u; }
DEV u32 wst_rd(int lane, int w) { const int q4 = (lane >> 2) & 3; return (u32)(q4 * 256 + ((w ^ q4) * 64) + ((lane >> 4) & 1) * 32 + (lane & 3) * 8); }

constexpr int QK_A = 65536;
#define QLEAD 4
#define QFIRST 2
static_assert(QFIRST < QLEAD, "a round of the lead carries the flag of the convolution");
#define QW0 10
#define QW1 3
#define QSAMP 72
DEV void role_qkv(const Params& p, lptr lds, int c) {
    const int grp = 64 + c;
    const int tid = threadIdx.x, lane = tid & 63, w = uni(tid >> 6), hh = lane >> 5, r32 = lane & 31;
    const int m = c >> 5, h = c & 31;
    const rsrc_t rs = mk_rsrc(p.ws, W_END);
    v4u wt[40];
    Filt flt;
    bool dead = false;
    u32 pol, h2flag, fo, ol, ar[8];
    const u32 ds = uu(w * 1024), dx = ds + QK_A;
#define XRD(ks) lds_r16(lds + ar[(ks) & 7] + ((ks) >> 3) * 8192)
    const u32 t16 = (u32)tid * 16u, lnf = tid < 96 ? foff(F_LN + tid) : OOB;
    MK_SRD();
#define QDMA(i, dst, vo, so) DMA16I(dst, (i) * 4096, vo, so, ((i) >> 1) * 16384 + ((i) & 1) * 4096)
    {
        const v4u sw = mk_srd(sel(m == 0, p.Wq, sel(m == 1, p.Wk, p.Wv)), DM * NQ * 2), sx = mk_srd(p.x, ACT);
        const u32 vo = wst_src(lane, NQ) + (u32)h * (DH * 2), x8 = (u32)lane * 8u, x4 = (u32)lane * 4u;
        v2u xa[6], xb[6]; u32 xc[6];
#pragma unroll
        for (int i = 0; i < 6; ++i)
            asm volatile("buffer_load_dwordx2 %0, %3, %5, %6 offen " XPOL "\n\tbuffer_load_dwordx2 %1, %3, %5, %6 offen offset:512 " XPOL "\n\tbuffer_load_dword %2, %4, %5, %6 offen offset:1024 " XPOL
                         : "=&v"(xa[i]), "=&v"(xb[i]), "=&v"(xc[i]) : "v"(x8), "v"(x4), "s"(sx), "s"(uu((4 * c + w + 384 * i) * (DM * 2))) : "memory");
        EV(1);
        auto wfill = [&](int i0, int n) {
#pragma unroll 1
            for (int i = i0; i < i0 + n; ++i) DMAIN(sw, uu((4 * i + w) * 1024), vo, uu((16 * i + 4 * w) * (NQ * 2)));
        };
        wfill(0, QW0);
        asm volatile("s_waitcnt vmcnt(%0) ; HZCLR" :: "i"(QW0) : "memory");
        EV(2);
#pragma unroll
        for (int i = 0; i < 6; ++i) {
            wfill(QW0 + QW1 * i, QW1);
            if (i == 2) { VMW(2 * QW1 + 3); BAR(); boot_set(p, rs, tid == 0 ? foff(F_LN + c) : OOB, 1u); }
            asm volatile("" : "+v"(xa[i]), "+v"(xb[i]), "+v"(xc[i]));
            const int row = 4 * c + w + 384 * i;
            if (i < 5 || row < SEQ) {
                const f32x2 x[5] = {up2(xa[i].x), up2(xa[i].y), up2(xb[i].x), up2(xb[i].y), up2(xc[i])};
                const f32x2 st = ln_stats(x);
                u32 hp[5];
#pragma unroll
                for (int q = 0; q < 5; ++q) hp[q] = pk2(up2(pk2(x[q] - st.x)) * st.y);
                store_h(rs, W_H1 + (u32)(row >> 5) * H_TILE, row, hp);
            }
        }
        EV(3);
        constexpr int K0 = QW0 + 6 * QW1;
        static_assert(K0 * 4096 >= QK_A + 40960, "the first tile's place is free");
        v4u fv, praw;
        asm volatile("buffer_load_dwordx4 %0, %2, %4, 0 offen sc1\n\tbuffer_load_dwordx4 %1, %3, %4, 0 offen sc1" : "=&v"(praw), "=&v"(fv) : "v"(pol_off()), "v"(lnf), "s"(srd) : "memory");
        wfill(K0, 40 - K0);
        asm volatile("s_waitcnt vmcnt(%2) ; HZCLR\n\ts_barrier" : "+v"(praw), "+v"(fv) : "i"(40 - K0) : "memory");
        boot_set(p, rs, tid == 0 ? foff(F_LN + c) : OOB, 2u);
        EV(4);
        lptr tb = lds + hh * 2048 + wst_rd(lane, w);
        SFOR(k, 40)
            if constexpr (k == K0) {
                if (!boot_ok(p, lnf, fv, 1u)) boot_wait(p, rs, lnf, 1u, dead);
                if (!boot_ok(p, pol_off(), praw, 0u)) praw.w = boot_wait(p, rs, pol_off(), 0u, dead);
                BAR();
                EV(5);
                const u32 so = W_H1;
                SFOR(i, 10) QDMA(i, dx, t16, so); SEND
                pol = pol_of(praw.w);
                h2flag = tid < 8 ? near_(foff(F_H2 + lane), 1u, pol) : OOB;
                ol = (m == 2 ? (u32)(2 * w + (r32 >> 4)) * 1024u : (u32)((r32 >> 4) * 4 + w) * 1024u) + (u32)(hh * 16 + (r32 & 15)) * 16u;
                fo = near_(foff(F_QKV + m * 64 + h), 0u, pol);
#pragma unroll
                for (int j = 0; j < 8; ++j) { ar[j] = (u32)(QK_A + r32 * 256 + ((2 * j + hh) ^ (r32 & 15)) * 16); PIN(ar[j]); }
                PIN(h2flag); PIN(ol); PINS(fo); PINS(pol);
                VMW(10); BAR();
            }
            lptr q = tb + (k >> 3) * 32768 + (k & 7) * 4096; wt[k] = tr2(q, q + 1024); asm volatile("" : "+a"(wt[k]));
        SEND
    }
    EV(6);
    int n0 = -1, n1 = -QLEAD;
    const u32 ksc = m == 1 ? 0x3e0293eeu : 0x3f800000u;
    const u32 bfo = tid < 96 ? lnf : tid - 128u < NKV ? foff(F_KV - 128 + tid) : OOB;
    v4u brk = {};
    auto run = [&](auto isv) __attribute__((always_inline)) {
      constexpr bool V = decltype(isv)::value;
      v4u x[8] = {};
      f32x16 a0 = {};
      bool fresh = true;
      u32 fv = 0, fo0 = tid == 192 ? fo : n1 > -QLEAD && tid == 194 ? foff(F_CV + grp) : OOB, fo1 = tid == 192 ? fo : tid == 193 ? fo + 32 * 128 : OOB;
      PIN(fo0); PIN(fo1);
#define QS(k) do {} while (0)
#define QPK1(d, a, a_, q_) do { if constexpr (V) VCVT(d[q_], a[4 * (a_) + 8 * ((q_) >> 1) + 2 * ((q_) & 1)], a[4 * (a_) + 8 * ((q_) >> 1) + 2 * ((q_) & 1) + 1]); else VCVT(d[q_], a[4 * (a_) + (q_)], a[4 * (a_) + (q_) + 8]); } while (0)
#define QSC(a, i) asm volatile("v_mul_f32 %0, %2, %0\n\tv_mul_f32 %1, %2, %1" : "+v"(a[i]), "+v"(a[(i) + 8]) : "s"(ksc))
#define QPK(d, a, a_, q_) do { if constexpr (!V) { QSC(a, 4 * (a_) + (q_)); QSC(a, 4 * (a_) + (q_) + 1); } QPK1(d, a, a_, q_); QPK1(d, a, a_, (q_) + 1); } while (0)
#pragma unroll 1
      for (const int end = n1 == -QLEAD ? QFIRST - QLEAD : NSEG; n1 < end; ++n1) {
        const int cur = n1 < 0 ? n0 + 1 : 64 | n1, nxt = n1 < 0 ? n0 + 2 : n0 + 1;
        u32 xs = n1 < 0 ? W_H1 + (u32)cur * H_TILE : near_(W_H2 + (u32)n1 * H_TILE, 1u, pol);
        PINS(xs);
        QS(0);
        if (w == 0 && n1 >= 0 && ((u32)__builtin_amdgcn_ballot_w64(fv <= (u32)n1) & 255u) != 0u) wait_flags(rs, h2flag, (u32)n1 + 1, dead);
        BAR();
        QS(1);
        f32x16 a1;
        u32 ss, sv, hb, oa, qfo, qfv;
        v4u pp, pq;
        SCHED();
        auto slot = [&](auto zc) __attribute__((always_inline)) {
            constexpr int z = decltype(zc)::value, ks = (z + 20) % 40;
            if constexpr (z < 20) { if constexpr (V) M32B(false, a0, x[ks & 7], wt[ks]); else M32A(false, a0, wt[ks], x[ks & 7]); }
            else if constexpr (z < 60) { if constexpr (V) M32B(z == 20, a1, x[ks & 7], wt[ks]); else M32A(z == 20, a1, wt[ks], x[ks & 7]); }
            else if constexpr (z == 60) {
                if constexpr (V) asm volatile("v_mfma_f32_32x32x16_bf16 %0, %1, %2, 0" : "+v"(a0) : "v"(x[ks & 7]), "a"(wt[ks]));
                else asm volatile("v_mfma_f32_32x32x16_bf16 %0, %1, %2, 0" : "+v"(a0) : "a"(wt[ks]), "v"(x[ks & 7]));
            } else { if constexpr (V) M32B(false, a0, x[ks & 7], wt[ks]); else M32A(false, a0, wt[ks], x[ks & 7]); }
            if constexpr (z == 18) { QS(2); asm volatile("s_waitcnt vmcnt(6) lgkmcnt(0)\n\ts_barrier" ::: "memory"); QS(3); }
            if constexpr (z < 15 || z > 20) x[(ks + 5) & 7] = XRD((ks + 5) % 40);
            if constexpr (z >= 18 && z <= 20) { x[2 * (z - 18)] = XRD(2 * (z - 18)); x[2 * (z - 18) + 1] = XRD(2 * (z - 18) + 1); }
            SCHED();
            if constexpr (z < 4) QDMA(z, dx, t16, xs);
            if constexpr (z >= 5 && z < 17 && (z & 1)) QDMA((z + 3) >> 1, dx, t16, xs);
            if constexpr ((z >= 7 && z < 15) || (z >= 47 && z < 55)) asm volatile("v_xor_b32 %0, 0x10000, %0" : "+v"(ar[(ks + 5) & 7]));
            if constexpr (z == 16) { ss = W_H1 + (u32)nxt * H_TILE; PINS(ss); }
            if constexpr (z == 17) { sv = t16 | (nxt >= NSEG ? OOB : 0u); PIN(sv); }
            if constexpr (z >= 21 && z < 41 && (z & 1)) QDMA((z - 21) >> 1, ds, sv, ss);
            if constexpr (z == 22 || z == 24) QPK(pp, a0, 0, z - 22);
            if constexpr (z == 26 || z == 28) QPK(pq, a0, 1, z - 26);
            if constexpr (z == 30) { QS(4); asm volatile("s_waitcnt vmcnt(5)\n\ts_barrier" ::: "memory"); QS(5); }
            if constexpr (z == 32 || z == 44) { hb = (m == 2 ? W_VO : W_KQ) + (u32)((z == 32 ? n0 : cur) * NH + h) * 16384u; PINS(hb); }
            if constexpr (z == 34 || z == 45) { hb = near_(hb, m == 1 ? 0u : 1u, pol); PINS(hb); }
            if constexpr (z == 36) { hb = n0 < NSEG && !fresh ? hb : OOB; PINS(hb); }
            if constexpr (z == 38 || z == 46) { oa = ol + hb; PIN(oa); }
            if constexpr (z == 41) st16(rs, oa, pp);
            if constexpr (z == 43) st16(rs, oa + 512, pq);
            if constexpr (z == 54) { QS(6); asm volatile("s_waitcnt vmcnt(4)\n\ts_barrier" ::: "memory"); QS(7); }
            if constexpr (z == 56) { if (__builtin_expect(n1 == QFIRST - QLEAD - 1, 0)) { filt_load<3, 0>(p, grp, flt); brk = ld16s(rs, bfo, 0); } }
            if constexpr (z == 60) NOPS(5);
            if constexpr (z == 63) st16(rs, oa, pp);
            if constexpr (z == 65) st16(rs, oa + 512, pq);
            if constexpr (z == 61 || z == 62) QPK(pp, a1, 0, 2 * (z - 61));
            if constexpr (z == 63 || z == 64) QPK(pq, a1, 1, 2 * (z - 63));
            if constexpr (z == 67) { qfo = n1 >= 0 ? fo1 : fo0; PIN(qfo); }
            if constexpr (z == 68) { qfv = tid == 193 ? (u32)n1 + 1u : (u32)min(nxt, NSEG); PIN(qfv); }
            if constexpr (z == QSAMP) LD4A(fv, h2flag, 0);
            SCHED();
        };
        if (__builtin_expect(!fresh, 1)) static_for<0, 16>(slot);
        static_for<16, 80>(slot);
        QS(8);
        asm volatile("s_waitcnt vmcnt(1)\n\ts_barrier" ::: "memory");
        st4s(rs, qfo, 0, qfv);
        QS(9);
        asm volatile("s_waitcnt vmcnt(1)" : "+v"(fv) :: "memory");
        n0 = nxt; fresh = false;
      }
      if (n1 < 0) {
        SFOR(z, 20)
            if constexpr (V) M32B(false, a0, x[(z + 20) & 7], wt[z + 20]); else M32A(false, a0, wt[z + 20], x[(z + 20) & 7]);
            if constexpr (z < 15) x[(z + 25) & 7] = XRD(z + 25);
            if constexpr (z >= 7 && z < 15) asm volatile("v_xor_b32 %0, 0x10000, %0" : "+v"(ar[(z + 25) & 7]));
            SCHED();
        SEND
        u32 oa = ol + near_((m == 2 ? W_VO : W_KQ) + (u32)(n0 * NH + h) * 16384u, m == 1 ? 0u : 1u, pol);
        v4u pp, pq;
        asm volatile("s_nop 11" : "+v"(oa));
        QPK(pp, a0, 0, 0); QPK(pp, a0, 0, 2); st16(rs, oa, pp);
        QPK(pq, a0, 1, 0); QPK(pq, a0, 1, 2); st16(rs, oa + 512, pq);
      }
    };
#pragma unroll 1
    for (;;) {
        if (m == 2) run(IC<1>{}); else run(IC<0>{});
        if (n1 > 0) break;
        if (!boot_ok(p, bfo, brk, 2u)) boot_wait(p, rs, bfo, 2u, dead);
        BAR();
        conv_job<1>(p, rs, lds, grp, flt, 0, tid == 1 ? fo : OOB, (u32)n0 + 1u);
        const u32 so = uu(W_H1 + (u32)(n0 + 1) * H_TILE);
        SFOR(i, 10) QDMA(i, dx, t16, so); SEND
    }
}

constexpr int OU_SEL = 131072;
#define OSAMP 46

DEV void role_out(const Params& p, lptr lds, int L, int kq, int ng) {
    const int tid = threadIdx.x, lane = tid & 63, w = uni(tid >> 6), hh = lane >> 5, r32 = lane & 31;
    const rsrc_t rs = mk_rsrc(p.ws, W_END);
    v4u wo[64];
    Filt flt;
    bool dead = false;
    {
        const v4u sw = mk_srd(p.Wo, DM * NQ * 2);
        const u32 vo = wst_src(lane, DM) + (ng == 4 ? 1024u : (u32)((ng >> 1) * 512 + (ng & 1) * 128 + ((lane ^ (lane >> 2)) & 8) * 16));
        auto fill = [&](int b) {
#pragma unroll 1
            for (int i = 0; i < 8; ++i) DMAIN(sw, uu((b % 5) * 32768 + (4 * i + w) * 1024), vo, uu((kq * 1024 + b * 128 + 16 * i + 4 * w) * (DM * 2)));
        };
#pragma unroll 1
        for (int b = 0; b < 5; ++b) fill(b);
        lptr tb = lds + hh * 1024 + wst_rd(lane, w);
        SFOR(b, 8)
            VMW(b < 3 ? 32 : 62 - 8 * b); BAR();
            lptr q = tb + (b % 5) * 32768;
#pragma unroll
            for (int k = 0; k < 8; ++k) { lptr a = q + (k >> 1) * 8192 + (k & 1) * 2048; wo[8 * b + k] = tr2(a, a + 4096); if (b < 7) asm volatile("" : "+a"(wo[8 * b + k])); else PIN(wo[8 * b + k]); }
            if constexpr (b < 3) { BAR(); fill(b + 5); }
            if constexpr (b == 2) filt_load<3, 0>(p, L == 0 ? 44 + kq * 5 + ng : 1 << 12, flt);
        SEND
    }
    EV(6);
    const u32 pol = get_pol(p, rs, dead);
    if (L == 0) {
        BAR();
        conv_job<3>(p, rs, lds, 44 + kq * 5 + ng, flt, 0);
        DRAIN(); BAR();
        st4s(rs, tid == 0 ? foff(F_CV + 44 + kq * 5 + ng) : OOB, 0, 2u);
    }
    EV(15);
    MK_SRD();
    const u32 ofl = (lane < 8 && w == 0) ? near_(foff(F_O + L * 32 + kq * 8 + lane), 0u, pol) : OOB;
    const u32 l16 = (u32)lane * 16u;
    const u32 fo = (tid == 192) ? near_(foff(F_P + L * 32 + kq * 5 + ng), 1u, pol) : OOB;
    auto osrc = [&](int s) { return uu(hblk(W_VO, L * NSEG + s, kq * 8, 0u, pol) + w * 1024); };
#define ODMA(i, dst, vo, so) DMA16I(dst, (i) * 4096, vo, so, ((i) >> 1) * 16384 + ((i) & 1) * 4096)
    const u32 pvo = (u32)(kq * 5 + ng) * 16384u + (u32)w * 2048u + l16;
    lptr tw = lds + OU_SEL + 256 + r32 * 272 + (ng == 4 ? hh * 32 + w * 4 : (w & 1) * 128 + hh * 16 + (w >> 1) * 8), tr = lds + OU_SEL + 256 + (8 * w + (lane >> 4)) * 272 + (lane & 15) * 16;
    lptr selw = lds + OU_SEL + (w == 0 ? 0 : 64);
    u32 pfo = OOB;
    u32 buf = 0;
    if (w == 0) wait_flags(rs, ofl, 1u, dead);
    BAR();
    for (int s = 0; s < NSEG; ++s) {
        u32 src = osrc(s), dst = uu(buf + w * 1024);
        lptr A = lds + buf + l16;
        asm volatile("" : "+s"(src), "+s"(dst));
#define OS(k) do {} while (0)
        OS(0);
        OS(1);
        SFOR(i, 10) ODMA(i, dst, l16, src); SEND
        OS(2);
        asm volatile("s_waitcnt vmcnt(7)\n\ts_barrier" ::: "memory");
        OS(3);
        LS2(L == 0 && w == 1, 96 + kq * 5 + ng);
        v4u o[64];
#pragma unroll
        for (int k = 0; k < 3; ++k) o[k] = lds_r16(A + k * 1024);
        st4s(rs, pfo, 0, (u32)s);
        u32 fv, pa; int rdy;
        f32x16 acc;
        SCHED();
        SFOR(k, 64)
            if constexpr (k < 56) M32A(k == 0, acc, wo[k], o[k]); else asm volatile("v_mfma_f32_32x32x16_bf16 %0, %1, %2, %0" : "+v"(acc) : "v"(wo[k]), "v"(o[k]));
            if constexpr (k < 61) o[k + 3] = lds_r16(A + (k + 3) * 1024);
            SCHED();
            if constexpr (k < 12 && (k & 1) == 0) ODMA(10 + (k >> 1), dst, l16, src);
            if constexpr (k == 7) OS(4);
            if constexpr (k == 8) OS(5);
            if constexpr (k == 23) OS(6);
            if constexpr (k == 24) OS(7);
            if constexpr (k == 43) OS(8);
            if constexpr (k == 44) OS(9);
            if constexpr (k == 59) OS(10);
            if constexpr (k == 63) OS(11);
            if constexpr (k == 7) asm volatile("s_waitcnt vmcnt(8)\n\ts_barrier" ::: "memory");
            if constexpr (k == 23) asm volatile("s_waitcnt vmcnt(4)\n\ts_barrier" ::: "memory");
            if constexpr (k == 30) { pa = pvo + near_(p_item(s), (u32)L, pol); PIN(pa); }
            if constexpr (k == 43) asm volatile("s_waitcnt vmcnt(0)\n\ts_barrier" ::: "memory");
            if constexpr (k == OSAMP) LD4A(fv, ofl, 0);
            if constexpr (k == 62) {
                asm volatile("s_waitcnt vmcnt(0)" : "+v"(fv) :: "memory");
                *(LAS int*)selw = (int)((u32)__builtin_amdgcn_ballot_w64(fv < (u32)s + 2) & 255u);
            }
            SCHED();
        SEND
        asm volatile("s_nop 11" : "+v"(acc));
        u32 pc[8];
#pragma unroll
        for (int i = 0; i < 8; ++i) pc[i] = pk(acc[2 * i], acc[2 * i + 1]);
        if (ng != 4) {
#pragma unroll
            for (int i = 0; i < 4; ++i) *(LAS v2u*)(tw + i * 32) = (v2u){pc[2 * i], pc[2 * i + 1]};
        } else {
#pragma unroll
            for (int i = 0; i < 8; ++i) *(LAS u32*)(tw + (i >> 1) * 64 + (i & 1) * 16) = pc[i];
        }
        BAR();
        const v4u p0 = lds_r16(tr), p1 = lds_r16(tr + 4 * 272);
        rdy = *(LAS int*)(lds + OU_SEL);
        st16(rs, pa, p0); st16(rs, pa + 1024, p1);
        const bool ready = uni(rdy) == 0;
        SCHED();
        OS(12);
        if (ready) pfo = fo;
        else {
            LD4A(fv, ofl, 0);
            asm volatile("s_waitcnt vmcnt(1)\n\ts_barrier" ::: "memory");
            st4s(rs, fo, 0, (u32)s + 1); pfo = OOB;
            OS(14);
            LS2(L == 0 && w == 1, 160 + kq * 5 + ng);
            asm volatile("s_waitcnt vmcnt(1)" : "+v"(fv) :: "memory");
            if (w == 0 && s + 1 < NSEG && ((u32)__builtin_amdgcn_ballot_w64(fv < (u32)s + 2) & 255u) != 0u) wait_flags(rs, ofl, (u32)s + 2, dead);
            BAR();
        }
        OS(13);
        buf ^= 65536u;
    }
}

template <int L> DEV void role_row(const Params& p, lptr lds, int ri) {
    const int lane = threadIdx.x & 63, w = uni(threadIdx.x >> 6);
    const rsrc_t rs = mk_rsrc(p.ws, W_END);
    bool dead = false;
    const u32 pol = get_pol(p, rs, dead);
    const u32 fp = threadIdx.x < 20 ? near_(foff(F_P + L * 32 + lane), 1u, pol) : OOB, fo = threadIdx.x == 192 && L == 0 ? near_(foff(F_H2 + (ri >> 2)), 1u, pol) : OOB;
    u32 fv = 0;
    MK_SRD();
    auto yfill = [&](int b, int i) { DMA32(uu((b & 1) * 40960 + w * 10240 + i * 2048), (u32)lane * 16u, uu(W_Y + (u32)((ri >> 1) * 8 + b) * 20480u + (w & 1) * 10240 + i * 2048)); };
    auto ywait = [&](u32 v) {
        wait_flags(rs, threadIdx.x < (v == 3u ? 36u : 160u) ? foff(F_CV + threadIdx.x) : OOB, v, dead);
    };
    v4u y8[10] = {};
    v2u xa, xb; u32 xc;
    const v4u sx = L == 0 ? mk_srd(p.x, ACT) : mk_srd(p.ws + W_X2, ACT);
    auto xload = [&](int s) {
        asm volatile("buffer_load_dwordx2 %0, %3, %5, %6 offen sc1\n\tbuffer_load_dwordx2 %1, %3, %5, %6 offen offset:512 sc1\n\tbuffer_load_dword %2, %4, %5, %6 offen offset:1024 sc1"
                     : "=&v"(xa), "=&v"(xb), "=&v"(xc) : "v"((u32)lane * 8u), "v"((u32)lane * 4u), "s"(sx), "s"(uu((u32)(s * 32 + ri) * (DM * 2))) : "memory");
        SCHED();
    };
    ywait(1u);
    BAR();
    EV(15);
#pragma unroll 1
    for (int i = 0; i < 5; ++i) yfill(0, i);
    if (L == 1) wait_flags(rs, lane == 0 ? near_(foff(F_H2 + (ri >> 2)), 1u, pol) : OOB, 1u, dead);
    xload(0);
    LD4A(fv, fp, 0);
    VMW(0); SCHED();
    asm volatile("" : "+v"(xa), "+v"(xb), "+v"(xc), "+v"(fv));
    BAR();
    for (int s = 0; s < NSEG; ++s) {
        u32 xi[5] = {xa.x, xa.y, xb.x, xb.y, xc};
        u32 pa = near_(p_item(s), (u32)L, pol) + (u32)ri * 256u, pc = pa + 65536u + (u32)(lane >> 4) * 81920u + (u32)(lane & 15) * 16u;
        pa += (u32)(lane >> 5) * 81920u + (u32)((lane >> 4) & 1) * 16384u + (u32)(lane & 15) * 16u;
        u32 yf = s == 1 ? foff(F_CV + min(threadIdx.x, 159u)) : s == 25 ? foff(F_CV + min(lane, 35)) : OOB;
        asm volatile("" : "+v"(xi[0]), "+v"(xi[1]), "+v"(xi[2]), "+v"(xi[3]), "+v"(xi[4]), "+v"(pa), "+v"(pc), "+v"(yf));
#define RS(k) do {} while (0)
        RS(0);
        if (w == 0 && ((u32)__builtin_amdgcn_ballot_w64(fv <= (u32)s) & 0xFFFFFu) != 0u) wait_flags(rs, fp, (u32)s + 1, dead);
        BAR();
        RS(1);
        v4u d[5];
        asm volatile("buffer_load_dwordx4 %0, %5, %7, 0 offen sc1\n\tbuffer_load_dwordx4 %1, %5, %7, %8 offen sc1\n\tbuffer_load_dwordx4 %2, %5, %7, %9 offen sc1\n\t"
                     "buffer_load_dwordx4 %3, %5, %7, %10 offen sc1\n\tbuffer_load_dwordx4 %4, %6, %7, 0 offen sc1"
                     : "=&v"(d[0]), "=&v"(d[1]), "=&v"(d[2]), "=&v"(d[3]), "=&v"(d[4]) : "v"(pa), "v"(pc), "s"(srd), "s"(163840), "s"(32768), "s"(196608) : "memory");
        RS(2);
        LS2(L == 0, 192 + ri);
        SCHED();
        f32x2 y[5];
        {
            if ((s & 7) == 0) {
                lptr ya = lds + ((s >> 3) & 1) * 40960 + (w >> 1) * 20480 + (w & 1) * 16, yc = ya + 4096 + (lane >> 1) * 32 + (lane & 1) * 10240;
#pragma unroll
                for (int i = 0; i < 8; ++i) y8[i] = lds_r16(ya + lane * 32 + (i >> 2) * 2048 + (i & 3) * 5120);
                y8[8] = lds_r16(yc); y8[9] = lds_r16(yc + 5120);
            }
#pragma unroll
            for (int i = 0; i < 10; ++i) {
                y[i >> 1][i & 1] = lo_f(y8[i].x);
                y8[i] = (v4u){__builtin_amdgcn_alignbit(y8[i].y, y8[i].x, 16), __builtin_amdgcn_alignbit(y8[i].z, y8[i].y, 16), __builtin_amdgcn_alignbit(y8[i].w, y8[i].z, 16), y8[i].w >> 16};
                PIN(y8[i]);
            }
            asm volatile("" : "+v"(y[0]), "+v"(y[1]), "+v"(y[2]), "+v"(y[3]), "+v"(y[4]));
        }
        u32 yv;
        {
            xload(s + 1);
            const bool yy = (s & 7) - 2u < 5u && s < NSEG - 8;
            u32 yo = yy ? (u32)lane * 16u : OOB;
            DMA32(uu(yy ? (((s >> 3) + 1) & 1) * 40960 + w * 10240 + ((s & 7) - 2) * 2048 : 81920), yo, uu(W_Y + (u32)((ri >> 1) * 8 + (s >> 3) + 1) * 20480u + (w & 1) * 10240 + ((s & 7) - 2) * 2048));
            LD4A(yv, yf, 0);
            asm volatile("s_waitcnt vmcnt(6)" : "+v"(d[0]), "+v"(d[1]), "+v"(d[2]), "+v"(d[3]), "+v"(d[4]));
        }
        f32x2 o[5];
        {
            f32x2 t[5][2];
#pragma unroll
            for (int i = 0; i < 5; ++i) {
                const auto e = __builtin_amdgcn_permlane32_swap(d[i].x, d[i].z, false, false), f = __builtin_amdgcn_permlane32_swap(d[i].y, d[i].w, false, false);
                t[i][0] = up2(e[0]) + up2(e[1]); t[i][1] = up2(f[0]) + up2(f[1]);
            }
            o[0] = t[0][0] + t[1][0]; o[1] = t[0][1] + t[1][1]; o[2] = t[2][0] + t[3][0]; o[3] = t[2][1] + t[3][1];
            const auto e = __builtin_amdgcn_permlane16_swap(u_(t[4][0].x), u_(t[4][1].x), false, false), f = __builtin_amdgcn_permlane16_swap(u_(t[4][0].y), u_(t[4][1].y), false, false);
            o[4] = (f32x2){f_(e[0]) + f_(e[1]), f_(f[0]) + f_(f[1])};
        }
        f32x2 x1[5];
#pragma unroll
        for (int i = 0; i < 5; ++i) x1[i] = up2(pk2(up2(xi[i]) + up2(pk2(o[i]))));
        asm volatile("" : "+v"(x1[0]), "+v"(x1[1]), "+v"(x1[2]), "+v"(x1[3]), "+v"(x1[4]));
        RS(3);
        const f32x2 st1 = ln_stats(x1);
        u32 xp[5];
#pragma unroll
        for (int i = 0; i < 5; ++i) xp[i] = pk2(x1[i] + ((x1[i] - st1.x) * st1.y) * y[i]);
        const int row = s * 32 + ri;
        RS(4);
        if (L == 0) {
            const u32 ro = (u32)row * (DM * 2);
            f32x2 x2[5];
#pragma unroll
            for (int i = 0; i < 5; ++i) x2[i] = up2(xp[i]);
            st8s(rs, (u32)lane * 8, W_X2 + ro, (v2u){xp[0], xp[1]});
            st8s(rs, (u32)lane * 8 + 512, W_X2 + ro, (v2u){xp[2], xp[3]});
            st4s(rs, (u32)lane * 4 + 1024, W_X2 + ro, xp[4]);
            SCHED();
            const f32x2 st2 = ln_stats(x2);
            u32 hp[5];
#pragma unroll
            for (int i = 0; i < 5; ++i) hp[i] = pk2((x2[i] - st2.x) * st2.y);
            store_h(rs, near_(W_H2 + (u32)s * H_TILE, 1u, pol), row, hp);
        } else {
            u16* o_ = p.out + (size_t)row * DM;
            *(v2u*)(o_ + 4 * lane) = (v2u){xp[0], xp[1]};
            *(v2u*)(o_ + 256 + 4 * lane) = (v2u){xp[2], xp[3]};
            *(u32*)(o_ + 512 + 2 * lane) = xp[4];
        }
        RS(5);
        LD4A(fv, fp, 0);
        if (L == 0) asm volatile("s_waitcnt vmcnt(1)\n\ts_barrier" ::: "memory");
        st4s(rs, fo, 0, (u32)s + 1);
        RS(6);
        VMW(1); SCHED();
        asm volatile("" : "+v"(fv), "+v"(yv), "+v"(xa), "+v"(xb), "+v"(xc));
        if (__builtin_expect(s == 1 || s == 25, 0) && !all_(yv >= (s == 1 ? 2u : 3u))) ywait(s == 1 ? 2u : 3u);
        RS(7);
        if (ri == 0) LST(320 + L * 64 + s, rt32());
        LS2(L == 0, 224 + ri);
    }
}

constexpr float SCALE = 0.08838834764831845f, LOG2E = 1.4426950408889634f;
constexpr int C_K = 0, C_V = 8192, C_QX = 16384, C_MEM = 24576, C_Q = C_MEM + 10240, C_KM = C_Q + 2304, C_VM = C_KM + 2304, C_DUMP = C_VM + 2048,
              C_ZERO = C_DUMP + 1024, C_WL = C_ZERO + 128;
constexpr int NLF = 14;
constexpr int C_MASK = C_WL + 4 * NLF * 2048;
static_assert(C_WL % 16 == 0 && C_MASK + 3072 <= LDS_BYTES, "head lds");
#define CH_VIS 10
#define CH_H0 104
#define CH_W0 82
#define CH_MH 2
#define CH_MW 6
#define CH_REC 8
#define CH_LATE 30
#define CH_SOLO 6
#define CH_KV 12
#define CH_KV0 60
#define CH_M0 6
#define CH_DEC 2u
#define CH_UP 4
#define CH_WX 6
DEV void wait_until(u32 t) {
    unsigned long long c;
    asm volatile("1:\n\ts_memrealtime %0\n\ts_waitcnt lgkmcnt(0)\n\ts_mov_b64 vcc, %0\n\ts_sub_u32 vcc_lo, %1, vcc_lo\n\ts_cmpk_lt_u32 vcc_lo, 400\n\ts_cbranch_scc1 1b" : "=&s"(c) : "s"(uu(t - 1u)) : "vcc", "scc", "memory");
}
#define WAIT_TXT(tmp, t1) "s_memrealtime vcc\n1:\n\ts_waitcnt lgkmcnt(0)\n\ts_sub_u32 " tmp ", " t1 ", vcc_lo\n\ts_memrealtime vcc\n\ts_cmpk_lt_u32 " tmp ", 400\n\ts_cbranch_scc1 1b\n\t"
#define SMF_A(acc, a, b) asm volatile("v_smfmac_f32_16x16x64_bf16 %0, %1, %2, %3" : "+v"(acc) : "v"(a), "a"(b), "v"(idx))
#define SMF_V(acc, a, b) asm volatile("v_smfmac_f32_16x16x64_bf16 %0, %1, %2, %3" : "+v"(acc) : "v"(a), "v"(b), "v"(idx))
#define MF0(d, a, b) asm volatile("v_mfma_f32_16x16x32_bf16 %0, %1, %2, 0" : "=&v"(d) : "v"(a), "v"(b))
#define MF1(d, a, b) asm volatile("v_mfma_f32_16x16x32_bf16 %0, %1, %2, %0" : "+v"(d) : "v"(a), "v"(b))
#define MF0H(d, a, b) asm volatile("v_mfma_f32_16x16x16_bf16 %0, %1, %2, 0" : "=&v"(d) : "v"(a), "v"(b))
#define MF1H(d, a, b) asm volatile("v_mfma_f32_16x16x16_bf16 %0, %1, %2, %0" : "+v"(d) : "v"(a), "v"(b))
DEV v8u tr_frag(lptr p0, lptr p1, lptr p2, lptr p3) { return cat8(tr2(p0, p1), tr2(p2, p3)); }
constexpr int qslot(int ks) { return (ks & 1) && ks < 8 ? ks >> 1 : -1; }
constexpr int kslot(int ks) { return ks < 4 ? 4 + ks : -1; }
constexpr int vslot(int ks) { return ks >= 4 ? 4 + ks : -1; }
DEV u32 ring_map(u32 off, u32 half) { return ((off >> 13) << 14) + (half << 13) + (off & 8191u); }
#define ZRD(x, i) x = __builtin_bit_cast(f32x4, lds_r16(ZP + 16 * (i)))
#define VMAX(d, a, b) asm volatile("v_max_f32 %0, %1, %2" : "=v"(d) : "v"(a), "v"(b))
#define VMAX3(d, a, b, c_) asm volatile("v_max3_f32 %0, %1, %2, %3" : "=v"(d) : "v"(a), "v"(b), "v"(c_))
#define VADD(d, a, b) asm volatile("v_add_f32 %0, %1, %2" : "=v"(d) : "v"(a), "v"(b))
#define VEXP(d, a) asm volatile("v_exp_f32 %0, %1" : "=v"(d) : "v"(a))
#define VFMC(d, a, b) asm volatile("v_fmamk_f32 %0, %1, 0x3e0293ee, %2" : "=v"(d) : "v"(a), "v"(b))
#define VMULC(d, a) asm volatile("v_mul_f32 %0, 0x3e0293ee, %1" : "=v"(d) : "v"(a))
#define SEL(d, a, b) asm volatile("v_cndmask_b32 %0, %1, %2, %3" : "=v"(d) : "v"(a), "v"(b), "s"(himask))
#define QPERM(d, a, q) asm volatile("v_mov_b32_dpp %0, %1 quad_perm:" q " row_mask:0xf bank_mask:0xf" : "=v"(d) : "v"(a))

#define SUMRANGE 0x28000000u
#define HSAMP 71
DEV void role_head(const Params& p, lptr lds, int L, int h) {
    const int tid = threadIdx.x, lane = tid & 63, w = uni(tid >> 6), g = lane >> 4, r = lane & 15;
    const rsrc_t rs = mk_rsrc(p.ws, W_END);
    v8u wq0[10], wq1[10], wk0[10], wk1[10], wv0[10], wv1[10];
    lptr WL = lds + C_WL + w * (NLF * 2048) + lane * 16;
    {
        constexpr int GS = 1056, CS = 48 * GS;
        const v4u sq = mk_srd(p.Wq, DM * NQ * 2), sk = mk_srd(p.Wk, DM * NQ * 2), sv = mk_srd(p.Wv, DM * NQ * 2);
        const u32 vo = (u32)(g * NQ + h * DH + 8 * (r ^ ((g & 2) << 2))) * 2u;
        auto fill = [&](int ch) {
#pragma unroll 1
            for (int i = 0; i < 4; ++i) {
                const u32 dst = uu((ch % 3) * CS + (4 * i + w) * GS), so = uu((ch * 64 + 16 * i + 4 * w) * (NQ * 2));
                DMAIN(sq, dst, vo, so); DMAIN(sk, dst + 16 * GS, vo, so); DMAIN(sv, dst + 32 * GS, vo, so);
            }
        };
        int vote = 0;
        if (L == 1) {
            if (w == 0) vote = probe(rs, h);
            BAR();
            EV(2);
            pre_rings(p, rs, h);
        }
        fill(0); fill(1); fill(2);
        const int q4 = r >> 2;
        lptr tb = lds + (4 * (g >> 1) + q4) * GS + (g & 1) * 512 + ((w * 64 + (r & 3) * 8) ^ ((g & 1) << 7));
        SFOR(ch, 10)
            VMW(ch == 1 || ch == 2 ? 25 : ch < 8 ? 24 : 108 - 12 * ch);
            BAR();
            if constexpr (ch == 0) boot_set(p, rs, tid == 0 && L == 1 ? foff(F_RG + h) : OOB, (u32)((1 << 20) + vote));
            auto frag = [&](int m, int t) { lptr b = tb + (ch % 3) * CS + m * 16 * GS + t * 32; return tr_frag(b, b + 256, b + 8 * GS, b + 8 * GS + 256); };
            wq0[ch] = frag(0, 0); wq1[ch] = frag(0, 1); wk0[ch] = frag(1, 0); wk1[ch] = frag(1, 1); wv0[ch] = frag(2, 0); wv1[ch] = frag(2, 1);
            asm volatile("" : "+a"(wq0[ch]), "+a"(wk0[ch]), "+v"(wv0[ch]), "+v"(wv1[ch]));
            if constexpr (qslot(ch) < 0) asm volatile("" : "+a"(wq1[ch])); else asm volatile("" : "+v"(wq1[ch]));
            if constexpr (kslot(ch) < 0) asm volatile("" : "+a"(wk1[ch])); else asm volatile("" : "+v"(wk1[ch]));
            BAR();
            if constexpr (ch < 7) fill(ch + 3);
        SEND
#pragma unroll
        for (int ch = 0; ch < 10; ++ch) {
            if (qslot(ch) >= 0) { lds_w16(WL + qslot(ch) * 2048, wq1[ch].lo); lds_w16(WL + qslot(ch) * 2048 + 1024, wq1[ch].hi); }
            if (kslot(ch) >= 0) { lds_w16(WL + kslot(ch) * 2048, wk1[ch].lo); lds_w16(WL + kslot(ch) * 2048 + 1024, wk1[ch].hi); }
            if (vslot(ch) >= 0) { lds_w16(WL + vslot(ch) * 2048, wv1[ch].lo); lds_w16(WL + vslot(ch) * 2048 + 1024, wv1[ch].hi); }
        }
        CFENCE();
    }
    const u32 idx = (r & 2) ? 0xEEEEu : 0x4444u;
    const unsigned long long himask = 0xCCCCCCCCCCCCCCCCull;
    bool dead = false;
    EV(6);
    const u32 pol = get_pol(p, rs, dead);
    const u32 pol0 = pol & 1u;
    const u32 l16 = (u32)lane * 16u;
    const u32 mring = W_MRING + (u32)L * (MB / 4) + pol0 * 8192u, hint = mring + 4 * 32768u;
    const u32 fkh = lane < 3 ? near_(foff(F_QKV + lane * 64 + L * 32 + h), 0u, pol) : lane < 6 ? hint + (u32)(lane - 3) * 4u : OOB;
    u32 tm = 0;
    MK_SRD();
    const unsigned long long kmask = __builtin_amdgcn_ballot_w64((hblk(W_KQ, L * NSEG + lane, h, 0u, pol) & 8192u) != 0u),
                             vmask = __builtin_amdgcn_ballot_w64((hblk(W_VO, L * NSEG + lane, h, 1u, pol) & 8192u) != 0u);
    u32 kb = uu(W_KQ + (u32)(L * NSEG * NH + h) * 16384u + w * 2048);
    u32 dk = uu(C_K + w * 2048), dv = dk + (C_V - C_K), dq = dk + (C_QX - C_K), dm = uu(C_MEM + w * 2048), dm2 = uu(w < 2 ? C_MEM + 8192 + w * 1024 : C_DUMP);
    asm volatile("" : "+s"(dk), "+s"(dv), "+s"(dq), "+s"(dm), "+s"(dm2));
    u32 ksrc = kb + ((u32)kmask & 1u) * 8192u, vsrc = kb + (W_VO - W_KQ) + ((u32)vmask & 1u) * 8192u;
    lptr qw = lds + C_Q + (2 * g) * 288 + (32 * w + 2 * r) * 2;
    lptr kw = qw + (C_KM - C_Q);
    lptr qr = lds + C_Q + (2 * (r >> 2) + (r & 1)) * 288 + g * 16;
    lptr LB = lds + l16, VX = lds + C_V + w * 2048 + l16;
    if (tid >= 64 && tid < 70) { const u32 z = tid == 68 ? 0x3F803F80u : tid == 69 ? PSENT : 0u; lds_w16(lds + C_ZERO + (tid - 64) * 16, (v4u){z, z, z, z}); }
    if (w < 3) {
        const u32 ng = u_(-1000000000.0f * LOG2E);
        v4u mk;
#pragma unroll
        for (int i = 0; i < 4; ++i) mk[i] = w == 0 || (w == 1 && 4 * g + i > r) ? ng : 0u;
        lds_w16(lds + C_MASK + w * 1024 + l16, mk);
    }
    u32 zaddr = 0; PIN(zaddr);
    lptr ZP = lds + zaddr + C_ZERO;
#define LF_RD(slot, hf) lf[slot][hf] = lds_r16(WL + (slot) * 2048 + (hf) * 1024)
    BAR();
    const u32 ofl = tid == 192 ? near_(foff(F_O + L * 32 + h), 0u, pol) : tid == 193 && (h & 15) == 0 ? hint + 128u + (u32)h * 8u : OOB;
    u32 oso = (g & 1) ? OOB : W_ORING + (u32)L * (MB / 2) + ring_map((u32)((r >> 3) * 32768 + h * 1024 + w * 256 + (g >> 1) * 128 + (r & 7) * 16), pol0);
    u32 missed = 0, dirty = 1, rep = PSENT;
    asm volatile("" : "+v"(oso), "+v"(qw), "+v"(kw), "+v"(qr), "+v"(VX));
    asm volatile("" : "+s"(ksrc), "+s"(vsrc));
    wait_flags(rs, lane < 3 ? fkh : OOB, 1u, dead);
    EV(7);
    DMA32(dv, l16, vsrc); DMA32(dk, l16, ksrc);
    EV(8);
    auto next_slot = [&](u32 now) {
        const u32 hv = ld4(rs, fkh), per = rdl(hv, 4);
        if (per < 400u) { tm = rdl(hv, 5) - per; for (int n = 0; n < 3 && (int)(tm - now) < 0; ++n) tm += per; }
    };
    if (L == 1) { next_slot(rt32() + CH_KV0); tm -= 24u; }
    for (int s = 0; s < NSEG;) {
        {
            *(LAS u32*)(lds + C_DUMP + 128 + w * 4) = tm;
            VMW(5); BAR();
            st4s(rs, ofl, 0, (l16 & 16u) ? rep : (u32)s);
            const v4u c = lds_r16(lds + C_DUMP + 128) - tm;
            const u32 add = uu((u32)max(max((int)c.x, (int)c.y), max((int)c.z, (int)c.w)));
            tm += add;
            if (__builtin_expect((dirty | add) != 0u, 0)) {
                wait_until(tm - 44u);
                st16(rs, oso, lds_r16(ZP + 80));
            }
        }
        u32 ms = mring + (u32)(s & 3) * 32768u + w * 2048, ms2 = mring + (u32)(s & 3) * 32768u + 16384u + (w & 1) * 1024;
        u32 l3 = w < 2 ? l16 : OOB;
        asm volatile("" : "+s"(ms), "+s"(ms2)); PIN(l3);
        f32x4 aq0, aq1, ak0, ak1;
        v4u lf[NLF][2];
        ZRD(aq0, 0); ZRD(aq1, 1); ZRD(ak0, 2); ZRD(ak1, 3);
#pragma unroll
        for (int i = 0; i < 4; ++i) { LF_RD(i, 0); LF_RD(i, 1); }
        SCHED();
        v4u A[10];
#define HT(k) do {} while (0)
        v4u kx[8], qf[4], kmf[4], vx[2], ones, P;
        float fa, fb, fc, fd, sx[4], t[4], sm0, sm1, tm0, tm1, inv; u32 pa, pb, fl, pmm, tmp = 0; unsigned long long odd;
        f32x4 av0, av1, s0, s1, sm, oa, ob, rsum;
        v2u vm, pm, vh, vl, on[2];
        f32x2 zz[4];
#define LW(sl) cat8(lf[sl][0], lf[sl][1])
#define PQ(ks, tl) do { if constexpr (!(tl)) SMF_A(aq0, A[ks], wq0[ks]); else if constexpr (qslot(ks) < 0) SMF_A(aq1, A[ks], wq1[ks]); else { const v8u lw_ = LW(qslot(ks)); SMF_V(aq1, A[ks], lw_); } } while (0)
#define PK(ks, tl) do { if constexpr (!(tl)) SMF_A(ak0, A[ks], wk0[ks]); else if constexpr (kslot(ks) < 0) SMF_A(ak1, A[ks], wk1[ks]); else { const v8u lw_ = LW(kslot(ks)); SMF_V(ak1, A[ks], lw_); } } while (0)
#define PV(ks, tl) do { if constexpr (!(tl)) SMF_V(av0, A[ks], wv0[ks]); else if constexpr (vslot(ks) < 0) SMF_V(av1, A[ks], wv1[ks]); else { const v8u lw_ = LW(vslot(ks)); SMF_V(av1, A[ks], lw_); } } while (0)
#define KXR(i) kx[i] = lds_r16(LB + C_K + ((i) & 1) * 4096 + ((i) >> 1) * 1024)
#define XW(q_) do { *(LAS u32*)(q_) = pa; *(LAS u32*)((q_) + 288) = pb; } while (0)
        auto slot = [&](auto zc) __attribute__((always_inline)) {
            constexpr int z = decltype(zc)::value;
            constexpr int vs[] = {0, 8, 1, 10, 9, 2, 11, -1, -1, -1, -1, 4, 6, 12, -1, -1, -1, 13, 14, -1, 16, 18, 15, 3, 5, 17, 7, 19};
            if constexpr (z < 12) PQ(z >> 1, z & 1);
            else if constexpr (z < 20) PQ(6 + (z & 3), z >> 4);
            else if constexpr (z < 32) PK((z - 20) >> 1, z & 1);
            else if constexpr (z < 40) PK(6 + (z & 3), (z >> 2) & 1);
            else if constexpr (z < 42) { if constexpr (z == 40) MF0(s0, kx[0], qf[0]); else MF0(s1, kx[1], qf[0]); }
            else if constexpr (z < 48) { if constexpr (z & 1) MF1(s1, kx[z - 40], qf[(z - 40) >> 1]); else MF1(s0, kx[z - 40], qf[(z - 40) >> 1]); }
            else if constexpr (z == 55) MF0(sm, kmf[0], qf[0]);
            else if constexpr (z > 55 && z < 59) MF1(sm, kmf[z - 55], qf[z - 55]);
            else if constexpr (z == 62) MF0(rsum, ones, P);
            else if constexpr (z == 63) MF0(oa, vx[0], P);
            else if constexpr (z == 64) MF0(ob, vx[1], P);
            else if constexpr (z == 67) { const v2u ones2 = ones.lo; MF1H(rsum, ones2, pm); }
            else PV(vs[z - 48] >> 1, vs[z - 48] & 1);
#define USE(x) asm volatile("" :: "v"(x))
            if constexpr (z == 20 || z == 24 || z == 26) USE(lf[4 + ((z - 20) >> 1)][1]);
            if constexpr (z == 41) USE(qf[3]);
            if constexpr (z == 45 || z == 46) USE(kx[z - 39]);
            if constexpr (z == 53) USE(lf[9][1]);
            if constexpr (z >= 54 && z < 58) USE(kmf[z - 54]);
            if constexpr (z == 61) USE(vl);
            if constexpr (z == 64) USE(lf[10][1]);
            if constexpr (z == 69) USE(lf[11][1]);
            if constexpr (z == 72) USE(lf[12][1]);
            if constexpr (z == 74) USE(lf[13][1]);
            if constexpr (z < 8) A[2 + z] = lds_r16(LB + C_MEM + (2 + z) * 1024);
            if constexpr (z >= 15 && z < 23) LF_RD(4 + ((z - 15) >> 1), (z - 15) & 1);
            if constexpr (z >= 24 && z < 27) KXR(z - 24);
            if constexpr (z == 27) XW(qw);
            if constexpr (z >= 28 && z < 31) KXR(z - 25);
            if constexpr (z == 32) asm volatile("s_waitcnt lgkmcnt(3)\n\ts_barrier" ::: "memory");
            if constexpr (z >= 33 && z < 37) qf[z - 33] = lds_r16(qr + (z - 33) * 64);
            if constexpr (z == 37 || z == 38) LF_RD(8, z - 37);
            if constexpr (z == 39 || z == 41) KXR(6 + (z == 41));
            if constexpr (z == 44) XW(kw);
            if constexpr (z == 45 || z == 46) LF_RD(9, z - 45);
            if constexpr (z == 47) vx[0] = lds_r16(VX);
            if constexpr (z == 48) { VMW(0); vh = *(LAS v2u*)(VX + 1032); }
            if constexpr (z == 54) { vl = *(LAS v2u*)(VX + 1024); vx[1] = (v4u){vh.x, vh.y, vl.x, vl.y}; }
            if constexpr (z == 49) { USE(lf[9][0]); asm volatile("s_barrier" ::: "memory"); }
            if constexpr (z >= 50 && z < 54) kmf[z - 50] = lds_r16(qr + (C_KM - C_Q) + (z - 50) * 64);
            if constexpr (z == 59 || z == 60) LF_RD(10, z - 59);
            if constexpr (z == 61 || z == 65) LF_RD(11, z == 65);
            if constexpr (z == 66 || z == 68) LF_RD(12, z == 68);
            if constexpr (z == 69 || z == 70) LF_RD(13, z - 69);
            if constexpr (z == HSAMP) fl = ld4(rs, fkh);
            SCHED();
            if constexpr (z == 19) HT(2);
            if constexpr (z == 39) HT(3);
            if constexpr (z == 51) HT(4);
            if constexpr (z == 64) HT(5);
            if constexpr (z == 20) VADD(fa, aq0[0], aq0[2]);
            if constexpr (z == 21) VADD(fb, aq0[1], aq0[3]);
            if constexpr (z == 23) VADD(fc, aq1[0], aq1[2]);
            if constexpr (z == 24) VADD(fd, aq1[1], aq1[3]);
            if constexpr (z == 25) VCVT(pa, fa, fc);
            if constexpr (z == 26) VCVT(pb, fb, fd);
            if constexpr (z >= 44 && z < 48) asm volatile("v_mov_b64 %0, 0" : "=v"(zz[z - 44]));
            if constexpr (z == 47) { av0 = __builtin_shufflevector(zz[0], zz[1], 0, 1, 2, 3); av1 = __builtin_shufflevector(zz[2], zz[3], 0, 1, 2, 3); }
            if constexpr (z == 48 || z == 49) asm volatile("v_mov_b64 %0, %1" : "=v"(on[z - 48]) : "s"(0x3F803F803F803F80ull));
            if constexpr (z == 49) ones = __builtin_shufflevector(on[0], on[1], 0, 1, 2, 3);
            if constexpr (z == 40) { VADD(fa, ak0[0], ak0[2]); VADD(fb, ak0[1], ak0[3]); }
            if constexpr (z == 42) { VADD(fd, ak1[1], ak1[3]); VADD(fc, ak1[0], ak1[2]); }
            if constexpr (z == 43) { VCVT(pb, fb, fd); VCVT(pa, fa, fc); }
            if constexpr (z >= 50 && z < 54) SEL(sx[z - 50], s0[z - 50], s1[z - 50]);
            if constexpr (z >= 54 && z < 58) VEXP(t[z - 54], sx[z - 54]);
            if constexpr (z == 58) asm volatile("v_cvt_pk_bf16_f32 %0, %2, %3\n\tv_cvt_pk_bf16_f32 %1, %4, %5" : "=&v"(P.x), "=v"(P.y) : "v"(t[0]), "v"(t[1]), "v"(t[2]), "v"(t[3]));
            if constexpr (z == 59) QPERM(P.z, P.x, "[2,3,0,1]");
            if constexpr (z == 60) QPERM(P.w, P.y, "[2,3,0,1]");
            if constexpr (z == 61) { USE(sm); VMULC(sm0, sm[0]); }
            if constexpr (z == 62) { VEXP(tm0, sm0); VMULC(sm1, sm[1]); }
            if constexpr (z == 63) VEXP(tm1, sm1);
            if constexpr (z == 64) asm volatile("v_cvt_pk_bf16_f32 %0, %2, %3\n\tv_cndmask_b32 %1, %0, 0, %4" : "=&v"(pmm), "=v"(pm.x) : "v"(tm0), "v"(tm1), "s"(himask));
            if constexpr (z == 65) asm volatile("v_cndmask_b32 %0, 0, %1, %2" : "=v"(pm.y) : "v"(pmm), "s"(himask));
            if constexpr (z == 67) { SEL(oa[0], oa[0], ob[0]); SEL(oa[1], oa[1], ob[1]); }
            if constexpr (z == 68 || z == 69) SEL(oa[z - 66], oa[z - 66], ob[z - 66]);
            if constexpr (z == 70) asm volatile("v_sub_u32 %0, %1, %2" : "=v"(pa) : "v"(rsum[0]), "s"(0x2B800000u), "v"(rsum));
            if constexpr (z == 71) asm volatile("v_cmp_le_u32 %0, %1, %2" : "=s"(odd) : "s"(SUMRANGE), "v"(pa));
            if constexpr (z == 72) VADD(fa, av0[0], av0[2]);
            if constexpr (z == 73) { NOPS(0); VADD(fb, av0[1], av0[3]); }
            if constexpr (z == 74) VCVT(vm.x, fa, fb);
            SCHED();
        };
        asm volatile("s_mov_b32 m0, %2\n\t" WAIT_TXT("%0", "%1") "buffer_load_dwordx4 %4, %5, %6 offen sc1 lds\n\tbuffer_load_dwordx4 %4, %5, %6 offen offset:1024 sc1 lds\n\t"
                     "s_mov_b32 m0, %3\n\ts_nop 0\n\tbuffer_load_dwordx4 %8, %5, %7 offen sc1 lds\n\ts_sleep 3\n\ts_mov_b32 m0, %9\n\ts_nop 0\n\t"
                     "buffer_load_dwordx4 %4, %5, %10 offen sc1 lds\n\tbuffer_load_dwordx4 %4, %5, %10 offen offset:1024 sc1 lds\n\ts_waitcnt vmcnt(2) lgkmcnt(0)\n\ts_barrier"
                     : "=&s"(tmp) : "s"(uu(tm - 1u)), "s"(dm), "s"(dm2), "v"(l16), "s"(srd), "s"(ms), "s"(ms2), "v"(l3), "s"(dq), "s"(ksrc ^ 8192u) : "vcc", "scc", "memory");
#pragma unroll
        for (int ks = 0; ks < 2; ++ks) A[ks] = lds_r16(LB + C_MEM + ks * 1024);
        SCHED();
        static_for<0, 76>(slot);
        u32 fail = 0;
        if (__builtin_expect(odd != 0ull, 0)) {
            u32 mn = ~0u;
#pragma unroll 5
            for (int i = 0; i < 10; ++i) { const v4u e = lds_r16(LB + C_MEM + i * 1024); mn = min(min(min(mn, e.x ^ PSENT), e.y ^ PSENT), min(e.z ^ PSENT, e.w ^ PSENT)); }
            fail = missed < SPIN_CAP && !all_(mn != 0u) ? OOB : 0u;
            float mx = fmaxf(fmaxf(fmaxf(sx[0], sx[1]), fmaxf(sx[2], sx[3])), fmaxf(sm0, sm1));
            mx = max32(max16(fmaxf(mx, dpp<0x4E>(mx))));
#pragma unroll
            for (int i = 0; i < 4; ++i) t[i] = __builtin_amdgcn_exp2f(sx[i] - mx);
            P.x = pk(t[0], t[1]); P.y = pk(t[2], t[3]); P.z = u_(dpp<0x4E>(f_(P.x))); P.w = u_(dpp<0x4E>(f_(P.y)));
            pmm = pk(__builtin_amdgcn_exp2f(sm0 - mx), __builtin_amdgcn_exp2f(sm1 - mx));
            pm = (r & 2) ? (v2u){0u, pmm} : (v2u){pmm, 0u};
            asm volatile("s_nop 1" : "+v"(P), "+v"(pm));
            MF0(rsum, ones, P); MF0(oa, vx[0], P); MF0(ob, vx[1], P);
            NOPS(7);
            { const v2u ones2 = ones.lo; MF1H(rsum, ones2, pm); }
#pragma unroll
            for (int i = 0; i < 4; ++i) SEL(oa[i], oa[i], ob[i]);
            asm volatile("s_nop 4" :: "v"(rsum));
            oso |= fail;
        }
        asm volatile("v_rcp_f32 %0, %3\n\ts_nop 4\n\tv_add_f32 %1, %4, %6\n\tv_add_f32 %2, %5, %7\n\tv_cvt_pk_bf16_f32 %1, %1, %2\n\ts_nop 0"
                     : "=&v"(inv), "=&v"(vm.y), "=&v"(fd) : "v"(rsum[0]), "v"(av1[0]), "v"(av1[1]), "v"(av1[2]), "v"(av1[3]));
        asm volatile("v_mfma_f32_16x16x16_bf16 %0, %1, %2, %0\n\ts_nop 6" : "+v"(oa) : "v"(vm), "v"(pm));
        {
            v4u oc;
            const f32x2 i2 = {inv, inv};
            const f32x2 u0 = oa.lo * i2, u1 = oa.hi * i2;
            asm volatile("v_cvt_pk_bf16_f32 %0, %4, %5\n\tv_cvt_pk_bf16_f32 %2, %4, %5\n\tv_cvt_pk_bf16_f32 %1, %6, %7\n\tv_cvt_pk_bf16_f32 %3, %6, %7\n\t"
                         "v_permlane16_swap_b32 %0, %2\n\ts_nop 0\n\tv_permlane16_swap_b32 %1, %3"
                         : "=&v"(oc.x), "=&v"(oc.y), "=&v"(oc.z), "=&v"(oc.w) : "v"(u0.x), "v"(u0.y), "v"(u1.x), "v"(u1.y));
            st16(rs, oso, oc);
        }
        SCHED();
        if (__builtin_expect(fail == 0u, 1)) {
        unsigned long long clk;
        asm volatile("s_memrealtime %0" : "=s"(clk));
        HT(6);
        v4u tq[4], tk[8], tv[2], px, pv;
        f32x4 c0, c1, cm, o[4], sum;
        float ex[10], cs0, cs1;
        v2u p0, p1, tvm;
        u32 oo, kvo, oz; bool kvok;
        u32 ln = l16; PIN(ln);
        lptr QX = lds + C_QX + (w & 1) * 4096 + ln, VO = lds + C_V + (w ^ 1) * 2048 + ln, VMX = lds + C_VM + w * 512 + (ln >> 1), MK = lds + C_MASK + (w & 1) * 1024 + ln;
#define TKR(i) tk[i] = lds_r16(LB + C_K + ((i) >> 2) * 4096 + ((i) & 3) * 1024)
#pragma unroll
        for (int i = 0; i < 4; ++i) tq[i] = lds_r16(QX + i * 1024);
        c0 = __builtin_bit_cast(f32x4, lds_r16(MK + 1024));
        TKR(0);
        *(LAS v2u*)VMX = vm;
        SCHED();
        SFOR(i, 12)
            if constexpr (i == 0) MF0(cm, kmf[0], tq[0]); else if constexpr (i < 4) MF1(cm, kmf[i], tq[i]);
            else if constexpr (i < 8) MF1(c0, tk[i - 4], tq[i - 4]); else MF1(c1, tk[i - 4], tq[i - 8]);
            if constexpr (i < 3) TKR(i + 1);
            if constexpr (i == 2) c1 = __builtin_bit_cast(f32x4, lds_r16(MK));
            if constexpr (i >= 3 && i < 7) TKR(i + 1);
            if constexpr (i == 7) { vx[1] = lds_r16(VX + 1024); tv[0] = lds_r16(VO); }
            if constexpr (i == 8) tv[1] = lds_r16(VO + 1024);
            SCHED();
            if constexpr (i == 0) { oo = ln + (ln & 0x300u) + ((vsrc ^ 8192u) + (u32)((w & 1) * 256)); PIN(oo); kb += NH * 16384; PINS(kb); }
            if constexpr (i == 1) { ksrc = kb + (((kmask >> ((s + 1) & 63)) & 1ull) ? 8192u : 0u); PINS(ksrc); }
            if constexpr (i == 3) { vsrc = kb + (((vmask >> ((s + 1) & 63)) & 1ull) ? W_VO - W_KQ + 8192u : W_VO - W_KQ); PINS(vsrc); }
            if constexpr (i == 6) { USE(cm); VMULC(cs0, cm[0]); }
            if constexpr (i == 8) VMULC(cs1, cm[1]);
            if constexpr (i == 9) VEXP(ex[8], cs0);
            if constexpr (i == 10) { VEXP(ex[9], cs1); VEXP(ex[0], c0[0]); }
            if constexpr (i == 11) { VEXP(ex[1], c0[1]); VEXP(ex[2], c0[2]); VEXP(ex[3], c0[3]); }
            SCHED();
        SEND
        asm volatile("v_cvt_pk_bf16_f32 %0, %6, %7\n\tv_mov_b32 %1, 0\n\tv_mov_b32 %2, 0\n\tv_cvt_pk_bf16_f32 %3, %6, %7\n\tv_cvt_pk_bf16_f32 %4, %8, %9\n\tv_cvt_pk_bf16_f32 %5, %10, %11"
                     : "=&v"(p0.x), "=&v"(p0.y), "=&v"(p1.x), "=&v"(p1.y), "=&v"(px.x), "=&v"(px.y) : "v"(ex[8]), "v"(ex[9]), "v"(ex[0]), "v"(ex[1]), "v"(ex[2]), "v"(ex[3]));
        asm volatile("s_waitcnt lgkmcnt(0)\n\ts_barrier" : "+s"(clk) :: "memory");
        tvm = *(LAS v2u*)(VMX + (512 - (w & 1) * 1024));
        pv = lds_r16(ZP + 80);
        SCHED();
#pragma unroll
        for (int i = 0; i < 4; ++i) VEXP(ex[4 + i], c1[i]);
        asm volatile("v_cvt_pk_bf16_f32 %0, %2, %3\n\tv_cvt_pk_bf16_f32 %1, %4, %5\n\ts_nop 1" : "=&v"(px.z), "=&v"(px.w) : "v"(ex[4]), "v"(ex[5]), "v"(ex[6]), "v"(ex[7]));
        {
            const v2u ones2 = ones.lo;
            const u32 now = (u32)clk;
            auto pvm = [&](auto first) __attribute__((always_inline)) {
                constexpr bool F = decltype(first)::value;
                SCHED();
                MF0(sum, ones, px); if constexpr (F) { oz = (oso & ~(3u << 17)) | (u32)((s + 3) & 3) << 17; PIN(oz); } SCHED();
                MF0(o[0], vx[0], px); if constexpr (F) { oso = (oso & ~(3u << 17)) | (u32)((s + 1) & 3) << 17; PIN(oso); } SCHED();
                MF0(o[1], vx[1], px); NOPS(1);
                MF0(o[2], tv[0], px); NOPS(0);
                MF1H(sum, ones2, p0); NOPS(1);
                MF0(o[3], tv[1], px); NOPS(1);
                MF1H(o[0], vm, p0);
                MF1H(o[1], vm, p1);
                MF1H(o[2], tvm, p0);
                asm volatile("v_sub_u32 %0, %2, %3\n\tv_rcp_f32 %1, %2" : "=&v"(pa), "=&v"(inv) : "v"(sum[0]), "s"(0x2B800000u), "v"(sum));
                MF1H(o[3], tvm, p1);
                SCHED();
            };
            pvm(IC<1>{});
            if (__builtin_expect(__builtin_amdgcn_ballot_w64(pa >= SUMRANGE) != 0ull, 0)) {
                const f32x4 m0 = __builtin_bit_cast(f32x4, lds_r16(MK + 1024)), m1 = __builtin_bit_cast(f32x4, lds_r16(MK));
                float mx = fmaxf(cs0, cs1);
#pragma unroll
                for (int i = 0; i < 4; ++i) { ex[i] = m0[i] == 0.f ? c0[i] : m0[i]; ex[4 + i] = m1[i] == 0.f ? c1[i] : m1[i]; mx = fmaxf(mx, fmaxf(ex[i], ex[4 + i])); }
                mx = max32(max16(mx));
#pragma unroll
                for (int i = 0; i < 4; ++i) px[i] = pk(__builtin_amdgcn_exp2f(ex[2 * i] - mx), __builtin_amdgcn_exp2f(ex[2 * i + 1] - mx));
                p0.x = p1.y = pk(__builtin_amdgcn_exp2f(cs0 - mx), __builtin_amdgcn_exp2f(cs1 - mx));
                asm volatile("s_nop 7" : "+v"(px), "+v"(p0), "+v"(p1));
                pvm(IC<0>{});
                NOPS(3);
            }
            asm volatile("s_nop 1" : "+v"(o[0]), "+v"(o[1]));
            st16(rs, oo, (v4u){pk2(o[0].lo * inv), pk2(o[0].hi * inv), pk2(o[1].lo * inv), pk2(o[1].hi * inv)});
            asm volatile("" : "+v"(o[2]), "+v"(o[3]));
            st16(rs, oo ^ 2048u, (v4u){pk2(o[2].lo * inv), pk2(o[2].hi * inv), pk2(o[3].lo * inv), pk2(o[3].hi * inv)});
            SCHED();
            PIN(fl); kvok = ((u32)__builtin_amdgcn_ballot_w64(fl < (u32)s + 2) & 7u) == 0u;
            kvo = l16 | (kvok ? 0u : OOB);
            DMA32(dv, kvo, vsrc); DMA32(dk, kvo, ksrc);
            st16(rs, oz, pv);
            SCHED();
            rep = (u32)s << 16 | min(missed, 1u) << 14 | min(now - tm, 0x3FFFu);
            if (h == 0 && w == 3) { LST(L * 64 + s, now); LST(128 + L * 64 + s, ((int)(rdl(fl, 3) - now) >= CH_VIS - 4) | ((int)kvok << 1) | ((int)min(missed, 3u) << 2)); LST(640 + L * 64 + s, tm); LST(1728 + L * 64 + s, __builtin_amdgcn_s_memtime()); }
            tm = rdl(fl, 5);
            missed = missed == SPIN_CAP ? missed : 0u;
            dirty = 0;
            if (__builtin_expect(rdl(fl, 3) - now + CH_LATE > 4000u && s + 1 < NSEG, 0)) {
                dirty = 1;
#pragma unroll 1
                for (int n = 0; n < 64; ++n) {
                    CFENCE();
                    const u32 hv = ld4(rs, fkh);
                    tm = rdl(hv, 5);
                    if (rdl(hv, 3) - now + CH_LATE <= 4000u) break;
                }
            }
        }
        LS2(L == 0 && w == 0, h);
        if (__builtin_expect(!kvok, 0) && s + 1 < NSEG) {
            st4s(rs, (ln == 0 && w == 0) ? hint + 4096u + (u32)h * 128u : OOB, 0, (u32)s + 1u);
            dirty = 1;
            wait_flags(rs, ln < 48u ? fkh : OOB, (u32)s + 2, dead);
            DMA32(dv, l16, vsrc); DMA32(dk, l16, ksrc);
            const u32 now = rt32() + CH_KV;
            if ((int)(tm - now) < 0) next_slot(now);
        }
        ++s;
        } else {
            u32 ln = l16; PIN(ln);
            oso = (ln & 256u) ? oso : oso & ~OOB;
            const u32 was = tm - tmp;
            tm = rdl(fl, 5); dirty = 1;
            if ((int)(tm - rdl(fl, 4) - was) > 24) tm -= rdl(fl, 4);
            if ((++missed & 7u) == 0u) {
                *(LAS u32*)(lds + C_DUMP + (w == 0 ? 0 : 64)) = ld4(rs, foff(F_ERR));
                BAR();
                missed = uu(*(LAS u32*)(lds + C_DUMP)) != 0u ? SPIN_CAP : missed;
                st4s(rs, missed == SPIN_CAP ? foff(F_ERR) : OOB, 0, 1u);
            }
        }
    }
    DRAIN(); BAR();
    st4s(rs, ofl, 0, (l16 & 16u) ? rep : NSEG);
}

#define WO_NI 4
#define WROT 0
DEV void role_wo(const Params& p, lptr lds, int j) {
    const int tid = threadIdx.x, lane = tid & 63, w = uni(tid >> 6), g = lane >> 4, r = lane & 15;
    const int rg = j & 1, cg = j >> 1, rot = cg & WROT;
    const rsrc_t rs = mk_rsrc(p.ws, W_END);
    v8u wo[8][5];
    {
        const v4u so_ = mk_srd(p.Wo, DM * NQ * 2);
        const int one = (0x3F2FF1F0u >> (4 * (cg & 7))) & 15;
        u32 vo[3];
#pragma unroll
        for (int i = 0; i < 3; ++i) { const int n = 64 * i + lane; vo[i] = n % 6 < 5 ? (u32)((n / 6) * (DM * 2) + cg * 80 + (n % 6) * 16) : ((n / 6) & 3) == one ? (u32)((n / 6) * (DM * 2) + (cg >> 3) * 640 + 304 + rg * 16) : OOB; }
        auto fill = [&](int b) {
            const u32 dst = uu(w * 36864 + (b % 3) * 12288), src = uu((8 * w + ((b + rot) & 7)) * (DH * DM * 2));
#pragma unroll
            for (int i = 0; i < 12; ++i) DMAIN(so_, dst + i * 1024, vo[i % 3], src + (i / 3) * (32 * DM * 2));
        };
#pragma unroll 1
        for (int b = 0; b < 3; ++b) fill(b);
        const int q4 = r >> 2;
        lptr tb = lds + w * 36864 + (32 * (g >> 1) + 16 * (q4 >> 1) + 8 * ((r >> 1) & 1) + 4 * (g & 1) + (q4 & 1)) * 96 + (r & 1) * 8;
        SFOR(b, 8)
            VMW(b < 6 ? 24 : 84 - 12 * b);
#pragma unroll
            for (int t = 0; t < 5; ++t) {
                lptr q = tb + (b % 3) * 12288 + (t == 0 ? 4 : (w + t) & 3) * 16;
                wo[b][t] = tr_frag(q, q + 192, q + 64 * 96, q + 64 * 96 + 192);
                if (t < 4) asm volatile("" : "+a"(wo[b][t])); else asm volatile("" : "+v"(wo[b][t]));
            }
            CFENCE();
            if constexpr (b < 5) fill(b + 3);
        SEND
    }
    BAR();
    EV(6);
    const u32 idx = (r & 2) ? 0xEEEEu : 0x4444u;
    bool dead = false;
    const u32 pol0 = get_pol(p, rs, dead) & 1u;
    const u32 l16 = (u32)lane * 16u;
    u32 oo[8];
#pragma unroll
    for (int i = 0; i < 8; ++i) oo[i] = uu(W_ORING + ring_map((u32)(rg * 32768 + (8 * w + ((i + rot) & 7)) * 1024), pol0));
    const u32 hint = W_MRING + 4 * 32768u + pol0 * 8192u;
    u32 hvo[2];
#pragma unroll
    for (int l = 0; l < 2; ++l) hvo[l] = lane < 2 ? hint + (u32)(l ^ 1) * (MB / 4) + (u32)lane * 4u : lane == 2 ? hint + (u32)l * (MB / 4) + 128u : lane == 3 ? foff(F_ERR) : lane == 4 ? hint + (u32)l * (MB / 4) + 256u :
                 (lane >= 32 && j == 0 && w == 3) ? hint + (u32)l * (MB / 4) + (u32)lane * 128u : OOB;
    const int xo = g * 128 + (r & 7) * 16, O = 5 * cg + (r < 8 ? w : 4), q2 = 2 + (r >> 3);
    lptr xw0 = lds + (r < 8 ? 2048 + w * 4 : ((w + 1) & 3) * 512 + 4) + xo, xw1 = lds + ((w + q2) & 3) * 512 + q2 * 4 + xo;
    lptr xr = lds + (r < 8 ? w : 4) * 512 + xo;
    if (r < 8) { *(LAS u32*)xr = 0u; *(LAS u32*)(xr + 8192) = 0u; }
    const u32 mpo = (r & 4) == 0 && (r < 8 || w == 0) ? W_MRING + ring_map((u32)((O >> 1) * 256 + rg * 128 + (4 * (g & 1) + 2 * (O & 1) + (g >> 1)) * 16 + (r & 3) * 4), pol0) : OOB;
    constexpr u32 PER0 = CH_W0 + CH_MW + CH_H0 + CH_MH + 2 * CH_VIS;
    const bool lead = j == 0 && w == 3;
    u32 hso = OOB;
    MK_SRD();
    u32 t = 0, tn = 0;
    int sn[2] = {0, 0};
    u32 hav = 4 * CH_H0, wav = 4 * CH_W0, mh = 8 * (CH_MH + CH_M0), mw = 8 * (CH_MW + CH_M0), ok[2] = {0u, 0u}, dw[2] = {CH_W0 + CH_VIS + CH_MW + CH_REC, CH_W0 + CH_VIS + CH_MW + CH_REC};
    u32 zero = 0, hv = 0xFFFFu, pst = 1, pdur = 0, pmp = OOB;
    const u32 psent = PSENT;
    lptr sw = lds + 5120 + w * 4;
    bool stop = false;
    asm volatile("" : "+v"(zero), "+v"(sw), "+v"(xw0), "+v"(xw1), "+v"(xr));
    auto turn = [&](auto lc) __attribute__((always_inline)) {
        constexpr int L = decltype(lc)::value, M = L ^ 1;
        u32 now, done, rep, rd, lost, dh, dn, nt, za; v4u hw;
        const int sc = sn[L];
        auto books = [&](auto ic) __attribute__((always_inline)) {
            constexpr int i = decltype(ic)::value;
            if constexpr (i == 0) { rep = max(rdl(hv, 2), rdl(hv, 4)); PINS(rep); }
            if constexpr (i == 1) { lost = (u32)(__builtin_amdgcn_ballot_w64(hv == (u32)sn[M]) >> 32); PINS(lost); }
            if constexpr (i == 2) { lost |= (rep >> 16) ^ (u32)sn[M]; PINS(lost); }
            if constexpr (i == 3) { lost |= ok[M] ^ 2u; rd = rep & 0xFFFFu; PINS(lost); PINS(rd); }
            if constexpr (i == 4) { lost = lost == 0u ? CH_UP * 8u : 0u; PINS(lost); }
            if constexpr (i == 5) { dn = wav >> 2; PINS(dn); pdur = min(pdur, dn + 8u); PINS(pdur); }
            if constexpr (i == 6) { pdur = (done & ok[M]) ? pdur : dn; PINS(pdur); }
            if constexpr (i == 7) { wav += pdur - dn; PINS(wav); }
            if constexpr (i == 8) { dh = hav >> 2; PINS(dh); nt = min(rd, dh + 8u); PINS(nt); }
            if constexpr (i == 9) { nt = rd < 0x3FFFu ? nt : dh; PINS(nt); }
            if constexpr (i == 10) { hav += nt - dh; PINS(hav); }
            if constexpr (i == 11) { mw = max(mw - CH_DEC, CH_MW * 8u); PINS(mw); }
            if constexpr (i == 12) { mh = max(mh - CH_DEC, CH_MH * 8u); PINS(mh); }
            if constexpr (i == 13) { rd = rd >> 14 == 1u ? lost : 0u; PINS(rd); }
            if constexpr (i == 14) { mw += rd; mh += lost - rd; PINS(mw); PINS(mh); }
            if constexpr (i == 15) { ok[M] = done ? 1u : (ok[M] & 1u) << 1; PINS(ok[M]); }
            if constexpr (i == 16) { dh = (2u * hav + mh) / 8u + (sn[1] < 2 ? CH_VIS + CH_SOLO : CH_VIS); PINS(dh); }
            if constexpr (i == 17) { dn = (2u * wav + mw) / 8u + CH_VIS; PINS(dn); }
            if constexpr (i == 18) { nt = dw[L] + dh; PINS(nt); }
            if constexpr (i == 19) { dh += dn; dw[L] = dn; sn[M] += (int)done; PINS(dh); PINS(sn[M]); }
            if constexpr (i == 20) { rd = tn - t + (dh >> 1); PINS(rd); }
            if constexpr (i == 21) { rd = sn[M] + 1 < NSEG ? rd : 0u; PINS(rd); }
            if constexpr (i == 22) { nt = t + (u32)max((int)nt, (int)rd); PINS(nt); }
            if constexpr (i == 23) { hw.x = nt; hw.y = dh; PIN(hw.x); PIN(hw.y); }
            if constexpr (i == 24) { hw.z = nt + dn; hw.w = 0u; PIN(hw); }
            if constexpr (i == 25) st16(rs, hso + L * (MB / 4), hw);
        };
        if (__builtin_expect(sc + 1 < NSEG, 1)) {
            u32 vo = l16 + (L * (MB / 2) + (u32)(sc & 3) * 131072u), mp = mpo + (L * (MB / 4) + (u32)((sc + 1) & 3) * 32768u), zb, hn;
            done = uu(pst) ^ 1u;
            za = done ? pmp : OOB;
            zb = ok[L] == 1u ? OOB : mp;
            pmp = mpo + (L * (MB / 4) + (u32)(sc & 3) * 32768u);
            *(LAS u32*)(sw + L * 8192) = zero;
            f32x4 acc[5] = {};
            asm volatile("" : "+v"(acc[0]), "+v"(acc[1]), "+v"(acc[2]), "+v"(acc[3]), "+v"(acc[4]), "+v"(mp), "+v"(pmp), "+v"(vo), "+v"(za), "+v"(zb));
            PINS(done);
#define WT(k) do {} while (0)
            v4u d[8];
            {
                u32 tmp;
                asm volatile(WAIT_TXT("%1", "%2") "buffer_load_dwordx4 %0, %3, %4, %5 offen sc1\n\ts_waitcnt lgkmcnt(0)"
                             : "=&v"(d[0]), "=&s"(tmp) : "s"(uu(t - 1u)), "v"(vo), "s"(srd), "s"(oo[0]) : "vcc", "scc", "memory");
            }
            WT(0);
            SFOR(i, 7)
                asm volatile("buffer_load_dwordx4 %0, %1, %2, %3 offen sc1" : "=v"(d[i + 1]) : "v"(vo), "s"(srd), "s"(oo[i + 1]) : "memory");
                SCHED();
                static_for<3 * i, 3 * i + 3>(books);
                SCHED();
            SEND
            LD4A(hn, hvo[L], 0);
            st4s(rs, za, 0, psent);
            st4s(rs, zb, 0, psent);
            SCHED();
            WT(1);
            float f0[5], f1[5], xs; unsigned long long bad; v4u sv;
#define WSW(o_) asm volatile("v_permlane32_swap_b32 %0, %1" : "+v"(f0[o_]), "+v"(f1[o_]))
#define WAD(o_) asm volatile("v_add_f32_dpp %0, %1, %0 row_ror:8 row_mask:0xf bank_mask:0xf\n\tv_add_f32_dpp %0, %2, %3 row_ror:8 row_mask:0xf bank_mask:0xc" : "+v"(f0[o_]) : "v"(f1[o_]), "v"(f0[o_ + 1]), "v"(f1[o_ + 1]))
#define WFO(o_) do { VADD(f0[o_], acc[o_][0], acc[o_][2]); VADD(f1[o_], acc[o_][1], acc[o_][3]); } while (0)
#define WOWN(k) do { if constexpr (WO_NI + (k) < 8) SMF_V(acc[4], d[(WO_NI + (k)) & 7], wo[(WO_NI + (k)) & 7][4]); SCHED(); } while (0)
            SFOR(i, 40)
                constexpr int pc = i / 5, oc = i % 5;
                if constexpr (oc == 0) VMW(10 - pc);
                if constexpr (oc < 4) SMF_A(acc[oc], d[pc], wo[pc][oc]); else if constexpr (pc < WO_NI) SMF_V(acc[4], d[pc], wo[pc][4]);
                if constexpr (i == 0) WT(2);
                if constexpr (i == 20) WT(3);
                if constexpr (i == 35) WT(4);
                if constexpr (i < 4) books(IC<21 + i>{});
                if constexpr (i == 6) { xs = 0.f; PIN(xs); }
                if constexpr (i == 36) NOPS(1);
                if constexpr (i == 37) NOPS(2);
                SCHED();
            SEND
            WFO(0); WFO(1);
            asm volatile("v_cmp_u_f32 %0, %1, %2" : "=s"(bad) : "v"(f0[0]), "v"(f1[0]));
            WSW(0); WSW(1); WFO(2); WAD(0); WFO(3);
            *(LAS float*)(xw0 + L * 8192) = f0[0]; SCHED();
            WSW(2); WSW(3);
            if (__builtin_expect(bad != 0ull, 0)) {
                u32 mn = ~0u;
#pragma unroll
                for (int i = 0; i < 8; ++i) { u32 e = d[i].x; PIN(e); mn = min(mn, e ^ PSENT); }
                if (all_(mn != 0u)) {
#pragma unroll
                    for (int i = 0; i < 8; ++i) { v4u e = d[i]; PIN(e); mn = min(min(mn, e.y ^ PSENT), min(e.z ^ PSENT, e.w ^ PSENT)); }
                }
                *(LAS u32*)(sw + L * 8192) = all_(mn != 0u) ? 0u : 1u;
            }
            WAD(2);
            *(LAS float*)(xw1 + L * 8192) = f0[2]; SCHED();
            WOWN(0); WOWN(1); WOWN(2); WOWN(3);
            WT(5);
            f32x4 xa; float nb; u32 cw, mq;
            asm volatile("s_waitcnt lgkmcnt(0)\n\ts_barrier\n\tds_read_b128 %0, %2 offset:%4\n\tds_read_b128 %1, %3 offset:%4+5120\n\ts_nop 3"
                         : "=&v"(xa), "=&v"(sv) : "v"(xr), "v"(zero), "i"(L * 8192) : "memory");
            WT(6);
            WFO(4);
            asm volatile("s_nop 0\n\tv_permlane32_swap_b32 %1, %2\n\ts_nop 1\n\tv_add_f32_dpp %0, %2, %1 row_ror:8 row_mask:0xf bank_mask:0x3\n\ts_waitcnt lgkmcnt(1)" : "+v"(xs), "+v"(f0[4]), "+v"(f1[4]));
            asm volatile("v_add_f32 %0, %0, %4\n\tv_add_f32 %1, %5, %6\n\tv_add_f32 %0, %0, %7\n\tv_add_f32 %0, %0, %1\n\tv_cmp_u_f32 vcc, %0, %0\n\ts_nop 0\n\t"
                         "v_mov_b32_dpp %1, %0 row_shl:4 row_mask:0xf bank_mask:0xf\n\tv_cndmask_b32 %3, %8, %9, vcc\n\tv_cvt_pk_bf16_f32 %2, %0, %1"
                         : "+v"(xs), "=&v"(nb), "=&v"(cw), "=&v"(mq) : "v"(xa[0]), "v"(xa[1]), "v"(xa[2]), "v"(xa[3]), "v"(mp), "v"(OOB) : "vcc");
            st4s(rs, mq, 0, cw);
            SCHED();
            WT(7);
            now = rt32();
            books(IC<25>{});
            asm volatile("s_waitcnt vmcnt(4) lgkmcnt(0)" : "+v"(hn), "+v"(sv));
            hv = hn;
            pst = sv.x | sv.y | sv.z | sv.w;
            if (__builtin_expect(__builtin_amdgcn_ballot_w64(mq != mp && pst == 0u) != 0ull, 0)) st4s(rs, mp, 0, cw);
        } else {
            done = uu(pst) ^ 1u;
            st4s(rs, done ? pmp : OOB, 0, psent);
            static_for<0, 25>(books);
            pst = 1;
            BAR();
            hv = ld4(rs, hvo[L]);
            PIN(hv);
            now = rt32();
        }
        pdur = now - t;
        const u32 per = rdl(hv, 1), th = rdl(hv, 0);
        t = lead ? tn : (int)(th - now) < -(int)(per >> 1) ? th + per : th;
        tn = nt;
        dw[M] += max((int)(now + CH_WX - t), 0);
        stop = stop || rdl(hv, 3) != 0u;
    };
#pragma unroll 1
    for (int k = 0; k < 2 * SPIN_CAP && !stop && (sn[0] + 1 < NSEG || sn[1] + 1 < NSEG); ++k) {
        turn(IC<0>{}); turn(IC<1>{});
        if (__builtin_expect(k != 0, 1)) continue;
        if (j == 0) {
            for (int n = 0; n < SPIN_CAP && !dead && sn[0] == 0; ++n) {
                CFENCE();
                if (all_(ld4(rs, W_ORING + pol0 * 8192u + (u32)w * 16384u + (u32)lane * 128u) != PSENT)) break;
                __builtin_amdgcn_s_sleep(1);
            }
            BAR();
        }
        if (lead) {
            t = rt32() + 56u; tn = t + PER0 / 2 + CH_REC;
            st16(rs, lane < 2 ? hint + (u32)lane * (MB / 4) : OOB, (v4u){lane == 0 ? t : tn, PER0, (lane == 0 ? t : tn) + (CH_W0 + CH_VIS + CH_MW + CH_REC), 0u});
        } else {
            for (int n = 0; n < SPIN_CAP && !dead; ++n) {
                CFENCE();
                const u32 hv = ld4(rs, hvo[1]);
                t = rdl(hv, 0);
                if (rdl(hv, 1) < 400u) break;
                __builtin_amdgcn_s_sleep(1);
            }
        }
        hso = (lead && lane == 0) ? hint : OOB;
        hv = 0xFFFFu; dw[0] = dw[1] = CH_W0 + CH_VIS + CH_MW + CH_REC;
    }
    if (sn[0] + 1 < NSEG || sn[1] + 1 < NSEG) st4s(rs, tid == 0 ? foff(F_ERR) : OOB, 0, 1u);
}

__global__ void __launch_bounds__(256, 1) k_main(Args args) {
    Params p;
    (Args&)p = args;
    lptr lds = (lptr)(u32)16 - 16;
    if (blockIdx.x == 0) LST(256, rt32());
    unsigned long long pc; int len;
    asm volatile("s_getpc_b64 %0\n.Lk_main_here:\n\ts_mov_b32 %1, .Lk_end_here-.Lk_main_here" : "=s"(pc), "=s"(len));
    const v4u code = ldin16(mk_rsrc((const void*)(pc & ~15ull), (u32)max(len, 0)), (((blockIdx.x >> 3) & 31u) * 256u + threadIdx.x) * 16u, 0);
    CFENCE();
    if (threadIdx.x == 0) {
        const v4u e = ld16s(mk_rsrc(p.ws, W_END), foff(F_EP), 0);
        const unsigned long long tag = (unsigned long long)e.y << 32 | (e.x + 1u);
        u32 role = ((u32)__builtin_amdgcn_s_getreg(20 | (3 << 11)) & 7u) * 32u + ((blockIdx.x >> 3) & 31u);
#pragma unroll 1
        for (int n = 0; n < 256; ++n, role = (role + 1u) & 255u)
            if (__hip_atomic_exchange((GAS unsigned long long*)(p.ws + W_SLOT) + role * 8u, tag, __ATOMIC_RELAXED, __HIP_MEMORY_SCOPE_AGENT) != tag) break;
        lds_w16(lds, (v4u){e.x + 1u, e.y, e.z, role});
    }
    __syncthreads();
    const v4u boot = lds_r16(lds);
    p.e0 = uu(boot.x); p.e1 = uu(boot.y); p.e2 = uu(boot.z);
    const int role = uni((int)boot.w), x = role >> 5, i = role & 31;
    asm volatile("" :: "v"(code));
    __syncthreads();
    LST(8192 + blockIdx.x * 16, role);
    if (x < 2) { role_head(p, lds, i >> 4, x * 16 + (i & 15)); __builtin_amdgcn_endpgm(); }
    if (x == 2) { role_wo(p, lds, i); __builtin_amdgcn_endpgm(); }
    const int no = x == 3 ? 30 : x < 6 ? 5 : 0;
    if (x > 3 && i >= 8) { role_qkv(p, lds, (x - 4) * 24 + i - 8); __builtin_amdgcn_endpgm(); }
    const bool isout = i < no;
    const int og = x == 3 ? i / 5 : x + 2, ng = i % 5, L = og >> 2, kq = og & 3;
    const int k = i - no, w = uni(threadIdx.x >> 6);
    const int j = isout ? (L == 1 ? 0 : 44) + kq * 5 + ng : x == 3 ? 36 + k : k < 2 ? 12 + 2 * x + k : x < 6 ? 24 + x : k < 5 ? 10 + 3 * x + k : 15 + 3 * x + k;
    const rsrc_t rs = mk_rsrc(p.ws, W_END);
    wait_until(rt32() + (j < NKV ? ST_KV : ST_OUT));
    if (j < NKV) {
        const int nt = j < 320 - 7 * NKV ? 8 : 7;
#pragma unroll 1
        for (int q = 0; q < 4; ++q) kv_fetch(p, j + NKV * q, uu(q * KT_BYTES));
        Filt f;
        u32 warm[3];
#pragma unroll 1
        for (int q = 0; q < nt; ++q) {
            VMW(15); BAR();
            if (q == 0) EV(2);
            kv_fetch(p, j + NKV * (q + 4), uu(((q + 4) & 7) * KT_BYTES));
            kv_tile(p, rs, lds + q * KT_BYTES, lds + (q == 0 ? 8 : q - 1) * KT_BYTES, j + NKV * q);
        }
        const int g = j < 20 ? j : j < 28 ? j + 16 : j - 8;
        filt_load<5, 0>(p, g, f);
        VMW(10); BAR();
        boot_set(p, rs, threadIdx.x == 0 ? foff(F_KV + j) : OOB, 2u);
        EV(4);
        { bool dead = false; boot_wait(p, rs, pol_off(), 0u, dead); }
#pragma unroll
        for (int q = 0; q < 3; ++q)
            warm[q] = __builtin_amdgcn_raw_buffer_load_b32(mk_rsrc(p.ssmk, ACT), 176u * 4096u + ((u32)((q * NKV + j) * 256) + threadIdx.x) * 64u, 0, 0);
        if (g >= 36) conv_job<3>(p, rs, lds, g, f, 0); else conv_job<0>(p, rs, lds, g, f, 0);
        asm volatile("" :: "v"(warm[0]), "v"(warm[1]), "v"(warm[2]));
        const int g0 = j < 20 ? 36 + j : j < 36 ? 28 + j : 44 + j, g1 = j < 20 ? g0 + 1 : j < 36 ? 80 : 160;
        if (g < 36) filt_load<5, 0>(p, g0, f);
        DRAIN(); BAR();
        st4s(rs, threadIdx.x == 0 ? foff(F_CV + g) : OOB, 0, 2u);
        if (g < 36) {
#pragma unroll 1
            for (int n = g0; n < g1; n += 8) conv_job<2>(p, rs, lds, n, f, min(n + 8, 159));
            DRAIN(); BAR();
            st4s(rs, threadIdx.x == 0 ? foff(F_CV + g) : OOB, 0, 3u);
            if (j >= 36) { EV(15); __builtin_amdgcn_endpgm(); }
        }
    }
    if (!isout) { if (j < 28) role_row<0>(p, lds, (j - 20) * 4 + w); else role_row<1>(p, lds, (j - 28) * 4 + w);
        boot_set(p, rs, j == 28 && threadIdx.x == 0 ? foff(F_EP) : OOB, 0u);
        __builtin_amdgcn_endpgm();
    }
    role_out(p, lds, L, kq, ng);
}

__global__ void k_end() { asm volatile(".Lk_end_here:"); }

extern "C" void kernel_launch(void* const* d_in, const int* in_sizes, int n_in, void* d_out, int out_size, void* d_ws, size_t ws_size,
                              hipStream_t stream) {
    Args p;
    p.x = (const u16*)d_in[0]; p.mem0 = (const u16*)d_in[1]; p.Wq = (const u16*)d_in[2]; p.Wk = (const u16*)d_in[3];
    p.Wv = (const u16*)d_in[4]; p.Wo = (const u16*)d_in[5];
    p.bk = (const u16*)d_in[8]; p.bv = (const u16*)d_in[9]; p.shift = (const u16*)d_in[10]; p.ssmk = (const u16*)d_in[11]; p.ssmD = (const u16*)d_in[12];
    p.out = (u16*)d_out;
    p.ws = (unsigned char*)(((size_t)d_ws + 2 * MB - 1) & ~(size_t)(2 * MB - 1));
    (void)hipFuncSetAttribute((const void*)k_main, hipFuncAttributeMaxDynamicSharedMemorySize, LDS_BYTES);
    hipLaunchKernelGGL(k_main, dim3(256), dim3(256), LDS_BYTES, stream, p);
}
```

```cpp
#include <hip/hip_runtime.h>
#include <hip/hip_bf16.h>

typedef unsigned short u16;
typedef unsigned int u32;
typedef u32 v2u __attribute__((ext_vector_type(2)));
typedef u32 v4u __attribute__((ext_vector_type(4)));
typedef u32 v8u __attribute__((ext_vector_type(8)));
typedef float f32x2 __attribute__((ext_vector_type(2)));
typedef float f32x4 __attribute__((ext_vector_type(4)));
typedef float f32x16 __attribute__((ext_vector_type(16)));
typedef __bf16 bf16x2 __attribute__((ext_vector_type(2)));
typedef __bf16 bf16x8 __attribute__((ext_vector_type(8)));
typedef short s16x4 __attribute__((ext_vector_type(4)));
#define LAS __attribute__((address_space(3)))
#define GAS __attribute__((address_space(1)))
typedef LAS unsigned char* lptr;
#define DEV __device__ __forceinline__

constexpr int SEQ = 2048, DM = 640, NH = 32, DH = 128, NSEG = 64, NQ = NH * DH;

constexpr u32 MB = 1u << 20;
constexpr u32 W_ORING = 0;
constexpr u32 W_MRING = MB;
constexpr u32 W_CTRL = 4 * MB;
constexpr u32 ACT = SEQ * DM * 2;
constexpr u32 H_TILE = 128 * 1024;
constexpr u32 W_H2 = 8 * MB;
constexpr u32 W_KQ = 16 * MB;
constexpr u32 W_VO = W_KQ + 64 * MB;
constexpr u32 W_P = W_VO + 64 * MB;
constexpr u32 P_ITEM = 512 * 1024;
constexpr u32 W_H1 = W_P + 44 * MB, W_Y = W_H1 + 8 * MB, W_X2 = W_Y + ACT;
constexpr u32 W_KVT = W_X2 + ACT;
constexpr u32 W_END = W_KVT + 3 * MB;
constexpr u32 NREG = 12;
static_assert((size_t)W_END + 2 * MB <= 256ull * MB && W_H1 >= (NREG - 1) * 16 * MB + 2 * MB && W_H1 <= NREG * 16 * MB, "workspace");
constexpr u32 F_EP = 0, F_RG = 32, F_LN = 64, F_KV = 192,
              F_QKV = 256, F_O = F_QKV + 192, F_P = F_O + 64, F_H2 = F_P + 64, F_CV = F_H2 + 64, F_ERR = F_CV + 191, F_N = F_CV + 192;
constexpr u32 W_SLOT = 3 * MB;
DEV u32 foff(u32 idx) { return W_CTRL + (idx >> 6) * 16384u + (idx & 63u) * 128u; }
DEV u32 near_(u32 o, u32 side, u32 pol) { return o + ((side ^ (pol >> (o >> 24)) ^ (o >> 21)) & 1u) * 8192u; }
DEV u32 p_item(int s) { return W_P + (u32)s * P_ITEM; }

constexpr u32 OOB = 0x80000000u;
constexpr u32 PSENT = 0x7F817F81u;
constexpr int SPIN_CAP = 40000;
constexpr int LDS_BYTES = 160 * 1024;
#define ST_KV 0
#define ST_OUT 0

#define EV(k) do {} while (0)
#define LST(idx, v) do {} while (0)
#define LS2(cond, idx) do {} while (0)
struct Args {
    const u16 *x, *mem0, *Wq, *Wk, *Wv, *Wo, *bk, *bv, *shift, *ssmk, *ssmD;
    u16* out;
    unsigned char* ws;
};
struct Params : Args { u32 e0, e1, e2; };

typedef __amdgpu_buffer_rsrc_t rsrc_t;
DEV rsrc_t mk_rsrc(const void* p, u32 bytes) { return __builtin_amdgcn_make_buffer_rsrc((void*)p, 0, bytes, 0x00020000); }
DEV v4u ld16s(rsrc_t r, u32 off, u32 so) { return __builtin_amdgcn_raw_buffer_load_b128(r, off, so, 16); }
DEV v2u ld8s(rsrc_t r, u32 off, u32 so) { return __builtin_amdgcn_raw_buffer_load_b64(r, off, so, 16); }
DEV v4u ldin16(rsrc_t r, u32 off, u32 so) { return __builtin_amdgcn_raw_buffer_load_b128(r, off, so, 0); }
DEV v2u ldin8(rsrc_t r, u32 off, u32 so) { return __builtin_amdgcn_raw_buffer_load_b64(r, off, so, 0); }
DEV u32 ld4(rsrc_t r, u32 off) { return __builtin_amdgcn_raw_buffer_load_b32(r, off, 0, 16); }
DEV void st16(rsrc_t r, u32 off, v4u v) { __builtin_amdgcn_raw_buffer_store_b128(v, r, off, 0, 16); }
DEV void st8s(rsrc_t r, u32 off, u32 so, v2u v) { __builtin_amdgcn_raw_buffer_store_b64(v, r, off, so, 16); }
DEV void st4s(rsrc_t r, u32 off, u32 so, u32 v) { __builtin_amdgcn_raw_buffer_store_b32(v, r, off, so, 16); }
#define DRAIN() asm volatile("s_waitcnt vmcnt(0)" ::: "memory")
#define VMW(n) asm volatile("s_waitcnt vmcnt(%0)" :: "i"(n) : "memory")
#define BAR() asm volatile("s_waitcnt lgkmcnt(0)\n\ts_barrier" ::: "memory")
#define SCHED() __builtin_amdgcn_sched_barrier(0)
#define CFENCE() asm volatile("" ::: "memory")
#define NOPS(n) asm volatile("s_nop %0" :: "i"(n))
#define PIN(x) asm volatile("" : "+v"(x))
#define PINS(x) asm volatile("" : "+s"(x))

template <int V> struct IC { static constexpr int value = V; };
template <int I, int N, class F> DEV void static_for(F&& f) { if constexpr (I < N) { f(IC<I>{}); static_for<I + 1, N>(f); } }
#define SFOR(var, n) static_for<0, n>([&](auto ic_) __attribute__((always_inline)) { constexpr int var = decltype(ic_)::value;
#define SEND });

DEV u32 pk(float a, float b) { bf16x2 v = {(__bf16)a, (__bf16)b}; return __builtin_bit_cast(u32, v); }
DEV float lo_f(u32 u) { return __builtin_bit_cast(float, u << 16); }
DEV float hi_f(u32 u) { return __builtin_bit_cast(float, u & 0xffff0000u); }
DEV f32x2 up2(u32 u) { return (f32x2){lo_f(u), hi_f(u)}; }
DEV u32 pk2(f32x2 v) { return pk(v.x, v.y); }
DEV f32x2 rb2(f32x2 a) { f32x2 r; asm("v_cvt_pk_bf16_f32 %0, 0, %1" : "=v"(r.x) : "v"(a.x)); asm("v_cvt_pk_bf16_f32 %0, 0, %1" : "=v"(r.y) : "v"(a.y)); return r; }
DEV float rbf(float a) { return (float)(__bf16)a; }
DEV v4u lds_r16(lptr p) { return *(LAS v4u*)p; }
DEV void lds_w16(lptr p, v4u v) { *(LAS v4u*)p = v; }
DEV v2u lds_tr(lptr p) { return __builtin_bit_cast(v2u, __builtin_amdgcn_ds_read_tr16_b64_v4i16((LAS s16x4*)p)); }
DEV v4u tr2(lptr p0, lptr p1) { const v2u a = lds_tr(p0), b = lds_tr(p1); return (v4u){a.x, a.y, b.x, b.y}; }
DEV v8u cat8(v4u a, v4u b) { return __builtin_shufflevector(a, b, 0, 1, 2, 3, 4, 5, 6, 7); }
DEV f32x4 mfma16(v4u a, v4u b, f32x4 c) { return __builtin_amdgcn_mfma_f32_16x16x32_bf16(__builtin_bit_cast(bf16x8, a), __builtin_bit_cast(bf16x8, b), c, 0, 0, 0); }
DEV f32x16 mfma32(v4u a, v4u b, f32x16 c) { return __builtin_amdgcn_mfma_f32_32x32x16_bf16(__builtin_bit_cast(bf16x8, a), __builtin_bit_cast(bf16x8, b), c, 0, 0, 0); }

DEV float f_(u32 u) { return __builtin_bit_cast(float, u); }
DEV u32 u_(float f) { return __builtin_bit_cast(u32, f); }
template <int CTRL> DEV float dpp(float v) { return f_((u32)__builtin_amdgcn_update_dpp(0, (int)u_(v), CTRL, 0xf, 0xf, false)); }
DEV float max16(float v) { auto r = __builtin_amdgcn_permlane16_swap(u_(v), u_(v), false, false); return fmaxf(f_(r[0]), f_(r[1])); }
DEV float max32(float v) { auto r = __builtin_amdgcn_permlane32_swap(u_(v), u_(v), false, false); return fmaxf(f_(r[0]), f_(r[1])); }
DEV float add16(float v) { auto r = __builtin_amdgcn_permlane16_swap(u_(v), u_(v), false, false); return f_(r[0]) + f_(r[1]); }
DEV float add32(float v) { auto r = __builtin_amdgcn_permlane32_swap(u_(v), u_(v), false, false); return f_(r[0]) + f_(r[1]); }
DEV float wave_sum(float v) {
    v += dpp<0xB1>(v);
    v += dpp<0x4E>(v);
    v += dpp<0x124>(v);
    v += dpp<0x128>(v);
    return add32(add16(v));
}
DEV bool all_(bool c) { return __builtin_amdgcn_ballot_w64(!c) == 0ull; }
DEV int uni(int v) { return __builtin_amdgcn_readfirstlane(v); }
DEV u32 uu(u32 v) { return (u32)__builtin_amdgcn_readfirstlane((int)v); }
DEV u32 rdl(u32 v, int l) { return (u32)__builtin_amdgcn_readlane((int)v, l); }
DEV u32 rt32() { return (u32)__builtin_amdgcn_s_memrealtime(); }

DEV void wait_flags(rsrc_t rs, u32 off, u32 want, bool& dead) {
    if (dead) return;
#pragma unroll 1
    for (int n = 0;; ++n) {
        CFENCE();
        const u32 v = ld4(rs, off);
        if (all_(off >= OOB || v >= want)) break;
        __builtin_amdgcn_s_sleep(1);
        if ((n & 63) == 63) { if (uni((int)ld4(rs, foff(F_ERR))) != 0) { dead = true; break; } }
        if (n > SPIN_CAP) { st4s(rs, foff(F_ERR), 0, 1u); dead = true; break; }
    }
    CFENCE();
}

DEV bool boot_ok(const Params& p, u32 off, v4u v, u32 want) { return all_(off >= OOB || (v.x == p.e0 && v.y == p.e1 && v.z == p.e2 && v.w >= want)); }
DEV void boot_set(const Params& p, rsrc_t rs, u32 off, u32 v) { st16(rs, off, (v4u){p.e0, p.e1, p.e2, v}); }
DEV u32 boot_wait(const Params& p, rsrc_t rs, u32 off, u32 want, bool& dead) {
    v4u v;
#pragma unroll 1
    for (int n = 0;; ++n) {
        CFENCE();
        v = ld16s(rs, off, 0);
        if (boot_ok(p, off, v, want) || dead) break;
        __builtin_amdgcn_s_sleep(1);
        if (n > SPIN_CAP) { st4s(rs, foff(F_ERR), 0, 1u); dead = true; }
    }
    CFENCE();
    return v.w;
}

DEV u32 pol_off() { const int lane = threadIdx.x & 63; return lane < 32 ? foff(F_RG + lane) : OOB; }
DEV u32 pol_of(u32 raw) {
    const int lane = threadIdx.x & 63;
    const float v = add16(__builtin_amdgcn_fmed3f((float)((int)raw - (1 << 20)), -250.f, 250.f));
    const float lo = dpp<0x121>(v), hi = dpp<0x12F>(v), ex = dpp<0x122>(v) + dpp<0x123>(v) + dpp<0x124>(v);
    const float sum = v + lo + ((lane & 15) == 11 ? 0.f : hi) + ((lane & 15) == 0 ? ex : 0.f);
    return (u32)__builtin_amdgcn_ballot_w64(sum >= 0.f) & 0xFFFFu;
}
DEV u32 get_pol(const Params& p, rsrc_t rs, bool& dead) { return pol_of(boot_wait(p, rs, pol_off(), 0u, dead)); }
DEV u32 hblk(u32 base, int ls, int h, u32 side, u32 pol) { return near_(base + (u32)(ls * NH + h) * 16384u, side, pol); }
DEV f32x2 ln_stats(const f32x2 (&x)[5]) {
    const float c = f_(uu(u_(x[0].x)));
    f32x2 d[5];
#pragma unroll
    for (int i = 0; i < 5; ++i) d[i] = x[i] - c;
    const f32x2 s2 = (d[0] + d[1]) + (d[2] + d[3]) + d[4], q = (d[0] * d[0] + d[1] * d[1]) + (d[2] * d[2] + (d[3] * d[3] + d[4] * d[4]));
    const float dm = wave_sum(s2.x + s2.y) * (1.0f / DM), mean = c + dm;
    const float var = fmaxf(wave_sum(q.x + q.y) * (1.0f / DM) - dm * dm, 0.f);
    const float sd = rbf(__builtin_amdgcn_sqrtf(rbf(rbf(var) + 1e-5f)));
    return (f32x2){rbf(mean), __builtin_amdgcn_rcpf(sd)};
}
DEV void store_h(rsrc_t rs, u32 tile, int row, const u32 (&hp)[5]) {
    const u32 lane = threadIdx.x & 63;
    const u32 sw = (u32)(row & 15) * 16u, o8 = (lane >> 5) * 16384u + (((lane & 31u) * 8u) ^ sw), hb = tile + (u32)(row & 31) * 256u;
    st8s(rs, o8, hb, (v2u){hp[0], hp[1]});
    st8s(rs, o8, hb + 32768, (v2u){hp[2], hp[3]});
    st4s(rs, (lane * 4) ^ sw, hb + 65536, hp[4]);
}

#define DMA16(ldsb, vo, so) asm volatile("s_mov_b32 m0, %0\n\ts_nop 0\n\tbuffer_load_dwordx4 %1, %2, %3 offen sc1 lds" :: "s"(ldsb), "v"(vo), "s"(srd), "s"(so) : "memory")
#define DMA16I(ldsb, lo, vo, so, go) do { u32 so_; asm volatile("s_add_u32 m0, %1, %5\n\ts_add_u32 %0, %4, %6\n\tbuffer_load_dwordx4 %2, %3, %0 offen sc1 lds" \
                         : "=&s"(so_) : "s"(ldsb), "v"(vo), "s"(srd), "s"(so), "i"(lo), "i"(go) : "memory", "scc"); } while (0)
#define DMA32(ldsb, vo, so) asm volatile("s_mov_b32 m0, %0\n\ts_nop 0\n\tbuffer_load_dwordx4 %1, %2, %3 offen sc1 lds\n\tbuffer_load_dwordx4 %1, %2, %3 offen offset:1024 sc1 lds" \
                         :: "s"(ldsb), "v"(vo), "s"(srd), "s"(so) : "memory")
#define INPOL "nt"
#define XPOL ""
#define INAUX 2
#define DMANT(sr, ldsb, vo, so) asm volatile("s_mov_b32 m0, %0\n\ts_nop 0\n\tbuffer_load_dwordx4 %1, %2, %3 offen " INPOL " lds" :: "s"(ldsb), "v"(vo), "s"(sr), "s"(so) : "memory")
#define DMAIN(sr, ldsb, vo, so) asm volatile("s_mov_b32 m0, %0\n\ts_nop 0\n\tbuffer_load_dwordx4 %1, %2, %3 offen lds" :: "s"(ldsb), "v"(vo), "s"(sr), "s"(so) : "memory")
DEV const u16* sel(bool c, const u16* a, const u16* b) { PINS(a); PINS(b); return c ? a : b; }
DEV v4u mk_srd(const void* q, u32 bytes) { return (v4u){uu((u32)(size_t)q), uu((u32)((size_t)q >> 32) & 0xffffu), bytes, 0x00020000u}; }
#define MK_SRD() const v4u srd = mk_srd(p.ws, W_END)

constexpr int CV_PAD = 40;
constexpr int CV_PL = 4416;
constexpr int CV_F = 8 * CV_PL;
constexpr int CV_KB = 80;
constexpr int CV_KV = 65 * CV_KB;
constexpr int CV_CH = CV_F + CV_KV;
static_assert(CV_CH % 16 == 0 && 4 * CV_CH <= LDS_BYTES, "conv lds");

struct Filt { v4u a[5], b[5]; };
DEV int chan(int grp, int w) { return 16 * (grp >> 2) + (grp & 3) + 4 * w; }
template <int N = 5, int AUX = INAUX> DEV void filt_load(const Params& p, int grp, Filt& f) {
    const int tid = threadIdx.x, lane = tid & 63, d = chan(grp, tid >> 6);
    const rsrc_t rf = mk_rsrc(p.ssmk, ACT);
#pragma unroll
    for (int i = 0; i < N; ++i) {
        const u32 s0 = (u32)(8 * (lane + 64 * i) - CV_PAD);
        f.a[i] = __builtin_amdgcn_raw_buffer_load_b128(rf, s0 < SEQ ? (u32)(d * SEQ + s0) * 2u : OOB, 0, AUX);
        f.b[i] = __builtin_amdgcn_raw_buffer_load_b128(rf, s0 + 8 < SEQ ? (u32)(d * SEQ + s0 + 8) * 2u : OOB, 0, AUX);
    }
}
constexpr int KT_BYTES = 18 * 1024, KT_X = 64 * 144, NKV = 44, KT_DUMP = 8 * KT_BYTES + KT_X;
DEV void kv_fetch(const Params& p, int tile, u32 dst) {
    const int lane = threadIdx.x & 63, w = uni(threadIdx.x >> 6);
    const v4u sk = mk_srd(p.bk, ACT), sv = mk_srd(p.bv, ACT), ss = mk_srd(sel(w == 0, p.bk, p.shift), w == 0 ? ACT : DM * 8);
    const u32 vo = tile < 320 ? (u32)((lane >> 3) * DM + (tile % 10) * 64 + (lane & 7) * 8) * 2u : OOB, r0 = uu((u32)((tile / 10) * 64 + 8 * w) * (DM * 2)), d = uu(dst + w * 1024);
    DMANT(sk, d + 1024, vo, r0); DMANT(sk, d + 5120, vo, r0 + 32 * DM * 2); DMANT(sv, d + 9216, vo, r0); DMANT(sv, d + 13312, vo, r0 + 32 * DM * 2);
    DMAIN(ss, uu(w == 0 ? dst : w == 1 ? dst + 17408 : KT_DUMP), w == 0 ? (tile >= 10 ? vo : OOB) : w == 1 && lane < 32 ? (u32)lane * 16u : OOB,
          uu(w == 0 ? (tile >= 10 ? r0 - 8 * DM * 2 : 0u) : (u32)(tile % 10) * 512u));
}
DEV void kv_tile(const Params& p, rsrc_t rs, lptr src, lptr xt, int tile) {
    const int tid = threadIdx.x;
    const v4u sv = lds_r16(src + 17408 + (tid & 31) * 16);
    lptr kp = src + ((tid >> 5) * 8 + 5) * 128 + (tid & 31) * 4;
    f32x2 kf[11];
#pragma unroll
    for (int i = 0; i < 11; ++i) kf[i] = up2(*(LAS u32*)(kp + i * 128));
    const f32x2 sh[4] = {{lo_f(sv.x), lo_f(sv.z)}, {hi_f(sv.x), hi_f(sv.z)}, {lo_f(sv.y), lo_f(sv.w)}, {hi_f(sv.y), hi_f(sv.w)}};
    u32 kv[8];
#pragma unroll
    for (int i = 0; i < 8; ++i) {
        const f32x2 acc = rb2(rb2(rb2(rb2(kf[i] * sh[3]) + rb2(kf[i + 1] * sh[2])) + rb2(kf[i + 2] * sh[1])) + rb2(kf[i + 3] * sh[0]));
        kv[i] = pk2(acc * up2(*(LAS u32*)(kp + 9216 + (i - 5) * 128)));
    }
    lds_w16(xt + (2 * (tid & 31)) * 144 + (tid >> 5) * 16, (v4u){__builtin_amdgcn_perm(kv[6], kv[7], 0x05040100u), __builtin_amdgcn_perm(kv[4], kv[5], 0x05040100u),
                                                             __builtin_amdgcn_perm(kv[2], kv[3], 0x05040100u), __builtin_amdgcn_perm(kv[0], kv[1], 0x05040100u)});
    lds_w16(xt + (2 * (tid & 31) + 1) * 144 + (tid >> 5) * 16, (v4u){__builtin_amdgcn_perm(kv[6], kv[7], 0x07060302u), __builtin_amdgcn_perm(kv[4], kv[5], 0x07060302u),
                                                                 __builtin_amdgcn_perm(kv[2], kv[3], 0x07060302u), __builtin_amdgcn_perm(kv[0], kv[1], 0x07060302u)});
    BAR();
#pragma unroll
    for (int i = 0; i < 2; ++i) {
        const int ch = i * 32 + (tid >> 3);
        st16(rs, W_KVT + (u32)(((tile % 10) * 64 + ch) * SEQ + (tile / 10) * 64 + (tid & 7) * 8) * 2u, lds_r16(xt + ch * 144 + (tid & 7) * 16));
    }
}
template <int MODE> DEV void conv_job(const Params& p, rsrc_t rs, lptr lds, int grp, Filt& f, int next, u32 fo2 = OOB, u32 fv2 = 0) {
    const int tid = threadIdx.x, lane = tid & 63, w = uni(tid >> 6), bq = lane & 31, hh = lane >> 5;
    lptr F = lds + w * CV_CH;
    lptr fa = F + (bq & 7) * CV_PL + ((bq >> 3) - hh + 4) * 16;
    lptr kb = F + CV_F + CV_KB + hh * 16, kh = kb + (bq + 32) * CV_KB;
    constexpr bool LO = MODE & 1, EARLY = MODE == 0 || MODE == 3;
    bool dead = false;
    v4u kvr[4];
    const u32 D = p.ssmD[chan(grp, w)];
    auto kv_load = [&]() {
        if (EARLY) boot_wait(p, rs, lane < NKV ? foff(F_KV + lane) : OOB, 2u, dead);
#pragma unroll
        for (int i = 0; i < (LO ? 2 : 4); ++i) kvr[i] = ld16s(rs, (u32)lane * 16u, W_KVT + (u32)(chan(grp, w) * SEQ + i * 512) * 2u);
    };
    auto windows = [&](int i) {
        if (i < 4 || lane < 16) {
            const u32 e[8] = {f.a[i].x, f.a[i].y, f.a[i].z, f.a[i].w, f.b[i].x, f.b[i].y, f.b[i].z, f.b[i].w};
            u32 o[7];
#pragma unroll
            for (int q = 0; q < 7; ++q) o[q] = __builtin_amdgcn_alignbit(e[q + 1], e[q], 16);
#pragma unroll
            for (int q = 0; q < 4; ++q) {
                lds_w16(F + (2 * q) * CV_PL + (64 * i + lane) * 16, (v4u){o[q], o[q + 1], o[q + 2], o[q + 3]});
                lds_w16(F + (2 * q + 1) * CV_PL + (64 * i + lane) * 16, (v4u){e[q + 1], e[q + 2], e[q + 3], e[q + 4]});
            }
        }
    };
    lptr yw = lds + (lane >> 4) * CV_CH + 3072 + (lane & 14) * 64 + w * 32 + (lane & 1) * 16, yr = F + 3072 + lane * 16;
    const u32 yo = W_Y + (u32)((((w & 1) * 8 + (lane >> 3)) * 8 + (w >> 1)) * DM + (grp & 3) * 160 + 4 * (grp >> 2)) * 32u + (u32)(lane & 7) * 16u;
    v4u dg[2];
    auto skip = [&](lptr k) { const f32x16 z = {}; return mfma32(lds_r16(k + 32), dg[1], mfma32(lds_r16(k), dg[0], z)); };
    auto pack = [&](const f32x16& ac, const f32x16& sk, int q) {
        const int r = 4 * (q & 3);
        u32 v[4];
#pragma unroll
        for (int i = 0; i < 4; ++i) v[i] = pk2(rb2((f32x2){ac[r + 2 * i], ac[r + 2 * i + 1]}) + rb2((f32x2){sk[r + 2 * i], sk[r + 2 * i + 1]}));
        const auto x = __builtin_amdgcn_permlane32_swap(v[0], v[2], false, false), y = __builtin_amdgcn_permlane32_swap(v[1], v[3], false, false);
        return (v4u){x[0], y[0], x[1], y[1]};
    };
    if (MODE != 2) EV(8);
    if (!EARLY) kv_load();
    if (lane < CV_KB / 16) lds_w16(F + CV_F + lane * 16, (v4u){0u, 0u, 0u, 0u});
    windows(0);
    if (MODE != 1) { windows(1); windows(2); }
    if (!LO) windows(4);
    if (EARLY) kv_load();
    if (MODE == 2) windows(3);
    if (MODE != 2) EV(9);
#pragma unroll
    for (int i = 0; i < (LO ? 2 : 4); ++i) lds_w16(F + CV_F + (16 * i + (lane >> 2) + 1) * CV_KB + (lane & 3) * 16, kvr[i]);
#pragma unroll
    for (int ks = 0; ks < 2; ++ks)
#pragma unroll
        for (int q = 0; q < 4; ++q) { const int j = 16 * ks + 8 * hh + 7 - bq; dg[ks][q] = j == 2 * q ? D : j == 2 * q + 1 ? D << 16 : 0u; }
    f32x16 acc0 = {}, acc1 = {};
    auto first = [&]() { asm volatile("s_waitcnt vmcnt(0)\n\ts_barrier" ::: "memory"); st4s(rs, tid == 0 ? foff(F_CV + grp) : fo2, 0, tid == 0 ? 1u : fv2); EV(12); };
    if (MODE != 2) {
        EV(10);
        v4u fr[2][4];
        auto rd = [&](int m) { lptr kl = kb + max(bq - m, -1) * CV_KB; fr[m & 1][0] = lds_r16(kl); fr[m & 1][1] = lds_r16(fa + m * 64); fr[m & 1][2] = lds_r16(kl + 32); fr[m & 1][3] = lds_r16(fa + m * 64 - 32); };
        rd(0); rd(1);
        const f32x16 sk = skip(kb + bq * CV_KB);
#pragma unroll
        for (int m = 0; m < 8; ++m) {
            acc0 = mfma32(fr[m & 1][0], fr[m & 1][1], acc0); acc0 = mfma32(fr[m & 1][2], fr[m & 1][3], acc0);
            if (m < 6) rd(m + 2);
            SCHED();
        }
        lds_w16(yw, pack(acc0, sk, 0));
        BAR();
        st16(rs, w < 2 ? yo : OOB, lds_r16(yr));
        EV(11);
    }
    if (LO) {
        if (MODE == 1) { windows(1); windows(2); first(); }
        v4u a[2][2], l[2][2];
#pragma unroll
        for (int u = 0; u < 2; ++u) { lptr kl = kb + max(bq - 8 - u, -1) * CV_KB; a[u][0] = lds_r16(fa + (8 + u) * 64); l[u][0] = lds_r16(kl); a[u][1] = lds_r16(fa + (8 + u) * 64 - 32); l[u][1] = lds_r16(kl + 32); }
#pragma unroll 1
        for (int m = 8; m < 32; m += 2) {
            if (MODE == 3 && m == 20) first();
#pragma unroll
            for (int u = 0; u < 2; ++u) {
                lptr kl = kb + max(bq - m - u - 2, -1) * CV_KB;
                acc0 = mfma32(l[u][0], a[u][0], acc0); a[u][0] = lds_r16(fa + (m + u + 2) * 64); l[u][0] = lds_r16(kl); SCHED();
                acc0 = mfma32(l[u][1], a[u][1], acc0); a[u][1] = lds_r16(fa + (m + u + 2) * 64 - 32); l[u][1] = lds_r16(kl + 32); SCHED();
            }
        }
    } else {
        if (MODE == 0) windows(3); else filt_load<5, 0>(p, next, f);
        v4u a0 = lds_r16(fa), a1 = lds_r16(fa - 32), c0 = lds_r16(fa + 2048), c1 = lds_r16(fa + 2048 - 32);
        v4u h0 = lds_r16(kh), h1 = lds_r16(kh + 32), l0 = lds_r16(kb + bq * CV_KB), l1 = lds_r16(kb + bq * CV_KB + 32);
#pragma unroll 2
        for (int m = 0; m < (MODE == 0 ? 8 : 32); ++m) {
            lptr kl = kb + max(bq - m - 1, -1) * CV_KB;
            if (MODE == 0 && m == 4) first();
            acc1 = mfma32(h0, a0, acc1); h0 = lds_r16(kh - (m + 1) * CV_KB); a0 = lds_r16(fa + (m + 1) * 64); SCHED();
            acc1 = mfma32(l0, c0, acc1); c0 = lds_r16(fa + (m + 1) * 64 + 2048); l0 = lds_r16(kl); SCHED();
            acc1 = mfma32(h1, a1, acc1); h1 = lds_r16(kh - (m + 1) * CV_KB + 32); a1 = lds_r16(fa + (m + 1) * 64 - 32); SCHED();
            acc1 = mfma32(l1, c1, acc1); c1 = lds_r16(fa + (m + 1) * 64 + 2048 - 32); l1 = lds_r16(kl + 32); SCHED();
        }
        if (MODE == 0) {
#pragma unroll 2
            for (int m = 8; m < 32; ++m) {
                lptr kl = kb + max(bq - m - 1, -1) * CV_KB;
                acc1 = mfma32(h0, a0, acc1); h0 = lds_r16(kh - (m + 1) * CV_KB); SCHED();
                acc0 = mfma32(l0, a0, acc0); a0 = lds_r16(fa + (m + 1) * 64); SCHED();
                acc1 = mfma32(l0, c0, acc1); c0 = lds_r16(fa + (m + 1) * 64 + 2048); l0 = lds_r16(kl); SCHED();
                acc1 = mfma32(h1, a1, acc1); h1 = lds_r16(kh - (m + 1) * CV_KB + 32); SCHED();
                acc0 = mfma32(l1, a1, acc0); a1 = lds_r16(fa + (m + 1) * 64 - 32); SCHED();
                acc1 = mfma32(l1, c1, acc1); c1 = lds_r16(fa + (m + 1) * 64 + 2048 - 32); l1 = lds_r16(kl + 32); SCHED();
            }
        }
    }
    if (MODE != 2) EV(13);
    v4u yy[4];
    if (MODE != 2) { const f32x16 sk = skip(kb + bq * CV_KB); yy[0] = pack(acc0, sk, 0); yy[1] = pack(acc0, sk, 2); }
    if (!LO) { const f32x16 sk = skip(kh); yy[2] = pack(acc1, sk, 0); yy[3] = pack(acc1, sk, 2); BAR(); }
#pragma unroll
    for (int q = (MODE == 2 ? 2 : 0); q < (LO ? 2 : 4); ++q) lds_w16(yw + q * CV_PL, yy[q]);
    BAR();
#pragma unroll
    for (int q = (MODE == 2 ? 2 : 0); q < (LO ? 2 : 4); ++q) st16(rs, q == 0 && w < 2 ? OOB : yo + (u32)q * (2 * DM * 32), lds_r16(yr + q * CV_PL));
    BAR();
    if (MODE != 2) EV(14);
}

#define NPROBE 6
DEV int probe(rsrc_t rs, int c) {
    const int lane = threadIdx.x & 63;
    const u32 k = (u32)(c & 15) < NREG ? (u32)(c & 15) : 0u;
    const u32 off0 = k * (16 * MB) + 2 * MB - 65536 + ((u32)(c & 15) >= NREG ? (u32)(c & 3) + 1u : 0u) * 256 + (u32)(c >> 4) * 128;
    st4s(rs, lane == 0 ? off0 : OOB, 0, 0u); DRAIN();
    int d0 = 1 << 16, d1 = 1 << 16;
#pragma unroll 1
    for (int q = 0; q < NPROBE; ++q) {
        const u32 o = lane == 0 ? off0 + (u32)(q & 1) * 8192 + (u32)(q >> 1) * 18432 : OOB;
        const unsigned long long t0 = __builtin_amdgcn_s_memtime();
        st4s(rs, o, 0, 0u); DRAIN();
        const int dt = (int)(__builtin_amdgcn_s_memtime() - t0);
        if (q & 1) d1 = min(d1, dt); else d0 = min(d0, dt);
    }
    const u32 xcc = (u32)__builtin_amdgcn_s_getreg(20 | (3 << 11)) & 0xfu;
    return (d0 - d1) * ((xcc & 4u) ? -1 : 1);
}
DEV void pre_rings(const Params& p, rsrc_t rs, int j) {
    const u32 gt = (u32)(j * 256 + threadIdx.x) * 16u, n = ((gt >> 14) << 9) + ((gt & 8191u) >> 4);
    const v4u ps = {PSENT, PSENT, PSENT, PSENT};
    v4u m = ps;
    if (n < 640u) m = *(const v4u*)(p.mem0 + (2 * ((n >> 2) & 3) + (n & 1)) * DM + (n >> 4) * 16 + (n & 2) * 4);
#pragma unroll
    for (u32 i = 0; i < 8; ++i) st16(rs, W_ORING + gt + i * 131072u, ps);
#pragma unroll
    for (u32 i = 0; i < 2; ++i) st16(rs, gt < 16384u ? W_MRING + i * (MB / 4) + 131072u + gt : OOB, ps);
    if (n < 640u) {
        m = (v4u){__builtin_amdgcn_perm(m.z, m.x, 0x05040100u), __builtin_amdgcn_perm(m.z, m.x, 0x07060302u), __builtin_amdgcn_perm(m.w, m.y, 0x05040100u), __builtin_amdgcn_perm(m.w, m.y, 0x07060302u)};
#pragma unroll
        for (int i = 0; i < 4; ++i) m[i] = m[i] == PSENT ? 0x7FC17FC1u : m[i];
    }
    st16(rs, W_MRING + gt, m);
    st16(rs, W_MRING + MB / 4 + gt, m);
    static_assert(2 * (F_N - F_QKV) == 32 * 36, "clear");
    st4s(rs, threadIdx.x < 36 ? foff(F_QKV) + (u32)(j * 36 + threadIdx.x) * 128u : OOB, 0, 0u);
}

#define M32A(first, d, a, b) do { if (first) asm volatile("v_mfma_f32_32x32x16_bf16 %0, %1, %2, 0" : "=&v"(d) : "a"(a), "v"(b)); \
                                  else asm volatile("v_mfma_f32_32x32x16_bf16 %0, %1, %2, %0" : "+v"(d) : "a"(a), "v"(b)); } while (0)
#define M32B(first, d, a, b) do { if (first) asm volatile("v_mfma_f32_32x32x16_bf16 %0, %1, %2, 0" : "=&v"(d) : "v"(a), "a"(b)); \
                                  else asm volatile("v_mfma_f32_32x32x16_bf16 %0, %1, %2, %0" : "+v"(d) : "v"(a), "a"(b)); } while (0)
#define VCVT(d, a, b) asm volatile("v_cvt_pk_bf16_f32 %0, %1, %2" : "=v"(d) : "v"(a), "v"(b))
#define LD4A(d, vo, so) asm volatile("buffer_load_dword %0, %1, %2, %3 offen sc1" : "=v"(d) : "v"(vo), "s"(srd), "s"(so) : "memory")
DEV u32 wst_src(int lane, int ld) { return (u32)((lane >> 4) * ld + 8 * ((lane & 15) ^ ((lane >> 4) << 2))) * 2u; }
DEV u32 wst_rd(int lane, int w) { const int q4 = (lane >> 2) & 3; return (u32)(q4 * 256 + ((w ^ q4) * 64) + ((lane >> 4) & 1) * 32 + (lane & 3) * 8); }

constexpr int QK_A = 65536;
#define QLEAD 4
#define QFIRST 2
static_assert(QFIRST < QLEAD, "a round of the lead carries the flag of the convolution");
#define QW0 10
#define QW1 3
#define QSAMP 72
DEV void role_qkv(const Params& p, lptr lds, int c) {
    const int grp = 64 + c;
    const int tid = threadIdx.x, lane = tid & 63, w = uni(tid >> 6), hh = lane >> 5, r32 = lane & 31;
    const int m = c >> 5, h = c & 31;
    const rsrc_t rs = mk_rsrc(p.ws, W_END);
    v4u wt[40];
    Filt flt;
    bool dead = false;
    u32 pol, h2flag, fo, ol, ar[8];
    const u32 ds = uu(w * 1024), dx = ds + QK_A;
#define XRD(ks) lds_r16(lds + ar[(ks) & 7] + ((ks) >> 3) * 8192)
    const u32 t16 = (u32)tid * 16u, lnf = tid < 96 ? foff(F_LN + tid) : OOB;
    MK_SRD();
#define QDMA(i, dst, vo, so) DMA16I(dst, (i) * 4096, vo, so, ((i) >> 1) * 16384 + ((i) & 1) * 4096)
    {
        const v4u sw = mk_srd(sel(m == 0, p.Wq, sel(m == 1, p.Wk, p.Wv)), DM * NQ * 2), sx = mk_srd(p.x, ACT);
        const u32 vo = wst_src(lane, NQ) + (u32)h * (DH * 2), x8 = (u32)lane * 8u, x4 = (u32)lane * 4u;
        v2u xa[6], xb[6]; u32 xc[6];
#pragma unroll
        for (int i = 0; i < 6; ++i)
            asm volatile("buffer_load_dwordx2 %0, %3, %5, %6 offen " XPOL "\n\tbuffer_load_dwordx2 %1, %3, %5, %6 offen offset:512 " XPOL "\n\tbuffer_load_dword %2, %4, %5, %6 offen offset:1024 " XPOL
                         : "=&v"(xa[i]), "=&v"(xb[i]), "=&v"(xc[i]) : "v"(x8), "v"(x4), "s"(sx), "s"(uu((4 * c + w + 384 * i) * (DM * 2))) : "memory");
        EV(1);
        auto wfill = [&](int i0, int n) {
#pragma unroll 1
            for (int i = i0; i < i0 + n; ++i) DMAIN(sw, uu((4 * i + w) * 1024), vo, uu((16 * i + 4 * w) * (NQ * 2)));
        };
        wfill(0, QW0);
        asm volatile("s_waitcnt vmcnt(%0) ; HZCLR" :: "i"(QW0) : "memory");
        EV(2);
#pragma unroll
        for (int i = 0; i < 6; ++i) {
            wfill(QW0 + QW1 * i, QW1);
            if (i == 2) { VMW(2 * QW1 + 3); BAR(); boot_set(p, rs, tid == 0 ? foff(F_LN + c) : OOB, 1u); }
            asm volatile("" : "+v"(xa[i]), "+v"(xb[i]), "+v"(xc[i]));
            const int row = 4 * c + w + 384 * i;
            if (i < 5 || row < SEQ) {
                const f32x2 x[5] = {up2(xa[i].x), up2(xa[i].y), up2(xb[i].x), up2(xb[i].y), up2(xc[i])};
                const f32x2 st = ln_stats(x);
                u32 hp[5];
#pragma unroll
                for (int q = 0; q < 5; ++q) hp[q] = pk2(up2(pk2(x[q] - st.x)) * st.y);
                store_h(rs, W_H1 + (u32)(row >> 5) * H_TILE, row, hp);
            }
        }
        EV(3);
        constexpr int K0 = QW0 + 6 * QW1;
        static_assert(K0 * 4096 >= QK_A + 40960, "the first tile's place is free");
        v4u fv, praw;
        asm volatile("buffer_load_dwordx4 %0, %2, %4, 0 offen sc1\n\tbuffer_load_dwordx4 %1, %3, %4, 0 offen sc1" : "=&v"(praw), "=&v"(fv) : "v"(pol_off()), "v"(lnf), "s"(srd) : "memory");
        wfill(K0, 40 - K0);
        asm volatile("s_waitcnt vmcnt(%2) ; HZCLR\n\ts_barrier" : "+v"(praw), "+v"(fv) : "i"(40 - K0) : "memory");
        boot_set(p, rs, tid == 0 ? foff(F_LN + c) : OOB, 2u);
        EV(4);
        lptr tb = lds + hh * 2048 + wst_rd(lane, w);
        SFOR(k, 40)
            if constexpr (k == K0) {
                if (!boot_ok(p, lnf, fv, 1u)) boot_wait(p, rs, lnf, 1u, dead);
                if (!boot_ok(p, pol_off(), praw, 0u)) praw.w = boot_wait(p, rs, pol_off(), 0u, dead);
                BAR();
                EV(5);
                const u32 so = W_H1;
                SFOR(i, 10) QDMA(i, dx, t16, so); SEND
                pol = pol_of(praw.w);
                h2flag = tid < 8 ? near_(foff(F_H2 + lane), 1u, pol) : OOB;
                ol = (m == 2 ? (u32)(2 * w + (r32 >> 4)) * 1024u : (u32)((r32 >> 4) * 4 + w) * 1024u) + (u32)(hh * 16 + (r32 & 15)) * 16u;
                fo = near_(foff(F_QKV + m * 64 + h), 0u, pol);
#pragma unroll
                for (int j = 0; j < 8; ++j) { ar[j] = (u32)(QK_A + r32 * 256 + ((2 * j + hh) ^ (r32 & 15)) * 16); PIN(ar[j]); }
                PIN(h2flag); PIN(ol); PINS(fo); PINS(pol);
                VMW(10); BAR();
            }
            lptr q = tb + (k >> 3) * 32768 + (k & 7) * 4096; wt[k] = tr2(q, q + 1024); asm volatile("" : "+a"(wt[k]));
        SEND
    }
    EV(6);
    int n0 = -1, n1 = -QLEAD;
    const u32 ksc = m == 1 ? 0x3e0293eeu : 0x3f800000u;
    const u32 bfo = tid < 96 ? lnf : tid - 128u < NKV ? foff(F_KV - 128 + tid) : OOB;
    v4u brk = {};
    auto run = [&](auto isv) __attribute__((always_inline)) {
      constexpr bool V = decltype(isv)::value;
      v4u x[8] = {};
      f32x16 a0 = {};
      bool fresh = true;
      u32 fv = 0, fo0 = tid == 192 ? fo : n1 > -QLEAD && tid == 194 ? foff(F_CV + grp) : OOB, fo1 = tid == 192 ? fo : tid == 193 ? fo + 32 * 128 : OOB;
      PIN(fo0); PIN(fo1);
#define QS(k) do {} while (0)
#define QPK1(d, a, a_, q_) do { if constexpr (V) VCVT(d[q_], a[4 * (a_) + 8 * ((q_) >> 1) + 2 * ((q_) & 1)], a[4 * (a_) + 8 * ((q_) >> 1) + 2 * ((q_) & 1) + 1]); else VCVT(d[q_], a[4 * (a_) + (q_)], a[4 * (a_) + (q_) + 8]); } while (0)
#define QSC(a, i) asm volatile("v_mul_f32 %0, %2, %0\n\tv_mul_f32 %1, %2, %1" : "+v"(a[i]), "+v"(a[(i) + 8]) : "s"(ksc))
#define QPK(d, a, a_, q_) do { if constexpr (!V) { QSC(a, 4 * (a_) + (q_)); QSC(a, 4 * (a_) + (q_) + 1); } QPK1(d, a, a_, q_); QPK1(d, a, a_, (q_) + 1); } while (0)
#pragma unroll 1
      for (const int end = n1 == -QLEAD ? QFIRST - QLEAD : NSEG; n1 < end; ++n1) {
        const int cur = n1 < 0 ? n0 + 1 : 64 | n1, nxt = n1 < 0 ? n0 + 2 : n0 + 1;
        u32 xs = n1 < 0 ? W_H1 + (u32)cur * H_TILE : near_(W_H2 + (u32)n1 * H_TILE, 1u, pol);
        PINS(xs);
        QS(0);
        if (w == 0 && n1 >= 0 && ((u32)__builtin_amdgcn_ballot_w64(fv <= (u32)n1) & 255u) != 0u) wait_flags(rs, h2flag, (u32)n1 + 1, dead);
        BAR();
        QS(1);
        f32x16 a1;
        u32 ss, sv, hb, oa, qfo, qfv;
        v4u pp, pq;
        SCHED();
        auto slot = [&](auto zc) __attribute__((always_inline)) {
            constexpr int z = decltype(zc)::value, ks = (z + 20) % 40;
            if constexpr (z < 20) { if constexpr (V) M32B(false, a0, x[ks & 7], wt[ks]); else M32A(false, a0, wt[ks], x[ks & 7]); }
            else if constexpr (z < 60) { if constexpr (V) M32B(z == 20, a1, x[ks & 7], wt[ks]); else M32A(z == 20, a1, wt[ks], x[ks & 7]); }
            else if constexpr (z == 60) {
                if constexpr (V) asm volatile("v_mfma_f32_32x32x16_bf16 %0, %1, %2, 0" : "+v"(a0) : "v"(x[ks & 7]), "a"(wt[ks]));
                else asm volatile("v_mfma_f32_32x32x16_bf16 %0, %1, %2, 0" : "+v"(a0) : "a"(wt[ks]), "v"(x[ks & 7]));
            } else { if constexpr (V) M32B(false, a0, x[ks & 7], wt[ks]); else M32A(false, a0, wt[ks], x[ks & 7]); }
            if constexpr (z == 18) { QS(2); asm volatile("s_waitcnt vmcnt(6) lgkmcnt(0)\n\ts_barrier" ::: "memory"); QS(3); }
            if constexpr (z < 15 || z > 20) x[(ks + 5) & 7] = XRD((ks + 5) % 40);
            if constexpr (z >= 18 && z <= 20) { x[2 * (z - 18)] = XRD(2 * (z - 18)); x[2 * (z - 18) + 1] = XRD(2 * (z - 18) + 1); }
            SCHED();
            if constexpr (z < 4) QDMA(z, dx, t16, xs);
            if constexpr (z >= 5 && z < 17 && (z & 1)) QDMA((z + 3) >> 1, dx, t16, xs);
            if constexpr ((z >= 7 && z < 15) || (z >= 47 && z < 55)) asm volatile("v_xor_b32 %0, 0x10000, %0" : "+v"(ar[(ks + 5) & 7]));
            if constexpr (z == 16) { ss = W_H1 + (u32)nxt * H_TILE; PINS(ss); }
            if constexpr (z == 17) { sv = t16 | (nxt >= NSEG ? OOB : 0u); PIN(sv); }
            if constexpr (z >= 21 && z < 41 && (z & 1)) QDMA((z - 21) >> 1, ds, sv, ss);
            if constexpr (z == 22 || z == 24) QPK(pp, a0, 0, z - 22);
            if constexpr (z == 26 || z == 28) QPK(pq, a0, 1, z - 26);
            if constexpr (z == 30) { QS(4); asm volatile("s_waitcnt vmcnt(5)\n\ts_barrier" ::: "memory"); QS(5); }
            if constexpr (z == 32 || z == 44) { hb = (m == 2 ? W_VO : W_KQ) + (u32)((z == 32 ? n0 : cur) * NH + h) * 16384u; PINS(hb); }
            if constexpr (z == 34 || z == 45) { hb = near_(hb, m == 1 ? 0u : 1u, pol); PINS(hb); }
            if constexpr (z == 36) { hb = n0 < NSEG && !fresh ? hb : OOB; PINS(hb); }
            if constexpr (z == 38 || z == 46) { oa = ol + hb; PIN(oa); }
            if constexpr (z == 41) st16(rs, oa, pp);
            if constexpr (z == 43) st16(rs, oa + 512, pq);
            if constexpr (z == 54) { QS(6); asm volatile("s_waitcnt vmcnt(4)\n\ts_barrier" ::: "memory"); QS(7); }
            if constexpr (z == 56) { if (__builtin_expect(n1 == QFIRST - QLEAD - 1, 0)) { filt_load<3, 0>(p, grp, flt); brk = ld16s(rs, bfo, 0); } }
            if constexpr (z == 60) NOPS(5);
            if constexpr (z == 63) st16(rs, oa, pp);
            if constexpr (z == 65) st16(rs, oa + 512, pq);
            if constexpr (z == 61 || z == 62) QPK(pp, a1, 0, 2 * (z - 61));
            if constexpr (z == 63 || z == 64) QPK(pq, a1, 1, 2 * (z - 63));
            if constexpr (z == 67) { qfo = n1 >= 0 ? fo1 : fo0; PIN(qfo); }
            if constexpr (z == 68) { qfv = tid == 193 ? (u32)n1 + 1u : (u32)min(nxt, NSEG); PIN(qfv); }
            if constexpr (z == QSAMP) LD4A(fv, h2flag, 0);
            SCHED();
        };
        if (__builtin_expect(!fresh, 1)) static_for<0, 16>(slot);
        static_for<16, 80>(slot);
        QS(8);
        asm volatile("s_waitcnt vmcnt(1)\n\ts_barrier" ::: "memory");
        st4s(rs, qfo, 0, qfv);
        QS(9);
        asm volatile("s_waitcnt vmcnt(1)" : "+v"(fv) :: "memory");
        n0 = nxt; fresh = false;
      }
      if (n1 < 0) {
        SFOR(z, 20)
            if constexpr (V) M32B(false, a0, x[(z + 20) & 7], wt[z + 20]); else M32A(false, a0, wt[z + 20], x[(z + 20) & 7]);
            if constexpr (z < 15) x[(z + 25) & 7] = XRD(z + 25);
            if constexpr (z >= 7 && z < 15) asm volatile("v_xor_b32 %0, 0x10000, %0" : "+v"(ar[(z + 25) & 7]));
            SCHED();
        SEND
        u32 oa = ol + near_((m == 2 ? W_VO : W_KQ) + (u32)(n0 * NH + h) * 16384u, m == 1 ? 0u : 1u, pol);
        v4u pp, pq;
        asm volatile("s_nop 11" : "+v"(oa));
        QPK(pp, a0, 0, 0); QPK(pp, a0, 0, 2); st16(rs, oa, pp);
        QPK(pq, a0, 1, 0); QPK(pq, a0, 1, 2); st16(rs, oa + 512, pq);
      }
    };
#pragma unroll 1
    for (;;) {
        if (m == 2) run(IC<1>{}); else run(IC<0>{});
        if (n1 > 0) break;
        if (!boot_ok(p, bfo, brk, 2u)) boot_wait(p, rs, bfo, 2u, dead);
        BAR();
        conv_job<1>(p, rs, lds, grp, flt, 0, tid == 1 ? fo : OOB, (u32)n0 + 1u);
        const u32 so = uu(W_H1 + (u32)(n0 + 1) * H_TILE);
        SFOR(i, 10) QDMA(i, dx, t16, so); SEND
    }
}

constexpr int OU_SEL = 131072;
#define OSAMP 46

DEV void role_out(const Params& p, lptr lds, int L, int kq, int ng) {
    const int tid = threadIdx.x, lane = tid & 63, w = uni(tid >> 6), hh = lane >> 5, r32 = lane & 31;
    const rsrc_t rs = mk_rsrc(p.ws, W_END);
    v4u wo[64];
    Filt flt;
    bool dead = false;
    {
        const v4u sw = mk_srd(p.Wo, DM * NQ * 2);
        const u32 vo = wst_src(lane, DM) + (ng == 4 ? 1024u : (u32)((ng >> 1) * 512 + (ng & 1) * 128 + ((lane ^ (lane >> 2)) & 8) * 16));
        auto fill = [&](int b) {
#pragma unroll 1
            for (int i = 0; i < 8; ++i) DMAIN(sw, uu((b % 5) * 32768 + (4 * i + w) * 1024), vo, uu((kq * 1024 + b * 128 + 16 * i + 4 * w) * (DM * 2)));
        };
#pragma unroll 1
        for (int b = 0; b < 5; ++b) fill(b);
        lptr tb = lds + hh * 1024 + wst_rd(lane, w);
        SFOR(b, 8)
            VMW(b < 3 ? 32 : 62 - 8 * b); BAR();
            lptr q = tb + (b % 5) * 32768;
#pragma unroll
            for (int k = 0; k < 8; ++k) { lptr a = q + (k >> 1) * 8192 + (k & 1) * 2048; wo[8 * b + k] = tr2(a, a + 4096); if (b < 7) asm volatile("" : "+a"(wo[8 * b + k])); else PIN(wo[8 * b + k]); }
            if constexpr (b < 3) { BAR(); fill(b + 5); }
            if constexpr (b == 2) filt_load<3, 0>(p, L == 0 ? 44 + kq * 5 + ng : 1 << 12, flt);
        SEND
    }
    EV(6);
    const u32 pol = get_pol(p, rs, dead);
    if (L == 0) {
        BAR();
        conv_job<3>(p, rs, lds, 44 + kq * 5 + ng, flt, 0);
        DRAIN(); BAR();
        st4s(rs, tid == 0 ? foff(F_CV + 44 + kq * 5 + ng) : OOB, 0, 2u);
    }
    EV(15);
    MK_SRD();
    const u32 ofl = (lane < 8 && w == 0) ? near_(foff(F_O + L * 32 + kq * 8 + lane), 0u, pol) : OOB;
    const u32 l16 = (u32)lane * 16u;
    const u32 fo = (tid == 192) ? near_(foff(F_P + L * 32 + kq * 5 + ng), 1u, pol) : OOB;
    auto osrc = [&](int s) { return uu(hblk(W_VO, L * NSEG + s, kq * 8, 0u, pol) + w * 1024); };
#define ODMA(i, dst, vo, so) DMA16I(dst, (i) * 4096, vo, so, ((i) >> 1) * 16384 + ((i) & 1) * 4096)
    const u32 pvo = (u32)(kq * 5 + ng) * 16384u + (u32)w * 2048u + l16;
    lptr tw = lds + OU_SEL + 256 + r32 * 272 + (ng == 4 ? hh * 32 + w * 4 : (w & 1) * 128 + hh * 16 + (w >> 1) * 8), tr = lds + OU_SEL + 256 + (8 * w + (lane >> 4)) * 272 + (lane & 15) * 16;
    lptr selw = lds + OU_SEL + (w == 0 ? 0 : 64);
    u32 pfo = OOB;
    u32 buf = 0;
    if (w == 0) wait_flags(rs, ofl, 1u, dead);
    BAR();
    for (int s = 0; s < NSEG; ++s) {
        u32 src = osrc(s), dst = uu(buf + w * 1024);
        lptr A = lds + buf + l16;
        asm volatile("" : "+s"(src), "+s"(dst));
#define OS(k) do {} while (0)
        OS(0);
        OS(1);
        SFOR(i, 10) ODMA(i, dst, l16, src); SEND
        OS(2);
        asm volatile("s_waitcnt vmcnt(7)\n\ts_barrier" ::: "memory");
        OS(3);
        LS2(L == 0 && w == 1, 96 + kq * 5 + ng);
        v4u o[64];
#pragma unroll
        for (int k = 0; k < 3; ++k) o[k] = lds_r16(A + k * 1024);
        st4s(rs, pfo, 0, (u32)s);
        u32 fv, pa; int rdy;
        f32x16 acc;
        SCHED();
        SFOR(k, 64)
            if constexpr (k < 56) M32A(k == 0, acc, wo[k], o[k]); else asm volatile("v_mfma_f32_32x32x16_bf16 %0, %1, %2, %0" : "+v"(acc) : "v"(wo[k]), "v"(o[k]));
            if constexpr (k < 61) o[k + 3] = lds_r16(A + (k + 3) * 1024);
            SCHED();
            if constexpr (k < 12 && (k & 1) == 0) ODMA(10 + (k >> 1), dst, l16, src);
            if constexpr (k == 7) OS(4);
            if constexpr (k == 8) OS(5);
            if constexpr (k == 23) OS(6);
            if constexpr (k == 24) OS(7);
            if constexpr (k == 43) OS(8);
            if constexpr (k == 44) OS(9);
            if constexpr (k == 59) OS(10);
            if constexpr (k == 63) OS(11);
            if constexpr (k == 7) asm volatile("s_waitcnt vmcnt(8)\n\ts_barrier" ::: "memory");
            if constexpr (k == 23) asm volatile("s_waitcnt vmcnt(4)\n\ts_barrier" ::: "memory");
            if constexpr (k == 30) { pa = pvo + near_(p_item(s), (u32)L, pol); PIN(pa); }
            if constexpr (k == 43) asm volatile("s_waitcnt vmcnt(0)\n\ts_barrier" ::: "memory");
            if constexpr (k == OSAMP) LD4A(fv, ofl, 0);
            if constexpr (k == 62) {
                asm volatile("s_waitcnt vmcnt(0)" : "+v"(fv) :: "memory");
                *(LAS int*)selw = (int)((u32)__builtin_amdgcn_ballot_w64(fv < (u32)s + 2) & 255u);
            }
            SCHED();
        SEND
        asm volatile("s_nop 11" : "+v"(acc));
        u32 pc[8];
#pragma unroll
        for (int i = 0; i < 8; ++i) pc[i] = pk(acc[2 * i], acc[2 * i + 1]);
        if (ng != 4) {
#pragma unroll
            for (int i = 0; i < 4; ++i) *(LAS v2u*)(tw + i * 32) = (v2u){pc[2 * i], pc[2 * i + 1]};
        } else {
#pragma unroll
            for (int i = 0; i < 8; ++i) *(LAS u32*)(tw + (i >> 1) * 64 + (i & 1) * 16) = pc[i];
        }
        BAR();
        const v4u p0 = lds_r16(tr), p1 = lds_r16(tr + 4 * 272);
        rdy = *(LAS int*)(lds + OU_SEL);
        st16(rs, pa, p0); st16(rs, pa + 1024, p1);
        const bool ready = uni(rdy) == 0;
        SCHED();
        OS(12);
        if (ready) pfo = fo;
        else {
            LD4A(fv, ofl, 0);
            asm volatile("s_waitcnt vmcnt(1)\n\ts_barrier" ::: "memory");
            st4s(rs, fo, 0, (u32)s + 1); pfo = OOB;
            OS(14);
            LS2(L == 0 && w == 1, 160 + kq * 5 + ng);
            asm volatile("s_waitcnt vmcnt(1)" : "+v"(fv) :: "memory");
            if (w == 0 && s + 1 < NSEG && ((u32)__builtin_amdgcn_ballot_w64(fv < (u32)s + 2) & 255u) != 0u) wait_flags(rs, ofl, (u32)s + 2, dead);
            BAR();
        }
        OS(13);
        buf ^= 65536u;
    }
}

template <int L> DEV void role_row(const Params& p, lptr lds, int ri) {
    const int lane = threadIdx.x & 63, w = uni(threadIdx.x >> 6);
    const rsrc_t rs = mk_rsrc(p.ws, W_END);
    bool dead = false;
    const u32 pol = get_pol(p, rs, dead);
    const u32 fp = threadIdx.x < 20 ? near_(foff(F_P + L * 32 + lane), 1u, pol) : OOB, fo = threadIdx.x == 192 && L == 0 ? near_(foff(F_H2 + (ri >> 2)), 1u, pol) : OOB;
    u32 fv = 0;
    MK_SRD();
    auto yfill = [&](int b, int i) { DMA32(uu((b & 1) * 40960 + w * 10240 + i * 2048), (u32)lane * 16u, uu(W_Y + (u32)((ri >> 1) * 8 + b) * 20480u + (w & 1) * 10240 + i * 2048)); };
    auto ywait = [&](u32 v) {
        wait_flags(rs, threadIdx.x < (v == 3u ? 36u : 160u) ? foff(F_CV + threadIdx.x) : OOB, v, dead);
    };
    v4u y8[10] = {};
    v2u xa, xb; u32 xc;
    const v4u sx = L == 0 ? mk_srd(p.x, ACT) : mk_srd(p.ws + W_X2, ACT);
    auto xload = [&](int s) {
        asm volatile("buffer_load_dwordx2 %0, %3, %5, %6 offen sc1\n\tbuffer_load_dwordx2 %1, %3, %5, %6 offen offset:512 sc1\n\tbuffer_load_dword %2, %4, %5, %6 offen offset:1024 sc1"
                     : "=&v"(xa), "=&v"(xb), "=&v"(xc) : "v"((u32)lane * 8u), "v"((u32)lane * 4u), "s"(sx), "s"(uu((u32)(s * 32 + ri) * (DM * 2))) : "memory");
        SCHED();
    };
    ywait(1u);
    BAR();
    EV(15);
#pragma unroll 1
    for (int i = 0; i < 5; ++i) yfill(0, i);
    if (L == 1) wait_flags(rs, lane == 0 ? near_(foff(F_H2 + (ri >> 2)), 1u, pol) : OOB, 1u, dead);
    xload(0);
    LD4A(fv, fp, 0);
    VMW(0); SCHED();
    asm volatile("" : "+v"(xa), "+v"(xb), "+v"(xc), "+v"(fv));
    BAR();
    for (int s = 0; s < NSEG; ++s) {
        u32 xi[5] = {xa.x, xa.y, xb.x, xb.y, xc};
        u32 pa = near_(p_item(s), (u32)L, pol) + (u32)ri * 256u, pc = pa + 65536u + (u32)(lane >> 4) * 81920u + (u32)(lane & 15) * 16u;
        pa += (u32)(lane >> 5) * 81920u + (u32)((lane >> 4) & 1) * 16384u + (u32)(lane & 15) * 16u;
        u32 yf = s == 1 ? foff(F_CV + min(threadIdx.x, 159u)) : s == 25 ? foff(F_CV + min(lane, 35)) : OOB;
        asm volatile("" : "+v"(xi[0]), "+v"(xi[1]), "+v"(xi[2]), "+v"(xi[3]), "+v"(xi[4]), "+v"(pa), "+v"(pc), "+v"(yf));
#define RS(k) do {} while (0)
        RS(0);
        if (w == 0 && ((u32)__builtin_amdgcn_ballot_w64(fv <= (u32)s) & 0xFFFFFu) != 0u) wait_flags(rs, fp, (u32)s + 1, dead);
        BAR();
        RS(1);
        v4u d[5];
        asm volatile("buffer_load_dwordx4 %0, %5, %7, 0 offen sc1\n\tbuffer_load_dwordx4 %1, %5, %7, %8 offen sc1\n\tbuffer_load_dwordx4 %2, %5, %7, %9 offen sc1\n\t"
                     "buffer_load_dwordx4 %3, %5, %7, %10 offen sc1\n\tbuffer_load_dwordx4 %4, %6, %7, 0 offen sc1"
                     : "=&v"(d[0]), "=&v"(d[1]), "=&v"(d[2]), "=&v"(d[3]), "=&v"(d[4]) : "v"(pa), "v"(pc), "s"(srd), "s"(163840), "s"(32768), "s"(196608) : "memory");
        RS(2);
        LS2(L == 0, 192 + ri);
        SCHED();
        f32x2 y[5];
        {
            if ((s & 7) == 0) {
                lptr ya = lds + ((s >> 3) & 1) * 40960 + (w >> 1) * 20480 + (w & 1) * 16, yc = ya + 4096 + (lane >> 1) * 32 + (lane & 1) * 10240;
#pragma unroll
                for (int i = 0; i < 8; ++i) y8[i] = lds_r16(ya + lane * 32 + (i >> 2) * 2048 + (i & 3) * 5120);
                y8[8] = lds_r16(yc); y8[9] = lds_r16(yc + 5120);
            }
#pragma unroll
            for (int i = 0; i < 10; ++i) {
                y[i >> 1][i & 1] = lo_f(y8[i].x);
                y8[i] = (v4u){__builtin_amdgcn_alignbit(y8[i].y, y8[i].x, 16), __builtin_amdgcn_alignbit(y8[i].z, y8[i].y, 16), __builtin_amdgcn_alignbit(y8[i].w, y8[i].z, 16), y8[i].w >> 16};
                PIN(y8[i]);
            }
            asm volatile("" : "+v"(y[0]), "+v"(y[1]), "+v"(y[2]), "+v"(y[3]), "+v"(y[4]));
        }
        u32 yv;
        {
            xload(s + 1);
            const bool yy = (s & 7) - 2u < 5u && s < NSEG - 8;
            u32 yo = yy ? (u32)lane * 16u : OOB;
            DMA32(uu(yy ? (((s >> 3) + 1) & 1) * 40960 + w * 10240 + ((s & 7) - 2) * 2048 : 81920), yo, uu(W_Y + (u32)((ri >> 1) * 8 + (s >> 3) + 1) * 20480u + (w & 1) * 10240 + ((s & 7) - 2) * 2048));
            LD4A(yv, yf, 0);
            asm volatile("s_waitcnt vmcnt(6)" : "+v"(d[0]), "+v"(d[1]), "+v"(d[2]), "+v"(d[3]), "+v"(d[4]));
        }
        f32x2 o[5];
        {
            f32x2 t[5][2];
#pragma unroll
            for (int i = 0; i < 5; ++i) {
                const auto e = __builtin_amdgcn_permlane32_swap(d[i].x, d[i].z, false, false), f = __builtin_amdgcn_permlane32_swap(d[i].y, d[i].w, false, false);
                t[i][0] = up2(e[0]) + up2(e[1]); t[i][1] = up2(f[0]) + up2(f[1]);
            }
            o[0] = t[0][0] + t[1][0]; o[1] = t[0][1] + t[1][1]; o[2] = t[2][0] + t[3][0]; o[3] = t[2][1] + t[3][1];
            const auto e = __builtin_amdgcn_permlane16_swap(u_(t[4][0].x), u_(t[4][1].x), false, false), f = __builtin_amdgcn_permlane16_swap(u_(t[4][0].y), u_(t[4][1].y), false, false);
            o[4] = (f32x2){f_(e[0]) + f_(e[1]), f_(f[0]) + f_(f[1])};
        }
        f32x2 x1[5];
#pragma unroll
        for (int i = 0; i < 5; ++i) x1[i] = up2(pk2(up2(xi[i]) + up2(pk2(o[i]))));
        asm volatile("" : "+v"(x1[0]), "+v"(x1[1]), "+v"(x1[2]), "+v"(x1[3]), "+v"(x1[4]));
        RS(3);
        const f32x2 st1 = ln_stats(x1);
        u32 xp[5];
#pragma unroll
        for (int i = 0; i < 5; ++i) xp[i] = pk2(x1[i] + ((x1[i] - st1.x) * st1.y) * y[i]);
        const int row = s * 32 + ri;
        RS(4);
        if (L == 0) {
            const u32 ro = (u32)row * (DM * 2);
            f32x2 x2[5];
#pragma unroll
            for (int i = 0; i < 5; ++i) x2[i] = up2(xp[i]);
            st8s(rs, (u32)lane * 8, W_X2 + ro, (v2u){xp[0], xp[1]});
            st8s(rs, (u32)lane * 8 + 512, W_X2 + ro, (v2u){xp[2], xp[3]});
            st4s(rs, (u32)lane * 4 + 1024, W_X2 + ro, xp[4]);
            SCHED();
            const f32x2 st2 = ln_stats(x2);
            u32 hp[5];
#pragma unroll
            for (int i = 0; i < 5; ++i) hp[i] = pk2((x2[i] - st2.x) * st2.y);
            store_h(rs, near_(W_H2 + (u32)s * H_TILE, 1u, pol), row, hp);
        } else {
            u16* o_ = p.out + (size_t)row * DM;
            *(v2u*)(o_ + 4 * lane) = (v2u){xp[0], xp[1]};
            *(v2u*)(o_ + 256 + 4 * lane) = (v2u){xp[2], xp[3]};
            *(u32*)(o_ + 512 + 2 * lane) = xp[4];
        }
        RS(5);
        LD4A(fv, fp, 0);
        if (L == 0) asm volatile("s_waitcnt vmcnt(1)\n\ts_barrier" ::: "memory");
        st4s(rs, fo, 0, (u32)s + 1);
        RS(6);
        VMW(1); SCHED();
        asm volatile("" : "+v"(fv), "+v"(yv), "+v"(xa), "+v"(xb), "+v"(xc));
        if (__builtin_expect(s == 1 || s == 25, 0) && !all_(yv >= (s == 1 ? 2u : 3u))) ywait(s == 1 ? 2u : 3u);
        RS(7);
        if (ri == 0) LST(320 + L * 64 + s, rt32());
        LS2(L == 0, 224 + ri);
    }
}

constexpr float SCALE = 0.08838834764831845f, LOG2E = 1.4426950408889634f;
constexpr int C_K = 0, C_V = 8192, C_QX = 16384, C_MEM = 24576, C_Q = C_MEM + 10240, C_KM = C_Q + 2304, C_VM = C_KM + 2304, C_DUMP = C_VM + 2048,
              C_ZERO = C_DUMP + 1024, C_WL = C_ZERO + 128;
constexpr int NLF = 14;
constexpr int C_MASK = C_WL + 4 * NLF * 2048;
static_assert(C_WL % 16 == 0 && C_MASK + 3072 <= LDS_BYTES, "head lds");
#define CH_VIS 8
#define CH_H0 98
#define CH_W0 80
#define CH_MH 3
#define CH_MW 11
#define CH_REC 8
#define CH_LATE 30
#define CH_SOLO 6
#define CH_KV 12
#define CH_KV0 60
#define CH_M0 6
#define CH_DEC 2u
#define CH_UP 4
#define CH_WX 6
DEV void wait_until(u32 t) {
    unsigned long long c;
    asm volatile("1:\n\ts_memrealtime %0\n\ts_waitcnt lgkmcnt(0)\n\ts_mov_b64 vcc, %0\n\ts_sub_u32 vcc_lo, %1, vcc_lo\n\ts_cmpk_lt_u32 vcc_lo, 400\n\ts_cbranch_scc1 1b" : "=&s"(c) : "s"(uu(t - 1u)) : "vcc", "scc", "memory");
}
#define CH_FD 14
#define CH_FC 34
#define FINE_T1(t) uu((t) - (CH_FD + 1u))
#define FINE_T4(t) uu(4u * (t) - CH_FC)
#define NOP8 "s_nop 0\n\ts_nop 0\n\ts_nop 0\n\ts_nop 0\n\ts_nop 0\n\ts_nop 0\n\ts_nop 0\n\ts_nop 0\n\t"
#define LOOP_TXT(tmp, t1) "s_memrealtime vcc\n1:\n\ts_waitcnt lgkmcnt(0)\n\ts_sub_u32 " tmp ", " t1 ", vcc_lo\n\ts_memrealtime vcc\n\ts_cmpk_lt_u32 " tmp ", 400\n\ts_cbranch_scc1 1b\n\t"
#define WAIT_TXT(tmp, t1, t4) LOOP_TXT(tmp, t1) \
    "s_nop 1\n\ts_memrealtime s[90:91]\n\ts_nop 3\n\ts_memrealtime s[92:93]\n\ts_nop 3\n\ts_memrealtime s[94:95]\n\ts_getpc_b64 s[96:97]\n2:\n\ts_waitcnt lgkmcnt(0)\n\t" \
    "s_add_u32 s90, s90, vcc_lo\n\ts_add_u32 s92, s92, s94\n\ts_sub_u32 s90, " t4 ", s90\n\ts_sub_u32 s90, s90, s92\n\ts_max_i32 s90, s90, 0\n\ts_min_i32 s90, s90, 29\n\t" \
    "s_mul_i32 s90, s90, 87\n\ts_lshr_b32 s90, s90, 4\n\ts_andn2_b32 s90, s90, 3\n\ts_sub_u32 s90, 3f-2b, s90\n\ts_add_u32 s96, s96, s90\n\ts_addc_u32 s97, s97, 0\n\t" \
    "s_setpc_b64 s[96:97]\n\t" NOP8 NOP8 NOP8 NOP8 NOP8 "3:\n\t"
#define WAIT_CLOB "vcc", "scc", "s90", "s91", "s92", "s93", "s94", "s95", "s96", "s97", "memory"
#define SMF_A(acc, a, b) asm volatile("v_smfmac_f32_16x16x64_bf16 %0, %1, %2, %3" : "+v"(acc) : "v"(a), "a"(b), "v"(idx))
#define SMF_V(acc, a, b) asm volatile("v_smfmac_f32_16x16x64_bf16 %0, %1, %2, %3" : "+v"(acc) : "v"(a), "v"(b), "v"(idx))
#define MF0(d, a, b) asm volatile("v_mfma_f32_16x16x32_bf16 %0, %1, %2, 0" : "=&v"(d) : "v"(a), "v"(b))
#define MF1(d, a, b) asm volatile("v_mfma_f32_16x16x32_bf16 %0, %1, %2, %0" : "+v"(d) : "v"(a), "v"(b))
#define MF0H(d, a, b) asm volatile("v_mfma_f32_16x16x16_bf16 %0, %1, %2, 0" : "=&v"(d) : "v"(a), "v"(b))
#define MF1H(d, a, b) asm volatile("v_mfma_f32_16x16x16_bf16 %0, %1, %2, %0" : "+v"(d) : "v"(a), "v"(b))
DEV v8u tr_frag(lptr p0, lptr p1, lptr p2, lptr p3) { return cat8(tr2(p0, p1), tr2(p2, p3)); }
constexpr int qslot(int ks) { return (ks & 1) && ks < 8 ? ks >> 1 : -1; }
constexpr int kslot(int ks) { return ks < 4 ? 4 + ks : -1; }
constexpr int vslot(int ks) { return ks >= 4 ? 4 + ks : -1; }
DEV u32 ring_map(u32 off, u32 half) { return ((off >> 13) << 14) + (half << 13) + (off & 8191u); }
#define ZRD(x, i) x = __builtin_bit_cast(f32x4, lds_r16(ZP + 16 * (i)))
#define VMAX(d, a, b) asm volatile("v_max_f32 %0, %1, %2" : "=v"(d) : "v"(a), "v"(b))
#define VMAX3(d, a, b, c_) asm volatile("v_max3_f32 %0, %1, %2, %3" : "=v"(d) : "v"(a), "v"(b), "v"(c_))
#define VADD(d, a, b) asm volatile("v_add_f32 %0, %1, %2" : "=v"(d) : "v"(a), "v"(b))
#define VEXP(d, a) asm volatile("v_exp_f32 %0, %1" : "=v"(d) : "v"(a))
#define VFMC(d, a, b) asm volatile("v_fmamk_f32 %0, %1, 0x3e0293ee, %2" : "=v"(d) : "v"(a), "v"(b))
#define VMULC(d, a) asm volatile("v_mul_f32 %0, 0x3e0293ee, %1" : "=v"(d) : "v"(a))
#define SEL(d, a, b) asm volatile("v_cndmask_b32 %0, %1, %2, %3" : "=v"(d) : "v"(a), "v"(b), "s"(himask))
#define QPERM(d, a, q) asm volatile("v_mov_b32_dpp %0, %1 quad_perm:" q " row_mask:0xf bank_mask:0xf" : "=v"(d) : "v"(a))

#define SUMRANGE 0x28000000u
#define HSAMP 71
DEV void role_head(const Params& p, lptr lds, int L, int h) {
    const int tid = threadIdx.x, lane = tid & 63, w = uni(tid >> 6), g = lane >> 4, r = lane & 15;
    const rsrc_t rs = mk_rsrc(p.ws, W_END);
    v8u wq0[10], wq1[10], wk0[10], wk1[10], wv0[10], wv1[10];
    lptr WL = lds + C_WL + w * (NLF * 2048) + lane * 16;
    {
        constexpr int GS = 1056, CS = 48 * GS;
        const v4u sq = mk_srd(p.Wq, DM * NQ * 2), sk = mk_srd(p.Wk, DM * NQ * 2), sv = mk_srd(p.Wv, DM * NQ * 2);
        const u32 vo = (u32)(g * NQ + h * DH + 8 * (r ^ ((g & 2) << 2))) * 2u;
        auto fill = [&](int ch) {
#pragma unroll 1
            for (int i = 0; i < 4; ++i) {
                const u32 dst = uu((ch % 3) * CS + (4 * i + w) * GS), so = uu((ch * 64 + 16 * i + 4 * w) * (NQ * 2));
                DMAIN(sq, dst, vo, so); DMAIN(sk, dst + 16 * GS, vo, so); DMAIN(sv, dst + 32 * GS, vo, so);
            }
        };
        int vote = 0;
        if (L == 1) {
            if (w == 0) vote = probe(rs, h);
            BAR();
            EV(2);
            pre_rings(p, rs, h);
        }
        fill(0); fill(1); fill(2);
        const int q4 = r >> 2;
        lptr tb = lds + (4 * (g >> 1) + q4) * GS + (g & 1) * 512 + ((w * 64 + (r & 3) * 8) ^ ((g & 1) << 7));
        SFOR(ch, 10)
            VMW(ch == 1 || ch == 2 ? 25 : ch < 8 ? 24 : 108 - 12 * ch);
            BAR();
            if constexpr (ch == 0) boot_set(p, rs, tid == 0 && L == 1 ? foff(F_RG + h) : OOB, (u32)((1 << 20) + vote));
            auto frag = [&](int m, int t) { lptr b = tb + (ch % 3) * CS + m * 16 * GS + t * 32; return tr_frag(b, b + 256, b + 8 * GS, b + 8 * GS + 256); };
            wq0[ch] = frag(0, 0); wq1[ch] = frag(0, 1); wk0[ch] = frag(1, 0); wk1[ch] = frag(1, 1); wv0[ch] = frag(2, 0); wv1[ch] = frag(2, 1);
            asm volatile("" : "+a"(wq0[ch]), "+a"(wk0[ch]), "+v"(wv0[ch]), "+v"(wv1[ch]));
            if constexpr (qslot(ch) < 0) asm volatile("" : "+a"(wq1[ch])); else asm volatile("" : "+v"(wq1[ch]));
            if constexpr (kslot(ch) < 0) asm volatile("" : "+a"(wk1[ch])); else asm volatile("" : "+v"(wk1[ch]));
            BAR();
            if constexpr (ch < 7) fill(ch + 3);
        SEND
#pragma unroll
        for (int ch = 0; ch < 10; ++ch) {
            if (qslot(ch) >= 0) { lds_w16(WL + qslot(ch) * 2048, wq1[ch].lo); lds_w16(WL + qslot(ch) * 2048 + 1024, wq1[ch].hi); }
            if (kslot(ch) >= 0) { lds_w16(WL + kslot(ch) * 2048, wk1[ch].lo); lds_w16(WL + kslot(ch) * 2048 + 1024, wk1[ch].hi); }
            if (vslot(ch) >= 0) { lds_w16(WL + vslot(ch) * 2048, wv1[ch].lo); lds_w16(WL + vslot(ch) * 2048 + 1024, wv1[ch].hi); }
        }
        CFENCE();
    }
    const u32 idx = (r & 2) ? 0xEEEEu : 0x4444u;
    const unsigned long long himask = 0xCCCCCCCCCCCCCCCCull;
    bool dead = false;
    EV(6);
    const u32 pol = get_pol(p, rs, dead);
    const u32 pol0 = pol & 1u;
    const u32 l16 = (u32)lane * 16u;
    const u32 mring = W_MRING + (u32)L * (MB / 4) + pol0 * 8192u, hint = mring + 4 * 32768u;
    const u32 fkh = lane < 3 ? near_(foff(F_QKV + lane * 64 + L * 32 + h), 0u, pol) : lane < 6 ? hint + (u32)(lane - 3) * 4u : OOB;
    u32 tm = 0;
    MK_SRD();
    const unsigned long long kmask = __builtin_amdgcn_ballot_w64((hblk(W_KQ, L * NSEG + lane, h, 0u, pol) & 8192u) != 0u),
                             vmask = __builtin_amdgcn_ballot_w64((hblk(W_VO, L * NSEG + lane, h, 1u, pol) & 8192u) != 0u);
    u32 kb = uu(W_KQ + (u32)(L * NSEG * NH + h) * 16384u + w * 2048);
    u32 dk = uu(C_K + w * 2048), dv = dk + (C_V - C_K), dq = dk + (C_QX - C_K), dm = uu(C_MEM + w * 2048), dm2 = uu(w < 2 ? C_MEM + 8192 + w * 1024 : C_DUMP);
    asm volatile("" : "+s"(dk), "+s"(dv), "+s"(dq), "+s"(dm), "+s"(dm2));
    u32 ksrc = kb + ((u32)kmask & 1u) * 8192u, vsrc = kb + (W_VO - W_KQ) + ((u32)vmask & 1u) * 8192u;
    lptr qw = lds + C_Q + (2 * g) * 288 + (32 * w + 2 * r) * 2;
    lptr kw = qw + (C_KM - C_Q);
    lptr qr = lds + C_Q + (2 * (r >> 2) + (r & 1)) * 288 + g * 16;
    lptr LB = lds + l16, VX = lds + C_V + w * 2048 + l16;
    if (tid >= 64 && tid < 70) { const u32 z = tid == 68 ? 0x3F803F80u : tid == 69 ? PSENT : 0u; lds_w16(lds + C_ZERO + (tid - 64) * 16, (v4u){z, z, z, z}); }
    if (w < 3) {
        const u32 ng = u_(-1000000000.0f * LOG2E);
        v4u mk;
#pragma unroll
        for (int i = 0; i < 4; ++i) mk[i] = w == 0 || (w == 1 && 4 * g + i > r) ? ng : 0u;
        lds_w16(lds + C_MASK + w * 1024 + l16, mk);
    }
    u32 zaddr = 0; PIN(zaddr);
    lptr ZP = lds + zaddr + C_ZERO;
#define LF_RD(slot, hf) lf[slot][hf] = lds_r16(WL + (slot) * 2048 + (hf) * 1024)
    BAR();
    const u32 ofl = tid == 192 ? near_(foff(F_O + L * 32 + h), 0u, pol) : tid == 193 && (h & 15) == 0 ? hint + 128u + (u32)h * 8u : OOB;
    u32 oso = (g & 1) ? OOB : W_ORING + (u32)L * (MB / 2) + ring_map((u32)((r >> 3) * 32768 + h * 1024 + w * 256 + (g >> 1) * 128 + (r & 7) * 16), pol0);
    u32 missed = 0, dirty = 1, rep = PSENT;
    asm volatile("" : "+v"(oso), "+v"(qw), "+v"(kw), "+v"(qr), "+v"(VX));
    asm volatile("" : "+s"(ksrc), "+s"(vsrc));
    wait_flags(rs, lane < 3 ? fkh : OOB, 1u, dead);
    EV(7);
    DMA32(dv, l16, vsrc); DMA32(dk, l16, ksrc);
    EV(8);
    auto next_slot = [&](u32 now) {
        const u32 hv = ld4(rs, fkh), per = rdl(hv, 4);
        if (per < 400u) { tm = rdl(hv, 5) - per; for (int n = 0; n < 3 && (int)(tm - now) < 0; ++n) tm += per; }
    };
    if (L == 1) { next_slot(rt32() + CH_KV0); tm -= 24u; }
    for (int s = 0; s < NSEG;) {
        {
            *(LAS u32*)(lds + C_DUMP + 128 + w * 4) = tm;
            VMW(5); BAR();
            st4s(rs, ofl, 0, (l16 & 16u) ? rep : (u32)s);
            const v4u c = lds_r16(lds + C_DUMP + 128) - tm;
            const u32 add = uu((u32)max(max((int)c.x, (int)c.y), max((int)c.z, (int)c.w)));
            tm += add;
            if (__builtin_expect((dirty | add) != 0u, 0)) {
                wait_until(tm - 44u);
                st16(rs, oso, lds_r16(ZP + 80));
            }
        }
        u32 ms = mring + (u32)(s & 3) * 32768u + w * 2048, ms2 = mring + (u32)(s & 3) * 32768u + 16384u + (w & 1) * 1024;
        u32 l3 = w < 2 ? l16 : OOB;
        asm volatile("" : "+s"(ms), "+s"(ms2)); PIN(l3);
        f32x4 aq0, aq1, ak0, ak1;
        v4u lf[NLF][2];
        ZRD(aq0, 0); ZRD(aq1, 1); ZRD(ak0, 2); ZRD(ak1, 3);
#pragma unroll
        for (int i = 0; i < 4; ++i) { LF_RD(i, 0); LF_RD(i, 1); }
        SCHED();
        v4u A[10];
#define HT(k) do {} while (0)
        v4u kx[8], qf[4], kmf[4], vx[2], ones, P;
        float fa, fb, fc, fd, sx[4], t[4], sm0, sm1, tm0, tm1, inv; u32 pa, pb, fl, pmm, tmp = 0; unsigned long long odd;
        f32x4 av0, av1, s0, s1, sm, oa, ob, rsum;
        v2u vm, pm, vh, vl, on[2];
        f32x2 zz[4];
#define LW(sl) cat8(lf[sl][0], lf[sl][1])
#define PQ(ks, tl) do { if constexpr (!(tl)) SMF_A(aq0, A[ks], wq0[ks]); else if constexpr (qslot(ks) < 0) SMF_A(aq1, A[ks], wq1[ks]); else { const v8u lw_ = LW(qslot(ks)); SMF_V(aq1, A[ks], lw_); } } while (0)
#define PK(ks, tl) do { if constexpr (!(tl)) SMF_A(ak0, A[ks], wk0[ks]); else if constexpr (kslot(ks) < 0) SMF_A(ak1, A[ks], wk1[ks]); else { const v8u lw_ = LW(kslot(ks)); SMF_V(ak1, A[ks], lw_); } } while (0)
#define PV(ks, tl) do { if constexpr (!(tl)) SMF_V(av0, A[ks], wv0[ks]); else if constexpr (vslot(ks) < 0) SMF_V(av1, A[ks], wv1[ks]); else { const v8u lw_ = LW(vslot(ks)); SMF_V(av1, A[ks], lw_); } } while (0)
#define KXR(i) kx[i] = lds_r16(LB + C_K + ((i) & 1) * 4096 + ((i) >> 1) * 1024)
#define XW(q_) do { *(LAS u32*)(q_) = pa; *(LAS u32*)((q_) + 288) = pb; } while (0)
        auto slot = [&](auto zc) __attribute__((always_inline)) {
            constexpr int z = decltype(zc)::value;
            constexpr int vs[] = {0, 8, 1, 10, 9, 2, 11, -1, -1, -1, -1, 4, 6, 12, -1, -1, -1, 13, 14, -1, 16, 18, 15, 3, 5, 17, 7, 19};
            if constexpr (z < 12) PQ(z >> 1, z & 1);
            else if constexpr (z < 20) PQ(6 + (z & 3), z >> 4);
            else if constexpr (z < 32) PK((z - 20) >> 1, z & 1);
            else if constexpr (z < 40) PK(6 + (z & 3), (z >> 2) & 1);
            else if constexpr (z < 42) { if constexpr (z == 40) MF0(s0, kx[0], qf[0]); else MF0(s1, kx[1], qf[0]); }
            else if constexpr (z < 48) { if constexpr (z & 1) MF1(s1, kx[z - 40], qf[(z - 40) >> 1]); else MF1(s0, kx[z - 40], qf[(z - 40) >> 1]); }
            else if constexpr (z == 55) MF0(sm, kmf[0], qf[0]);
            else if constexpr (z > 55 && z < 59) MF1(sm, kmf[z - 55], qf[z - 55]);
            else if constexpr (z == 62) MF0(rsum, ones, P);
            else if constexpr (z == 63) MF0(oa, vx[0], P);
            else if constexpr (z == 64) MF0(ob, vx[1], P);
            else if constexpr (z == 67) { const v2u ones2 = ones.lo; MF1H(rsum, ones2, pm); }
            else PV(vs[z - 48] >> 1, vs[z - 48] & 1);
#define USE(x) asm volatile("" :: "v"(x))
            if constexpr (z == 20 || z == 24 || z == 26) USE(lf[4 + ((z - 20) >> 1)][1]);
            if constexpr (z == 41) USE(qf[3]);
            if constexpr (z == 45 || z == 46) USE(kx[z - 39]);
            if constexpr (z == 53) USE(lf[9][1]);
            if constexpr (z >= 54 && z < 58) USE(kmf[z - 54]);
            if constexpr (z == 61) USE(vl);
            if constexpr (z == 64) USE(lf[10][1]);
            if constexpr (z == 69) USE(lf[11][1]);
            if constexpr (z == 72) USE(lf[12][1]);
            if constexpr (z == 74) USE(lf[13][1]);
            if constexpr (z < 8) A[2 + z] = lds_r16(LB + C_MEM + (2 + z) * 1024);
            if constexpr (z >= 15 && z < 23) LF_RD(4 + ((z - 15) >> 1), (z - 15) & 1);
            if constexpr (z >= 24 && z < 27) KXR(z - 24);
            if constexpr (z == 27) XW(qw);
            if constexpr (z >= 28 && z < 31) KXR(z - 25);
            if constexpr (z == 32) asm volatile("s_waitcnt lgkmcnt(3)\n\ts_barrier" ::: "memory");
            if constexpr (z >= 33 && z < 37) qf[z - 33] = lds_r16(qr + (z - 33) * 64);
            if constexpr (z == 37 || z == 38) LF_RD(8, z - 37);
            if constexpr (z == 39 || z == 41) KXR(6 + (z == 41));
            if constexpr (z == 44) XW(kw);
            if constexpr (z == 45 || z == 46) LF_RD(9, z - 45);
            if constexpr (z == 47) vx[0] = lds_r16(VX);
            if constexpr (z == 48) { VMW(0); vh = *(LAS v2u*)(VX + 1032); }
            if constexpr (z == 54) { vl = *(LAS v2u*)(VX + 1024); vx[1] = (v4u){vh.x, vh.y, vl.x, vl.y}; }
            if constexpr (z == 49) { USE(lf[9][0]); asm volatile("s_barrier" ::: "memory"); }
            if constexpr (z >= 50 && z < 54) kmf[z - 50] = lds_r16(qr + (C_KM - C_Q) + (z - 50) * 64);
            if constexpr (z == 59 || z == 60) LF_RD(10, z - 59);
            if constexpr (z == 61 || z == 65) LF_RD(11, z == 65);
            if constexpr (z == 66 || z == 68) LF_RD(12, z == 68);
            if constexpr (z == 69 || z == 70) LF_RD(13, z - 69);
            if constexpr (z == HSAMP) fl = ld4(rs, fkh);
            SCHED();
            if constexpr (z == 19) HT(2);
            if constexpr (z == 39) HT(3);
            if constexpr (z == 51) HT(4);
            if constexpr (z == 64) HT(5);
            if constexpr (z == 20) VADD(fa, aq0[0], aq0[2]);
            if constexpr (z == 21) VADD(fb, aq0[1], aq0[3]);
            if constexpr (z == 23) VADD(fc, aq1[0], aq1[2]);
            if constexpr (z == 24) VADD(fd, aq1[1], aq1[3]);
            if constexpr (z == 25) VCVT(pa, fa, fc);
            if constexpr (z == 26) VCVT(pb, fb, fd);
            if constexpr (z >= 44 && z < 48) asm volatile("v_mov_b64 %0, 0" : "=v"(zz[z - 44]));
            if constexpr (z == 47) { av0 = __builtin_shufflevector(zz[0], zz[1], 0, 1, 2, 3); av1 = __builtin_shufflevector(zz[2], zz[3], 0, 1, 2, 3); }
            if constexpr (z == 48 || z == 49) asm volatile("v_mov_b64 %0, %1" : "=v"(on[z - 48]) : "s"(0x3F803F803F803F80ull));
            if constexpr (z == 49) ones = __builtin_shufflevector(on[0], on[1], 0, 1, 2, 3);
            if constexpr (z == 40) { VADD(fa, ak0[0], ak0[2]); VADD(fb, ak0[1], ak0[3]); }
            if constexpr (z == 42) { VADD(fd, ak1[1], ak1[3]); VADD(fc, ak1[0], ak1[2]); }
            if constexpr (z == 43) { VCVT(pb, fb, fd); VCVT(pa, fa, fc); }
            if constexpr (z >= 50 && z < 54) SEL(sx[z - 50], s0[z - 50], s1[z - 50]);
            if constexpr (z >= 54 && z < 58) VEXP(t[z - 54], sx[z - 54]);
            if constexpr (z == 58) asm volatile("v_cvt_pk_bf16_f32 %0, %2, %3\n\tv_cvt_pk_bf16_f32 %1, %4, %5" : "=&v"(P.x), "=v"(P.y) : "v"(t[0]), "v"(t[1]), "v"(t[2]), "v"(t[3]));
            if constexpr (z == 59) QPERM(P.z, P.x, "[2,3,0,1]");
            if constexpr (z == 60) QPERM(P.w, P.y, "[2,3,0,1]");
            if constexpr (z == 61) { USE(sm); VMULC(sm0, sm[0]); }
            if constexpr (z == 62) { VEXP(tm0, sm0); VMULC(sm1, sm[1]); }
            if constexpr (z == 63) VEXP(tm1, sm1);
            if constexpr (z == 64) asm volatile("v_cvt_pk_bf16_f32 %0, %2, %3\n\tv_cndmask_b32 %1, %0, 0, %4" : "=&v"(pmm), "=v"(pm.x) : "v"(tm0), "v"(tm1), "s"(himask));
            if constexpr (z == 65) asm volatile("v_cndmask_b32 %0, 0, %1, %2" : "=v"(pm.y) : "v"(pmm), "s"(himask));
            if constexpr (z == 67) { SEL(oa[0], oa[0], ob[0]); SEL(oa[1], oa[1], ob[1]); }
            if constexpr (z == 68 || z == 69) SEL(oa[z - 66], oa[z - 66], ob[z - 66]);
            if constexpr (z == 70) asm volatile("v_sub_u32 %0, %1, %2" : "=v"(pa) : "v"(rsum[0]), "s"(0x2B800000u), "v"(rsum));
            if constexpr (z == 71) asm volatile("v_cmp_le_u32 %0, %1, %2" : "=s"(odd) : "s"(SUMRANGE), "v"(pa));
            if constexpr (z == 72) VADD(fa, av0[0], av0[2]);
            if constexpr (z == 73) { NOPS(0); VADD(fb, av0[1], av0[3]); }
            if constexpr (z == 74) VCVT(vm.x, fa, fb);
            SCHED();
        };
        asm volatile("s_mov_b32 m0, %2\n\t" WAIT_TXT("%0", "%1", "%11") "buffer_load_dwordx4 %4, %5, %6 offen sc1 lds\n\tbuffer_load_dwordx4 %4, %5, %6 offen offset:1024 sc1 lds\n\t"
                     "s_mov_b32 m0, %3\n\ts_nop 0\n\tbuffer_load_dwordx4 %8, %5, %7 offen sc1 lds\n\ts_sleep 3\n\ts_mov_b32 m0, %9\n\ts_nop 0\n\t"
                     "buffer_load_dwordx4 %4, %5, %10 offen sc1 lds\n\tbuffer_load_dwordx4 %4, %5, %10 offen offset:1024 sc1 lds\n\ts_waitcnt vmcnt(2)\n\ts_barrier"
                     : "=&s"(tmp) : "s"(FINE_T1(tm)), "s"(dm), "s"(dm2), "v"(l16), "s"(srd), "s"(ms), "s"(ms2), "v"(l3), "s"(dq), "s"(ksrc ^ 8192u), "s"(FINE_T4(tm)) : WAIT_CLOB);
#pragma unroll
        for (int ks = 0; ks < 2; ++ks) A[ks] = lds_r16(LB + C_MEM + ks * 1024);
        SCHED();
        static_for<0, 76>(slot);
        u32 fail = 0;
        if (__builtin_expect(odd != 0ull, 0)) {
            u32 mn = ~0u;
#pragma unroll 5
            for (int i = 0; i < 10; ++i) { const v4u e = lds_r16(LB + C_MEM + i * 1024); mn = min(min(min(mn, e.x ^ PSENT), e.y ^ PSENT), min(e.z ^ PSENT, e.w ^ PSENT)); }
            fail = missed < SPIN_CAP && !all_(mn != 0u) ? OOB : 0u;
            float mx = fmaxf(fmaxf(fmaxf(sx[0], sx[1]), fmaxf(sx[2], sx[3])), fmaxf(sm0, sm1));
            mx = max32(max16(fmaxf(mx, dpp<0x4E>(mx))));
#pragma unroll
            for (int i = 0; i < 4; ++i) t[i] = __builtin_amdgcn_exp2f(sx[i] - mx);
            P.x = pk(t[0], t[1]); P.y = pk(t[2], t[3]); P.z = u_(dpp<0x4E>(f_(P.x))); P.w = u_(dpp<0x4E>(f_(P.y)));
            pmm = pk(__builtin_amdgcn_exp2f(sm0 - mx), __builtin_amdgcn_exp2f(sm1 - mx));
            pm = (r & 2) ? (v2u){0u, pmm} : (v2u){pmm, 0u};
            asm volatile("s_nop 1" : "+v"(P), "+v"(pm));
            MF0(rsum, ones, P); MF0(oa, vx[0], P); MF0(ob, vx[1], P);
            NOPS(7);
            { const v2u ones2 = ones.lo; MF1H(rsum, ones2, pm); }
#pragma unroll
            for (int i = 0; i < 4; ++i) SEL(oa[i], oa[i], ob[i]);
            asm volatile("s_nop 4" :: "v"(rsum));
            oso |= fail;
        }
        asm volatile("v_rcp_f32 %0, %3\n\ts_nop 4\n\tv_add_f32 %1, %4, %6\n\tv_add_f32 %2, %5, %7\n\tv_cvt_pk_bf16_f32 %1, %1, %2\n\ts_nop 0"
                     : "=&v"(inv), "=&v"(vm.y), "=&v"(fd) : "v"(rsum[0]), "v"(av1[0]), "v"(av1[1]), "v"(av1[2]), "v"(av1[3]));
        asm volatile("v_mfma_f32_16x16x16_bf16 %0, %1, %2, %0\n\ts_nop 6" : "+v"(oa) : "v"(vm), "v"(pm));
        {
            v4u oc;
            const f32x2 i2 = {inv, inv};
            const f32x2 u0 = oa.lo * i2, u1 = oa.hi * i2;
            asm volatile("v_cvt_pk_bf16_f32 %0, %4, %5\n\tv_cvt_pk_bf16_f32 %2, %4, %5\n\tv_cvt_pk_bf16_f32 %1, %6, %7\n\tv_cvt_pk_bf16_f32 %3, %6, %7\n\t"
                         "v_permlane16_swap_b32 %0, %2\n\ts_nop 0\n\tv_permlane16_swap_b32 %1, %3"
                         : "=&v"(oc.x), "=&v"(oc.y), "=&v"(oc.z), "=&v"(oc.w) : "v"(u0.x), "v"(u0.y), "v"(u1.x), "v"(u1.y));
            st16(rs, oso, oc);
        }
        SCHED();
        if (__builtin_expect(fail == 0u, 1)) {
        unsigned long long clk;
        asm volatile("s_memrealtime %0" : "=s"(clk));
        HT(6);
        v4u tq[4], tk[8], tv[2], px, pv;
        f32x4 c0, c1, cm, o[4], sum;
        float ex[10], cs0, cs1;
        v2u p0, p1, tvm;
        u32 oo, kvo, oz; bool kvok;
        u32 ln = l16; PIN(ln);
        lptr QX = lds + C_QX + (w & 1) * 4096 + ln, VO = lds + C_V + (w ^ 1) * 2048 + ln, VMX = lds + C_VM + w * 512 + (ln >> 1), MK = lds + C_MASK + (w & 1) * 1024 + ln;
#define TKR(i) tk[i] = lds_r16(LB + C_K + ((i) >> 2) * 4096 + ((i) & 3) * 1024)
#pragma unroll
        for (int i = 0; i < 4; ++i) tq[i] = lds_r16(QX + i * 1024);
        c0 = __builtin_bit_cast(f32x4, lds_r16(MK + 1024));
        TKR(0);
        *(LAS v2u*)VMX = vm;
        SCHED();
        SFOR(i, 12)
            if constexpr (i == 0) MF0(cm, kmf[0], tq[0]); else if constexpr (i < 4) MF1(cm, kmf[i], tq[i]);
            else if constexpr (i < 8) MF1(c0, tk[i - 4], tq[i - 4]); else MF1(c1, tk[i - 4], tq[i - 8]);
            if constexpr (i < 3) TKR(i + 1);
            if constexpr (i == 2) c1 = __builtin_bit_cast(f32x4, lds_r16(MK));
            if constexpr (i >= 3 && i < 7) TKR(i + 1);
            if constexpr (i == 7) { vx[1] = lds_r16(VX + 1024); tv[0] = lds_r16(VO); }
            if constexpr (i == 8) tv[1] = lds_r16(VO + 1024);
            SCHED();
            if constexpr (i == 0) { oo = ln + (ln & 0x300u) + ((vsrc ^ 8192u) + (u32)((w & 1) * 256)); PIN(oo); kb += NH * 16384; PINS(kb); }
            if constexpr (i == 1) { ksrc = kb + (((kmask >> ((s + 1) & 63)) & 1ull) ? 8192u : 0u); PINS(ksrc); }
            if constexpr (i == 3) { vsrc = kb + (((vmask >> ((s + 1) & 63)) & 1ull) ? W_VO - W_KQ + 8192u : W_VO - W_KQ); PINS(vsrc); }
            if constexpr (i == 6) { USE(cm); VMULC(cs0, cm[0]); }
            if constexpr (i == 8) VMULC(cs1, cm[1]);
            if constexpr (i == 9) VEXP(ex[8], cs0);
            if constexpr (i == 10) { VEXP(ex[9], cs1); VEXP(ex[0], c0[0]); }
            if constexpr (i == 11) { VEXP(ex[1], c0[1]); VEXP(ex[2], c0[2]); VEXP(ex[3], c0[3]); }
            SCHED();
        SEND
        asm volatile("v_cvt_pk_bf16_f32 %0, %6, %7\n\tv_mov_b32 %1, 0\n\tv_mov_b32 %2, 0\n\tv_cvt_pk_bf16_f32 %3, %6, %7\n\tv_cvt_pk_bf16_f32 %4, %8, %9\n\tv_cvt_pk_bf16_f32 %5, %10, %11"
                     : "=&v"(p0.x), "=&v"(p0.y), "=&v"(p1.x), "=&v"(p1.y), "=&v"(px.x), "=&v"(px.y) : "v"(ex[8]), "v"(ex[9]), "v"(ex[0]), "v"(ex[1]), "v"(ex[2]), "v"(ex[3]));
        asm volatile("s_waitcnt lgkmcnt(0)\n\ts_barrier" : "+s"(clk) :: "memory");
        tvm = *(LAS v2u*)(VMX + (512 - (w & 1) * 1024));
        pv = lds_r16(ZP + 80);
        SCHED();
#pragma unroll
        for (int i = 0; i < 4; ++i) VEXP(ex[4 + i], c1[i]);
        asm volatile("v_cvt_pk_bf16_f32 %0, %2, %3\n\tv_cvt_pk_bf16_f32 %1, %4, %5\n\ts_nop 1" : "=&v"(px.z), "=&v"(px.w) : "v"(ex[4]), "v"(ex[5]), "v"(ex[6]), "v"(ex[7]));
        {
            const v2u ones2 = ones.lo;
            const u32 now = (u32)clk;
            auto pvm = [&](auto first) __attribute__((always_inline)) {
                constexpr bool F = decltype(first)::value;
                SCHED();
                MF0(sum, ones, px); if constexpr (F) { oz = (oso & ~(3u << 17)) | (u32)((s + 3) & 3) << 17; PIN(oz); } SCHED();
                MF0(o[0], vx[0], px); if constexpr (F) { oso = (oso & ~(3u << 17)) | (u32)((s + 1) & 3) << 17; PIN(oso); } SCHED();
                MF0(o[1], vx[1], px); NOPS(1);
                MF0(o[2], tv[0], px); NOPS(0);
                MF1H(sum, ones2, p0); NOPS(1);
                MF0(o[3], tv[1], px); NOPS(1);
                MF1H(o[0], vm, p0);
                MF1H(o[1], vm, p1);
                MF1H(o[2], tvm, p0);
                asm volatile("v_sub_u32 %0, %2, %3\n\tv_rcp_f32 %1, %2" : "=&v"(pa), "=&v"(inv) : "v"(sum[0]), "s"(0x2B800000u), "v"(sum));
                MF1H(o[3], tvm, p1);
                SCHED();
            };
            pvm(IC<1>{});
            if (__builtin_expect(__builtin_amdgcn_ballot_w64(pa >= SUMRANGE) != 0ull, 0)) {
                const f32x4 m0 = __builtin_bit_cast(f32x4, lds_r16(MK + 1024)), m1 = __builtin_bit_cast(f32x4, lds_r16(MK));
                float mx = fmaxf(cs0, cs1);
#pragma unroll
                for (int i = 0; i < 4; ++i) { ex[i] = m0[i] == 0.f ? c0[i] : m0[i]; ex[4 + i] = m1[i] == 0.f ? c1[i] : m1[i]; mx = fmaxf(mx, fmaxf(ex[i], ex[4 + i])); }
                mx = max32(max16(mx));
#pragma unroll
                for (int i = 0; i < 4; ++i) px[i] = pk(__builtin_amdgcn_exp2f(ex[2 * i] - mx), __builtin_amdgcn_exp2f(ex[2 * i + 1] - mx));
                p0.x = p1.y = pk(__builtin_amdgcn_exp2f(cs0 - mx), __builtin_amdgcn_exp2f(cs1 - mx));
                asm volatile("s_nop 7" : "+v"(px), "+v"(p0), "+v"(p1));
                pvm(IC<0>{});
                NOPS(3);
            }
            asm volatile("s_nop 1" : "+v"(o[0]), "+v"(o[1]));
            st16(rs, oo, (v4u){pk2(o[0].lo * inv), pk2(o[0].hi * inv), pk2(o[1].lo * inv), pk2(o[1].hi * inv)});
            asm volatile("" : "+v"(o[2]), "+v"(o[3]));
            st16(rs, oo ^ 2048u, (v4u){pk2(o[2].lo * inv), pk2(o[2].hi * inv), pk2(o[3].lo * inv), pk2(o[3].hi * inv)});
            SCHED();
            PIN(fl); kvok = ((u32)__builtin_amdgcn_ballot_w64(fl < (u32)s + 2) & 7u) == 0u;
            kvo = l16 | (kvok ? 0u : OOB);
            DMA32(dv, kvo, vsrc); DMA32(dk, kvo, ksrc);
            st16(rs, oz, pv);
            SCHED();
            rep = (u32)s << 16 | min(missed, 1u) << 14 | min(now - tm, 0x3FFFu);
            if (h == 0 && w == 3) { LST(L * 64 + s, now); LST(128 + L * 64 + s, ((int)(rdl(fl, 3) - now) >= CH_VIS - 4) | ((int)kvok << 1) | ((int)min(missed, 3u) << 2)); LST(640 + L * 64 + s, tm); LST(1728 + L * 64 + s, __builtin_amdgcn_s_memtime()); }
            tm = rdl(fl, 5);
            missed = missed == SPIN_CAP ? missed : 0u;
            dirty = 0;
            if (__builtin_expect(rdl(fl, 3) - now + CH_LATE > 4000u && s + 1 < NSEG, 0)) {
                dirty = 1;
#pragma unroll 1
                for (int n = 0; n < 64; ++n) {
                    CFENCE();
                    const u32 hv = ld4(rs, fkh);
                    tm = rdl(hv, 5);
                    if (rdl(hv, 3) - now + CH_LATE <= 4000u) break;
                }
            }
        }
        LS2(L == 0 && w == 0, h);
        if (__builtin_expect(!kvok, 0) && s + 1 < NSEG) {
            st4s(rs, (ln == 0 && w == 0) ? hint + 4096u + (u32)h * 128u : OOB, 0, (u32)s + 1u);
            dirty = 1;
            wait_flags(rs, ln < 48u ? fkh : OOB, (u32)s + 2, dead);
            DMA32(dv, l16, vsrc); DMA32(dk, l16, ksrc);
            const u32 now = rt32() + CH_KV;
            if ((int)(tm - now) < 0) next_slot(now);
        }
        ++s;
        } else {
            u32 ln = l16; PIN(ln);
            oso = (ln & 256u) ? oso : oso & ~OOB;
            const u32 was = tm - tmp;
            tm = rdl(fl, 5); dirty = 1;
            if ((int)(tm - rdl(fl, 4) - was) > 24) tm -= rdl(fl, 4);
            if ((++missed & 7u) == 0u) {
                *(LAS u32*)(lds + C_DUMP + (w == 0 ? 0 : 64)) = ld4(rs, foff(F_ERR));
                BAR();
                missed = uu(*(LAS u32*)(lds + C_DUMP)) != 0u ? SPIN_CAP : missed;
                st4s(rs, missed == SPIN_CAP ? foff(F_ERR) : OOB, 0, 1u);
            }
        }
    }
    DRAIN(); BAR();
    st4s(rs, ofl, 0, (l16 & 16u) ? rep : NSEG);
}

#define WO_NI 4
#define WROT 0
DEV void role_wo(const Params& p, lptr lds, int j) {
    const int tid = threadIdx.x, lane = tid & 63, w = uni(tid >> 6), g = lane >> 4, r = lane & 15;
    const int rg = j & 1, cg = j >> 1, rot = cg & WROT;
    const rsrc_t rs = mk_rsrc(p.ws, W_END);
    v8u wo[8][5];
    {
        const v4u so_ = mk_srd(p.Wo, DM * NQ * 2);
        const int one = (0x3F2FF1F0u >> (4 * (cg & 7))) & 15;
        u32 vo[3];
#pragma unroll
        for (int i = 0; i < 3; ++i) { const int n = 64 * i + lane; vo[i] = n % 6 < 5 ? (u32)((n / 6) * (DM * 2) + cg * 80 + (n % 6) * 16) : ((n / 6) & 3) == one ? (u32)((n / 6) * (DM * 2) + (cg >> 3) * 640 + 304 + rg * 16) : OOB; }
        auto fill = [&](int b) {
            const u32 dst = uu(w * 36864 + (b % 3) * 12288), src = uu((8 * w + ((b + rot) & 7)) * (DH * DM * 2));
#pragma unroll
            for (int i = 0; i < 12; ++i) DMAIN(so_, dst + i * 1024, vo[i % 3], src + (i / 3) * (32 * DM * 2));
        };
#pragma unroll 1
        for (int b = 0; b < 3; ++b) fill(b);
        const int q4 = r >> 2;
        lptr tb = lds + w * 36864 + (32 * (g >> 1) + 16 * (q4 >> 1) + 8 * ((r >> 1) & 1) + 4 * (g & 1) + (q4 & 1)) * 96 + (r & 1) * 8;
        SFOR(b, 8)
            VMW(b < 6 ? 24 : 84 - 12 * b);
#pragma unroll
            for (int t = 0; t < 5; ++t) {
                lptr q = tb + (b % 3) * 12288 + (t == 0 ? 4 : (w + t) & 3) * 16;
                wo[b][t] = tr_frag(q, q + 192, q + 64 * 96, q + 64 * 96 + 192);
                if (t < 4) asm volatile("" : "+a"(wo[b][t])); else asm volatile("" : "+v"(wo[b][t]));
            }
            CFENCE();
            if constexpr (b < 5) fill(b + 3);
        SEND
    }
    BAR();
    EV(6);
    const u32 idx = (r & 2) ? 0xEEEEu : 0x4444u;
    bool dead = false;
    const u32 pol0 = get_pol(p, rs, dead) & 1u;
    const u32 l16 = (u32)lane * 16u;
    u32 oo[8];
#pragma unroll
    for (int i = 0; i < 8; ++i) oo[i] = uu(W_ORING + ring_map((u32)(rg * 32768 + (8 * w + ((i + rot) & 7)) * 1024), pol0));
    const u32 hint = W_MRING + 4 * 32768u + pol0 * 8192u;
    u32 hvo[2];
#pragma unroll
    for (int l = 0; l < 2; ++l) hvo[l] = lane < 2 ? hint + (u32)(l ^ 1) * (MB / 4) + (u32)lane * 4u : lane == 2 ? hint + (u32)l * (MB / 4) + 128u : lane == 3 ? foff(F_ERR) : lane == 4 ? hint + (u32)l * (MB / 4) + 256u :
                 (lane >= 32 && j == 0 && w == 3) ? hint + (u32)l * (MB / 4) + (u32)lane * 128u : OOB;
    const int xo = g * 128 + (r & 7) * 16, O = 5 * cg + (r < 8 ? w : 4), q2 = 2 + (r >> 3);
    lptr xw0 = lds + (r < 8 ? 2048 + w * 4 : ((w + 1) & 3) * 512 + 4) + xo, xw1 = lds + ((w + q2) & 3) * 512 + q2 * 4 + xo;
    lptr xr = lds + (r < 8 ? w : 4) * 512 + xo;
    if (r < 8) { *(LAS u32*)xr = 0u; *(LAS u32*)(xr + 8192) = 0u; }
    const u32 mpo = (r & 4) == 0 && (r < 8 || w == 0) ? W_MRING + ring_map((u32)((O >> 1) * 256 + rg * 128 + (4 * (g & 1) + 2 * (O & 1) + (g >> 1)) * 16 + (r & 3) * 4), pol0) : OOB;
    constexpr u32 PER0 = CH_W0 + CH_MW + CH_H0 + CH_MH + 2 * CH_VIS;
    const bool lead = j == 0 && w == 3;
    u32 hso = OOB;
    MK_SRD();
    u32 t = 0, tn = 0;
    int sn[2] = {0, 0};
    u32 hav = 4 * CH_H0, wav = 4 * CH_W0, mh = 8 * (CH_MH + CH_M0), mw = 8 * (CH_MW + CH_M0), ok[2] = {0u, 0u}, dw[2] = {CH_W0 + CH_VIS + CH_MW + CH_REC, CH_W0 + CH_VIS + CH_MW + CH_REC};
    u32 zero = 0, hv = 0xFFFFu, pst = 1, pdur = 0, pmp = OOB;
    const u32 psent = PSENT;
    lptr sw = lds + 5120 + w * 4;
    bool stop = false;
    asm volatile("" : "+v"(zero), "+v"(sw), "+v"(xw0), "+v"(xw1), "+v"(xr));
    auto turn = [&](auto lc) __attribute__((always_inline)) {
        constexpr int L = decltype(lc)::value, M = L ^ 1;
        u32 now, done, rep, rd, lost, dh, dn, nt, za; v4u hw;
        const int sc = sn[L];
        auto books = [&](auto ic) __attribute__((always_inline)) {
            constexpr int i = decltype(ic)::value;
            if constexpr (i == 0) { rep = max(rdl(hv, 2), rdl(hv, 4)); PINS(rep); }
            if constexpr (i == 1) { lost = (u32)(__builtin_amdgcn_ballot_w64(hv == (u32)sn[M]) >> 32); PINS(lost); }
            if constexpr (i == 2) { lost |= (rep >> 16) ^ (u32)sn[M]; PINS(lost); }
            if constexpr (i == 3) { lost |= ok[M] ^ 2u; rd = rep & 0xFFFFu; PINS(lost); PINS(rd); }
            if constexpr (i == 4) { lost = lost == 0u ? CH_UP * 8u : 0u; PINS(lost); }
            if constexpr (i == 5) { dn = wav >> 2; PINS(dn); pdur = min(pdur, dn + 8u); PINS(pdur); }
            if constexpr (i == 6) { pdur = (done & ok[M]) ? pdur : dn; PINS(pdur); }
            if constexpr (i == 7) { wav += pdur - dn; PINS(wav); }
            if constexpr (i == 8) { dh = hav >> 2; PINS(dh); nt = min(rd, dh + 8u); PINS(nt); }
            if constexpr (i == 9) { nt = rd < 0x3FFFu ? nt : dh; PINS(nt); }
            if constexpr (i == 10) { hav += nt - dh; PINS(hav); }
            if constexpr (i == 11) { mw = max(mw - CH_DEC, CH_MW * 8u); PINS(mw); }
            if constexpr (i == 12) { mh = max(mh - CH_DEC, CH_MH * 8u); PINS(mh); }
            if constexpr (i == 13) { rd = rd >> 14 == 1u ? lost : 0u; PINS(rd); }
            if constexpr (i == 14) { mw += rd; mh += lost - rd; PINS(mw); PINS(mh); }
            if constexpr (i == 15) { ok[M] = done ? 1u : (ok[M] & 1u) << 1; PINS(ok[M]); }
            if constexpr (i == 16) { dh = (2u * hav + mh) / 8u + (sn[1] < 2 ? CH_VIS + CH_SOLO : CH_VIS); PINS(dh); }
            if constexpr (i == 17) { dn = (2u * wav + mw) / 8u + CH_VIS; PINS(dn); }
            if constexpr (i == 18) { nt = dw[L] + dh; PINS(nt); }
            if constexpr (i == 19) { dh += dn; dw[L] = dn; sn[M] += (int)done; PINS(dh); PINS(sn[M]); }
            if constexpr (i == 20) { rd = tn - t + (dh >> 1); PINS(rd); }
            if constexpr (i == 21) { rd = sn[M] + 1 < NSEG ? rd : 0u; PINS(rd); }
            if constexpr (i == 22) { nt = t + (u32)max((int)nt, (int)rd); PINS(nt); }
            if constexpr (i == 23) { hw.x = nt; hw.y = dh; PIN(hw.x); PIN(hw.y); }
            if constexpr (i == 24) { hw.z = nt + dn; hw.w = 0u; PIN(hw); }
            if constexpr (i == 25) st16(rs, hso + L * (MB / 4), hw);
        };
        if (__builtin_expect(sc + 1 < NSEG, 1)) {
            u32 vo = l16 + (L * (MB / 2) + (u32)(sc & 3) * 131072u), mp = mpo + (L * (MB / 4) + (u32)((sc + 1) & 3) * 32768u), zb, hn;
            done = uu(pst) ^ 1u;
            za = done ? pmp : OOB;
            zb = ok[L] == 1u ? OOB : mp;
            pmp = mpo + (L * (MB / 4) + (u32)(sc & 3) * 32768u);
            *(LAS u32*)(sw + L * 8192) = zero;
            f32x4 acc[5] = {};
            asm volatile("" : "+v"(acc[0]), "+v"(acc[1]), "+v"(acc[2]), "+v"(acc[3]), "+v"(acc[4]), "+v"(mp), "+v"(pmp), "+v"(vo), "+v"(za), "+v"(zb));
            PINS(done);
#define WT(k) do {} while (0)
            v4u d[8];
            {
                u32 tmp;
                asm volatile(LOOP_TXT("%1", "%2") "buffer_load_dwordx4 %0, %3, %4, %5 offen sc1\n\ts_waitcnt lgkmcnt(0)"
                             : "=&v"(d[0]), "=&s"(tmp) : "s"(uu(t - 1u)), "v"(vo), "s"(srd), "s"(oo[0]) : "vcc", "scc", "memory");
            }
            WT(0);
            SFOR(i, 7)
                asm volatile("buffer_load_dwordx4 %0, %1, %2, %3 offen sc1" : "=v"(d[i + 1]) : "v"(vo), "s"(srd), "s"(oo[i + 1]) : "memory");
                SCHED();
                static_for<3 * i, 3 * i + 3>(books);
                SCHED();
            SEND
            LD4A(hn, hvo[L], 0);
            st4s(rs, za, 0, psent);
            st4s(rs, zb, 0, psent);
            SCHED();
            WT(1);
            float f0[5], f1[5], xs; unsigned long long bad; v4u sv;
#define WSW(o_) asm volatile("v_permlane32_swap_b32 %0, %1" : "+v"(f0[o_]), "+v"(f1[o_]))
#define WAD(o_) asm volatile("v_add_f32_dpp %0, %1, %0 row_ror:8 row_mask:0xf bank_mask:0xf\n\tv_add_f32_dpp %0, %2, %3 row_ror:8 row_mask:0xf bank_mask:0xc" : "+v"(f0[o_]) : "v"(f1[o_]), "v"(f0[o_ + 1]), "v"(f1[o_ + 1]))
#define WFO(o_) do { VADD(f0[o_], acc[o_][0], acc[o_][2]); VADD(f1[o_], acc[o_][1], acc[o_][3]); } while (0)
#define WOWN(k) do { if constexpr (WO_NI + (k) < 8) SMF_V(acc[4], d[(WO_NI + (k)) & 7], wo[(WO_NI + (k)) & 7][4]); SCHED(); } while (0)
            SFOR(i, 40)
                constexpr int pc = i / 5, oc = i % 5;
                if constexpr (oc == 0) VMW(10 - pc);
                if constexpr (oc < 4) SMF_A(acc[oc], d[pc], wo[pc][oc]); else if constexpr (pc < WO_NI) SMF_V(acc[4], d[pc], wo[pc][4]);
                if constexpr (i == 0) WT(2);
                if constexpr (i == 20) WT(3);
                if constexpr (i == 35) WT(4);
                if constexpr (i < 4) books(IC<21 + i>{});
                if constexpr (i == 6) { xs = 0.f; PIN(xs); }
                if constexpr (i == 36) NOPS(1);
                if constexpr (i == 37) NOPS(2);
                SCHED();
            SEND
            WFO(0); WFO(1);
            asm volatile("v_cmp_u_f32 %0, %1, %2" : "=s"(bad) : "v"(f0[0]), "v"(f1[0]));
            WSW(0); WSW(1); WFO(2); WAD(0); WFO(3);
            *(LAS float*)(xw0 + L * 8192) = f0[0]; SCHED();
            WSW(2); WSW(3);
            if (__builtin_expect(bad != 0ull, 0)) {
                u32 mn = ~0u;
#pragma unroll
                for (int i = 0; i < 8; ++i) { u32 e = d[i].x; PIN(e); mn = min(mn, e ^ PSENT); }
                if (all_(mn != 0u)) {
#pragma unroll
                    for (int i = 0; i < 8; ++i) { v4u e = d[i]; PIN(e); mn = min(min(mn, e.y ^ PSENT), min(e.z ^ PSENT, e.w ^ PSENT)); }
                }
                *(LAS u32*)(sw + L * 8192) = all_(mn != 0u) ? 0u : 1u;
            }
            WAD(2);
            *(LAS float*)(xw1 + L * 8192) = f0[2]; SCHED();
            WOWN(0); WOWN(1); WOWN(2); WOWN(3);
            WT(5);
            f32x4 xa; float nb; u32 cw, mq;
            asm volatile("s_waitcnt lgkmcnt(0)\n\ts_barrier\n\tds_read_b128 %0, %2 offset:%4\n\tds_read_b128 %1, %3 offset:%4+5120\n\ts_nop 3"
                         : "=&v"(xa), "=&v"(sv) : "v"(xr), "v"(zero), "i"(L * 8192) : "memory");
            WT(6);
            WFO(4);
            asm volatile("s_nop 0\n\tv_permlane32_swap_b32 %1, %2\n\ts_nop 1\n\tv_add_f32_dpp %0, %2, %1 row_ror:8 row_mask:0xf bank_mask:0x3\n\ts_waitcnt lgkmcnt(1)" : "+v"(xs), "+v"(f0[4]), "+v"(f1[4]));
            asm volatile("v_add_f32 %0, %0, %4\n\tv_add_f32 %1, %5, %6\n\tv_add_f32 %0, %0, %7\n\tv_add_f32 %0, %0, %1\n\tv_cmp_u_f32 vcc, %0, %0\n\ts_nop 0\n\t"
                         "v_mov_b32_dpp %1, %0 row_shl:4 row_mask:0xf bank_mask:0xf\n\tv_cndmask_b32 %3, %8, %9, vcc\n\tv_cvt_pk_bf16_f32 %2, %0, %1"
                         : "+v"(xs), "=&v"(nb), "=&v"(cw), "=&v"(mq) : "v"(xa[0]), "v"(xa[1]), "v"(xa[2]), "v"(xa[3]), "v"(mp), "v"(OOB) : "vcc");
            st4s(rs, mq, 0, cw);
            SCHED();
            WT(7);
            now = rt32();
            books(IC<25>{});
            asm volatile("s_waitcnt vmcnt(4) lgkmcnt(0)" : "+v"(hn), "+v"(sv));
            hv = hn;
            pst = sv.x | sv.y | sv.z | sv.w;
            if (__builtin_expect(__builtin_amdgcn_ballot_w64(mq != mp && pst == 0u) != 0ull, 0)) st4s(rs, mp, 0, cw);
        } else {
            done = uu(pst) ^ 1u;
            st4s(rs, done ? pmp : OOB, 0, psent);
            static_for<0, 25>(books);
            pst = 1;
            BAR();
            hv = ld4(rs, hvo[L]);
            PIN(hv);
            now = rt32();
        }
        pdur = now - t;
        const u32 per = rdl(hv, 1), th = rdl(hv, 0);
        t = lead ? tn : (int)(th - now) < -(int)(per >> 1) ? th + per : th;
        tn = nt;
        dw[M] += max((int)(now + CH_WX - t), 0);
        stop = stop || rdl(hv, 3) != 0u;
    };
#pragma unroll 1
    for (int k = 0; k < 2 * SPIN_CAP && !stop && (sn[0] + 1 < NSEG || sn[1] + 1 < NSEG); ++k) {
        turn(IC<0>{}); turn(IC<1>{});
        if (__builtin_expect(k != 0, 1)) continue;
        if (j == 0) {
            for (int n = 0; n < SPIN_CAP && !dead && sn[0] == 0; ++n) {
                CFENCE();
                if (all_(ld4(rs, W_ORING + pol0 * 8192u + (u32)w * 16384u + (u32)lane * 128u) != PSENT)) break;
                __builtin_amdgcn_s_sleep(1);
            }
            BAR();
        }
        if (lead) {
            t = rt32() + 56u; tn = t + PER0 / 2 + CH_REC;
            st16(rs, lane < 2 ? hint + (u32)lane * (MB / 4) : OOB, (v4u){lane == 0 ? t : tn, PER0, (lane == 0 ? t : tn) + (CH_W0 + CH_VIS + CH_MW + CH_REC), 0u});
        } else {
            for (int n = 0; n < SPIN_CAP && !dead; ++n) {
                CFENCE();
                const u32 hv = ld4(rs, hvo[1]);
                t = rdl(hv, 0);
                if (rdl(hv, 1) < 400u) break;
                __builtin_amdgcn_s_sleep(1);
            }
        }
        hso = (lead && lane == 0) ? hint : OOB;
        hv = 0xFFFFu; dw[0] = dw[1] = CH_W0 + CH_VIS + CH_MW + CH_REC;
    }
    if (sn[0] + 1 < NSEG || sn[1] + 1 < NSEG) st4s(rs, tid == 0 ? foff(F_ERR) : OOB, 0, 1u);
}

__global__ void __launch_bounds__(256, 1) k_main(Args args) {
    Params p;
    (Args&)p = args;
    lptr lds = (lptr)(u32)16 - 16;
    if (blockIdx.x == 0) LST(256, rt32());
    unsigned long long pc; int len;
    asm volatile("s_getpc_b64 %0\n.Lk_main_here:\n\ts_mov_b32 %1, .Lk_end_here-.Lk_main_here" : "=s"(pc), "=s"(len));
    const v4u code = ldin16(mk_rsrc((const void*)(pc & ~15ull), (u32)max(len, 0)), (((blockIdx.x >> 3) & 31u) * 256u + threadIdx.x) * 16u, 0);
    CFENCE();
    if (threadIdx.x == 0) {
        const v4u e = ld16s(mk_rsrc(p.ws, W_END), foff(F_EP), 0);
        const unsigned long long tag = (unsigned long long)e.y << 32 | (e.x + 1u);
        u32 role = ((u32)__builtin_amdgcn_s_getreg(20 | (3 << 11)) & 7u) * 32u + ((blockIdx.x >> 3) & 31u);
#pragma unroll 1
        for (int n = 0; n < 256; ++n, role = (role + 1u) & 255u)
            if (__hip_atomic_exchange((GAS unsigned long long*)(p.ws + W_SLOT) + role * 8u, tag, __ATOMIC_RELAXED, __HIP_MEMORY_SCOPE_AGENT) != tag) break;
        lds_w16(lds, (v4u){e.x + 1u, e.y, e.z, role});
    }
    __syncthreads();
    const v4u boot = lds_r16(lds);
    p.e0 = uu(boot.x); p.e1 = uu(boot.y); p.e2 = uu(boot.z);
    const int role = uni((int)boot.w), x = role >> 5, i = role & 31;
    asm volatile("" :: "v"(code));
    __syncthreads();
    LST(8192 + blockIdx.x * 16, role);
    if (x < 2) { role_head(p, lds, i >> 4, x * 16 + (i & 15)); __builtin_amdgcn_endpgm(); }
    if (x == 2) { role_wo(p, lds, i); __builtin_amdgcn_endpgm(); }
    const int no = x == 3 ? 30 : x < 6 ? 5 : 0;
    if (x > 3 && i >= 8) { role_qkv(p, lds, (x - 4) * 24 + i - 8); __builtin_amdgcn_endpgm(); }
    const bool isout = i < no;
    const int og = x == 3 ? i / 5 : x + 2, ng = i % 5, L = og >> 2, kq = og & 3;
    const int k = i - no, w = uni(threadIdx.x >> 6);
    const int j = isout ? (L == 1 ? 0 : 44) + kq * 5 + ng : x == 3 ? 36 + k : k < 2 ? 12 + 2 * x + k : x < 6 ? 24 + x : k < 5 ? 10 + 3 * x + k : 15 + 3 * x + k;
    const rsrc_t rs = mk_rsrc(p.ws, W_END);
    wait_until(rt32() + (j < NKV ? ST_KV : ST_OUT));
    if (j < NKV) {
        const int nt = j < 320 - 7 * NKV ? 8 : 7;
#pragma unroll 1
        for (int q = 0; q < 4; ++q) kv_fetch(p, j + NKV * q, uu(q * KT_BYTES));
        Filt f;
        u32 warm[3];
#pragma unroll 1
        for (int q = 0; q < nt; ++q) {
            VMW(15); BAR();
            if (q == 0) EV(2);
            kv_fetch(p, j + NKV * (q + 4), uu(((q + 4) & 7) * KT_BYTES));
            kv_tile(p, rs, lds + q * KT_BYTES, lds + (q == 0 ? 8 : q - 1) * KT_BYTES, j + NKV * q);
        }
        const int g = j < 20 ? j : j < 28 ? j + 16 : j - 8;
        filt_load<5, 0>(p, g, f);
        VMW(10); BAR();
        boot_set(p, rs, threadIdx.x == 0 ? foff(F_KV + j) : OOB, 2u);
        EV(4);
        { bool dead = false; boot_wait(p, rs, pol_off(), 0u, dead); }
#pragma unroll
        for (int q = 0; q < 3; ++q)
            warm[q] = __builtin_amdgcn_raw_buffer_load_b32(mk_rsrc(p.ssmk, ACT), 176u * 4096u + ((u32)((q * NKV + j) * 256) + threadIdx.x) * 64u, 0, 0);
        if (g >= 36) conv_job<3>(p, rs, lds, g, f, 0); else conv_job<0>(p, rs, lds, g, f, 0);
        asm volatile("" :: "v"(warm[0]), "v"(warm[1]), "v"(warm[2]));
        const int g0 = j < 20 ? 36 + j : j < 36 ? 28 + j : 44 + j, g1 = j < 20 ? g0 + 1 : j < 36 ? 80 : 160;
        if (g < 36) filt_load<5, 0>(p, g0, f);
        DRAIN(); BAR();
        st4s(rs, threadIdx.x == 0 ? foff(F_CV + g) : OOB, 0, 2u);
        if (g < 36) {
#pragma unroll 1
            for (int n = g0; n < g1; n += 8) conv_job<2>(p, rs, lds, n, f, min(n + 8, 159));
            DRAIN(); BAR();
            st4s(rs, threadIdx.x == 0 ? foff(F_CV + g) : OOB, 0, 3u);
            if (j >= 36) { EV(15); __builtin_amdgcn_endpgm(); }
        }
    }
    if (!isout) { if (j < 28) role_row<0>(p, lds, (j - 20) * 4 + w); else role_row<1>(p, lds, (j - 28) * 4 + w);
        boot_set(p, rs, j == 28 && threadIdx.x == 0 ? foff(F_EP) : OOB, 0u);
        __builtin_amdgcn_endpgm();
    }
    role_out(p, lds, L, kq, ng);
}

__global__ void k_end() { asm volatile(".Lk_end_here:"); }

extern "C" void kernel_launch(void* const* d_in, const int* in_sizes, int n_in, void* d_out, int out_size, void* d_ws, size_t ws_size,
                              hipStream_t stream) {
    Args p;
    p.x = (const u16*)d_in[0]; p.mem0 = (const u16*)d_in[1]; p.Wq = (const u16*)d_in[2]; p.Wk = (const u16*)d_in[3];
    p.Wv = (const u16*)d_in[4]; p.Wo = (const u16*)d_in[5];
    p.bk = (const u16*)d_in[8]; p.bv = (const u16*)d_in[9]; p.shift = (const u16*)d_in[10]; p.ssmk = (const u16*)d_in[11]; p.ssmD = (const u16*)d_in[12];
    p.out = (u16*)d_out;
    p.ws = (unsigned char*)(((size_t)d_ws + 2 * MB - 1) & ~(size_t)(2 * MB - 1));
    (void)hipFuncSetAttribute((const void*)k_main, hipFuncAttributeMaxDynamicSharedMemorySize, LDS_BYTES);
    hipLaunchKernelGGL(k_main, dim3(256), dim3(256), LDS_BYTES, stream, p);
}
```

```cpp
#include <hip/hip_runtime.h>
#include <hip/hip_bf16.h>

typedef unsigned short u16;
typedef unsigned int u32;
typedef u32 v2u __attribute__((ext_vector_type(2)));
typedef u32 v3u __attribute__((ext_vector_type(3)));
typedef u32 v4u __attribute__((ext_vector_type(4)));
typedef u32 v8u __attribute__((ext_vector_type(8)));
typedef float f32x2 __attribute__((ext_vector_type(2)));
typedef float f32x4 __attribute__((ext_vector_type(4)));
typedef float f32x16 __attribute__((ext_vector_type(16)));
typedef __bf16 bf16x2 __attribute__((ext_vector_type(2)));
typedef __bf16 bf16x8 __attribute__((ext_vector_type(8)));
typedef short s16x4 __attribute__((ext_vector_type(4)));
#define LAS __attribute__((address_space(3)))
#define GAS __attribute__((address_space(1)))
typedef LAS unsigned char* lptr;
#define DEV __device__ __forceinline__

constexpr int SEQ = 2048, DM = 640, NH = 32, DH = 128, NSEG = 64, NQ = NH * DH;

constexpr u32 MB = 1u << 20;
constexpr u32 W_ORING = 0;
constexpr u32 W_MRING = MB;
constexpr u32 W_CTRL = 4 * MB;
constexpr u32 ACT = SEQ * DM * 2;
constexpr u32 H_TILE = 128 * 1024;
constexpr u32 W_H2 = 8 * MB;
constexpr u32 W_KQ = 16 * MB;
constexpr u32 W_VO = W_KQ + 64 * MB;
constexpr u32 W_P = W_VO + 64 * MB;
constexpr u32 P_ITEM = 512 * 1024;
constexpr u32 W_H1 = W_P + 44 * MB, W_Y = W_H1 + 8 * MB, W_X2 = W_Y + ACT;
constexpr u32 W_KVT = W_X2 + ACT;
constexpr u32 W_END = W_KVT + 3 * MB;
constexpr u32 NREG = 12;
static_assert((size_t)W_END + 2 * MB <= 256ull * MB && W_H1 >= (NREG - 1) * 16 * MB + 2 * MB && W_H1 <= NREG * 16 * MB, "workspace");
constexpr u32 F_EP = 0, F_RG = 32, F_LN = 64, F_KV = 192,
              F_QKV = 256, F_O = F_QKV + 192, F_P = F_O + 64, F_H2 = F_P + 64, F_CV = F_H2 + 64, F_ERR = F_CV + 191, F_N = F_CV + 192;
constexpr u32 W_SLOT = 3 * MB;
DEV u32 foff(u32 idx) { return W_CTRL + (idx >> 6) * 16384u + (idx & 63u) * 128u; }
DEV u32 near_(u32 o, u32 side, u32 pol) { return o + ((side ^ (pol >> (o >> 24)) ^ (o >> 21)) & 1u) * 8192u; }
DEV u32 p_item(int s) { return W_P + (u32)s * P_ITEM; }

constexpr u32 OOB = 0x80000000u;
constexpr u32 PSENT = 0x7F817F81u;
constexpr int SPIN_CAP = 40000;
constexpr int LDS_BYTES = 160 * 1024;
#define ST_KV 0
#define ST_OUT 0

#define EV(k) do {} while (0)
#define LST(idx, v) do {} while (0)
#define LS2(cond, idx) do {} while (0)
struct Args {
    const u16 *x, *mem0, *Wq, *Wk, *Wv, *Wo, *bk, *bv, *shift, *ssmk, *ssmD;
    u16* out;
    unsigned char* ws;
};
struct Params : Args { u32 e0, e1, e2; };

typedef __amdgpu_buffer_rsrc_t rsrc_t;
DEV rsrc_t mk_rsrc(const void* p, u32 bytes) { return __builtin_amdgcn_make_buffer_rsrc((void*)p, 0, bytes, 0x00020000); }
DEV v4u ld16s(rsrc_t r, u32 off, u32 so) { return __builtin_amdgcn_raw_buffer_load_b128(r, off, so, 16); }
DEV v2u ld8s(rsrc_t r, u32 off, u32 so) { return __builtin_amdgcn_raw_buffer_load_b64(r, off, so, 16); }
DEV v4u ldin16(rsrc_t r, u32 off, u32 so) { return __builtin_amdgcn_raw_buffer_load_b128(r, off, so, 0); }
DEV v2u ldin8(rsrc_t r, u32 off, u32 so) { return __builtin_amdgcn_raw_buffer_load_b64(r, off, so, 0); }
DEV u32 ld4(rsrc_t r, u32 off) { return __builtin_amdgcn_raw_buffer_load_b32(r, off, 0, 16); }
DEV void st16(rsrc_t r, u32 off, v4u v) { __builtin_amdgcn_raw_buffer_store_b128(v, r, off, 0, 16); }
DEV void st8s(rsrc_t r, u32 off, u32 so, v2u v) { __builtin_amdgcn_raw_buffer_store_b64(v, r, off, so, 16); }
DEV void st4s(rsrc_t r, u32 off, u32 so, u32 v) { __builtin_amdgcn_raw_buffer_store_b32(v, r, off, so, 16); }
#define DRAIN() asm volatile("s_waitcnt vmcnt(0)" ::: "memory")
#define VMW(n) asm volatile("s_waitcnt vmcnt(%0)" :: "i"(n) : "memory")
#define BAR() asm volatile("s_waitcnt lgkmcnt(0)\n\ts_barrier" ::: "memory")
#define SCHED() __builtin_amdgcn_sched_barrier(0)
#define CFENCE() asm volatile("" ::: "memory")
#define NOPS(n) asm volatile("s_nop %0" :: "i"(n))
#define PIN(x) asm volatile("" : "+v"(x))
#define PINS(x) asm volatile("" : "+s"(x))

template <int V> struct IC { static constexpr int value = V; };
template <int I, int N, class F> DEV void static_for(F&& f) { if constexpr (I < N) { f(IC<I>{}); static_for<I + 1, N>(f); } }
#define SFOR(var, n) static_for<0, n>([&](auto ic_) __attribute__((always_inline)) { constexpr int var = decltype(ic_)::value;
#define SEND });

DEV u32 pk(float a, float b) { bf16x2 v = {(__bf16)a, (__bf16)b}; return __builtin_bit_cast(u32, v); }
DEV float lo_f(u32 u) { return __builtin_bit_cast(float, u << 16); }
DEV float hi_f(u32 u) { return __builtin_bit_cast(float, u & 0xffff0000u); }
DEV f32x2 up2(u32 u) { return (f32x2){lo_f(u), hi_f(u)}; }
DEV u32 pk2(f32x2 v) { return pk(v.x, v.y); }
DEV f32x2 rb2(f32x2 a) { f32x2 r; asm("v_cvt_pk_bf16_f32 %0, 0, %1" : "=v"(r.x) : "v"(a.x)); asm("v_cvt_pk_bf16_f32 %0, 0, %1" : "=v"(r.y) : "v"(a.y)); return r; }
DEV float rbf(float a) { return (float)(__bf16)a; }
DEV v4u lds_r16(lptr p) { return *(LAS v4u*)p; }
DEV void lds_w16(lptr p, v4u v) { *(LAS v4u*)p = v; }
DEV v2u lds_tr(lptr p) { return __builtin_bit_cast(v2u, __builtin_amdgcn_ds_read_tr16_b64_v4i16((LAS s16x4*)p)); }
DEV v4u tr2(lptr p0, lptr p1) { const v2u a = lds_tr(p0), b = lds_tr(p1); return (v4u){a.x, a.y, b.x, b.y}; }
DEV v8u cat8(v4u a, v4u b) { return __builtin_shufflevector(a, b, 0, 1, 2, 3, 4, 5, 6, 7); }
DEV f32x4 mfma16(v4u a, v4u b, f32x4 c) { return __builtin_amdgcn_mfma_f32_16x16x32_bf16(__builtin_bit_cast(bf16x8, a), __builtin_bit_cast(bf16x8, b), c, 0, 0, 0); }
DEV f32x16 mfma32(v4u a, v4u b, f32x16 c) { return __builtin_amdgcn_mfma_f32_32x32x16_bf16(__builtin_bit_cast(bf16x8, a), __builtin_bit_cast(bf16x8, b), c, 0, 0, 0); }

DEV float f_(u32 u) { return __builtin_bit_cast(float, u); }
DEV u32 u_(float f) { return __builtin_bit_cast(u32, f); }
template <int CTRL> DEV float dpp(float v) { return f_((u32)__builtin_amdgcn_update_dpp(0, (int)u_(v), CTRL, 0xf, 0xf, false)); }
DEV float max16(float v) { auto r = __builtin_amdgcn_permlane16_swap(u_(v), u_(v), false, false); return fmaxf(f_(r[0]), f_(r[1])); }
DEV float max32(float v) { auto r = __builtin_amdgcn_permlane32_swap(u_(v), u_(v), false, false); return fmaxf(f_(r[0]), f_(r[1])); }
DEV float add16(float v) { auto r = __builtin_amdgcn_permlane16_swap(u_(v), u_(v), false, false); return f_(r[0]) + f_(r[1]); }
DEV float add32(float v) { auto r = __builtin_amdgcn_permlane32_swap(u_(v), u_(v), false, false); return f_(r[0]) + f_(r[1]); }
DEV float wave_sum(float v) {
    v += dpp<0xB1>(v);
    v += dpp<0x4E>(v);
    v += dpp<0x124>(v);
    v += dpp<0x128>(v);
    return add32(add16(v));
}
DEV bool all_(bool c) { return __builtin_amdgcn_ballot_w64(!c) == 0ull; }
DEV int uni(int v) { return __builtin_amdgcn_readfirstlane(v); }
DEV u32 uu(u32 v) { return (u32)__builtin_amdgcn_readfirstlane((int)v); }
DEV u32 rdl(u32 v, int l) { return (u32)__builtin_amdgcn_readlane((int)v, l); }
DEV u32 rt32() { return (u32)__builtin_amdgcn_s_memrealtime(); }

DEV void wait_flags(rsrc_t rs, u32 off, u32 want, bool& dead) {
    if (dead) return;
#pragma unroll 1
    for (int n = 0;; ++n) {
        CFENCE();
        const u32 v = ld4(rs, off);
        if (all_(off >= OOB || v >= want)) break;
        __builtin_amdgcn_s_sleep(1);
        if ((n & 63) == 63) { if (uni((int)ld4(rs, foff(F_ERR))) != 0) { dead = true; break; } }
        if (n > SPIN_CAP) { st4s(rs, foff(F_ERR), 0, 1u); dead = true; break; }
    }
    CFENCE();
}

DEV bool boot_ok(const Params& p, u32 off, v4u v, u32 want) { return all_(off >= OOB || (v.x == p.e0 && v.y == p.e1 && v.z == p.e2 && v.w >= want)); }
DEV void boot_set(const Params& p, rsrc_t rs, u32 off, u32 v) { st16(rs, off, (v4u){p.e0, p.e1, p.e2, v}); }
DEV u32 boot_wait(const Params& p, rsrc_t rs, u32 off, u32 want, bool& dead) {
    v4u v;
#pragma unroll 1
    for (int n = 0;; ++n) {
        CFENCE();
        v = ld16s(rs, off, 0);
        if (boot_ok(p, off, v, want) || dead) break;
        __builtin_amdgcn_s_sleep(1);
        if (n > SPIN_CAP) { st4s(rs, foff(F_ERR), 0, 1u); dead = true; }
    }
    CFENCE();
    return v.w;
}

DEV u32 pol_off() { const int lane = threadIdx.x & 63; return lane < 32 ? foff(F_RG + lane) : OOB; }
DEV u32 pol_of(u32 raw) {
    const int lane = threadIdx.x & 63;
    const float v = add16(__builtin_amdgcn_fmed3f((float)((int)raw - (1 << 20)), -250.f, 250.f));
    const float lo = dpp<0x121>(v), hi = dpp<0x12F>(v), ex = dpp<0x122>(v) + dpp<0x123>(v) + dpp<0x124>(v);
    const float sum = v + lo + ((lane & 15) == 11 ? 0.f : hi) + ((lane & 15) == 0 ? ex : 0.f);
    return (u32)__builtin_amdgcn_ballot_w64(sum >= 0.f) & 0xFFFFu;
}
DEV u32 get_pol(const Params& p, rsrc_t rs, bool& dead) { return pol_of(boot_wait(p, rs, pol_off(), 0u, dead)); }
DEV u32 hblk(u32 base, int ls, int h, u32 side, u32 pol) { return near_(base + (u32)(ls * NH + h) * 16384u, side, pol); }
DEV f32x2 ln_stats(const f32x2 (&x)[5]) {
    const float c = f_(uu(u_(x[0].x)));
    f32x2 d[5];
#pragma unroll
    for (int i = 0; i < 5; ++i) d[i] = x[i] - c;
    const f32x2 s2 = (d[0] + d[1]) + (d[2] + d[3]) + d[4], q = (d[0] * d[0] + d[1] * d[1]) + (d[2] * d[2] + (d[3] * d[3] + d[4] * d[4]));
    const float dm = wave_sum(s2.x + s2.y) * (1.0f / DM), mean = c + dm;
    const float var = fmaxf(wave_sum(q.x + q.y) * (1.0f / DM) - dm * dm, 0.f);
    const float sd = rbf(__builtin_amdgcn_sqrtf(rbf(rbf(var) + 1e-5f)));
    return (f32x2){rbf(mean), __builtin_amdgcn_rcpf(sd)};
}
DEV void store_h(rsrc_t rs, u32 tile, int row, const u32 (&hp)[5]) {
    const u32 lane = threadIdx.x & 63;
    const u32 sw = (u32)(row & 15) * 16u, o8 = (lane >> 5) * 16384u + (((lane & 31u) * 8u) ^ sw), hb = tile + (u32)(row & 31) * 256u;
    st8s(rs, o8, hb, (v2u){hp[0], hp[1]});
    st8s(rs, o8, hb + 32768, (v2u){hp[2], hp[3]});
    st4s(rs, (lane * 4) ^ sw, hb + 65536, hp[4]);
}

#define DMA16(ldsb, vo, so) asm volatile("s_mov_b32 m0, %0\n\ts_nop 0\n\tbuffer_load_dwordx4 %1, %2, %3 offen sc1 lds" :: "s"(ldsb), "v"(vo), "s"(srd), "s"(so) : "memory")
#define DMA16I(ldsb, lo, vo, so, go) do { u32 so_; asm volatile("s_add_u32 m0, %1, %5\n\ts_add_u32 %0, %4, %6\n\tbuffer_load_dwordx4 %2, %3, %0 offen sc1 lds" \
                         : "=&s"(so_) : "s"(ldsb), "v"(vo), "s"(srd), "s"(so), "i"(lo), "i"(go) : "memory", "scc"); } while (0)
#define DMA32(ldsb, vo, so) asm volatile("s_mov_b32 m0, %0\n\ts_nop 0\n\tbuffer_load_dwordx4 %1, %2, %3 offen sc1 lds\n\tbuffer_load_dwordx4 %1, %2, %3 offen offset:1024 sc1 lds" \
                         :: "s"(ldsb), "v"(vo), "s"(srd), "s"(so) : "memory")
#define INPOL "nt"
#define XPOL ""
#define INAUX 2
#define DMANT(sr, ldsb, vo, so) asm volatile("s_mov_b32 m0, %0\n\ts_nop 0\n\tbuffer_load_dwordx4 %1, %2, %3 offen " INPOL " lds" :: "s"(ldsb), "v"(vo), "s"(sr), "s"(so) : "memory")
#define DMAIN(sr, ldsb, vo, so) asm volatile("s_mov_b32 m0, %0\n\ts_nop 0\n\tbuffer_load_dwordx4 %1, %2, %3 offen lds" :: "s"(ldsb), "v"(vo), "s"(sr), "s"(so) : "memory")
DEV const u16* sel(bool c, const u16* a, const u16* b) { PINS(a); PINS(b); return c ? a : b; }
DEV v4u mk_srd(const void* q, u32 bytes) { return (v4u){uu((u32)(size_t)q), uu((u32)((size_t)q >> 32) & 0xffffu), bytes, 0x00020000u}; }
#define MK_SRD() const v4u srd = mk_srd(p.ws, W_END)

constexpr int CV_PAD = 40;
constexpr int CV_PL = 4416;
constexpr int CV_F = 8 * CV_PL;
constexpr int CV_KB = 80;
constexpr int CV_KV = 65 * CV_KB;
constexpr int CV_CH = CV_F + CV_KV;
static_assert(CV_CH % 16 == 0 && 4 * CV_CH <= LDS_BYTES, "conv lds");

struct Filt { v4u a[5], b[5]; };
DEV int chan(int grp, int w) { return 16 * (grp >> 2) + (grp & 3) + 4 * w; }
template <int N = 5, int AUX = INAUX> DEV void filt_load(const Params& p, int grp, Filt& f) {
    const int tid = threadIdx.x, lane = tid & 63, d = chan(grp, tid >> 6);
    const rsrc_t rf = mk_rsrc(p.ssmk, ACT);
#pragma unroll
    for (int i = 0; i < N; ++i) {
        const u32 s0 = (u32)(8 * (lane + 64 * i) - CV_PAD);
        f.a[i] = __builtin_amdgcn_raw_buffer_load_b128(rf, s0 < SEQ ? (u32)(d * SEQ + s0) * 2u : OOB, 0, AUX);
        f.b[i] = __builtin_amdgcn_raw_buffer_load_b128(rf, s0 + 8 < SEQ ? (u32)(d * SEQ + s0 + 8) * 2u : OOB, 0, AUX);
    }
}
constexpr int KT_BYTES = 18 * 1024, KT_X = 64 * 144, NKV = 44, KT_DUMP = 8 * KT_BYTES + KT_X;
DEV void kv_fetch(const Params& p, int tile, u32 dst) {
    const int lane = threadIdx.x & 63, w = uni(threadIdx.x >> 6);
    const v4u sk = mk_srd(p.bk, ACT), sv = mk_srd(p.bv, ACT), ss = mk_srd(sel(w == 0, p.bk, p.shift), w == 0 ? ACT : DM * 8);
    const u32 vo = tile < 320 ? (u32)((lane >> 3) * DM + (tile % 10) * 64 + (lane & 7) * 8) * 2u : OOB, r0 = uu((u32)((tile / 10) * 64 + 8 * w) * (DM * 2)), d = uu(dst + w * 1024);
    DMANT(sk, d + 1024, vo, r0); DMANT(sk, d + 5120, vo, r0 + 32 * DM * 2); DMANT(sv, d + 9216, vo, r0); DMANT(sv, d + 13312, vo, r0 + 32 * DM * 2);
    DMAIN(ss, uu(w == 0 ? dst : w == 1 ? dst + 17408 : KT_DUMP), w == 0 ? (tile >= 10 ? vo : OOB) : w == 1 && lane < 32 ? (u32)lane * 16u : OOB,
          uu(w == 0 ? (tile >= 10 ? r0 - 8 * DM * 2 : 0u) : (u32)(tile % 10) * 512u));
}
DEV void kv_tile(const Params& p, rsrc_t rs, lptr src, lptr xt, int tile) {
    const int tid = threadIdx.x;
    const v4u sv = lds_r16(src + 17408 + (tid & 31) * 16);
    lptr kp = src + ((tid >> 5) * 8 + 5) * 128 + (tid & 31) * 4;
    f32x2 kf[11];
#pragma unroll
    for (int i = 0; i < 11; ++i) kf[i] = up2(*(LAS u32*)(kp + i * 128));
    const f32x2 sh[4] = {{lo_f(sv.x), lo_f(sv.z)}, {hi_f(sv.x), hi_f(sv.z)}, {lo_f(sv.y), lo_f(sv.w)}, {hi_f(sv.y), hi_f(sv.w)}};
    u32 kv[8];
#pragma unroll
    for (int i = 0; i < 8; ++i) {
        const f32x2 acc = rb2(rb2(rb2(rb2(kf[i] * sh[3]) + rb2(kf[i + 1] * sh[2])) + rb2(kf[i + 2] * sh[1])) + rb2(kf[i + 3] * sh[0]));
        kv[i] = pk2(acc * up2(*(LAS u32*)(kp + 9216 + (i - 5) * 128)));
    }
    lds_w16(xt + (2 * (tid & 31)) * 144 + (tid >> 5) * 16, (v4u){__builtin_amdgcn_perm(kv[6], kv[7], 0x05040100u), __builtin_amdgcn_perm(kv[4], kv[5], 0x05040100u),
                                                             __builtin_amdgcn_perm(kv[2], kv[3], 0x05040100u), __builtin_amdgcn_perm(kv[0], kv[1], 0x05040100u)});
    lds_w16(xt + (2 * (tid & 31) + 1) * 144 + (tid >> 5) * 16, (v4u){__builtin_amdgcn_perm(kv[6], kv[7], 0x07060302u), __builtin_amdgcn_perm(kv[4], kv[5], 0x07060302u),
                                                                 __builtin_amdgcn_perm(kv[2], kv[3], 0x07060302u), __builtin_amdgcn_perm(kv[0], kv[1], 0x07060302u)});
    BAR();
#pragma unroll
    for (int i = 0; i < 2; ++i) {
        const int ch = i * 32 + (tid >> 3);
        st16(rs, W_KVT + (u32)(((tile % 10) * 64 + ch) * SEQ + (tile / 10) * 64 + (tid & 7) * 8) * 2u, lds_r16(xt + ch * 144 + (tid & 7) * 16));
    }
}
template <int MODE> DEV void conv_job(const Params& p, rsrc_t rs, lptr lds, int grp, Filt& f, int next, u32 fo2 = OOB, u32 fv2 = 0) {
    const int tid = threadIdx.x, lane = tid & 63, w = uni(tid >> 6), bq = lane & 31, hh = lane >> 5;
    lptr F = lds + w * CV_CH;
    lptr fa = F + (bq & 7) * CV_PL + ((bq >> 3) - hh + 4) * 16;
    lptr kb = F + CV_F + CV_KB + hh * 16, kh = kb + (bq + 32) * CV_KB;
    constexpr bool LO = MODE & 1, EARLY = MODE == 0 || MODE == 3;
    bool dead = false;
    v4u kvr[4];
    const u32 D = p.ssmD[chan(grp, w)];
    auto kv_load = [&]() {
        if (EARLY) boot_wait(p, rs, lane < NKV ? foff(F_KV + lane) : OOB, 2u, dead);
#pragma unroll
        for (int i = 0; i < (LO ? 2 : 4); ++i) kvr[i] = ld16s(rs, (u32)lane * 16u, W_KVT + (u32)(chan(grp, w) * SEQ + i * 512) * 2u);
    };
    auto windows = [&](int i) {
        if (i < 4 || lane < 16) {
            const u32 e[8] = {f.a[i].x, f.a[i].y, f.a[i].z, f.a[i].w, f.b[i].x, f.b[i].y, f.b[i].z, f.b[i].w};
            u32 o[7];
#pragma unroll
            for (int q = 0; q < 7; ++q) o[q] = __builtin_amdgcn_alignbit(e[q + 1], e[q], 16);
#pragma unroll
            for (int q = 0; q < 4; ++q) {
                lds_w16(F + (2 * q) * CV_PL + (64 * i + lane) * 16, (v4u){o[q], o[q + 1], o[q + 2], o[q + 3]});
                lds_w16(F + (2 * q + 1) * CV_PL + (64 * i + lane) * 16, (v4u){e[q + 1], e[q + 2], e[q + 3], e[q + 4]});
            }
        }
    };
    lptr yw = lds + (lane >> 4) * CV_CH + 3072 + (lane & 14) * 64 + w * 32 + (lane & 1) * 16, yr = F + 3072 + lane * 16;
    const u32 yo = W_Y + (u32)((((w & 1) * 8 + (lane >> 3)) * 8 + (w >> 1)) * DM + (grp & 3) * 160 + 4 * (grp >> 2)) * 32u + (u32)(lane & 7) * 16u;
    v4u dg[2];
    auto skip = [&](lptr k) { const f32x16 z = {}; return mfma32(lds_r16(k + 32), dg[1], mfma32(lds_r16(k), dg[0], z)); };
    auto pack = [&](const f32x16& ac, const f32x16& sk, int q) {
        const int r = 4 * (q & 3);
        u32 v[4];
#pragma unroll
        for (int i = 0; i < 4; ++i) v[i] = pk2(rb2((f32x2){ac[r + 2 * i], ac[r + 2 * i + 1]}) + rb2((f32x2){sk[r + 2 * i], sk[r + 2 * i + 1]}));
        const auto x = __builtin_amdgcn_permlane32_swap(v[0], v[2], false, false), y = __builtin_amdgcn_permlane32_swap(v[1], v[3], false, false);
        return (v4u){x[0], y[0], x[1], y[1]};
    };
    if (MODE != 2) EV(8);
    if (!EARLY) kv_load();
    if (lane < CV_KB / 16) lds_w16(F + CV_F + lane * 16, (v4u){0u, 0u, 0u, 0u});
    windows(0);
    if (MODE != 1) { windows(1); windows(2); }
    if (!LO) windows(4);
    if (EARLY) kv_load();
    if (MODE == 2) windows(3);
    if (MODE != 2) EV(9);
#pragma unroll
    for (int i = 0; i < (LO ? 2 : 4); ++i) lds_w16(F + CV_F + (16 * i + (lane >> 2) + 1) * CV_KB + (lane & 3) * 16, kvr[i]);
#pragma unroll
    for (int ks = 0; ks < 2; ++ks)
#pragma unroll
        for (int q = 0; q < 4; ++q) { const int j = 16 * ks + 8 * hh + 7 - bq; dg[ks][q] = j == 2 * q ? D : j == 2 * q + 1 ? D << 16 : 0u; }
    f32x16 acc0 = {}, acc1 = {};
    auto first = [&]() { asm volatile("s_waitcnt vmcnt(0)\n\ts_barrier" ::: "memory"); st4s(rs, tid == 0 ? foff(F_CV + grp) : fo2, 0, tid == 0 ? 1u : fv2); EV(12); };
    if (MODE != 2) {
        EV(10);
        v4u fr[2][4];
        auto rd = [&](int m) { lptr kl = kb + max(bq - m, -1) * CV_KB; fr[m & 1][0] = lds_r16(kl); fr[m & 1][1] = lds_r16(fa + m * 64); fr[m & 1][2] = lds_r16(kl + 32); fr[m & 1][3] = lds_r16(fa + m * 64 - 32); };
        rd(0); rd(1);
        const f32x16 sk = skip(kb + bq * CV_KB);
#pragma unroll
        for (int m = 0; m < 8; ++m) {
            acc0 = mfma32(fr[m & 1][0], fr[m & 1][1], acc0); acc0 = mfma32(fr[m & 1][2], fr[m & 1][3], acc0);
            if (m < 6) rd(m + 2);
            SCHED();
        }
        lds_w16(yw, pack(acc0, sk, 0));
        BAR();
        st16(rs, w < 2 ? yo : OOB, lds_r16(yr));
        EV(11);
    }
    if (LO) {
        if (MODE == 1) { windows(1); windows(2); first(); }
        v4u a[2][2], l[2][2];
#pragma unroll
        for (int u = 0; u < 2; ++u) { lptr kl = kb + max(bq - 8 - u, -1) * CV_KB; a[u][0] = lds_r16(fa + (8 + u) * 64); l[u][0] = lds_r16(kl); a[u][1] = lds_r16(fa + (8 + u) * 64 - 32); l[u][1] = lds_r16(kl + 32); }
#pragma unroll 1
        for (int m = 8; m < 32; m += 2) {
            if (MODE == 3 && m == 20) first();
#pragma unroll
            for (int u = 0; u < 2; ++u) {
                lptr kl = kb + max(bq - m - u - 2, -1) * CV_KB;
                acc0 = mfma32(l[u][0], a[u][0], acc0); a[u][0] = lds_r16(fa + (m + u + 2) * 64); l[u][0] = lds_r16(kl); SCHED();
                acc0 = mfma32(l[u][1], a[u][1], acc0); a[u][1] = lds_r16(fa + (m + u + 2) * 64 - 32); l[u][1] = lds_r16(kl + 32); SCHED();
            }
        }
    } else {
        if (MODE == 0) windows(3); else filt_load<5, 0>(p, next, f);
        v4u a0 = lds_r16(fa), a1 = lds_r16(fa - 32), c0 = lds_r16(fa + 2048), c1 = lds_r16(fa + 2048 - 32);
        v4u h0 = lds_r16(kh), h1 = lds_r16(kh + 32), l0 = lds_r16(kb + bq * CV_KB), l1 = lds_r16(kb + bq * CV_KB + 32);
#pragma unroll 2
        for (int m = 0; m < (MODE == 0 ? 8 : 32); ++m) {
            lptr kl = kb + max(bq - m - 1, -1) * CV_KB;
            if (MODE == 0 && m == 4) first();
            acc1 = mfma32(h0, a0, acc1); h0 = lds_r16(kh - (m + 1) * CV_KB); a0 = lds_r16(fa + (m + 1) * 64); SCHED();
            acc1 = mfma32(l0, c0, acc1); c0 = lds_r16(fa + (m + 1) * 64 + 2048); l0 = lds_r16(kl); SCHED();
            acc1 = mfma32(h1, a1, acc1); h1 = lds_r16(kh - (m + 1) * CV_KB + 32); a1 = lds_r16(fa + (m + 1) * 64 - 32); SCHED();
            acc1 = mfma32(l1, c1, acc1); c1 = lds_r16(fa + (m + 1) * 64 + 2048 - 32); l1 = lds_r16(kl + 32); SCHED();
        }
        if (MODE == 0) {
#pragma unroll 2
            for (int m = 8; m < 32; ++m) {
                lptr kl = kb + max(bq - m - 1, -1) * CV_KB;
                acc1 = mfma32(h0, a0, acc1); h0 = lds_r16(kh - (m + 1) * CV_KB); SCHED();
                acc0 = mfma32(l0, a0, acc0); a0 = lds_r16(fa + (m + 1) * 64); SCHED();
                acc1 = mfma32(l0, c0, acc1); c0 = lds_r16(fa + (m + 1) * 64 + 2048); l0 = lds_r16(kl); SCHED();
                acc1 = mfma32(h1, a1, acc1); h1 = lds_r16(kh - (m + 1) * CV_KB + 32); SCHED();
                acc0 = mfma32(l1, a1, acc0); a1 = lds_r16(fa + (m + 1) * 64 - 32); SCHED();
                acc1 = mfma32(l1, c1, acc1); c1 = lds_r16(fa + (m + 1) * 64 + 2048 - 32); l1 = lds_r16(kl + 32); SCHED();
            }
        }
    }
    if (MODE != 2) EV(13);
    v4u yy[4];
    if (MODE != 2) { const f32x16 sk = skip(kb + bq * CV_KB); yy[0] = pack(acc0, sk, 0); yy[1] = pack(acc0, sk, 2); }
    if (!LO) { const f32x16 sk = skip(kh); yy[2] = pack(acc1, sk, 0); yy[3] = pack(acc1, sk, 2); BAR(); }
#pragma unroll
    for (int q = (MODE == 2 ? 2 : 0); q < (LO ? 2 : 4); ++q) lds_w16(yw + q * CV_PL, yy[q]);
    BAR();
#pragma unroll
    for (int q = (MODE == 2 ? 2 : 0); q < (LO ? 2 : 4); ++q) st16(rs, q == 0 && w < 2 ? OOB : yo + (u32)q * (2 * DM * 32), lds_r16(yr + q * CV_PL));
    BAR();
    if (MODE != 2) EV(14);
}

#define NPROBE 6
DEV int probe(rsrc_t rs, int c) {
    const int lane = threadIdx.x & 63;
    const u32 k = (u32)(c & 15) < NREG ? (u32)(c & 15) : 0u;
    const u32 off0 = k * (16 * MB) + 2 * MB - 65536 + ((u32)(c & 15) >= NREG ? (u32)(c & 3) + 1u : 0u) * 256 + (u32)(c >> 4) * 128;
    st4s(rs, lane == 0 ? off0 : OOB, 0, 0u); DRAIN();
    int d0 = 1 << 16, d1 = 1 << 16;
#pragma unroll 1
    for (int q = 0; q < NPROBE; ++q) {
        const u32 o = lane == 0 ? off0 + (u32)(q & 1) * 8192 + (u32)(q >> 1) * 18432 : OOB;
        const unsigned long long t0 = __builtin_amdgcn_s_memtime();
        st4s(rs, o, 0, 0u); DRAIN();
        const int dt = (int)(__builtin_amdgcn_s_memtime() - t0);
        if (q & 1) d1 = min(d1, dt); else d0 = min(d0, dt);
    }
    const u32 xcc = (u32)__builtin_amdgcn_s_getreg(20 | (3 << 11)) & 0xfu;
    return (d0 - d1) * ((xcc & 4u) ? -1 : 1);
}
DEV void pre_rings(const Params& p, rsrc_t rs, int j) {
    const u32 gt = (u32)(j * 256 + threadIdx.x) * 16u, n = ((gt >> 14) << 9) + ((gt & 8191u) >> 4);
    const v4u ps = {PSENT, PSENT, PSENT, PSENT};
    v4u m = ps;
    if (n < 640u) m = *(const v4u*)(p.mem0 + (2 * ((n >> 2) & 3) + (n & 1)) * DM + (n >> 4) * 16 + (n & 2) * 4);
#pragma unroll
    for (u32 i = 0; i < 8; ++i) st16(rs, W_ORING + gt + i * 131072u, ps);
#pragma unroll
    for (u32 i = 0; i < 2; ++i) st16(rs, gt < 16384u ? W_MRING + i * (MB / 4) + 131072u + gt : OOB, ps);
    if (n < 640u) {
        m = (v4u){__builtin_amdgcn_perm(m.z, m.x, 0x05040100u), __builtin_amdgcn_perm(m.z, m.x, 0x07060302u), __builtin_amdgcn_perm(m.w, m.y, 0x05040100u), __builtin_amdgcn_perm(m.w, m.y, 0x07060302u)};
#pragma unroll
        for (int i = 0; i < 4; ++i) m[i] = m[i] == PSENT ? 0x7FC17FC1u : m[i];
    }
    st16(rs, W_MRING + gt, m);
    st16(rs, W_MRING + MB / 4 + gt, m);
    static_assert(2 * (F_N - F_QKV) == 32 * 36, "clear");
    st4s(rs, threadIdx.x < 36 ? foff(F_QKV) + (u32)(j * 36 + threadIdx.x) * 128u : OOB, 0, 0u);
}

#define M32A(first, d, a, b) do { if (first) asm volatile("v_mfma_f32_32x32x16_bf16 %0, %1, %2, 0" : "=&v"(d) : "a"(a), "v"(b)); \
                                  else asm volatile("v_mfma_f32_32x32x16_bf16 %0, %1, %2, %0" : "+v"(d) : "a"(a), "v"(b)); } while (0)
#define M32B(first, d, a, b) do { if (first) asm volatile("v_mfma_f32_32x32x16_bf16 %0, %1, %2, 0" : "=&v"(d) : "v"(a), "a"(b)); \
                                  else asm volatile("v_mfma_f32_32x32x16_bf16 %0, %1, %2, %0" : "+v"(d) : "v"(a), "a"(b)); } while (0)
#define VCVT(d, a, b) asm volatile("v_cvt_pk_bf16_f32 %0, %1, %2" : "=v"(d) : "v"(a), "v"(b))
#define LD4A(d, vo, so) asm volatile("buffer_load_dword %0, %1, %2, %3 offen sc1" : "=v"(d) : "v"(vo), "s"(srd), "s"(so) : "memory")
DEV u32 wst_src(int lane, int ld) { return (u32)((lane >> 4) * ld + 8 * ((lane & 15) ^ ((lane >> 4) << 2))) * 2u; }
DEV u32 wst_rd(int lane, int w) { const int q4 = (lane >> 2) & 3; return (u32)(q4 * 256 + ((w ^ q4) * 64) + ((lane >> 4) & 1) * 32 + (lane & 3) * 8); }

constexpr int QK_A = 65536;
#define QLEAD 4
#define QFIRST 2
static_assert(QFIRST < QLEAD, "a round of the lead carries the flag of the convolution");
#define QW0 10
#define QW1 3
#define QSAMP 72
DEV void role_qkv(const Params& p, lptr lds, int c) {
    const int grp = 64 + c;
    const int tid = threadIdx.x, lane = tid & 63, w = uni(tid >> 6), hh = lane >> 5, r32 = lane & 31;
    const int m = c >> 5, h = c & 31;
    const rsrc_t rs = mk_rsrc(p.ws, W_END);
    v4u wt[40];
    Filt flt;
    bool dead = false;
    u32 pol, h2flag, fo, ol, ar[8];
    const u32 ds = uu(w * 1024), dx = ds + QK_A;
#define XRD(ks) lds_r16(lds + ar[(ks) & 7] + ((ks) >> 3) * 8192)
    const u32 t16 = (u32)tid * 16u, lnf = tid < 96 ? foff(F_LN + tid) : OOB;
    MK_SRD();
#define QDMA(i, dst, vo, so) DMA16I(dst, (i) * 4096, vo, so, ((i) >> 1) * 16384 + ((i) & 1) * 4096)
    {
        const v4u sw = mk_srd(sel(m == 0, p.Wq, sel(m == 1, p.Wk, p.Wv)), DM * NQ * 2), sx = mk_srd(p.x, ACT);
        const u32 vo = wst_src(lane, NQ) + (u32)h * (DH * 2), x8 = (u32)lane * 8u, x4 = (u32)lane * 4u;
        v2u xa[6], xb[6]; u32 xc[6];
#pragma unroll
        for (int i = 0; i < 6; ++i)
            asm volatile("buffer_load_dwordx2 %0, %3, %5, %6 offen " XPOL "\n\tbuffer_load_dwordx2 %1, %3, %5, %6 offen offset:512 " XPOL "\n\tbuffer_load_dword %2, %4, %5, %6 offen offset:1024 " XPOL
                         : "=&v"(xa[i]), "=&v"(xb[i]), "=&v"(xc[i]) : "v"(x8), "v"(x4), "s"(sx), "s"(uu((4 * c + w + 384 * i) * (DM * 2))) : "memory");
        EV(1);
        auto wfill = [&](int i0, int n) {
#pragma unroll 1
            for (int i = i0; i < i0 + n; ++i) DMAIN(sw, uu((4 * i + w) * 1024), vo, uu((16 * i + 4 * w) * (NQ * 2)));
        };
        wfill(0, QW0);
        asm volatile("s_waitcnt vmcnt(%0) ; HZCLR" :: "i"(QW0) : "memory");
        EV(2);
#pragma unroll
        for (int i = 0; i < 6; ++i) {
            wfill(QW0 + QW1 * i, QW1);
            if (i == 2) { VMW(2 * QW1 + 3); BAR(); boot_set(p, rs, tid == 0 ? foff(F_LN + c) : OOB, 1u); }
            asm volatile("" : "+v"(xa[i]), "+v"(xb[i]), "+v"(xc[i]));
            const int row = 4 * c + w + 384 * i;
            if (i < 5 || row < SEQ) {
                const f32x2 x[5] = {up2(xa[i].x), up2(xa[i].y), up2(xb[i].x), up2(xb[i].y), up2(xc[i])};
                const f32x2 st = ln_stats(x);
                u32 hp[5];
#pragma unroll
                for (int q = 0; q < 5; ++q) hp[q] = pk2(up2(pk2(x[q] - st.x)) * st.y);
                store_h(rs, W_H1 + (u32)(row >> 5) * H_TILE, row, hp);
            }
        }
        EV(3);
        constexpr int K0 = QW0 + 6 * QW1;
        static_assert(K0 * 4096 >= QK_A + 40960, "the first tile's place is free");
        v4u fv, praw;
        asm volatile("buffer_load_dwordx4 %0, %2, %4, 0 offen sc1\n\tbuffer_load_dwordx4 %1, %3, %4, 0 offen sc1" : "=&v"(praw), "=&v"(fv) : "v"(pol_off()), "v"(lnf), "s"(srd) : "memory");
        wfill(K0, 40 - K0);
        asm volatile("s_waitcnt vmcnt(%2) ; HZCLR\n\ts_barrier" : "+v"(praw), "+v"(fv) : "i"(40 - K0) : "memory");
        boot_set(p, rs, tid == 0 ? foff(F_LN + c) : OOB, 2u);
        EV(4);
        lptr tb = lds + hh * 2048 + wst_rd(lane, w);
        SFOR(k, 40)
            if constexpr (k == K0) {
                if (!boot_ok(p, lnf, fv, 1u)) boot_wait(p, rs, lnf, 1u, dead);
                if (!boot_ok(p, pol_off(), praw, 0u)) praw.w = boot_wait(p, rs, pol_off(), 0u, dead);
                BAR();
                EV(5);
                const u32 so = W_H1;
                SFOR(i, 10) QDMA(i, dx, t16, so); SEND
                pol = pol_of(praw.w);
                h2flag = tid < 8 ? near_(foff(F_H2 + lane), 1u, pol) : OOB;
                ol = (m == 2 ? (u32)(2 * w + (r32 >> 4)) * 1024u : (u32)((r32 >> 4) * 4 + w) * 1024u) + (u32)(hh * 16 + (r32 & 15)) * 16u;
                fo = near_(foff(F_QKV + m * 64 + h), 0u, pol);
#pragma unroll
                for (int j = 0; j < 8; ++j) { ar[j] = (u32)(QK_A + r32 * 256 + ((2 * j + hh) ^ (r32 & 15)) * 16); PIN(ar[j]); }
                PIN(h2flag); PIN(ol); PINS(fo); PINS(pol);
                VMW(10); BAR();
            }
            lptr q = tb + (k >> 3) * 32768 + (k & 7) * 4096; wt[k] = tr2(q, q + 1024); asm volatile("" : "+a"(wt[k]));
        SEND
    }
    EV(6);
    int n0 = -1, n1 = -QLEAD;
    const u32 ksc = m == 1 ? 0x3e0293eeu : 0x3f800000u;
    const u32 bfo = tid < 96 ? lnf : tid - 128u < NKV ? foff(F_KV - 128 + tid) : OOB;
    v4u brk = {};
    auto run = [&](auto isv) __attribute__((always_inline)) {
      constexpr bool V = decltype(isv)::value;
      v4u x[8] = {};
      f32x16 a0 = {};
      bool fresh = true;
      u32 fv = 0, fo0 = tid == 192 ? fo : n1 > -QLEAD && tid == 194 ? foff(F_CV + grp) : OOB, fo1 = tid == 192 ? fo : tid == 193 ? fo + 32 * 128 : OOB;
      PIN(fo0); PIN(fo1);
#define QS(k) do {} while (0)
#define QPK1(d, a, a_, q_) do { if constexpr (V) VCVT(d[q_], a[4 * (a_) + 8 * ((q_) >> 1) + 2 * ((q_) & 1)], a[4 * (a_) + 8 * ((q_) >> 1) + 2 * ((q_) & 1) + 1]); else VCVT(d[q_], a[4 * (a_) + (q_)], a[4 * (a_) + (q_) + 8]); } while (0)
#define QSC(a, i) asm volatile("v_mul_f32 %0, %2, %0\n\tv_mul_f32 %1, %2, %1" : "+v"(a[i]), "+v"(a[(i) + 8]) : "s"(ksc))
#define QPK(d, a, a_, q_) do { if constexpr (!V) { QSC(a, 4 * (a_) + (q_)); QSC(a, 4 * (a_) + (q_) + 1); } QPK1(d, a, a_, q_); QPK1(d, a, a_, (q_) + 1); } while (0)
#pragma unroll 1
      for (const int end = n1 == -QLEAD ? QFIRST - QLEAD : NSEG; n1 < end; ++n1) {
        const int cur = n1 < 0 ? n0 + 1 : 64 | n1, nxt = n1 < 0 ? n0 + 2 : n0 + 1;
        u32 xs = n1 < 0 ? W_H1 + (u32)cur * H_TILE : near_(W_H2 + (u32)n1 * H_TILE, 1u, pol);
        PINS(xs);
        QS(0);
        if (w == 0 && n1 >= 0 && ((u32)__builtin_amdgcn_ballot_w64(fv <= (u32)n1) & 255u) != 0u) wait_flags(rs, h2flag, (u32)n1 + 1, dead);
        BAR();
        QS(1);
        f32x16 a1;
        u32 ss, sv, hb, oa, qfo, qfv;
        v4u pp, pq;
        SCHED();
        auto slot = [&](auto zc) __attribute__((always_inline)) {
            constexpr int z = decltype(zc)::value, ks = (z + 20) % 40;
            if constexpr (z < 20) { if constexpr (V) M32B(false, a0, x[ks & 7], wt[ks]); else M32A(false, a0, wt[ks], x[ks & 7]); }
            else if constexpr (z < 60) { if constexpr (V) M32B(z == 20, a1, x[ks & 7], wt[ks]); else M32A(z == 20, a1, wt[ks], x[ks & 7]); }
            else if constexpr (z == 60) {
                if constexpr (V) asm volatile("v_mfma_f32_32x32x16_bf16 %0, %1, %2, 0" : "+v"(a0) : "v"(x[ks & 7]), "a"(wt[ks]));
                else asm volatile("v_mfma_f32_32x32x16_bf16 %0, %1, %2, 0" : "+v"(a0) : "a"(wt[ks]), "v"(x[ks & 7]));
            } else { if constexpr (V) M32B(false, a0, x[ks & 7], wt[ks]); else M32A(false, a0, wt[ks], x[ks & 7]); }
            if constexpr (z == 18) { QS(2); asm volatile("s_waitcnt vmcnt(6) lgkmcnt(0)\n\ts_barrier" ::: "memory"); QS(3); }
            if constexpr (z < 15 || z > 20) x[(ks + 5) & 7] = XRD((ks + 5) % 40);
            if constexpr (z >= 18 && z <= 20) { x[2 * (z - 18)] = XRD(2 * (z - 18)); x[2 * (z - 18) + 1] = XRD(2 * (z - 18) + 1); }
            SCHED();
            if constexpr (z < 4) QDMA(z, dx, t16, xs);
            if constexpr (z >= 5 && z < 17 && (z & 1)) QDMA((z + 3) >> 1, dx, t16, xs);
            if constexpr ((z >= 7 && z < 15) || (z >= 47 && z < 55)) asm volatile("v_xor_b32 %0, 0x10000, %0" : "+v"(ar[(ks + 5) & 7]));
            if constexpr (z == 16) { ss = W_H1 + (u32)nxt * H_TILE; PINS(ss); }
            if constexpr (z == 17) { sv = t16 | (nxt >= NSEG ? OOB : 0u); PIN(sv); }
            if constexpr (z >= 21 && z < 41 && (z & 1)) QDMA((z - 21) >> 1, ds, sv, ss);
            if constexpr (z == 22 || z == 24) QPK(pp, a0, 0, z - 22);
            if constexpr (z == 26 || z == 28) QPK(pq, a0, 1, z - 26);
            if constexpr (z == 30) { QS(4); asm volatile("s_waitcnt vmcnt(5)\n\ts_barrier" ::: "memory"); QS(5); }
            if constexpr (z == 32 || z == 44) { hb = (m == 2 ? W_VO : W_KQ) + (u32)((z == 32 ? n0 : cur) * NH + h) * 16384u; PINS(hb); }
            if constexpr (z == 34 || z == 45) { hb = near_(hb, m == 1 ? 0u : 1u, pol); PINS(hb); }
            if constexpr (z == 36) { hb = n0 < NSEG && !fresh ? hb : OOB; PINS(hb); }
            if constexpr (z == 38 || z == 46) { oa = ol + hb; PIN(oa); }
            if constexpr (z == 41) st16(rs, oa, pp);
            if constexpr (z == 43) st16(rs, oa + 512, pq);
            if constexpr (z == 54) { QS(6); asm volatile("s_waitcnt vmcnt(4)\n\ts_barrier" ::: "memory"); QS(7); }
            if constexpr (z == 56) { if (__builtin_expect(n1 == QFIRST - QLEAD - 1, 0)) { filt_load<3, 0>(p, grp, flt); brk = ld16s(rs, bfo, 0); } }
            if constexpr (z == 60) NOPS(5);
            if constexpr (z == 63) st16(rs, oa, pp);
            if constexpr (z == 65) st16(rs, oa + 512, pq);
            if constexpr (z == 61 || z == 62) QPK(pp, a1, 0, 2 * (z - 61));
            if constexpr (z == 63 || z == 64) QPK(pq, a1, 1, 2 * (z - 63));
            if constexpr (z == 67) { qfo = n1 >= 0 ? fo1 : fo0; PIN(qfo); }
            if constexpr (z == 68) { qfv = tid == 193 ? (u32)n1 + 1u : (u32)min(nxt, NSEG); PIN(qfv); }
            if constexpr (z == QSAMP) LD4A(fv, h2flag, 0);
            SCHED();
        };
        if (__builtin_expect(!fresh, 1)) static_for<0, 16>(slot);
        static_for<16, 80>(slot);
        QS(8);
        asm volatile("s_waitcnt vmcnt(1)\n\ts_barrier" ::: "memory");
        st4s(rs, qfo, 0, qfv);
        QS(9);
        asm volatile("s_waitcnt vmcnt(1)" : "+v"(fv) :: "memory");
        n0 = nxt; fresh = false;
      }
      if (n1 < 0) {
        SFOR(z, 20)
            if constexpr (V) M32B(false, a0, x[(z + 20) & 7], wt[z + 20]); else M32A(false, a0, wt[z + 20], x[(z + 20) & 7]);
            if constexpr (z < 15) x[(z + 25) & 7] = XRD(z + 25);
            if constexpr (z >= 7 && z < 15) asm volatile("v_xor_b32 %0, 0x10000, %0" : "+v"(ar[(z + 25) & 7]));
            SCHED();
        SEND
        u32 oa = ol + near_((m == 2 ? W_VO : W_KQ) + (u32)(n0 * NH + h) * 16384u, m == 1 ? 0u : 1u, pol);
        v4u pp, pq;
        asm volatile("s_nop 11" : "+v"(oa));
        QPK(pp, a0, 0, 0); QPK(pp, a0, 0, 2); st16(rs, oa, pp);
        QPK(pq, a0, 1, 0); QPK(pq, a0, 1, 2); st16(rs, oa + 512, pq);
      }
    };
#pragma unroll 1
    for (;;) {
        if (m == 2) run(IC<1>{}); else run(IC<0>{});
        if (n1 > 0) break;
        if (!boot_ok(p, bfo, brk, 2u)) boot_wait(p, rs, bfo, 2u, dead);
        BAR();
        conv_job<1>(p, rs, lds, grp, flt, 0, tid == 1 ? fo : OOB, (u32)n0 + 1u);
        const u32 so = uu(W_H1 + (u32)(n0 + 1) * H_TILE);
        SFOR(i, 10) QDMA(i, dx, t16, so); SEND
    }
}

constexpr int OU_SEL = 131072;
#define OSAMP 46

DEV void role_out(const Params& p, lptr lds, int L, int kq, int ng) {
    const int tid = threadIdx.x, lane = tid & 63, w = uni(tid >> 6), hh = lane >> 5, r32 = lane & 31;
    const rsrc_t rs = mk_rsrc(p.ws, W_END);
    v4u wo[64];
    Filt flt;
    bool dead = false;
    {
        const v4u sw = mk_srd(p.Wo, DM * NQ * 2);
        const u32 vo = wst_src(lane, DM) + (ng == 4 ? 1024u : (u32)((ng >> 1) * 512 + (ng & 1) * 128 + ((lane ^ (lane >> 2)) & 8) * 16));
        auto fill = [&](int b) {
#pragma unroll 1
            for (int i = 0; i < 8; ++i) DMAIN(sw, uu((b % 5) * 32768 + (4 * i + w) * 1024), vo, uu((kq * 1024 + b * 128 + 16 * i + 4 * w) * (DM * 2)));
        };
#pragma unroll 1
        for (int b = 0; b < 5; ++b) fill(b);
        lptr tb = lds + hh * 1024 + wst_rd(lane, w);
        SFOR(b, 8)
            VMW(b < 3 ? 32 : 62 - 8 * b); BAR();
            lptr q = tb + (b % 5) * 32768;
#pragma unroll
            for (int k = 0; k < 8; ++k) { lptr a = q + (k >> 1) * 8192 + (k & 1) * 2048; wo[8 * b + k] = tr2(a, a + 4096); if (b < 7) asm volatile("" : "+a"(wo[8 * b + k])); else PIN(wo[8 * b + k]); }
            if constexpr (b < 3) { BAR(); fill(b + 5); }
            if constexpr (b == 2) filt_load<3, 0>(p, L == 0 ? 44 + kq * 5 + ng : 1 << 12, flt);
        SEND
    }
    EV(6);
    const u32 pol = get_pol(p, rs, dead);
    if (L == 0) {
        BAR();
        conv_job<3>(p, rs, lds, 44 + kq * 5 + ng, flt, 0);
        DRAIN(); BAR();
        st4s(rs, tid == 0 ? foff(F_CV + 44 + kq * 5 + ng) : OOB, 0, 2u);
    }
    EV(15);
    MK_SRD();
    const u32 ofl = (lane < 8 && w == 0) ? near_(foff(F_O + L * 32 + kq * 8 + lane), 0u, pol) : OOB;
    const u32 l16 = (u32)lane * 16u;
    const u32 fo = (tid == 192) ? near_(foff(F_P + L * 32 + kq * 5 + ng), 1u, pol) : OOB;
    auto osrc = [&](int s) { return uu(hblk(W_VO, L * NSEG + s, kq * 8, 0u, pol) + w * 1024); };
#define ODMA(i, dst, vo, so) DMA16I(dst, (i) * 4096, vo, so, ((i) >> 1) * 16384 + ((i) & 1) * 4096)
    const u32 pvo = (u32)(kq * 5 + ng) * 16384u + (u32)w * 2048u + l16;
    lptr tw = lds + OU_SEL + 256 + r32 * 272 + (ng == 4 ? hh * 32 + w * 4 : (w & 1) * 128 + hh * 16 + (w >> 1) * 8), tr = lds + OU_SEL + 256 + (8 * w + (lane >> 4)) * 272 + (lane & 15) * 16;
    lptr selw = lds + OU_SEL + (w == 0 ? 0 : 64);
    u32 pfo = OOB;
    u32 buf = 0;
    if (w == 0) wait_flags(rs, ofl, 1u, dead);
    BAR();
    for (int s = 0; s < NSEG; ++s) {
        u32 src = osrc(s), dst = uu(buf + w * 1024);
        lptr A = lds + buf + l16;
        asm volatile("" : "+s"(src), "+s"(dst));
#define OS(k) do {} while (0)
        OS(0);
        OS(1);
        SFOR(i, 10) ODMA(i, dst, l16, src); SEND
        OS(2);
        asm volatile("s_waitcnt vmcnt(7)\n\ts_barrier" ::: "memory");
        OS(3);
        LS2(L == 0 && w == 1, 96 + kq * 5 + ng);
        v4u o[64];
#pragma unroll
        for (int k = 0; k < 3; ++k) o[k] = lds_r16(A + k * 1024);
        st4s(rs, pfo, 0, (u32)s);
        u32 fv, pa; int rdy;
        f32x16 acc;
        SCHED();
        SFOR(k, 64)
            if constexpr (k < 56) M32A(k == 0, acc, wo[k], o[k]); else asm volatile("v_mfma_f32_32x32x16_bf16 %0, %1, %2, %0" : "+v"(acc) : "v"(wo[k]), "v"(o[k]));
            if constexpr (k < 61) o[k + 3] = lds_r16(A + (k + 3) * 1024);
            SCHED();
            if constexpr (k < 12 && (k & 1) == 0) ODMA(10 + (k >> 1), dst, l16, src);
            if constexpr (k == 7) OS(4);
            if constexpr (k == 8) OS(5);
            if constexpr (k == 23) OS(6);
            if constexpr (k == 24) OS(7);
            if constexpr (k == 43) OS(8);
            if constexpr (k == 44) OS(9);
            if constexpr (k == 59) OS(10);
            if constexpr (k == 63) OS(11);
            if constexpr (k == 7) asm volatile("s_waitcnt vmcnt(8)\n\ts_barrier" ::: "memory");
            if constexpr (k == 23) asm volatile("s_waitcnt vmcnt(4)\n\ts_barrier" ::: "memory");
            if constexpr (k == 30) { pa = pvo + near_(p_item(s), (u32)L, pol); PIN(pa); }
            if constexpr (k == 43) asm volatile("s_waitcnt vmcnt(0)\n\ts_barrier" ::: "memory");
            if constexpr (k == OSAMP) LD4A(fv, ofl, 0);
            if constexpr (k == 62) {
                asm volatile("s_waitcnt vmcnt(0)" : "+v"(fv) :: "memory");
                *(LAS int*)selw = (int)((u32)__builtin_amdgcn_ballot_w64(fv < (u32)s + 2) & 255u);
            }
            SCHED();
        SEND
        asm volatile("s_nop 11" : "+v"(acc));
        u32 pc[8];
#pragma unroll
        for (int i = 0; i < 8; ++i) pc[i] = pk(acc[2 * i], acc[2 * i + 1]);
        if (ng != 4) {
#pragma unroll
            for (int i = 0; i < 4; ++i) *(LAS v2u*)(tw + i * 32) = (v2u){pc[2 * i], pc[2 * i + 1]};
        } else {
#pragma unroll
            for (int i = 0; i < 8; ++i) *(LAS u32*)(tw + (i >> 1) * 64 + (i & 1) * 16) = pc[i];
        }
        BAR();
        const v4u p0 = lds_r16(tr), p1 = lds_r16(tr + 4 * 272);
        rdy = *(LAS int*)(lds + OU_SEL);
        st16(rs, pa, p0); st16(rs, pa + 1024, p1);
        const bool ready = uni(rdy) == 0;
        SCHED();
        OS(12);
        if (ready) pfo = fo;
        else {
            LD4A(fv, ofl, 0);
            asm volatile("s_waitcnt vmcnt(1)\n\ts_barrier" ::: "memory");
            st4s(rs, fo, 0, (u32)s + 1); pfo = OOB;
            OS(14);
            LS2(L == 0 && w == 1, 160 + kq * 5 + ng);
            asm volatile("s_waitcnt vmcnt(1)" : "+v"(fv) :: "memory");
            if (w == 0 && s + 1 < NSEG && ((u32)__builtin_amdgcn_ballot_w64(fv < (u32)s + 2) & 255u) != 0u) wait_flags(rs, ofl, (u32)s + 2, dead);
            BAR();
        }
        OS(13);
        buf ^= 65536u;
    }
}

template <int L> DEV void role_row(const Params& p, lptr lds, int ri) {
    const int lane = threadIdx.x & 63, w = uni(threadIdx.x >> 6);
    const rsrc_t rs = mk_rsrc(p.ws, W_END);
    bool dead = false;
    const u32 pol = get_pol(p, rs, dead);
    const u32 fp = threadIdx.x < 20 ? near_(foff(F_P + L * 32 + lane), 1u, pol) : OOB, fo = threadIdx.x == 192 && L == 0 ? near_(foff(F_H2 + (ri >> 2)), 1u, pol) : OOB;
    u32 fv = 0;
    MK_SRD();
    auto yfill = [&](int b, int i) { DMA32(uu((b & 1) * 40960 + w * 10240 + i * 2048), (u32)lane * 16u, uu(W_Y + (u32)((ri >> 1) * 8 + b) * 20480u + (w & 1) * 10240 + i * 2048)); };
    auto ywait = [&](u32 v) {
        wait_flags(rs, threadIdx.x < (v == 3u ? 36u : 160u) ? foff(F_CV + threadIdx.x) : OOB, v, dead);
    };
    v4u y8[10] = {};
    v2u xa, xb; u32 xc;
    const v4u sx = L == 0 ? mk_srd(p.x, ACT) : mk_srd(p.ws + W_X2, ACT);
    auto xload = [&](int s) {
        asm volatile("buffer_load_dwordx2 %0, %3, %5, %6 offen sc1\n\tbuffer_load_dwordx2 %1, %3, %5, %6 offen offset:512 sc1\n\tbuffer_load_dword %2, %4, %5, %6 offen offset:1024 sc1"
                     : "=&v"(xa), "=&v"(xb), "=&v"(xc) : "v"((u32)lane * 8u), "v"((u32)lane * 4u), "s"(sx), "s"(uu((u32)(s * 32 + ri) * (DM * 2))) : "memory");
        SCHED();
    };
    ywait(1u);
    BAR();
    EV(15);
#pragma unroll 1
    for (int i = 0; i < 5; ++i) yfill(0, i);
    if (L == 1) wait_flags(rs, lane == 0 ? near_(foff(F_H2 + (ri >> 2)), 1u, pol) : OOB, 1u, dead);
    xload(0);
    LD4A(fv, fp, 0);
    VMW(0); SCHED();
    asm volatile("" : "+v"(xa), "+v"(xb), "+v"(xc), "+v"(fv));
    BAR();
    for (int s = 0; s < NSEG; ++s) {
        u32 xi[5] = {xa.x, xa.y, xb.x, xb.y, xc};
        u32 pa = near_(p_item(s), (u32)L, pol) + (u32)ri * 256u, pc = pa + 65536u + (u32)(lane >> 4) * 81920u + (u32)(lane & 15) * 16u;
        pa += (u32)(lane >> 5) * 81920u + (u32)((lane >> 4) & 1) * 16384u + (u32)(lane & 15) * 16u;
        u32 yf = s == 1 ? foff(F_CV + min(threadIdx.x, 159u)) : s == 25 ? foff(F_CV + min(lane, 35)) : OOB;
        asm volatile("" : "+v"(xi[0]), "+v"(xi[1]), "+v"(xi[2]), "+v"(xi[3]), "+v"(xi[4]), "+v"(pa), "+v"(pc), "+v"(yf));
#define RS(k) do {} while (0)
        RS(0);
        if (w == 0 && ((u32)__builtin_amdgcn_ballot_w64(fv <= (u32)s) & 0xFFFFFu) != 0u) wait_flags(rs, fp, (u32)s + 1, dead);
        BAR();
        RS(1);
        v4u d[5];
        asm volatile("buffer_load_dwordx4 %0, %5, %7, 0 offen sc1\n\tbuffer_load_dwordx4 %1, %5, %7, %8 offen sc1\n\tbuffer_load_dwordx4 %2, %5, %7, %9 offen sc1\n\t"
                     "buffer_load_dwordx4 %3, %5, %7, %10 offen sc1\n\tbuffer_load_dwordx4 %4, %6, %7, 0 offen sc1"
                     : "=&v"(d[0]), "=&v"(d[1]), "=&v"(d[2]), "=&v"(d[3]), "=&v"(d[4]) : "v"(pa), "v"(pc), "s"(srd), "s"(163840), "s"(32768), "s"(196608) : "memory");
        RS(2);
        LS2(L == 0, 192 + ri);
        SCHED();
        f32x2 y[5];
        {
            if ((s & 7) == 0) {
                lptr ya = lds + ((s >> 3) & 1) * 40960 + (w >> 1) * 20480 + (w & 1) * 16, yc = ya + 4096 + (lane >> 1) * 32 + (lane & 1) * 10240;
#pragma unroll
                for (int i = 0; i < 8; ++i) y8[i] = lds_r16(ya + lane * 32 + (i >> 2) * 2048 + (i & 3) * 5120);
                y8[8] = lds_r16(yc); y8[9] = lds_r16(yc + 5120);
            }
#pragma unroll
            for (int i = 0; i < 10; ++i) {
                y[i >> 1][i & 1] = lo_f(y8[i].x);
                y8[i] = (v4u){__builtin_amdgcn_alignbit(y8[i].y, y8[i].x, 16), __builtin_amdgcn_alignbit(y8[i].z, y8[i].y, 16), __builtin_amdgcn_alignbit(y8[i].w, y8[i].z, 16), y8[i].w >> 16};
                PIN(y8[i]);
            }
            asm volatile("" : "+v"(y[0]), "+v"(y[1]), "+v"(y[2]), "+v"(y[3]), "+v"(y[4]));
        }
        u32 yv;
        {
            xload(s + 1);
            const bool yy = (s & 7) - 2u < 5u && s < NSEG - 8;
            u32 yo = yy ? (u32)lane * 16u : OOB;
            DMA32(uu(yy ? (((s >> 3) + 1) & 1) * 40960 + w * 10240 + ((s & 7) - 2) * 2048 : 81920), yo, uu(W_Y + (u32)((ri >> 1) * 8 + (s >> 3) + 1) * 20480u + (w & 1) * 10240 + ((s & 7) - 2) * 2048));
            LD4A(yv, yf, 0);
            asm volatile("s_waitcnt vmcnt(6)" : "+v"(d[0]), "+v"(d[1]), "+v"(d[2]), "+v"(d[3]), "+v"(d[4]));
        }
        f32x2 o[5];
        {
            f32x2 t[5][2];
#pragma unroll
            for (int i = 0; i < 5; ++i) {
                const auto e = __builtin_amdgcn_permlane32_swap(d[i].x, d[i].z, false, false), f = __builtin_amdgcn_permlane32_swap(d[i].y, d[i].w, false, false);
                t[i][0] = up2(e[0]) + up2(e[1]); t[i][1] = up2(f[0]) + up2(f[1]);
            }
            o[0] = t[0][0] + t[1][0]; o[1] = t[0][1] + t[1][1]; o[2] = t[2][0] + t[3][0]; o[3] = t[2][1] + t[3][1];
            const auto e = __builtin_amdgcn_permlane16_swap(u_(t[4][0].x), u_(t[4][1].x), false, false), f = __builtin_amdgcn_permlane16_swap(u_(t[4][0].y), u_(t[4][1].y), false, false);
            o[4] = (f32x2){f_(e[0]) + f_(e[1]), f_(f[0]) + f_(f[1])};
        }
        f32x2 x1[5];
#pragma unroll
        for (int i = 0; i < 5; ++i) x1[i] = up2(pk2(up2(xi[i]) + up2(pk2(o[i]))));
        asm volatile("" : "+v"(x1[0]), "+v"(x1[1]), "+v"(x1[2]), "+v"(x1[3]), "+v"(x1[4]));
        RS(3);
        const f32x2 st1 = ln_stats(x1);
        u32 xp[5];
#pragma unroll
        for (int i = 0; i < 5; ++i) xp[i] = pk2(x1[i] + ((x1[i] - st1.x) * st1.y) * y[i]);
        const int row = s * 32 + ri;
        RS(4);
        if (L == 0) {
            const u32 ro = (u32)row * (DM * 2);
            f32x2 x2[5];
#pragma unroll
            for (int i = 0; i < 5; ++i) x2[i] = up2(xp[i]);
            st8s(rs, (u32)lane * 8, W_X2 + ro, (v2u){xp[0], xp[1]});
            st8s(rs, (u32)lane * 8 + 512, W_X2 + ro, (v2u){xp[2], xp[3]});
            st4s(rs, (u32)lane * 4 + 1024, W_X2 + ro, xp[4]);
            SCHED();
            const f32x2 st2 = ln_stats(x2);
            u32 hp[5];
#pragma unroll
            for (int i = 0; i < 5; ++i) hp[i] = pk2((x2[i] - st2.x) * st2.y);
            store_h(rs, near_(W_H2 + (u32)s * H_TILE, 1u, pol), row, hp);
        } else {
            u16* o_ = p.out + (size_t)row * DM;
            *(v2u*)(o_ + 4 * lane) = (v2u){xp[0], xp[1]};
            *(v2u*)(o_ + 256 + 4 * lane) = (v2u){xp[2], xp[3]};
            *(u32*)(o_ + 512 + 2 * lane) = xp[4];
        }
        RS(5);
        LD4A(fv, fp, 0);
        if (L == 0) asm volatile("s_waitcnt vmcnt(1)\n\ts_barrier" ::: "memory");
        st4s(rs, fo, 0, (u32)s + 1);
        RS(6);
        VMW(1); SCHED();
        asm volatile("" : "+v"(fv), "+v"(yv), "+v"(xa), "+v"(xb), "+v"(xc));
        if (__builtin_expect(s == 1 || s == 25, 0) && !all_(yv >= (s == 1 ? 2u : 3u))) ywait(s == 1 ? 2u : 3u);
        RS(7);
        if (ri == 0) LST(320 + L * 64 + s, rt32());
        LS2(L == 0, 224 + ri);
    }
}

constexpr float SCALE = 0.08838834764831845f, LOG2E = 1.4426950408889634f;
constexpr int C_K = 0, C_V = 8192, C_QX = 16384, C_MEM = 24576, C_Q = C_MEM + 10240, C_KM = C_Q + 2304, C_VM = C_KM + 2304, C_DUMP = C_VM + 2048,
              C_ZERO = C_DUMP + 1024, C_WL = C_ZERO + 128;
constexpr int NLF = 14;
constexpr int C_MASK = C_WL + 4 * NLF * 2048;
static_assert(C_WL % 16 == 0 && C_MASK + 3072 <= LDS_BYTES, "head lds");
#define CH_H0 98
#define CH_W0 80
#define CH_MH 11
#define CH_MW 18
#define CH_REC 12
#define CH_LATE 30
#define CH_SOLO 6
#define CH_KV 12
#define CH_KV0 60
#define CH_M0 3
#define CH_DEC 1u
#define CH_EARLY 10
#define CH_NEAR 4
#define CH_UP 3
#define CH_WX 6
DEV void wait_until(u32 t) {
    unsigned long long c;
    asm volatile("1:\n\ts_memrealtime %0\n\ts_waitcnt lgkmcnt(0)\n\ts_mov_b64 vcc, %0\n\ts_sub_u32 vcc_lo, %1, vcc_lo\n\ts_cmpk_lt_u32 vcc_lo, 400\n\ts_cbranch_scc1 1b" : "=&s"(c) : "s"(uu(t - 1u)) : "vcc", "scc", "memory");
}
#define CH_FD 14
#define CH_FC 34
#define FINE_T1(t) uu((t) - (CH_FD + 1u))
#define FINE_T4(t) uu(4u * (t) - CH_FC)
#define NOP8 "s_nop 0\n\ts_nop 0\n\ts_nop 0\n\ts_nop 0\n\ts_nop 0\n\ts_nop 0\n\ts_nop 0\n\ts_nop 0\n\t"
#define LOOP_TXT(tmp, t1) "s_memrealtime vcc\n1:\n\ts_waitcnt lgkmcnt(0)\n\ts_sub_u32 " tmp ", " t1 ", vcc_lo\n\ts_memrealtime vcc\n\ts_cmpk_lt_u32 " tmp ", 400\n\ts_cbranch_scc1 1b\n\t"
#define WAIT_TXT(tmp, t1, t4) LOOP_TXT(tmp, t1) \
    "s_nop 1\n\ts_memrealtime s[90:91]\n\ts_nop 3\n\ts_memrealtime s[92:93]\n\ts_nop 3\n\ts_memrealtime s[94:95]\n\ts_getpc_b64 s[96:97]\n2:\n\ts_waitcnt lgkmcnt(0)\n\t" \
    "s_add_u32 s90, s90, vcc_lo\n\ts_add_u32 s92, s92, s94\n\ts_sub_u32 s90, " t4 ", s90\n\ts_sub_u32 s90, s90, s92\n\ts_max_i32 s90, s90, 0\n\ts_min_i32 s90, s90, 29\n\t" \
    "s_mul_i32 s90, s90, 87\n\ts_lshr_b32 s90, s90, 4\n\ts_andn2_b32 s90, s90, 3\n\ts_sub_u32 s90, 3f-2b, s90\n\ts_add_u32 s96, s96, s90\n\ts_addc_u32 s97, s97, 0\n\t" \
    "s_setpc_b64 s[96:97]\n\t" NOP8 NOP8 NOP8 NOP8 NOP8 "3:\n\t"
#define WAIT_CLOB "vcc", "scc", "s90", "s91", "s92", "s93", "s94", "s95", "s96", "s97", "memory"
#define SMF_A(acc, a, b) asm volatile("v_smfmac_f32_16x16x64_bf16 %0, %1, %2, %3" : "+v"(acc) : "v"(a), "a"(b), "v"(idx))
#define SMF_V(acc, a, b) asm volatile("v_smfmac_f32_16x16x64_bf16 %0, %1, %2, %3" : "+v"(acc) : "v"(a), "v"(b), "v"(idx))
#define MF0(d, a, b) asm volatile("v_mfma_f32_16x16x32_bf16 %0, %1, %2, 0" : "=&v"(d) : "v"(a), "v"(b))
#define MF1(d, a, b) asm volatile("v_mfma_f32_16x16x32_bf16 %0, %1, %2, %0" : "+v"(d) : "v"(a), "v"(b))
#define MF0H(d, a, b) asm volatile("v_mfma_f32_16x16x16_bf16 %0, %1, %2, 0" : "=&v"(d) : "v"(a), "v"(b))
#define MF1H(d, a, b) asm volatile("v_mfma_f32_16x16x16_bf16 %0, %1, %2, %0" : "+v"(d) : "v"(a), "v"(b))
DEV v8u tr_frag(lptr p0, lptr p1, lptr p2, lptr p3) { return cat8(tr2(p0, p1), tr2(p2, p3)); }
constexpr int qslot(int ks) { return (ks & 1) && ks < 8 ? ks >> 1 : -1; }
constexpr int kslot(int ks) { return ks < 4 ? 4 + ks : -1; }
constexpr int vslot(int ks) { return ks >= 4 ? 4 + ks : -1; }
DEV u32 ring_map(u32 off, u32 half) { return ((off >> 13) << 14) + (half << 13) + (off & 8191u); }
#define ZRD(x, i) x = __builtin_bit_cast(f32x4, lds_r16(ZP + 16 * (i)))
#define VMAX(d, a, b) asm volatile("v_max_f32 %0, %1, %2" : "=v"(d) : "v"(a), "v"(b))
#define VMAX3(d, a, b, c_) asm volatile("v_max3_f32 %0, %1, %2, %3" : "=v"(d) : "v"(a), "v"(b), "v"(c_))
#define VADD(d, a, b) asm volatile("v_add_f32 %0, %1, %2" : "=v"(d) : "v"(a), "v"(b))
#define VEXP(d, a) asm volatile("v_exp_f32 %0, %1" : "=v"(d) : "v"(a))
#define VFMC(d, a, b) asm volatile("v_fmamk_f32 %0, %1, 0x3e0293ee, %2" : "=v"(d) : "v"(a), "v"(b))
#define VMULC(d, a) asm volatile("v_mul_f32 %0, 0x3e0293ee, %1" : "=v"(d) : "v"(a))
#define SEL(d, a, b) asm volatile("v_cndmask_b32 %0, %1, %2, %3" : "=v"(d) : "v"(a), "v"(b), "s"(himask))
#define QPERM(d, a, q) asm volatile("v_mov_b32_dpp %0, %1 quad_perm:" q " row_mask:0xf bank_mask:0xf" : "=v"(d) : "v"(a))

#define SUMRANGE 0x28000000u
#define HSAMP 71
DEV void role_head(const Params& p, lptr lds, int L, int h) {
    const int tid = threadIdx.x, lane = tid & 63, w = uni(tid >> 6), g = lane >> 4, r = lane & 15;
    const rsrc_t rs = mk_rsrc(p.ws, W_END);
    v8u wq0[10], wq1[10], wk0[10], wk1[10], wv0[10], wv1[10];
    lptr WL = lds + C_WL + w * (NLF * 2048) + lane * 16;
    {
        constexpr int GS = 1056, CS = 48 * GS;
        const v4u sq = mk_srd(p.Wq, DM * NQ * 2), sk = mk_srd(p.Wk, DM * NQ * 2), sv = mk_srd(p.Wv, DM * NQ * 2);
        const u32 vo = (u32)(g * NQ + h * DH + 8 * (r ^ ((g & 2) << 2))) * 2u;
        auto fill = [&](int ch) {
#pragma unroll 1
            for (int i = 0; i < 4; ++i) {
                const u32 dst = uu((ch % 3) * CS + (4 * i + w) * GS), so = uu((ch * 64 + 16 * i + 4 * w) * (NQ * 2));
                DMAIN(sq, dst, vo, so); DMAIN(sk, dst + 16 * GS, vo, so); DMAIN(sv, dst + 32 * GS, vo, so);
            }
        };
        int vote = 0;
        if (L == 1) {
            if (w == 0) vote = probe(rs, h);
            BAR();
            EV(2);
            pre_rings(p, rs, h);
        }
        fill(0); fill(1); fill(2);
        const int q4 = r >> 2;
        lptr tb = lds + (4 * (g >> 1) + q4) * GS + (g & 1) * 512 + ((w * 64 + (r & 3) * 8) ^ ((g & 1) << 7));
        SFOR(ch, 10)
            VMW(ch == 1 || ch == 2 ? 25 : ch < 8 ? 24 : 108 - 12 * ch);
            BAR();
            if constexpr (ch == 0) boot_set(p, rs, tid == 0 && L == 1 ? foff(F_RG + h) : OOB, (u32)((1 << 20) + vote));
            auto frag = [&](int m, int t) { lptr b = tb + (ch % 3) * CS + m * 16 * GS + t * 32; return tr_frag(b, b + 256, b + 8 * GS, b + 8 * GS + 256); };
            wq0[ch] = frag(0, 0); wq1[ch] = frag(0, 1); wk0[ch] = frag(1, 0); wk1[ch] = frag(1, 1); wv0[ch] = frag(2, 0); wv1[ch] = frag(2, 1);
            asm volatile("" : "+a"(wq0[ch]), "+a"(wk0[ch]), "+v"(wv0[ch]), "+v"(wv1[ch]));
            if constexpr (qslot(ch) < 0) asm volatile("" : "+a"(wq1[ch])); else asm volatile("" : "+v"(wq1[ch]));
            if constexpr (kslot(ch) < 0) asm volatile("" : "+a"(wk1[ch])); else asm volatile("" : "+v"(wk1[ch]));
            BAR();
            if constexpr (ch < 7) fill(ch + 3);
        SEND
#pragma unroll
        for (int ch = 0; ch < 10; ++ch) {
            if (qslot(ch) >= 0) { lds_w16(WL + qslot(ch) * 2048, wq1[ch].lo); lds_w16(WL + qslot(ch) * 2048 + 1024, wq1[ch].hi); }
            if (kslot(ch) >= 0) { lds_w16(WL + kslot(ch) * 2048, wk1[ch].lo); lds_w16(WL + kslot(ch) * 2048 + 1024, wk1[ch].hi); }
            if (vslot(ch) >= 0) { lds_w16(WL + vslot(ch) * 2048, wv1[ch].lo); lds_w16(WL + vslot(ch) * 2048 + 1024, wv1[ch].hi); }
        }
        CFENCE();
    }
    const u32 idx = (r & 2) ? 0xEEEEu : 0x4444u;
    const unsigned long long himask = 0xCCCCCCCCCCCCCCCCull;
    bool dead = false;
    EV(6);
    const u32 pol = get_pol(p, rs, dead);
    const u32 pol0 = pol & 1u;
    const u32 l16 = (u32)lane * 16u;
    const u32 mring = W_MRING + (u32)L * (MB / 4) + pol0 * 8192u, hint = mring + 4 * 32768u;
    const u32 fkh = lane < 3 ? near_(foff(F_QKV + lane * 64 + L * 32 + h), 0u, pol) : lane < 6 ? hint + (u32)(lane - 3) * 4u : OOB;
    u32 tm = 0;
    MK_SRD();
    const unsigned long long kmask = __builtin_amdgcn_ballot_w64((hblk(W_KQ, L * NSEG + lane, h, 0u, pol) & 8192u) != 0u),
                             vmask = __builtin_amdgcn_ballot_w64((hblk(W_VO, L * NSEG + lane, h, 1u, pol) & 8192u) != 0u);
    u32 kb = uu(W_KQ + (u32)(L * NSEG * NH + h) * 16384u + w * 2048);
    u32 dk = uu(C_K + w * 2048), dv = dk + (C_V - C_K), dq = dk + (C_QX - C_K), dm = uu(C_MEM + w * 2048), dm2 = uu(w < 2 ? C_MEM + 8192 + w * 1024 : C_DUMP);
    asm volatile("" : "+s"(dk), "+s"(dv), "+s"(dq), "+s"(dm), "+s"(dm2));
    u32 ksrc = kb + ((u32)kmask & 1u) * 8192u, vsrc = kb + (W_VO - W_KQ) + ((u32)vmask & 1u) * 8192u;
    lptr qw = lds + C_Q + (2 * g) * 288 + (32 * w + 2 * r) * 2;
    lptr kw = qw + (C_KM - C_Q);
    lptr qr = lds + C_Q + (2 * (r >> 2) + (r & 1)) * 288 + g * 16;
    lptr LB = lds + l16, VX = lds + C_V + w * 2048 + l16;
    if (tid >= 64 && tid < 70) { const u32 z = tid == 68 ? 0x3F803F80u : tid == 69 ? PSENT : 0u; lds_w16(lds + C_ZERO + (tid - 64) * 16, (v4u){z, z, z, z}); }
    if (w < 3) {
        const u32 ng = u_(-1000000000.0f * LOG2E);
        v4u mk;
#pragma unroll
        for (int i = 0; i < 4; ++i) mk[i] = w == 0 || (w == 1 && 4 * g + i > r) ? ng : 0u;
        lds_w16(lds + C_MASK + w * 1024 + l16, mk);
    }
    u32 zaddr = 0; PIN(zaddr);
    lptr ZP = lds + zaddr + C_ZERO;
#define LF_RD(slot, hf) lf[slot][hf] = lds_r16(WL + (slot) * 2048 + (hf) * 1024)
    BAR();
    const u32 ofl = tid == 192 ? near_(foff(F_O + L * 32 + h), 0u, pol) : tid == 193 && (h & 15) == 0 ? hint + 128u + (u32)h * 8u : OOB;
    u32 oso = (g & 1) ? OOB : W_ORING + (u32)L * (MB / 2) + ring_map((u32)((r >> 3) * 32768 + h * 1024 + w * 256 + (g >> 1) * 128 + (r & 7) * 16), pol0);
    u32 missed = 0, dirty = 1, rep = PSENT;
    asm volatile("" : "+v"(oso), "+v"(qw), "+v"(kw), "+v"(qr), "+v"(VX));
    asm volatile("" : "+s"(ksrc), "+s"(vsrc));
    wait_flags(rs, lane < 3 ? fkh : OOB, 1u, dead);
    EV(7);
    DMA32(dv, l16, vsrc); DMA32(dk, l16, ksrc);
    EV(8);
    auto next_slot = [&](u32 now) {
        const u32 hv = ld4(rs, fkh), per = rdl(hv, 4);
        if (per < 400u) { tm = rdl(hv, 5) - per; for (int n = 0; n < 3 && (int)(tm - now) < 0; ++n) tm += per; }
    };
    if (L == 1) { next_slot(rt32() + CH_KV0); tm -= 24u; }
    for (int s = 0; s < NSEG;) {
        {
            *(LAS u32*)(lds + C_DUMP + 128 + w * 4) = tm;
            VMW(5); BAR();
            st4s(rs, ofl, 0, (l16 & 16u) ? rep : (u32)s);
            const v4u c = lds_r16(lds + C_DUMP + 128) - tm;
            const u32 add = uu((u32)max(max((int)c.x, (int)c.y), max((int)c.z, (int)c.w)));
            tm += add;
            if (__builtin_expect((dirty | add) != 0u, 0)) {
                wait_until(tm - 44u);
                st16(rs, oso, lds_r16(ZP + 80));
            }
        }
        u32 ms = mring + (u32)(s & 3) * 32768u + w * 2048, ms2 = mring + (u32)(s & 3) * 32768u + 16384u + (w & 1) * 1024;
        u32 l3 = w < 2 ? l16 : OOB;
        asm volatile("" : "+s"(ms), "+s"(ms2)); PIN(l3);
        f32x4 aq0, aq1, ak0, ak1;
        v4u lf[NLF][2];
        ZRD(aq0, 0); ZRD(aq1, 1); ZRD(ak0, 2); ZRD(ak1, 3);
#pragma unroll
        for (int i = 0; i < 4; ++i) { LF_RD(i, 0); LF_RD(i, 1); }
        SCHED();
        v4u A[10];
#define HT(k) do {} while (0)
        v4u kx[8], qf[4], kmf[4], vx[2], ones, P;
        float fa, fb, fc, fd, sx[4], t[4], sm0, sm1, tm0, tm1, inv; u32 pa, pb, fl, pmm, tmp = 0; unsigned long long odd;
        f32x4 av0, av1, s0, s1, sm, oa, ob, rsum;
        v2u vm, pm, vh, vl, on[2];
        f32x2 zz[4];
#define LW(sl) cat8(lf[sl][0], lf[sl][1])
#define PQ(ks, tl) do { if constexpr (!(tl)) SMF_A(aq0, A[ks], wq0[ks]); else if constexpr (qslot(ks) < 0) SMF_A(aq1, A[ks], wq1[ks]); else { const v8u lw_ = LW(qslot(ks)); SMF_V(aq1, A[ks], lw_); } } while (0)
#define PK(ks, tl) do { if constexpr (!(tl)) SMF_A(ak0, A[ks], wk0[ks]); else if constexpr (kslot(ks) < 0) SMF_A(ak1, A[ks], wk1[ks]); else { const v8u lw_ = LW(kslot(ks)); SMF_V(ak1, A[ks], lw_); } } while (0)
#define PV(ks, tl) do { if constexpr (!(tl)) SMF_V(av0, A[ks], wv0[ks]); else if constexpr (vslot(ks) < 0) SMF_V(av1, A[ks], wv1[ks]); else { const v8u lw_ = LW(vslot(ks)); SMF_V(av1, A[ks], lw_); } } while (0)
#define KXR(i) kx[i] = lds_r16(LB + C_K + ((i) & 1) * 4096 + ((i) >> 1) * 1024)
#define XW(q_) do { *(LAS u32*)(q_) = pa; *(LAS u32*)((q_) + 288) = pb; } while (0)
        auto slot = [&](auto zc) __attribute__((always_inline)) {
            constexpr int z = decltype(zc)::value;
            constexpr int vs[] = {0, 8, 1, 10, 9, 2, 11, -1, -1, -1, -1, 4, 6, 12, -1, -1, -1, 13, 14, -1, 16, 18, 15, 3, 5, 17, 7, 19};
            if constexpr (z < 12) PQ(z >> 1, z & 1);
            else if constexpr (z < 20) PQ(6 + (z & 3), z >> 4);
            else if constexpr (z < 32) PK((z - 20) >> 1, z & 1);
            else if constexpr (z < 40) PK(6 + (z & 3), (z >> 2) & 1);
            else if constexpr (z < 42) { if constexpr (z == 40) MF0(s0, kx[0], qf[0]); else MF0(s1, kx[1], qf[0]); }
            else if constexpr (z < 48) { if constexpr (z & 1) MF1(s1, kx[z - 40], qf[(z - 40) >> 1]); else MF1(s0, kx[z - 40], qf[(z - 40) >> 1]); }
            else if constexpr (z == 55) MF0(sm, kmf[0], qf[0]);
            else if constexpr (z > 55 && z < 59) MF1(sm, kmf[z - 55], qf[z - 55]);
            else if constexpr (z == 62) MF0(rsum, ones, P);
            else if constexpr (z == 63) MF0(oa, vx[0], P);
            else if constexpr (z == 64) MF0(ob, vx[1], P);
            else if constexpr (z == 67) { const v2u ones2 = ones.lo; MF1H(rsum, ones2, pm); }
            else PV(vs[z - 48] >> 1, vs[z - 48] & 1);
#define USE(x) asm volatile("" :: "v"(x))
            if constexpr (z == 20 || z == 24 || z == 26) USE(lf[4 + ((z - 20) >> 1)][1]);
            if constexpr (z == 41) USE(qf[3]);
            if constexpr (z == 45 || z == 46) USE(kx[z - 39]);
            if constexpr (z == 53) USE(lf[9][1]);
            if constexpr (z >= 54 && z < 58) USE(kmf[z - 54]);
            if constexpr (z == 61) USE(vl);
            if constexpr (z == 64) USE(lf[10][1]);
            if constexpr (z == 69) USE(lf[11][1]);
            if constexpr (z == 72) USE(lf[12][1]);
            if constexpr (z == 74) USE(lf[13][1]);
            if constexpr (z < 8) A[2 + z] = lds_r16(LB + C_MEM + (2 + z) * 1024);
            if constexpr (z >= 15 && z < 23) LF_RD(4 + ((z - 15) >> 1), (z - 15) & 1);
            if constexpr (z >= 24 && z < 27) KXR(z - 24);
            if constexpr (z == 27) XW(qw);
            if constexpr (z >= 28 && z < 31) KXR(z - 25);
            if constexpr (z == 32) asm volatile("s_waitcnt lgkmcnt(3)\n\ts_barrier" ::: "memory");
            if constexpr (z >= 33 && z < 37) qf[z - 33] = lds_r16(qr + (z - 33) * 64);
            if constexpr (z == 37 || z == 38) LF_RD(8, z - 37);
            if constexpr (z == 39 || z == 41) KXR(6 + (z == 41));
            if constexpr (z == 44) XW(kw);
            if constexpr (z == 45 || z == 46) LF_RD(9, z - 45);
            if constexpr (z == 47) vx[0] = lds_r16(VX);
            if constexpr (z == 48) { VMW(0); vh = *(LAS v2u*)(VX + 1032); }
            if constexpr (z == 54) { vl = *(LAS v2u*)(VX + 1024); vx[1] = (v4u){vh.x, vh.y, vl.x, vl.y}; }
            if constexpr (z == 49) { USE(lf[9][0]); asm volatile("s_barrier" ::: "memory"); }
            if constexpr (z >= 50 && z < 54) kmf[z - 50] = lds_r16(qr + (C_KM - C_Q) + (z - 50) * 64);
            if constexpr (z == 59 || z == 60) LF_RD(10, z - 59);
            if constexpr (z == 61 || z == 65) LF_RD(11, z == 65);
            if constexpr (z == 66 || z == 68) LF_RD(12, z == 68);
            if constexpr (z == 69 || z == 70) LF_RD(13, z - 69);
            if constexpr (z == HSAMP) fl = ld4(rs, fkh);
            SCHED();
            if constexpr (z == 19) HT(2);
            if constexpr (z == 39) HT(3);
            if constexpr (z == 51) HT(4);
            if constexpr (z == 64) HT(5);
            if constexpr (z == 20) VADD(fa, aq0[0], aq0[2]);
            if constexpr (z == 21) VADD(fb, aq0[1], aq0[3]);
            if constexpr (z == 23) VADD(fc, aq1[0], aq1[2]);
            if constexpr (z == 24) VADD(fd, aq1[1], aq1[3]);
            if constexpr (z == 25) VCVT(pa, fa, fc);
            if constexpr (z == 26) VCVT(pb, fb, fd);
            if constexpr (z >= 44 && z < 48) asm volatile("v_mov_b64 %0, 0" : "=v"(zz[z - 44]));
            if constexpr (z == 47) { av0 = __builtin_shufflevector(zz[0], zz[1], 0, 1, 2, 3); av1 = __builtin_shufflevector(zz[2], zz[3], 0, 1, 2, 3); }
            if constexpr (z == 48 || z == 49) asm volatile("v_mov_b64 %0, %1" : "=v"(on[z - 48]) : "s"(0x3F803F803F803F80ull));
            if constexpr (z == 49) ones = __builtin_shufflevector(on[0], on[1], 0, 1, 2, 3);
            if constexpr (z == 40) { VADD(fa, ak0[0], ak0[2]); VADD(fb, ak0[1], ak0[3]); }
            if constexpr (z == 42) { VADD(fd, ak1[1], ak1[3]); VADD(fc, ak1[0], ak1[2]); }
            if constexpr (z == 43) { VCVT(pb, fb, fd); VCVT(pa, fa, fc); }
            if constexpr (z >= 50 && z < 54) SEL(sx[z - 50], s0[z - 50], s1[z - 50]);
            if constexpr (z >= 54 && z < 58) VEXP(t[z - 54], sx[z - 54]);
            if constexpr (z == 58) asm volatile("v_cvt_pk_bf16_f32 %0, %2, %3\n\tv_cvt_pk_bf16_f32 %1, %4, %5" : "=&v"(P.x), "=v"(P.y) : "v"(t[0]), "v"(t[1]), "v"(t[2]), "v"(t[3]));
            if constexpr (z == 59) QPERM(P.z, P.x, "[2,3,0,1]");
            if constexpr (z == 60) QPERM(P.w, P.y, "[2,3,0,1]");
            if constexpr (z == 61) { USE(sm); VMULC(sm0, sm[0]); }
            if constexpr (z == 62) { VEXP(tm0, sm0); VMULC(sm1, sm[1]); }
            if constexpr (z == 63) VEXP(tm1, sm1);
            if constexpr (z == 64) asm volatile("v_cvt_pk_bf16_f32 %0, %2, %3\n\tv_cndmask_b32 %1, %0, 0, %4" : "=&v"(pmm), "=v"(pm.x) : "v"(tm0), "v"(tm1), "s"(himask));
            if constexpr (z == 65) asm volatile("v_cndmask_b32 %0, 0, %1, %2" : "=v"(pm.y) : "v"(pmm), "s"(himask));
            if constexpr (z == 67) { SEL(oa[0], oa[0], ob[0]); SEL(oa[1], oa[1], ob[1]); }
            if constexpr (z == 68 || z == 69) SEL(oa[z - 66], oa[z - 66], ob[z - 66]);
            if constexpr (z == 70) asm volatile("v_sub_u32 %0, %1, %2" : "=v"(pa) : "v"(rsum[0]), "s"(0x2B800000u), "v"(rsum));
            if constexpr (z == 71) asm volatile("v_cmp_le_u32 %0, %1, %2" : "=s"(odd) : "s"(SUMRANGE), "v"(pa));
            if constexpr (z == 72) VADD(fa, av0[0], av0[2]);
            if constexpr (z == 73) { NOPS(0); VADD(fb, av0[1], av0[3]); }
            if constexpr (z == 74) VCVT(vm.x, fa, fb);
            SCHED();
        };
        asm volatile("s_mov_b32 m0, %2\n\t" WAIT_TXT("%0", "%1", "%11") "buffer_load_dwordx4 %4, %5, %6 offen sc1 lds\n\tbuffer_load_dwordx4 %4, %5, %6 offen offset:1024 sc1 lds\n\t"
                     "s_mov_b32 m0, %3\n\ts_nop 0\n\tbuffer_load_dwordx4 %8, %5, %7 offen sc1 lds\n\ts_sleep 3\n\ts_mov_b32 m0, %9\n\ts_nop 0\n\t"
                     "buffer_load_dwordx4 %4, %5, %10 offen sc1 lds\n\tbuffer_load_dwordx4 %4, %5, %10 offen offset:1024 sc1 lds\n\ts_waitcnt vmcnt(2)\n\ts_barrier"
                     : "=&s"(tmp) : "s"(FINE_T1(tm)), "s"(dm), "s"(dm2), "v"(l16), "s"(srd), "s"(ms), "s"(ms2), "v"(l3), "s"(dq), "s"(ksrc ^ 8192u), "s"(FINE_T4(tm)) : WAIT_CLOB);
#pragma unroll
        for (int ks = 0; ks < 2; ++ks) A[ks] = lds_r16(LB + C_MEM + ks * 1024);
        SCHED();
        static_for<0, 76>(slot);
        u32 fail = 0;
        if (__builtin_expect(odd != 0ull, 0)) {
            u32 mn = ~0u;
#pragma unroll 5
            for (int i = 0; i < 10; ++i) { const v4u e = lds_r16(LB + C_MEM + i * 1024); mn = min(min(min(mn, e.x ^ PSENT), e.y ^ PSENT), min(e.z ^ PSENT, e.w ^ PSENT)); }
            fail = missed < SPIN_CAP && !all_(mn != 0u) ? OOB : 0u;
            float mx = fmaxf(fmaxf(fmaxf(sx[0], sx[1]), fmaxf(sx[2], sx[3])), fmaxf(sm0, sm1));
            mx = max32(max16(fmaxf(mx, dpp<0x4E>(mx))));
#pragma unroll
            for (int i = 0; i < 4; ++i) t[i] = __builtin_amdgcn_exp2f(sx[i] - mx);
            P.x = pk(t[0], t[1]); P.y = pk(t[2], t[3]); P.z = u_(dpp<0x4E>(f_(P.x))); P.w = u_(dpp<0x4E>(f_(P.y)));
            pmm = pk(__builtin_amdgcn_exp2f(sm0 - mx), __builtin_amdgcn_exp2f(sm1 - mx));
            pm = (r & 2) ? (v2u){0u, pmm} : (v2u){pmm, 0u};
            asm volatile("s_nop 1" : "+v"(P), "+v"(pm));
            MF0(rsum, ones, P); MF0(oa, vx[0], P); MF0(ob, vx[1], P);
            NOPS(7);
            { const v2u ones2 = ones.lo; MF1H(rsum, ones2, pm); }
#pragma unroll
            for (int i = 0; i < 4; ++i) SEL(oa[i], oa[i], ob[i]);
            asm volatile("s_nop 4" :: "v"(rsum));
            oso |= fail;
        }
        asm volatile("v_rcp_f32 %0, %3\n\ts_nop 4\n\tv_add_f32 %1, %4, %6\n\tv_add_f32 %2, %5, %7\n\tv_cvt_pk_bf16_f32 %1, %1, %2\n\ts_nop 0"
                     : "=&v"(inv), "=&v"(vm.y), "=&v"(fd) : "v"(rsum[0]), "v"(av1[0]), "v"(av1[1]), "v"(av1[2]), "v"(av1[3]));
        asm volatile("v_mfma_f32_16x16x16_bf16 %0, %1, %2, %0\n\ts_nop 6" : "+v"(oa) : "v"(vm), "v"(pm));
        {
            v4u oc;
            const f32x2 i2 = {inv, inv};
            const f32x2 u0 = oa.lo * i2, u1 = oa.hi * i2;
            asm volatile("v_cvt_pk_bf16_f32 %0, %4, %5\n\tv_cvt_pk_bf16_f32 %2, %4, %5\n\tv_cvt_pk_bf16_f32 %1, %6, %7\n\tv_cvt_pk_bf16_f32 %3, %6, %7\n\t"
                         "v_permlane16_swap_b32 %0, %2\n\ts_nop 0\n\tv_permlane16_swap_b32 %1, %3"
                         : "=&v"(oc.x), "=&v"(oc.y), "=&v"(oc.z), "=&v"(oc.w) : "v"(u0.x), "v"(u0.y), "v"(u1.x), "v"(u1.y));
            st16(rs, oso, oc);
        }
        SCHED();
        if (__builtin_expect(fail == 0u, 1)) {
        unsigned long long clk;
        asm volatile("s_memrealtime %0" : "=s"(clk));
        HT(6);
        v4u tq[4], tk[8], tv[2], px, pv;
        f32x4 c0, c1, cm, o[4], sum;
        float ex[10], cs0, cs1;
        v2u p0, p1, tvm;
        u32 oo, kvo, oz; bool kvok;
        u32 ln = l16; PIN(ln);
        lptr QX = lds + C_QX + (w & 1) * 4096 + ln, VO = lds + C_V + (w ^ 1) * 2048 + ln, VMX = lds + C_VM + w * 512 + (ln >> 1), MK = lds + C_MASK + (w & 1) * 1024 + ln;
#define TKR(i) tk[i] = lds_r16(LB + C_K + ((i) >> 2) * 4096 + ((i) & 3) * 1024)
#pragma unroll
        for (int i = 0; i < 4; ++i) tq[i] = lds_r16(QX + i * 1024);
        c0 = __builtin_bit_cast(f32x4, lds_r16(MK + 1024));
        TKR(0);
        *(LAS v2u*)VMX = vm;
        SCHED();
        SFOR(i, 12)
            if constexpr (i == 0) MF0(cm, kmf[0], tq[0]); else if constexpr (i < 4) MF1(cm, kmf[i], tq[i]);
            else if constexpr (i < 8) MF1(c0, tk[i - 4], tq[i - 4]); else MF1(c1, tk[i - 4], tq[i - 8]);
            if constexpr (i < 3) TKR(i + 1);
            if constexpr (i == 2) c1 = __builtin_bit_cast(f32x4, lds_r16(MK));
            if constexpr (i >= 3 && i < 7) TKR(i + 1);
            if constexpr (i == 7) { vx[1] = lds_r16(VX + 1024); tv[0] = lds_r16(VO); }
            if constexpr (i == 8) tv[1] = lds_r16(VO + 1024);
            SCHED();
            if constexpr (i == 0) { oo = ln + (ln & 0x300u) + ((vsrc ^ 8192u) + (u32)((w & 1) * 256)); PIN(oo); kb += NH * 16384; PINS(kb); }
            if constexpr (i == 1) { ksrc = kb + (((kmask >> ((s + 1) & 63)) & 1ull) ? 8192u : 0u); PINS(ksrc); }
            if constexpr (i == 3) { vsrc = kb + (((vmask >> ((s + 1) & 63)) & 1ull) ? W_VO - W_KQ + 8192u : W_VO - W_KQ); PINS(vsrc); }
            if constexpr (i == 6) { USE(cm); VMULC(cs0, cm[0]); }
            if constexpr (i == 8) VMULC(cs1, cm[1]);
            if constexpr (i == 9) VEXP(ex[8], cs0);
            if constexpr (i == 10) { VEXP(ex[9], cs1); VEXP(ex[0], c0[0]); }
            if constexpr (i == 11) { VEXP(ex[1], c0[1]); VEXP(ex[2], c0[2]); VEXP(ex[3], c0[3]); }
            SCHED();
        SEND
        asm volatile("v_cvt_pk_bf16_f32 %0, %6, %7\n\tv_mov_b32 %1, 0\n\tv_mov_b32 %2, 0\n\tv_cvt_pk_bf16_f32 %3, %6, %7\n\tv_cvt_pk_bf16_f32 %4, %8, %9\n\tv_cvt_pk_bf16_f32 %5, %10, %11"
                     : "=&v"(p0.x), "=&v"(p0.y), "=&v"(p1.x), "=&v"(p1.y), "=&v"(px.x), "=&v"(px.y) : "v"(ex[8]), "v"(ex[9]), "v"(ex[0]), "v"(ex[1]), "v"(ex[2]), "v"(ex[3]));
        asm volatile("s_waitcnt lgkmcnt(0)\n\ts_barrier" : "+s"(clk) :: "memory");
        tvm = *(LAS v2u*)(VMX + (512 - (w & 1) * 1024));
        pv = lds_r16(ZP + 80);
        SCHED();
#pragma unroll
        for (int i = 0; i < 4; ++i) VEXP(ex[4 + i], c1[i]);
        asm volatile("v_cvt_pk_bf16_f32 %0, %2, %3\n\tv_cvt_pk_bf16_f32 %1, %4, %5\n\ts_nop 1" : "=&v"(px.z), "=&v"(px.w) : "v"(ex[4]), "v"(ex[5]), "v"(ex[6]), "v"(ex[7]));
        {
            const v2u ones2 = ones.lo;
            const u32 now = (u32)clk;
            auto pvm = [&](auto first) __attribute__((always_inline)) {
                constexpr bool F = decltype(first)::value;
                SCHED();
                MF0(sum, ones, px); if constexpr (F) { oz = (oso & ~(3u << 17)) | (u32)((s + 3) & 3) << 17; PIN(oz); } SCHED();
                MF0(o[0], vx[0], px); if constexpr (F) { oso = (oso & ~(3u << 17)) | (u32)((s + 1) & 3) << 17; PIN(oso); } SCHED();
                MF0(o[1], vx[1], px); NOPS(1);
                MF0(o[2], tv[0], px); NOPS(0);
                MF1H(sum, ones2, p0); NOPS(1);
                MF0(o[3], tv[1], px); NOPS(1);
                MF1H(o[0], vm, p0);
                MF1H(o[1], vm, p1);
                MF1H(o[2], tvm, p0);
                asm volatile("v_sub_u32 %0, %2, %3\n\tv_rcp_f32 %1, %2" : "=&v"(pa), "=&v"(inv) : "v"(sum[0]), "s"(0x2B800000u), "v"(sum));
                MF1H(o[3], tvm, p1);
                SCHED();
            };
            pvm(IC<1>{});
            if (__builtin_expect(__builtin_amdgcn_ballot_w64(pa >= SUMRANGE) != 0ull, 0)) {
                const f32x4 m0 = __builtin_bit_cast(f32x4, lds_r16(MK + 1024)), m1 = __builtin_bit_cast(f32x4, lds_r16(MK));
                float mx = fmaxf(cs0, cs1);
#pragma unroll
                for (int i = 0; i < 4; ++i) { ex[i] = m0[i] == 0.f ? c0[i] : m0[i]; ex[4 + i] = m1[i] == 0.f ? c1[i] : m1[i]; mx = fmaxf(mx, fmaxf(ex[i], ex[4 + i])); }
                mx = max32(max16(mx));
#pragma unroll
                for (int i = 0; i < 4; ++i) px[i] = pk(__builtin_amdgcn_exp2f(ex[2 * i] - mx), __builtin_amdgcn_exp2f(ex[2 * i + 1] - mx));
                p0.x = p1.y = pk(__builtin_amdgcn_exp2f(cs0 - mx), __builtin_amdgcn_exp2f(cs1 - mx));
                asm volatile("s_nop 7" : "+v"(px), "+v"(p0), "+v"(p1));
                pvm(IC<0>{});
                NOPS(3);
            }
            asm volatile("s_nop 1" : "+v"(o[0]), "+v"(o[1]));
            st16(rs, oo, (v4u){pk2(o[0].lo * inv), pk2(o[0].hi * inv), pk2(o[1].lo * inv), pk2(o[1].hi * inv)});
            asm volatile("" : "+v"(o[2]), "+v"(o[3]));
            st16(rs, oo ^ 2048u, (v4u){pk2(o[2].lo * inv), pk2(o[2].hi * inv), pk2(o[3].lo * inv), pk2(o[3].hi * inv)});
            SCHED();
            PIN(fl); kvok = ((u32)__builtin_amdgcn_ballot_w64(fl < (u32)s + 2) & 7u) == 0u;
            kvo = l16 | (kvok ? 0u : OOB);
            DMA32(dv, kvo, vsrc); DMA32(dk, kvo, ksrc);
            st16(rs, oz, pv);
            SCHED();
            rep = (u32)s << 16 | min(missed, 1u) << 14 | min(now - tm, 0x3FFFu);
            if (h == 0 && w == 3) { LST(L * 64 + s, now); LST(128 + L * 64 + s, ((int)(rdl(fl, 3) - now) >= 4) | ((int)kvok << 1) | ((int)min(missed, 3u) << 2)); LST(640 + L * 64 + s, tm); LST(1728 + L * 64 + s, __builtin_amdgcn_s_memtime()); }
            tm = rdl(fl, 5);
            missed = missed == SPIN_CAP ? missed : 0u;
            dirty = 0;
            if (__builtin_expect(rdl(fl, 3) - now + CH_LATE > 4000u && s + 1 < NSEG, 0)) {
                dirty = 1;
#pragma unroll 1
                for (int n = 0; n < 64; ++n) {
                    CFENCE();
                    const u32 hv = ld4(rs, fkh);
                    tm = rdl(hv, 5);
                    if (rdl(hv, 3) - now + CH_LATE <= 4000u) break;
                }
            }
        }
        LS2(L == 0 && w == 0, h);
        if (__builtin_expect(!kvok, 0) && s + 1 < NSEG) {
            dirty = 1;
            wait_flags(rs, ln < 48u ? fkh : OOB, (u32)s + 2, dead);
            DMA32(dv, l16, vsrc); DMA32(dk, l16, ksrc);
            const u32 now = rt32() + CH_KV;
            if ((int)(tm - now) < 0) next_slot(now);
        }
        ++s;
        } else {
            u32 ln = l16; PIN(ln);
            oso = (ln & 256u) ? oso : oso & ~OOB;
            const u32 was = tm - tmp;
            tm = rdl(fl, 5); dirty = 1;
            if ((int)(tm - rdl(fl, 4) - was) > 24) tm -= rdl(fl, 4);
            if ((++missed & 7u) == 0u) {
                *(LAS u32*)(lds + C_DUMP + (w == 0 ? 0 : 64)) = ld4(rs, foff(F_ERR));
                BAR();
                missed = uu(*(LAS u32*)(lds + C_DUMP)) != 0u ? SPIN_CAP : missed;
                st4s(rs, missed == SPIN_CAP ? foff(F_ERR) : OOB, 0, 1u);
            }
        }
    }
    DRAIN(); BAR();
    st4s(rs, ofl, 0, (l16 & 16u) ? rep : NSEG);
}

#define WO_NI 4
#define WROT 0
DEV void role_wo(const Params& p, lptr lds, int j) {
    const int tid = threadIdx.x, lane = tid & 63, w = uni(tid >> 6), g = lane >> 4, r = lane & 15;
    const int rg = j & 1, cg = j >> 1, rot = cg & WROT;
    const rsrc_t rs = mk_rsrc(p.ws, W_END);
    v8u wo[8][5];
    {
        const v4u so_ = mk_srd(p.Wo, DM * NQ * 2);
        const int one = (0x3F2FF1F0u >> (4 * (cg & 7))) & 15;
        u32 vo[3];
#pragma unroll
        for (int i = 0; i < 3; ++i) { const int n = 64 * i + lane; vo[i] = n % 6 < 5 ? (u32)((n / 6) * (DM * 2) + cg * 80 + (n % 6) * 16) : ((n / 6) & 3) == one ? (u32)((n / 6) * (DM * 2) + (cg >> 3) * 640 + 304 + rg * 16) : OOB; }
        auto fill = [&](int b) {
            const u32 dst = uu(w * 36864 + (b % 3) * 12288), src = uu((8 * w + ((b + rot) & 7)) * (DH * DM * 2));
#pragma unroll
            for (int i = 0; i < 12; ++i) DMAIN(so_, dst + i * 1024, vo[i % 3], src + (i / 3) * (32 * DM * 2));
        };
#pragma unroll 1
        for (int b = 0; b < 3; ++b) fill(b);
        const int q4 = r >> 2;
        lptr tb = lds + w * 36864 + (32 * (g >> 1) + 16 * (q4 >> 1) + 8 * ((r >> 1) & 1) + 4 * (g & 1) + (q4 & 1)) * 96 + (r & 1) * 8;
        SFOR(b, 8)
            VMW(b < 6 ? 24 : 84 - 12 * b);
#pragma unroll
            for (int t = 0; t < 5; ++t) {
                lptr q = tb + (b % 3) * 12288 + (t == 0 ? 4 : (w + t) & 3) * 16;
                wo[b][t] = tr_frag(q, q + 192, q + 64 * 96, q + 64 * 96 + 192);
                if (t < 4) asm volatile("" : "+a"(wo[b][t])); else asm volatile("" : "+v"(wo[b][t]));
            }
            CFENCE();
            if constexpr (b < 5) fill(b + 3);
        SEND
    }
    BAR();
    EV(6);
    const u32 idx = (r & 2) ? 0xEEEEu : 0x4444u;
    bool dead = false;
    const u32 pol0 = get_pol(p, rs, dead) & 1u;
    const u32 l16 = (u32)lane * 16u;
    u32 oo[8];
#pragma unroll
    for (int i = 0; i < 8; ++i) oo[i] = uu(W_ORING + ring_map((u32)(rg * 32768 + (8 * w + ((i + rot) & 7)) * 1024), pol0));
    const u32 hint = W_MRING + 4 * 32768u + pol0 * 8192u;
    u32 hvo[2];
#pragma unroll
    for (int l = 0; l < 2; ++l) hvo[l] = lane < 2 ? hint + (u32)(l ^ 1) * (MB / 4) + (u32)lane * 4u : lane == 2 ? hint + (u32)l * (MB / 4) + 128u : lane == 3 ? foff(F_ERR) : lane == 4 ? hint + (u32)l * (MB / 4) + 256u : OOB;
    const int xo = g * 128 + (r & 7) * 16, O = 5 * cg + (r < 8 ? w : 4), q2 = 2 + (r >> 3);
    lptr xw0 = lds + (r < 8 ? 2048 + w * 4 : ((w + 1) & 3) * 512 + 4) + xo, xw1 = lds + ((w + q2) & 3) * 512 + q2 * 4 + xo;
    lptr xr = lds + (r < 8 ? w : 4) * 512 + xo;
    if (r < 8) { *(LAS u32*)xr = 0u; *(LAS u32*)(xr + 8192) = 0u; }
    const u32 mpo = (r & 4) == 0 && (r < 8 || w == 0) ? W_MRING + ring_map((u32)((O >> 1) * 256 + rg * 128 + (4 * (g & 1) + 2 * (O & 1) + (g >> 1)) * 16 + (r & 3) * 4), pol0) : OOB;
    constexpr u32 PER0 = CH_W0 + CH_MW + CH_H0 + CH_MH;
    const bool lead = j == 0 && w == 3;
    u32 hso[2] = {OOB, OOB};
    MK_SRD();
    u32 t = 0, tn = 0;
    int sn[2] = {0, 0};
    u32 hav = 4 * CH_H0, wav = 4 * CH_W0, mx = 8 * CH_M0, ok[2] = {0u, 0u}, dw[2] = {0u, 0u};
    u32 zero = 0, rep = 0xFFFFu, failed = 1, pmp = OOB, mp = OOB, mq = OOB, cw = 0, vo = l16, tp = 0, tw1 = 0, stop = 0;
    f32x4 acc[5] = {};
    if (tid < 20) *(LAS u32*)(lds + 5632 + tid * 4) = 0u;
    unsigned long long clk = 0;
    const u32 psent = PSENT;
    lptr sw = lds + 5120 + w * 4;
    asm volatile("" : "+v"(zero), "+v"(sw), "+v"(xw0), "+v"(xw1), "+v"(xr));
    auto turn = [&](auto lc) __attribute__((always_inline)) {
        constexpr int L = decltype(lc)::value, M = L ^ 1;
        u32 done, rd, dh, dn, nt, za, zn, pdur, hn; v3u hw;
        t = uu(t); tn = uu(tn); tp = uu(tp); dw[L] = uu(dw[L]); failed = uu(failed); rep = uu(rep);
        const int sc = sn[L];
        auto settle = [&](auto ic) __attribute__((always_inline)) {
            constexpr int i = decltype(ic)::value;
            if constexpr (i == 0) { pdur = (u32)clk - tp; dw[L] += max((int)((u32)clk + CH_WX - t), 0); PINS(pdur); PINS(dw[L]); }
            if constexpr (i == 1) {
                asm volatile("v_or_b32 %0, %2, %3\n\tv_cmp_ne_u32 vcc, %4, %5\n\tv_or_b32 %1, %2, %5\n\tv_cndmask_b32 %1, %6, %1, vcc" : "=&v"(za), "=&v"(zn) : "s"(failed << 31), "v"(pmp), "v"(mq), "v"(mp), "v"(OOB) : "vcc");
                done = failed ^ 1u; PINS(done);
            }
            SCHED();
        };
        auto ahead = [&]() __attribute__((always_inline)) {
            u32 h0 = rdl(hn, 0), h1 = rdl(hn, 1), h2 = rdl(hn, 2), h3 = rdl(hn, 3), h4 = rdl(hn, 4);
            asm volatile("" : "+s"(h0), "+s"(h1), "+s"(h2), "+s"(h3), "+s"(h4));
            tp = t; t = lead ? tn : (int)(h0 - t) < 0 ? h0 + h1 : h0;
            tn = nt; stop |= h3; rep = max(h2, h4); tw1 = t - 1u;
            asm volatile("" : "+s"(tp), "+s"(t), "+s"(tn), "+s"(stop), "+s"(rep), "+s"(tw1));
            SCHED();
        };
        auto books = [&](auto ic) __attribute__((always_inline)) {
            constexpr int i = decltype(ic)::value;
            if constexpr (i == 0) { rd = rep & 0xFFFFu; PINS(rd); }
            if constexpr (i == 1) { dn = wav >> 2; PINS(dn); pdur = min(pdur, dn + 8u); PINS(pdur); }
            if constexpr (i == 2) { pdur = (done & ok[M]) ? pdur : dn; PINS(pdur); }
            if constexpr (i == 3) { wav += pdur - dn; PINS(wav); }
            if constexpr (i == 4) { dh = hav >> 2; PINS(dh); nt = min(rd, dh + 8u); PINS(nt); }
            if constexpr (i == 5) { nt = rd < 0x3FFFu ? nt : dh; PINS(nt); }
            if constexpr (i == 6) { hav += nt - dh; PINS(hav); }
            if constexpr (i == 7) { mx = (u32)max((int)(mx - CH_DEC), 0) + (ok[M] & ~done) * (CH_UP * 8u); ok[M] = done; PINS(mx); }
            if constexpr (i == 8) { dh = (2u * hav + mx) / 8u + (sn[1] < 2 ? CH_MH + CH_SOLO : CH_MH); PINS(dh); }
            if constexpr (i == 9) { dn = (2u * wav + mx) / 8u + (sn[0] < CH_NEAR ? CH_MW + CH_EARLY : CH_MW); PINS(dn); }
            if constexpr (i == 10) { nt = dw[L] + dh; dh += dn; dw[L] = dn; sn[M] += (int)done; PINS(nt); PINS(dh); PINS(sn[M]); }
            if constexpr (i == 11) { rd = tn - t + (dh >> 1); PINS(rd); }
            if constexpr (i == 12) { rd = sn[M] + 1 < NSEG ? rd : 0u; PINS(rd); }
            if constexpr (i == 13) { nt = t + (u32)max((int)nt, (int)rd); PINS(nt); }
            if constexpr (i == 14) asm volatile("v_mov_b32 %0, %1" : "=v"(hw.x) : "s"(nt));
            if constexpr (i == 15) asm volatile("v_mov_b32 %0, %1" : "=v"(hw.y) : "s"(dh));
            if constexpr (i == 16) asm volatile("v_mov_b32 %0, %1" : "=v"(hw.z) : "s"(nt + dn));
            SCHED();
        };
        if (__builtin_expect(sc + 1 < NSEG, 1)) {
            u32 zb;
#define WT(k) do {} while (0)
            v4u d[8];
            {
                u32 tmp;
                asm volatile(LOOP_TXT("%1", "%2") "buffer_load_dwordx4 %0, %3, %4, %5 offen sc1\n\ts_waitcnt lgkmcnt(0)"
                             : "=&v"(d[0]), "=&s"(tmp) : "s"(uu(tw1)), "v"(vo), "s"(srd), "s"(oo[0]) : "vcc", "scc", "memory");
            }
            WT(0);
            SFOR(i, 7)
                asm volatile("buffer_load_dwordx4 %0, %1, %2, %3 offen sc1" : "=v"(d[i + 1]) : "v"(vo), "s"(srd), "s"(oo[i + 1]) : "memory");
                SCHED();
                if constexpr (i == 0) { settle(IC<0>{}); static_for<0, 2>(books); }
                if constexpr (i == 1) { settle(IC<1>{}); books(IC<2>{}); }
                if constexpr (i == 2) {
                    mp = mpo + (L * (MB / 4) + (u32)((sc + 1) & 3) * 32768u); pmp = mpo + (L * (MB / 4) + (u32)(sc & 3) * 32768u);
                    zb = mp | ok[L] << 31;
                    PIN(mp); PIN(pmp); PIN(zb);
                }
                if constexpr (i == 3) static_for<3, 7>(books);
                if constexpr (i == 4) { books(IC<7>{}); books(IC<9>{}); }
                if constexpr (i == 5) { books(IC<8>{}); books(IC<10>{}); }
                SCHED();
            SEND
            LD4A(hn, hvo[L], 0);
            *(LAS u32*)(sw + L * 8192) = zero;
            SCHED();
            WT(1);
            float f0[5], f1[5], xs; unsigned long long bad; v4u sv;
#define WSW(o_) asm volatile("v_permlane32_swap_b32 %0, %1" : "+v"(f0[o_]), "+v"(f1[o_]))
#define WAD(o_) asm volatile("v_add_f32_dpp %0, %1, %0 row_ror:8 row_mask:0xf bank_mask:0xf\n\tv_add_f32_dpp %0, %2, %3 row_ror:8 row_mask:0xf bank_mask:0xc" : "+v"(f0[o_]) : "v"(f1[o_]), "v"(f0[o_ + 1]), "v"(f1[o_ + 1]))
#define WFO(o_) do { VADD(f0[o_], acc[o_][0], acc[o_][2]); VADD(f1[o_], acc[o_][1], acc[o_][3]); } while (0)
#define WOWN(k) do { if constexpr (WO_NI + (k) < 8) SMF_V(acc[4], d[(WO_NI + (k)) & 7], wo[(WO_NI + (k)) & 7][4]); SCHED(); } while (0)
            SFOR(i, 40)
                constexpr int pc = i / 5, oc = i % 5;
                if constexpr (oc == 0) VMW((pc == 0 ? 8 : 11) - pc);
                if constexpr (oc < 4) SMF_A(acc[oc], d[pc], wo[pc][oc]); else if constexpr (pc < WO_NI) SMF_V(acc[4], d[pc], wo[pc][4]);
                SCHED();
                if constexpr (i == 0) WT(2);
                if constexpr (i == 20) WT(3);
                if constexpr (i == 35) WT(4);
                if constexpr (i == 0) st4s(rs, zb, 0, psent);
                if constexpr (i == 1) st4s(rs, za, 0, psent);
                if constexpr (i == 2) st4s(rs, zn, 0, cw);
                if constexpr (i >= 5 && i < 8) books(IC<6 + i>{});
                if constexpr (i >= 10 && i < 13) books(IC<4 + i>{});
                if constexpr (i == 13) { xs = 0.f; PIN(xs); }
                if constexpr (i == 15) { vo = l16 + (M * (MB / 2) + (u32)(sn[M] & 3) * 131072u); PIN(vo); }
                if constexpr (i == 36) NOPS(1);
                if constexpr (i == 37) NOPS(2);
                SCHED();
            SEND
            WFO(0); WFO(1);
            asm volatile("v_cmp_u_f32 %0, %1, %2" : "=s"(bad) : "v"(f0[0]), "v"(f1[0]));
            WSW(0); WSW(1); WFO(2); WAD(0); WFO(3);
            *(LAS float*)(xw0 + L * 8192) = f0[0]; SCHED();
            WSW(2); WSW(3);
            if (__builtin_expect(bad != 0ull, 0)) {
                u32 mn = ~0u;
#pragma unroll
                for (int i = 0; i < 8; ++i) { u32 e = d[i].x; PIN(e); mn = min(mn, e ^ PSENT); }
                if (all_(mn != 0u)) {
#pragma unroll
                    for (int i = 0; i < 8; ++i) { v4u e = d[i]; PIN(e); mn = min(min(mn, e.y ^ PSENT), min(e.z ^ PSENT, e.w ^ PSENT)); }
                }
                *(LAS u32*)(sw + L * 8192) = all_(mn != 0u) ? 0u : 1u;
            }
            WAD(2);
            *(LAS float*)(xw1 + L * 8192) = f0[2]; SCHED();
            WOWN(0); WOWN(1); WOWN(2); WOWN(3);
            WT(5);
            f32x4 xa; float nb;
            asm volatile("s_waitcnt lgkmcnt(0)\n\ts_barrier\n\tds_read_b128 %0, %2 offset:%4\n\tds_read_b128 %1, %3 offset:%4+5120\n\ts_nop 3"
                         : "=&v"(xa), "=&v"(sv) : "v"(xr), "v"(zero), "i"(L * 8192) : "memory");
            WT(6);
            WFO(4);
            asm volatile("s_nop 0\n\tv_permlane32_swap_b32 %1, %2\n\ts_nop 1\n\tv_add_f32_dpp %0, %2, %1 row_ror:8 row_mask:0xf bank_mask:0x3\n\ts_waitcnt lgkmcnt(1)" : "+v"(xs), "+v"(f0[4]), "+v"(f1[4]));
            asm volatile("v_add_f32 %0, %0, %4\n\tv_add_f32 %1, %5, %6\n\tv_add_f32 %0, %0, %7\n\tv_add_f32 %0, %0, %1\n\tv_cmp_u_f32 vcc, %0, %0\n\ts_nop 0\n\t"
                         "v_mov_b32_dpp %1, %0 row_shl:4 row_mask:0xf bank_mask:0xf\n\tv_cndmask_b32 %3, %8, %9, vcc\n\tv_cvt_pk_bf16_f32 %2, %0, %1"
                         : "+v"(xs), "=&v"(nb), "=&v"(cw), "=&v"(mq) : "v"(xa[0]), "v"(xa[1]), "v"(xa[2]), "v"(xa[3]), "v"(mp), "v"(OOB) : "vcc");
            st4s(rs, mq, 0, cw);
            SCHED();
            WT(7);
            asm volatile("s_memrealtime %0" : "=s"(clk));
            __builtin_amdgcn_raw_buffer_store_b96(hw, rs, hso[L], 0, 16);
#pragma unroll
            for (int q = 0; q < 5; ++q) acc[q] = __builtin_bit_cast(f32x4, lds_r16(lds + zero + (5632 + 16 * q)));
            VMW(5); SCHED(); PIN(hn);
            asm volatile("s_waitcnt lgkmcnt(5)" ::: "memory");
            SCHED(); PIN(sv);
            failed = uu(sv.x | sv.y | sv.z | sv.w);
            ahead();
            asm volatile("s_waitcnt lgkmcnt(0)" : "+s"(clk) :: "memory");
        } else {
            BAR();
            static_for<0, 2>(settle);
            st4s(rs, za, 0, psent);
            st4s(rs, zn, 0, cw);
            static_for<0, 14>(books);
            failed = 1u;
            hn = ld4(rs, hvo[L]);
            vo = l16 + (M * (MB / 2) + (u32)(sn[M] & 3) * 131072u);
            ahead();
            clk = __builtin_amdgcn_s_memrealtime();
        }
    };
#pragma unroll 1
    for (int k = 0; k < 2 * SPIN_CAP && !stop && (sn[0] + 1 < NSEG || sn[1] + 1 < NSEG); ++k) {
        turn(IC<0>{}); turn(IC<1>{});
        if (__builtin_expect(k != 0, 1)) continue;
        if (j == 0) {
            for (int n = 0; n < SPIN_CAP && !dead && sn[0] == 0; ++n) {
                CFENCE();
                if (all_(ld4(rs, W_ORING + pol0 * 8192u + (u32)w * 16384u + (u32)lane * 128u) != PSENT)) break;
                __builtin_amdgcn_s_sleep(1);
            }
            BAR();
        }
        if (lead) {
            t = rt32() + 56u; tn = t + PER0 / 2 + CH_REC;
            st16(rs, lane < 2 ? hint + (u32)lane * (MB / 4) : OOB, (v4u){lane == 0 ? t : tn, PER0, (lane == 0 ? t : tn) + (CH_W0 + CH_MW + CH_REC), 0u});
        } else {
            for (int n = 0; n < SPIN_CAP && !dead; ++n) {
                CFENCE();
                const u32 hv = ld4(rs, hvo[1]);
                t = rdl(hv, 0);
                if (rdl(hv, 1) < 400u) break;
                __builtin_amdgcn_s_sleep(1);
            }
        }
        hso[0] = (lead && lane == 0) ? hint : OOB; hso[1] = hso[0] + MB / 4;
        rep = 0xFFFFu; dw[0] = dw[1] = CH_W0 + CH_MW + CH_REC;
        tw1 = t - 1u;
    }
    if (sn[0] + 1 < NSEG || sn[1] + 1 < NSEG) st4s(rs, tid == 0 ? foff(F_ERR) : OOB, 0, 1u);
}

__global__ void __launch_bounds__(256, 1) k_main(Args args) {
    Params p;
    (Args&)p = args;
    lptr lds = (lptr)(u32)16 - 16;
    if (blockIdx.x == 0) LST(256, rt32());
    unsigned long long pc; int len;
    asm volatile("s_getpc_b64 %0\n.Lk_main_here:\n\ts_mov_b32 %1, .Lk_end_here-.Lk_main_here" : "=s"(pc), "=s"(len));
    const v4u code = ldin16(mk_rsrc((const void*)(pc & ~15ull), (u32)max(len, 0)), (((blockIdx.x >> 3) & 31u) * 256u + threadIdx.x) * 16u, 0);
    CFENCE();
    if (threadIdx.x == 0) {
        const v4u e = ld16s(mk_rsrc(p.ws, W_END), foff(F_EP), 0);
        const unsigned long long tag = (unsigned long long)e.y << 32 | (e.x + 1u);
        u32 role = ((u32)__builtin_amdgcn_s_getreg(20 | (3 << 11)) & 7u) * 32u + ((blockIdx.x >> 3) & 31u);
#pragma unroll 1
        for (int n = 0; n < 256; ++n, role = (role + 1u) & 255u)
            if (__hip_atomic_exchange((GAS unsigned long long*)(p.ws + W_SLOT) + role * 8u, tag, __ATOMIC_RELAXED, __HIP_MEMORY_SCOPE_AGENT) != tag) break;
        lds_w16(lds, (v4u){e.x + 1u, e.y, e.z, role});
    }
    __syncthreads();
    const v4u boot = lds_r16(lds);
    p.e0 = uu(boot.x); p.e1 = uu(boot.y); p.e2 = uu(boot.z);
    const int role = uni((int)boot.w), x = role >> 5, i = role & 31;
    asm volatile("" :: "v"(code));
    __syncthreads();
    LST(8192 + blockIdx.x * 16, role);
    if (x < 2) { role_head(p, lds, i >> 4, x * 16 + (i & 15)); __builtin_amdgcn_endpgm(); }
    if (x == 2) { role_wo(p, lds, i); __builtin_amdgcn_endpgm(); }
    const int no = x == 3 ? 30 : x < 6 ? 5 : 0;
    if (x > 3 && i >= 8) { role_qkv(p, lds, (x - 4) * 24 + i - 8); __builtin_amdgcn_endpgm(); }
    const bool isout = i < no;
    const int og = x == 3 ? i / 5 : x + 2, ng = i % 5, L = og >> 2, kq = og & 3;
    const int k = i - no, w = uni(threadIdx.x >> 6);
    const int j = isout ? (L == 1 ? 0 : 44) + kq * 5 + ng : x == 3 ? 36 + k : k < 2 ? 12 + 2 * x + k : x < 6 ? 24 + x : k < 5 ? 10 + 3 * x + k : 15 + 3 * x + k;
    const rsrc_t rs = mk_rsrc(p.ws, W_END);
    wait_until(rt32() + (j < NKV ? ST_KV : ST_OUT));
    if (j < NKV) {
        const int nt = j < 320 - 7 * NKV ? 8 : 7;
#pragma unroll 1
        for (int q = 0; q < 4; ++q) kv_fetch(p, j + NKV * q, uu(q * KT_BYTES));
        Filt f;
        u32 warm[3];
#pragma unroll 1
        for (int q = 0; q < nt; ++q) {
            VMW(15); BAR();
            if (q == 0) EV(2);
            kv_fetch(p, j + NKV * (q + 4), uu(((q + 4) & 7) * KT_BYTES));
            kv_tile(p, rs, lds + q * KT_BYTES, lds + (q == 0 ? 8 : q - 1) * KT_BYTES, j + NKV * q);
        }
        const int g = j < 20 ? j : j < 28 ? j + 16 : j - 8;
        filt_load<5, 0>(p, g, f);
        VMW(10); BAR();
        boot_set(p, rs, threadIdx.x == 0 ? foff(F_KV + j) : OOB, 2u);
        EV(4);
        { bool dead = false; boot_wait(p, rs, pol_off(), 0u, dead); }
#pragma unroll
        for (int q = 0; q < 3; ++q)
            warm[q] = __builtin_amdgcn_raw_buffer_load_b32(mk_rsrc(p.ssmk, ACT), 176u * 4096u + ((u32)((q * NKV + j) * 256) + threadIdx.x) * 64u, 0, 0);
        if (g >= 36) conv_job<3>(p, rs, lds, g, f, 0); else conv_job<0>(p, rs, lds, g, f, 0);
        asm volatile("" :: "v"(warm[0]), "v"(warm[1]), "v"(warm[2]));
        const int g0 = j < 20 ? 36 + j : j < 36 ? 28 + j : 44 + j, g1 = j < 20 ? g0 + 1 : j < 36 ? 80 : 160;
        if (g < 36) filt_load<5, 0>(p, g0, f);
        DRAIN(); BAR();
        st4s(rs, threadIdx.x == 0 ? foff(F_CV + g) : OOB, 0, 2u);
        if (g < 36) {
#pragma unroll 1
            for (int n = g0; n < g1; n += 8) conv_job<2>(p, rs, lds, n, f, min(n + 8, 159));
            DRAIN(); BAR();
            st4s(rs, threadIdx.x == 0 ? foff(F_CV + g) : OOB, 0, 3u);
            if (j >= 36) { EV(15); __builtin_amdgcn_endpgm(); }
        }
    }
    if (!isout) { if (j < 28) role_row<0>(p, lds, (j - 20) * 4 + w); else role_row<1>(p, lds, (j - 28) * 4 + w);
        boot_set(p, rs, j == 28 && threadIdx.x == 0 ? foff(F_EP) : OOB, 0u);
        __builtin_amdgcn_endpgm();
    }
    role_out(p, lds, L, kq, ng);
}

__global__ void k_end() { asm volatile(".Lk_end_here:"); }

extern "C" void kernel_launch(void* const* d_in, const int* in_sizes, int n_in, void* d_out, int out_size, void* d_ws, size_t ws_size,
                              hipStream_t stream) {
    Args p;
    p.x = (const u16*)d_in[0]; p.mem0 = (const u16*)d_in[1]; p.Wq = (const u16*)d_in[2]; p.Wk = (const u16*)d_in[3];
    p.Wv = (const u16*)d_in[4]; p.Wo = (const u16*)d_in[5];
    p.bk = (const u16*)d_in[8]; p.bv = (const u16*)d_in[9]; p.shift = (const u16*)d_in[10]; p.ssmk = (const u16*)d_in[11]; p.ssmD = (const u16*)d_in[12];
    p.out = (u16*)d_out;
    p.ws = (unsigned char*)(((size_t)d_ws + 2 * MB - 1) & ~(size_t)(2 * MB - 1));
    (void)hipFuncSetAttribute((const void*)k_main, hipFuncAttributeMaxDynamicSharedMemorySize, LDS_BYTES);
    hipLaunchKernelGGL(k_main, dim3(256), dim3(256), LDS_BYTES, stream, p);
}
```

```cpp
#include <hip/hip_runtime.h>
#include <hip/hip_bf16.h>

typedef unsigned short u16;
typedef unsigned int u32;
typedef u32 v2u __attribute__((ext_vector_type(2)));
typedef u32 v3u __attribute__((ext_vector_type(3)));
typedef u32 v4u __attribute__((ext_vector_type(4)));
typedef u32 v8u __attribute__((ext_vector_type(8)));
typedef float f32x2 __attribute__((ext_vector_type(2)));
typedef float f32x4 __attribute__((ext_vector_type(4)));
typedef float f32x16 __attribute__((ext_vector_type(16)));
typedef __bf16 bf16x2 __attribute__((ext_vector_type(2)));
typedef __bf16 bf16x8 __attribute__((ext_vector_type(8)));
typedef short s16x4 __attribute__((ext_vector_type(4)));
#define LAS __attribute__((address_space(3)))
#define GAS __attribute__((address_space(1)))
typedef LAS unsigned char* lptr;
#define DEV __device__ __forceinline__

constexpr int SEQ = 2048, DM = 640, NH = 32, DH = 128, NSEG = 64, NQ = NH * DH;

constexpr u32 MB = 1u << 20;
constexpr u32 W_ORING = 0;
constexpr u32 W_MRING = MB;
constexpr u32 W_CTRL = 4 * MB;
constexpr u32 ACT = SEQ * DM * 2;
constexpr u32 H_TILE = 128 * 1024;
constexpr u32 W_H2 = 8 * MB;
constexpr u32 W_KQ = 16 * MB;
constexpr u32 W_VO = W_KQ + 64 * MB;
constexpr u32 W_P = W_VO + 64 * MB;
constexpr u32 P_ITEM = 512 * 1024;
constexpr u32 W_H1 = W_P + 44 * MB, W_Y = W_H1 + 8 * MB, W_X2 = W_Y + ACT;
constexpr u32 W_KVT = W_X2 + ACT;
constexpr u32 W_END = W_KVT + 3 * MB;
constexpr u32 NREG = 12;
static_assert((size_t)W_END + 2 * MB <= 256ull * MB && W_H1 >= (NREG - 1) * 16 * MB + 2 * MB && W_H1 <= NREG * 16 * MB, "workspace");
constexpr u32 F_EP = 0, F_RG = 32, F_LN = 64, F_KV = 192,
              F_QKV = 256, F_O = F_QKV + 192, F_P = F_O + 64, F_H2 = F_P + 64, F_CV = F_H2 + 64, F_ERR = F_CV + 191, F_N = F_CV + 192;
constexpr u32 W_SLOT = 3 * MB;
DEV u32 foff(u32 idx) { return W_CTRL + (idx >> 6) * 16384u + (idx & 63u) * 128u; }
DEV u32 near_(u32 o, u32 side, u32 pol) { return o + ((side ^ (pol >> (o >> 24)) ^ (o >> 21)) & 1u) * 8192u; }
DEV u32 p_item(int s) { return W_P + (u32)s * P_ITEM; }

constexpr u32 OOB = 0x80000000u;
constexpr u32 PSENT = 0x7F817F81u;
constexpr int SPIN_CAP = 40000;
constexpr int LDS_BYTES = 160 * 1024;
#define ST_KV 0
#define ST_OUT 0

#define EV(k) do {} while (0)
#define LST(idx, v) do {} while (0)
#define LS2(cond, idx) do {} while (0)
struct Args {
    const u16 *x, *mem0, *Wq, *Wk, *Wv, *Wo, *bk, *bv, *shift, *ssmk, *ssmD;
    u16* out;
    unsigned char* ws;
};
struct Params : Args { u32 e0, e1, e2; };

typedef __amdgpu_buffer_rsrc_t rsrc_t;
DEV rsrc_t mk_rsrc(const void* p, u32 bytes) { return __builtin_amdgcn_make_buffer_rsrc((void*)p, 0, bytes, 0x00020000); }
DEV v4u ld16s(rsrc_t r, u32 off, u32 so) { return __builtin_amdgcn_raw_buffer_load_b128(r, off, so, 16); }
DEV v2u ld8s(rsrc_t r, u32 off, u32 so) { return __builtin_amdgcn_raw_buffer_load_b64(r, off, so, 16); }
DEV v4u ldin16(rsrc_t r, u32 off, u32 so) { return __builtin_amdgcn_raw_buffer_load_b128(r, off, so, 0); }
DEV v2u ldin8(rsrc_t r, u32 off, u32 so) { return __builtin_amdgcn_raw_buffer_load_b64(r, off, so, 0); }
DEV u32 ld4(rsrc_t r, u32 off) { return __builtin_amdgcn_raw_buffer_load_b32(r, off, 0, 16); }
DEV void st16(rsrc_t r, u32 off, v4u v) { __builtin_amdgcn_raw_buffer_store_b128(v, r, off, 0, 16); }
DEV void st8s(rsrc_t r, u32 off, u32 so, v2u v) { __builtin_amdgcn_raw_buffer_store_b64(v, r, off, so, 16); }
DEV void st4s(rsrc_t r, u32 off, u32 so, u32 v) { __builtin_amdgcn_raw_buffer_store_b32(v, r, off, so, 16); }
#define DRAIN() asm volatile("s_waitcnt vmcnt(0)" ::: "memory")
#define VMW(n) asm volatile("s_waitcnt vmcnt(%0)" :: "i"(n) : "memory")
#define BAR() asm volatile("s_waitcnt lgkmcnt(0)\n\ts_barrier" ::: "memory")
#define SCHED() __builtin_amdgcn_sched_barrier(0)
#define CFENCE() asm volatile("" ::: "memory")
#define NOPS(n) asm volatile("s_nop %0" :: "i"(n))
#define PIN(x) asm volatile("" : "+v"(x))
#define PINS(x) asm volatile("" : "+s"(x))

template <int V> struct IC { static constexpr int value = V; };
template <int I, int N, class F> DEV void static_for(F&& f) { if constexpr (I < N) { f(IC<I>{}); static_for<I + 1, N>(f); } }
#define SFOR(var, n) static_for<0, n>([&](auto ic_) __attribute__((always_inline)) { constexpr int var = decltype(ic_)::value;
#define SEND });

DEV u32 pk(float a, float b) { bf16x2 v = {(__bf16)a, (__bf16)b}; return __builtin_bit_cast(u32, v); }
DEV float lo_f(u32 u) { return __builtin_bit_cast(float, u << 16); }
DEV float hi_f(u32 u) { return __builtin_bit_cast(float, u & 0xffff0000u); }
DEV f32x2 up2(u32 u) { return (f32x2){lo_f(u), hi_f(u)}; }
DEV u32 pk2(f32x2 v) { return pk(v.x, v.y); }
DEV f32x2 rb2(f32x2 a) { f32x2 r; asm("v_cvt_pk_bf16_f32 %0, 0, %1" : "=v"(r.x) : "v"(a.x)); asm("v_cvt_pk_bf16_f32 %0, 0, %1" : "=v"(r.y) : "v"(a.y)); return r; }
DEV float rbf(float a) { return (float)(__bf16)a; }
DEV v4u lds_r16(lptr p) { return *(LAS v4u*)p; }
DEV void lds_w16(lptr p, v4u v) { *(LAS v4u*)p = v; }
DEV v2u lds_tr(lptr p) { return __builtin_bit_cast(v2u, __builtin_amdgcn_ds_read_tr16_b64_v4i16((LAS s16x4*)p)); }
DEV v4u tr2(lptr p0, lptr p1) { const v2u a = lds_tr(p0), b = lds_tr(p1); return (v4u){a.x, a.y, b.x, b.y}; }
DEV v8u cat8(v4u a, v4u b) { return __builtin_shufflevector(a, b, 0, 1, 2, 3, 4, 5, 6, 7); }
DEV f32x4 mfma16(v4u a, v4u b, f32x4 c) { return __builtin_amdgcn_mfma_f32_16x16x32_bf16(__builtin_bit_cast(bf16x8, a), __builtin_bit_cast(bf16x8, b), c, 0, 0, 0); }
DEV f32x16 mfma32(v4u a, v4u b, f32x16 c) { return __builtin_amdgcn_mfma_f32_32x32x16_bf16(__builtin_bit_cast(bf16x8, a), __builtin_bit_cast(bf16x8, b), c, 0, 0, 0); }

DEV float f_(u32 u) { return __builtin_bit_cast(float, u); }
DEV u32 u_(float f) { return __builtin_bit_cast(u32, f); }
template <int CTRL> DEV float dpp(float v) { return f_((u32)__builtin_amdgcn_update_dpp(0, (int)u_(v), CTRL, 0xf, 0xf, false)); }
DEV float max16(float v) { auto r = __builtin_amdgcn_permlane16_swap(u_(v), u_(v), false, false); return fmaxf(f_(r[0]), f_(r[1])); }
DEV float max32(float v) { auto r = __builtin_amdgcn_permlane32_swap(u_(v), u_(v), false, false); return fmaxf(f_(r[0]), f_(r[1])); }
DEV float add16(float v) { auto r = __builtin_amdgcn_permlane16_swap(u_(v), u_(v), false, false); return f_(r[0]) + f_(r[1]); }
DEV float add32(float v) { auto r = __builtin_amdgcn_permlane32_swap(u_(v), u_(v), false, false); return f_(r[0]) + f_(r[1]); }
DEV float wave_sum(float v) {
    v += dpp<0xB1>(v);
    v += dpp<0x4E>(v);
    v += dpp<0x124>(v);
    v += dpp<0x128>(v);
    return add32(add16(v));
}
DEV bool all_(bool c) { return __builtin_amdgcn_ballot_w64(!c) == 0ull; }
DEV int uni(int v) { return __builtin_amdgcn_readfirstlane(v); }
DEV u32 uu(u32 v) { return (u32)__builtin_amdgcn_readfirstlane((int)v); }
DEV u32 rdl(u32 v, int l) { return (u32)__builtin_amdgcn_readlane((int)v, l); }
DEV u32 rt32() { return (u32)__builtin_amdgcn_s_memrealtime(); }

DEV void wait_flags(rsrc_t rs, u32 off, u32 want, bool& dead) {
    if (dead) return;
#pragma unroll 1
    for (int n = 0;; ++n) {
        CFENCE();
        const u32 v = ld4(rs, off);
        if (all_(off >= OOB || v >= want)) break;
        __builtin_amdgcn_s_sleep(1);
        if ((n & 63) == 63) { if (uni((int)ld4(rs, foff(F_ERR))) != 0) { dead = true; break; } }
        if (n > SPIN_CAP) { st4s(rs, foff(F_ERR), 0, 1u); dead = true; break; }
    }
    CFENCE();
}

DEV bool boot_ok(const Params& p, u32 off, v4u v, u32 want) { return all_(off >= OOB || (v.x == p.e0 && v.y == p.e1 && v.z == p.e2 && v.w >= want)); }
DEV void boot_set(const Params& p, rsrc_t rs, u32 off, u32 v) { st16(rs, off, (v4u){p.e0, p.e1, p.e2, v}); }
DEV u32 boot_wait(const Params& p, rsrc_t rs, u32 off, u32 want, bool& dead) {
    v4u v;
#pragma unroll 1
    for (int n = 0;; ++n) {
        CFENCE();
        v = ld16s(rs, off, 0);
        if (boot_ok(p, off, v, want) || dead) break;
        __builtin_amdgcn_s_sleep(1);
        if (n > SPIN_CAP) { st4s(rs, foff(F_ERR), 0, 1u); dead = true; }
    }
    CFENCE();
    return v.w;
}

DEV u32 pol_off() { const int lane = threadIdx.x & 63; return lane < 32 ? foff(F_RG + lane) : OOB; }
DEV u32 pol_of(u32 raw) {
    const int lane = threadIdx.x & 63;
    const float v = add16(__builtin_amdgcn_fmed3f((float)((int)raw - (1 << 20)), -250.f, 250.f));
    const float lo = dpp<0x121>(v), hi = dpp<0x12F>(v), ex = dpp<0x122>(v) + dpp<0x123>(v) + dpp<0x124>(v);
    const float sum = v + lo + ((lane & 15) == 11 ? 0.f : hi) + ((lane & 15) == 0 ? ex : 0.f);
    return (u32)__builtin_amdgcn_ballot_w64(sum >= 0.f) & 0xFFFFu;
}
DEV u32 get_pol(const Params& p, rsrc_t rs, bool& dead) { return pol_of(boot_wait(p, rs, pol_off(), 0u, dead)); }
DEV u32 hblk(u32 base, int ls, int h, u32 side, u32 pol) { return near_(base + (u32)(ls * NH + h) * 16384u, side, pol); }
DEV f32x2 ln_stats(const f32x2 (&x)[5]) {
    const float c = f_(uu(u_(x[0].x)));
    f32x2 d[5];
#pragma unroll
    for (int i = 0; i < 5; ++i) d[i] = x[i] - c;
    const f32x2 s2 = (d[0] + d[1]) + (d[2] + d[3]) + d[4], q = (d[0] * d[0] + d[1] * d[1]) + (d[2] * d[2] + (d[3] * d[3] + d[4] * d[4]));
    const float dm = wave_sum(s2.x + s2.y) * (1.0f / DM), mean = c + dm;
    const float var = fmaxf(wave_sum(q.x + q.y) * (1.0f / DM) - dm * dm, 0.f);
    const float sd = rbf(__builtin_amdgcn_sqrtf(rbf(rbf(var) + 1e-5f)));
    return (f32x2){rbf(mean), __builtin_amdgcn_rcpf(sd)};
}
DEV void store_h(rsrc_t rs, u32 tile, int row, const u32 (&hp)[5]) {
    const u32 lane = threadIdx.x & 63;
    const u32 sw = (u32)(row & 15) * 16u, o8 = (lane >> 5) * 16384u + (((lane & 31u) * 8u) ^ sw), hb = tile + (u32)(row & 31) * 256u;
    st8s(rs, o8, hb, (v2u){hp[0], hp[1]});
    st8s(rs, o8, hb + 32768, (v2u){hp[2], hp[3]});
    st4s(rs, (lane * 4) ^ sw, hb + 65536, hp[4]);
}

#define DMA16(ldsb, vo, so) asm volatile("s_mov_b32 m0, %0\n\ts_nop 0\n\tbuffer_load_dwordx4 %1, %2, %3 offen sc1 lds" :: "s"(ldsb), "v"(vo), "s"(srd), "s"(so) : "memory")
#define DMA16I(ldsb, lo, vo, so, go) do { u32 so_; asm volatile("s_add_u32 m0, %1, %5\n\ts_add_u32 %0, %4, %6\n\tbuffer_load_dwordx4 %2, %3, %0 offen sc1 lds" \
                         : "=&s"(so_) : "s"(ldsb), "v"(vo), "s"(srd), "s"(so), "i"(lo), "i"(go) : "memory", "scc"); } while (0)
#define DMA32(ldsb, vo, so) asm volatile("s_mov_b32 m0, %0\n\ts_nop 0\n\tbuffer_load_dwordx4 %1, %2, %3 offen sc1 lds\n\tbuffer_load_dwordx4 %1, %2, %3 offen offset:1024 sc1 lds" \
                         :: "s"(ldsb), "v"(vo), "s"(srd), "s"(so) : "memory")
#define INPOL "nt"
#define XPOL ""
#define OUTAUX 16
#define INAUX 2
#define DMANT(sr, ldsb, vo, so) asm volatile("s_mov_b32 m0, %0\n\ts_nop 0\n\tbuffer_load_dwordx4 %1, %2, %3 offen " INPOL " lds" :: "s"(ldsb), "v"(vo), "s"(sr), "s"(so) : "memory")
#define DMAIN(sr, ldsb, vo, so) asm volatile("s_mov_b32 m0, %0\n\ts_nop 0\n\tbuffer_load_dwordx4 %1, %2, %3 offen lds" :: "s"(ldsb), "v"(vo), "s"(sr), "s"(so) : "memory")
DEV const u16* sel(bool c, const u16* a, const u16* b) { PINS(a); PINS(b); return c ? a : b; }
DEV v4u mk_srd(const void* q, u32 bytes) { return (v4u){uu((u32)(size_t)q), uu((u32)((size_t)q >> 32) & 0xffffu), bytes, 0x00020000u}; }
#define MK_SRD() const v4u srd = mk_srd(p.ws, W_END)

constexpr int CV_PAD = 40;
constexpr int CV_PL = 4416;
constexpr int CV_F = 8 * CV_PL;
constexpr int CV_KB = 80;
constexpr int CV_KV = 65 * CV_KB;
constexpr int CV_CH = CV_F + CV_KV;
static_assert(CV_CH % 16 == 0 && 4 * CV_CH <= LDS_BYTES, "conv lds");

struct Filt { v4u a[5], b[5]; };
DEV int chan(int grp, int w) { return 16 * (grp >> 2) + (grp & 3) + 4 * w; }
template <int N = 5, int AUX = INAUX> DEV void filt_load(const Params& p, int grp, Filt& f) {
    const int tid = threadIdx.x, lane = tid & 63, d = chan(grp, tid >> 6);
    const rsrc_t rf = mk_rsrc(p.ssmk, ACT);
#pragma unroll
    for (int i = 0; i < N; ++i) {
        const u32 s0 = (u32)(8 * (lane + 64 * i) - CV_PAD);
        f.a[i] = __builtin_amdgcn_raw_buffer_load_b128(rf, s0 < SEQ ? (u32)(d * SEQ + s0) * 2u : OOB, 0, AUX);
        f.b[i] = __builtin_amdgcn_raw_buffer_load_b128(rf, s0 + 8 < SEQ ? (u32)(d * SEQ + s0 + 8) * 2u : OOB, 0, AUX);
    }
}
constexpr int KT_BYTES = 18 * 1024, KT_X = 64 * 144, NKV = 44, KT_DUMP = 8 * KT_BYTES + KT_X;
DEV void kv_fetch(const Params& p, int tile, u32 dst) {
    const int lane = threadIdx.x & 63, w = uni(threadIdx.x >> 6);
    const v4u sk = mk_srd(p.bk, ACT), sv = mk_srd(p.bv, ACT), ss = mk_srd(sel(w == 0, p.bk, p.shift), w == 0 ? ACT : DM * 8);
    const u32 vo = tile < 320 ? (u32)((lane >> 3) * DM + (tile % 10) * 64 + (lane & 7) * 8) * 2u : OOB, r0 = uu((u32)((tile / 10) * 64 + 8 * w) * (DM * 2)), d = uu(dst + w * 1024);
    DMANT(sk, d + 1024, vo, r0); DMANT(sk, d + 5120, vo, r0 + 32 * DM * 2); DMANT(sv, d + 9216, vo, r0); DMANT(sv, d + 13312, vo, r0 + 32 * DM * 2);
    DMAIN(ss, uu(w == 0 ? dst : w == 1 ? dst + 17408 : KT_DUMP), w == 0 ? (tile >= 10 ? vo : OOB) : w == 1 && lane < 32 ? (u32)lane * 16u : OOB,
          uu(w == 0 ? (tile >= 10 ? r0 - 8 * DM * 2 : 0u) : (u32)(tile % 10) * 512u));
}
DEV void kv_tile(const Params& p, rsrc_t rs, lptr src, lptr xt, int tile) {
    const int tid = threadIdx.x;
    const v4u sv = lds_r16(src + 17408 + (tid & 31) * 16);
    lptr kp = src + ((tid >> 5) * 8 + 5) * 128 + (tid & 31) * 4;
    f32x2 kf[11];
#pragma unroll
    for (int i = 0; i < 11; ++i) kf[i] = up2(*(LAS u32*)(kp + i * 128));
    const f32x2 sh[4] = {{lo_f(sv.x), lo_f(sv.z)}, {hi_f(sv.x), hi_f(sv.z)}, {lo_f(sv.y), lo_f(sv.w)}, {hi_f(sv.y), hi_f(sv.w)}};
    u32 kv[8];
#pragma unroll
    for (int i = 0; i < 8; ++i) {
        const f32x2 acc = rb2(rb2(rb2(rb2(kf[i] * sh[3]) + rb2(kf[i + 1] * sh[2])) + rb2(kf[i + 2] * sh[1])) + rb2(kf[i + 3] * sh[0]));
        kv[i] = pk2(acc * up2(*(LAS u32*)(kp + 9216 + (i - 5) * 128)));
    }
    lds_w16(xt + (2 * (tid & 31)) * 144 + (tid >> 5) * 16, (v4u){__builtin_amdgcn_perm(kv[6], kv[7], 0x05040100u), __builtin_amdgcn_perm(kv[4], kv[5], 0x05040100u),
                                                             __builtin_amdgcn_perm(kv[2], kv[3], 0x05040100u), __builtin_amdgcn_perm(kv[0], kv[1], 0x05040100u)});
    lds_w16(xt + (2 * (tid & 31) + 1) * 144 + (tid >> 5) * 16, (v4u){__builtin_amdgcn_perm(kv[6], kv[7], 0x07060302u), __builtin_amdgcn_perm(kv[4], kv[5], 0x07060302u),
                                                                 __builtin_amdgcn_perm(kv[2], kv[3], 0x07060302u), __builtin_amdgcn_perm(kv[0], kv[1], 0x07060302u)});
    BAR();
#pragma unroll
    for (int i = 0; i < 2; ++i) {
        const int ch = i * 32 + (tid >> 3);
        st16(rs, W_KVT + (u32)(((tile % 10) * 64 + ch) * SEQ + (tile / 10) * 64 + (tid & 7) * 8) * 2u, lds_r16(xt + ch * 144 + (tid & 7) * 16));
    }
}
template <int MODE> DEV void conv_job(const Params& p, rsrc_t rs, lptr lds, int grp, Filt& f, int next, u32 fo2 = OOB, u32 fv2 = 0) {
    const int tid = threadIdx.x, lane = tid & 63, w = uni(tid >> 6), bq = lane & 31, hh = lane >> 5;
    lptr F = lds + w * CV_CH;
    lptr fa = F + (bq & 7) * CV_PL + ((bq >> 3) - hh + 4) * 16;
    lptr kb = F + CV_F + CV_KB + hh * 16, kh = kb + (bq + 32) * CV_KB;
    constexpr bool LO = MODE & 1, EARLY = MODE == 0 || MODE == 3;
    bool dead = false;
    v4u kvr[4];
    const u32 D = p.ssmD[chan(grp, w)];
    auto kv_load = [&]() {
        if (EARLY) boot_wait(p, rs, lane < NKV ? foff(F_KV + lane) : OOB, 2u, dead);
#pragma unroll
        for (int i = 0; i < (LO ? 2 : 4); ++i) kvr[i] = ld16s(rs, (u32)lane * 16u, W_KVT + (u32)(chan(grp, w) * SEQ + i * 512) * 2u);
    };
    auto windows = [&](int i) {
        if (i < 4 || lane < 16) {
            const u32 e[8] = {f.a[i].x, f.a[i].y, f.a[i].z, f.a[i].w, f.b[i].x, f.b[i].y, f.b[i].z, f.b[i].w};
            u32 o[7];
#pragma unroll
            for (int q = 0; q < 7; ++q) o[q] = __builtin_amdgcn_alignbit(e[q + 1], e[q], 16);
#pragma unroll
            for (int q = 0; q < 4; ++q) {
                lds_w16(F + (2 * q) * CV_PL + (64 * i + lane) * 16, (v4u){o[q], o[q + 1], o[q + 2], o[q + 3]});
                lds_w16(F + (2 * q + 1) * CV_PL + (64 * i + lane) * 16, (v4u){e[q + 1], e[q + 2], e[q + 3], e[q + 4]});
            }
        }
    };
    lptr yw = lds + (lane >> 4) * CV_CH + 3072 + (lane & 14) * 64 + w * 32 + (lane & 1) * 16, yr = F + 3072 + lane * 16;
    const u32 yo = W_Y + (u32)((((w & 1) * 8 + (lane >> 3)) * 8 + (w >> 1)) * DM + (grp & 3) * 160 + 4 * (grp >> 2)) * 32u + (u32)(lane & 7) * 16u;
    v4u dg[2];
    auto skip = [&](lptr k) { const f32x16 z = {}; return mfma32(lds_r16(k + 32), dg[1], mfma32(lds_r16(k), dg[0], z)); };
    auto pack = [&](const f32x16& ac, const f32x16& sk, int q) {
        const int r = 4 * (q & 3);
        u32 v[4];
#pragma unroll
        for (int i = 0; i < 4; ++i) v[i] = pk2(rb2((f32x2){ac[r + 2 * i], ac[r + 2 * i + 1]}) + rb2((f32x2){sk[r + 2 * i], sk[r + 2 * i + 1]}));
        const auto x = __builtin_amdgcn_permlane32_swap(v[0], v[2], false, false), y = __builtin_amdgcn_permlane32_swap(v[1], v[3], false, false);
        return (v4u){x[0], y[0], x[1], y[1]};
    };
    if (MODE != 2) EV(8);
    if (!EARLY) kv_load();
    if (lane < CV_KB / 16) lds_w16(F + CV_F + lane * 16, (v4u){0u, 0u, 0u, 0u});
    windows(0);
    if (MODE != 1) { windows(1); windows(2); }
    if (!LO) windows(4);
    if (EARLY) kv_load();
    if (MODE == 2) windows(3);
    if (MODE != 2) EV(9);
#pragma unroll
    for (int i = 0; i < (LO ? 2 : 4); ++i) lds_w16(F + CV_F + (16 * i + (lane >> 2) + 1) * CV_KB + (lane & 3) * 16, kvr[i]);
#pragma unroll
    for (int ks = 0; ks < 2; ++ks)
#pragma unroll
        for (int q = 0; q < 4; ++q) { const int j = 16 * ks + 8 * hh + 7 - bq; dg[ks][q] = j == 2 * q ? D : j == 2 * q + 1 ? D << 16 : 0u; }
    f32x16 acc0 = {}, acc1 = {};
    auto first = [&]() { asm volatile("s_waitcnt vmcnt(0)\n\ts_barrier" ::: "memory"); st4s(rs, tid == 0 ? foff(F_CV + grp) : fo2, 0, tid == 0 ? 1u : fv2); EV(12); };
    if (MODE != 2) {
        EV(10);
        v4u fr[2][4];
        auto rd = [&](int m) { lptr kl = kb + max(bq - m, -1) * CV_KB; fr[m & 1][0] = lds_r16(kl); fr[m & 1][1] = lds_r16(fa + m * 64); fr[m & 1][2] = lds_r16(kl + 32); fr[m & 1][3] = lds_r16(fa + m * 64 - 32); };
        rd(0); rd(1);
        const f32x16 sk = skip(kb + bq * CV_KB);
#pragma unroll
        for (int m = 0; m < 8; ++m) {
            acc0 = mfma32(fr[m & 1][0], fr[m & 1][1], acc0); acc0 = mfma32(fr[m & 1][2], fr[m & 1][3], acc0);
            if (m < 6) rd(m + 2);
            SCHED();
        }
        lds_w16(yw, pack(acc0, sk, 0));
        BAR();
        st16(rs, w < 2 ? yo : OOB, lds_r16(yr));
        EV(11);
    }
    if (LO) {
        if (MODE == 1) { windows(1); windows(2); first(); }
        v4u a[2][2], l[2][2];
#pragma unroll
        for (int u = 0; u < 2; ++u) { lptr kl = kb + max(bq - 8 - u, -1) * CV_KB; a[u][0] = lds_r16(fa + (8 + u) * 64); l[u][0] = lds_r16(kl); a[u][1] = lds_r16(fa + (8 + u) * 64 - 32); l[u][1] = lds_r16(kl + 32); }
#pragma unroll 1
        for (int m = 8; m < 32; m += 2) {
            if (MODE == 3 && m == 20) first();
#pragma unroll
            for (int u = 0; u < 2; ++u) {
                lptr kl = kb + max(bq - m - u - 2, -1) * CV_KB;
                acc0 = mfma32(l[u][0], a[u][0], acc0); a[u][0] = lds_r16(fa + (m + u + 2) * 64); l[u][0] = lds_r16(kl); SCHED();
                acc0 = mfma32(l[u][1], a[u][1], acc0); a[u][1] = lds_r16(fa + (m + u + 2) * 64 - 32); l[u][1] = lds_r16(kl + 32); SCHED();
            }
        }
    } else {
        if (MODE == 0) windows(3); else filt_load<5, 0>(p, next, f);
        v4u a0 = lds_r16(fa), a1 = lds_r16(fa - 32), c0 = lds_r16(fa + 2048), c1 = lds_r16(fa + 2048 - 32);
        v4u h0 = lds_r16(kh), h1 = lds_r16(kh + 32), l0 = lds_r16(kb + bq * CV_KB), l1 = lds_r16(kb + bq * CV_KB + 32);
#pragma unroll 2
        for (int m = 0; m < (MODE == 0 ? 8 : 32); ++m) {
            lptr kl = kb + max(bq - m - 1, -1) * CV_KB;
            if (MODE == 0 && m == 4) first();
            acc1 = mfma32(h0, a0, acc1); h0 = lds_r16(kh - (m + 1) * CV_KB); a0 = lds_r16(fa + (m + 1) * 64); SCHED();
            acc1 = mfma32(l0, c0, acc1); c0 = lds_r16(fa + (m + 1) * 64 + 2048); l0 = lds_r16(kl); SCHED();
            acc1 = mfma32(h1, a1, acc1); h1 = lds_r16(kh - (m + 1) * CV_KB + 32); a1 = lds_r16(fa + (m + 1) * 64 - 32); SCHED();
            acc1 = mfma32(l1, c1, acc1); c1 = lds_r16(fa + (m + 1) * 64 + 2048 - 32); l1 = lds_r16(kl + 32); SCHED();
        }
        if (MODE == 0) {
#pragma unroll 2
            for (int m = 8; m < 32; ++m) {
                lptr kl = kb + max(bq - m - 1, -1) * CV_KB;
                acc1 = mfma32(h0, a0, acc1); h0 = lds_r16(kh - (m + 1) * CV_KB); SCHED();
                acc0 = mfma32(l0, a0, acc0); a0 = lds_r16(fa + (m + 1) * 64); SCHED();
                acc1 = mfma32(l0, c0, acc1); c0 = lds_r16(fa + (m + 1) * 64 + 2048); l0 = lds_r16(kl); SCHED();
                acc1 = mfma32(h1, a1, acc1); h1 = lds_r16(kh - (m + 1) * CV_KB + 32); SCHED();
                acc0 = mfma32(l1, a1, acc0); a1 = lds_r16(fa + (m + 1) * 64 - 32); SCHED();
                acc1 = mfma32(l1, c1, acc1); c1 = lds_r16(fa + (m + 1) * 64 + 2048 - 32); l1 = lds_r16(kl + 32); SCHED();
            }
        }
    }
    if (MODE != 2) EV(13);
    v4u yy[4];
    if (MODE != 2) { const f32x16 sk = skip(kb + bq * CV_KB); yy[0] = pack(acc0, sk, 0); yy[1] = pack(acc0, sk, 2); }
    if (!LO) { const f32x16 sk = skip(kh); yy[2] = pack(acc1, sk, 0); yy[3] = pack(acc1, sk, 2); BAR(); }
#pragma unroll
    for (int q = (MODE == 2 ? 2 : 0); q < (LO ? 2 : 4); ++q) lds_w16(yw + q * CV_PL, yy[q]);
    BAR();
#pragma unroll
    for (int q = (MODE == 2 ? 2 : 0); q < (LO ? 2 : 4); ++q) st16(rs, q == 0 && w < 2 ? OOB : yo + (u32)q * (2 * DM * 32), lds_r16(yr + q * CV_PL));
    BAR();
    if (MODE != 2) EV(14);
}

#define NPROBE 6
DEV int probe(rsrc_t rs, int c) {
    const int lane = threadIdx.x & 63;
    const u32 k = (u32)(c & 15) < NREG ? (u32)(c & 15) : 0u;
    const u32 off0 = k * (16 * MB) + 2 * MB - 65536 + ((u32)(c & 15) >= NREG ? (u32)(c & 3) + 1u : 0u) * 256 + (u32)(c >> 4) * 128;
    st4s(rs, lane == 0 ? off0 : OOB, 0, 0u); DRAIN();
    int d0 = 1 << 16, d1 = 1 << 16;
#pragma unroll 1
    for (int q = 0; q < NPROBE; ++q) {
        const u32 o = lane == 0 ? off0 + (u32)(q & 1) * 8192 + (u32)(q >> 1) * 18432 : OOB;
        const unsigned long long t0 = __builtin_amdgcn_s_memtime();
        st4s(rs, o, 0, 0u); DRAIN();
        const int dt = (int)(__builtin_amdgcn_s_memtime() - t0);
        if (q & 1) d1 = min(d1, dt); else d0 = min(d0, dt);
    }
    const u32 xcc = (u32)__builtin_amdgcn_s_getreg(20 | (3 << 11)) & 0xfu;
    return (d0 - d1) * ((xcc & 4u) ? -1 : 1);
}
DEV void pre_rings(const Params& p, rsrc_t rs, int j) {
    const u32 gt = (u32)(j * 256 + threadIdx.x) * 16u, n = ((gt >> 14) << 9) + ((gt & 8191u) >> 4);
    const v4u ps = {PSENT, PSENT, PSENT, PSENT};
    v4u m = ps;
    if (n < 640u) m = *(const v4u*)(p.mem0 + (2 * ((n >> 2) & 3) + (n & 1)) * DM + (n >> 4) * 16 + (n & 2) * 4);
#pragma unroll
    for (u32 i = 0; i < 8; ++i) st16(rs, W_ORING + gt + i * 131072u, ps);
#pragma unroll
    for (u32 i = 0; i < 2; ++i) st16(rs, gt < 16384u ? W_MRING + i * (MB / 4) + 131072u + gt : OOB, ps);
    if (n < 640u) {
        m = (v4u){__builtin_amdgcn_perm(m.z, m.x, 0x05040100u), __builtin_amdgcn_perm(m.z, m.x, 0x07060302u), __builtin_amdgcn_perm(m.w, m.y, 0x05040100u), __builtin_amdgcn_perm(m.w, m.y, 0x07060302u)};
#pragma unroll
        for (int i = 0; i < 4; ++i) m[i] = m[i] == PSENT ? 0x7FC17FC1u : m[i];
    }
    st16(rs, W_MRING + gt, m);
    st16(rs, W_MRING + MB / 4 + gt, m);
    static_assert(2 * (F_N - F_QKV) == 32 * 36, "clear");
    st4s(rs, threadIdx.x < 36 ? foff(F_QKV) + (u32)(j * 36 + threadIdx.x) * 128u : OOB, 0, 0u);
}

#define M32A(first, d, a, b) do { if (first) asm volatile("v_mfma_f32_32x32x16_bf16 %0, %1, %2, 0" : "=&v"(d) : "a"(a), "v"(b)); \
                                  else asm volatile("v_mfma_f32_32x32x16_bf16 %0, %1, %2, %0" : "+v"(d) : "a"(a), "v"(b)); } while (0)
#define M32B(first, d, a, b) do { if (first) asm volatile("v_mfma_f32_32x32x16_bf16 %0, %1, %2, 0" : "=&v"(d) : "v"(a), "a"(b)); \
                                  else asm volatile("v_mfma_f32_32x32x16_bf16 %0, %1, %2, %0" : "+v"(d) : "v"(a), "a"(b)); } while (0)
#define VCVT(d, a, b) asm volatile("v_cvt_pk_bf16_f32 %0, %1, %2" : "=v"(d) : "v"(a), "v"(b))
#define LD4A(d, vo, so) asm volatile("buffer_load_dword %0, %1, %2, %3 offen sc1" : "=v"(d) : "v"(vo), "s"(srd), "s"(so) : "memory")
DEV u32 wst_src(int lane, int ld) { return (u32)((lane >> 4) * ld + 8 * ((lane & 15) ^ ((lane >> 4) << 2))) * 2u; }
DEV u32 wst_rd(int lane, int w) { const int q4 = (lane >> 2) & 3; return (u32)(q4 * 256 + ((w ^ q4) * 64) + ((lane >> 4) & 1) * 32 + (lane & 3) * 8); }

constexpr int QK_A = 65536;
#define QLEAD 4
#define QFIRST 2
static_assert(QFIRST < QLEAD, "a round of the lead carries the flag of the convolution");
#define QW0 10
#define QW1 3
#define QSAMP 72
DEV void role_qkv(const Params& p, lptr lds, int c) {
    const int grp = 64 + c;
    const int tid = threadIdx.x, lane = tid & 63, w = uni(tid >> 6), hh = lane >> 5, r32 = lane & 31;
    const int m = c >> 5, h = c & 31;
    const rsrc_t rs = mk_rsrc(p.ws, W_END);
    v4u wt[40];
    Filt flt;
    bool dead = false;
    u32 pol, h2flag, fo, ol, ar[8];
    const u32 ds = uu(w * 1024), dx = ds + QK_A;
#define XRD(ks) lds_r16(lds + ar[(ks) & 7] + ((ks) >> 3) * 8192)
    const u32 t16 = (u32)tid * 16u, lnf = tid < 96 ? foff(F_LN + tid) : OOB;
    MK_SRD();
#define QDMA(i, dst, vo, so) DMA16I(dst, (i) * 4096, vo, so, ((i) >> 1) * 16384 + ((i) & 1) * 4096)
    {
        const v4u sw = mk_srd(sel(m == 0, p.Wq, sel(m == 1, p.Wk, p.Wv)), DM * NQ * 2), sx = mk_srd(p.x, ACT);
        const u32 vo = wst_src(lane, NQ) + (u32)h * (DH * 2), x8 = (u32)lane * 8u, x4 = (u32)lane * 4u;
        v2u xa[6], xb[6]; u32 xc[6];
#pragma unroll
        for (int i = 0; i < 6; ++i)
            asm volatile("buffer_load_dwordx2 %0, %3, %5, %6 offen " XPOL "\n\tbuffer_load_dwordx2 %1, %3, %5, %6 offen offset:512 " XPOL "\n\tbuffer_load_dword %2, %4, %5, %6 offen offset:1024 " XPOL
                         : "=&v"(xa[i]), "=&v"(xb[i]), "=&v"(xc[i]) : "v"(x8), "v"(x4), "s"(sx), "s"(uu((4 * c + w + 384 * i) * (DM * 2))) : "memory");
        EV(1);
        auto wfill = [&](int i0, int n) {
#pragma unroll 1
            for (int i = i0; i < i0 + n; ++i) DMAIN(sw, uu((4 * i + w) * 1024), vo, uu((16 * i + 4 * w) * (NQ * 2)));
        };
        wfill(0, QW0);
        asm volatile("s_waitcnt vmcnt(%0) ; HZCLR" :: "i"(QW0) : "memory");
        EV(2);
#pragma unroll
        for (int i = 0; i < 6; ++i) {
            wfill(QW0 + QW1 * i, QW1);
            if (i == 2) { VMW(2 * QW1 + 3); BAR(); boot_set(p, rs, tid == 0 ? foff(F_LN + c) : OOB, 1u); }
            asm volatile("" : "+v"(xa[i]), "+v"(xb[i]), "+v"(xc[i]));
            const int row = 4 * c + w + 384 * i;
            if (i < 5 || row < SEQ) {
                const f32x2 x[5] = {up2(xa[i].x), up2(xa[i].y), up2(xb[i].x), up2(xb[i].y), up2(xc[i])};
                const f32x2 st = ln_stats(x);
                u32 hp[5];
#pragma unroll
                for (int q = 0; q < 5; ++q) hp[q] = pk2(up2(pk2(x[q] - st.x)) * st.y);
                store_h(rs, W_H1 + (u32)(row >> 5) * H_TILE, row, hp);
            }
        }
        EV(3);
        constexpr int K0 = QW0 + 6 * QW1;
        static_assert(K0 * 4096 >= QK_A + 40960, "the first tile's place is free");
        v4u fv, praw;
        asm volatile("buffer_load_dwordx4 %0, %2, %4, 0 offen sc1\n\tbuffer_load_dwordx4 %1, %3, %4, 0 offen sc1" : "=&v"(praw), "=&v"(fv) : "v"(pol_off()), "v"(lnf), "s"(srd) : "memory");
        wfill(K0, 40 - K0);
        asm volatile("s_waitcnt vmcnt(%2) ; HZCLR\n\ts_barrier" : "+v"(praw), "+v"(fv) : "i"(40 - K0) : "memory");
        boot_set(p, rs, tid == 0 ? foff(F_LN + c) : OOB, 2u);
        EV(4);
        lptr tb = lds + hh * 2048 + wst_rd(lane, w);
        SFOR(k, 40)
            if constexpr (k == K0) {
                if (!boot_ok(p, lnf, fv, 1u)) boot_wait(p, rs, lnf, 1u, dead);
                if (!boot_ok(p, pol_off(), praw, 0u)) praw.w = boot_wait(p, rs, pol_off(), 0u, dead);
                BAR();
                EV(5);
                const u32 so = W_H1;
                SFOR(i, 10) QDMA(i, dx, t16, so); SEND
                pol = pol_of(praw.w);
                h2flag = tid < 8 ? near_(foff(F_H2 + lane), 1u, pol) : OOB;
                ol = (m == 2 ? (u32)(2 * w + (r32 >> 4)) * 1024u : (u32)((r32 >> 4) * 4 + w) * 1024u) + (u32)(hh * 16 + (r32 & 15)) * 16u;
                fo = near_(foff(F_QKV + m * 64 + h), 0u, pol);
#pragma unroll
                for (int j = 0; j < 8; ++j) { ar[j] = (u32)(QK_A + r32 * 256 + ((2 * j + hh) ^ (r32 & 15)) * 16); PIN(ar[j]); }
                PIN(h2flag); PIN(ol); PINS(fo); PINS(pol);
                VMW(10); BAR();
            }
            lptr q = tb + (k >> 3) * 32768 + (k & 7) * 4096; wt[k] = tr2(q, q + 1024); asm volatile("" : "+a"(wt[k]));
        SEND
    }
    EV(6);
    int n0 = -1, n1 = -QLEAD;
    const u32 ksc = m == 1 ? 0x3e0293eeu : 0x3f800000u;
    const u32 bfo = tid < 96 ? lnf : tid - 128u < NKV ? foff(F_KV - 128 + tid) : OOB;
    v4u brk = {};
    auto run = [&](auto isv) __attribute__((always_inline)) {
      constexpr bool V = decltype(isv)::value;
      v4u x[8] = {};
      f32x16 a0 = {};
      bool fresh = true;
      u32 fv = 0, fo0 = tid == 192 ? fo : n1 > -QLEAD && tid == 194 ? foff(F_CV + grp) : OOB, fo1 = tid == 192 ? fo : tid == 193 ? fo + 32 * 128 : OOB;
      PIN(fo0); PIN(fo1);
#define QS(k) do {} while (0)
#define QPK1(d, a, a_, q_) do { if constexpr (V) VCVT(d[q_], a[4 * (a_) + 8 * ((q_) >> 1) + 2 * ((q_) & 1)], a[4 * (a_) + 8 * ((q_) >> 1) + 2 * ((q_) & 1) + 1]); else VCVT(d[q_], a[4 * (a_) + (q_)], a[4 * (a_) + (q_) + 8]); } while (0)
#define QSC(a, i) asm volatile("v_mul_f32 %0, %2, %0\n\tv_mul_f32 %1, %2, %1" : "+v"(a[i]), "+v"(a[(i) + 8]) : "s"(ksc))
#define QPK(d, a, a_, q_) do { if constexpr (!V) { QSC(a, 4 * (a_) + (q_)); QSC(a, 4 * (a_) + (q_) + 1); } QPK1(d, a, a_, q_); QPK1(d, a, a_, (q_) + 1); } while (0)
#pragma unroll 1
      for (const int end = n1 == -QLEAD ? QFIRST - QLEAD : NSEG; n1 < end; ++n1) {
        const int cur = n1 < 0 ? n0 + 1 : 64 | n1, nxt = n1 < 0 ? n0 + 2 : n0 + 1;
        u32 xs = n1 < 0 ? W_H1 + (u32)cur * H_TILE : near_(W_H2 + (u32)n1 * H_TILE, 1u, pol);
        PINS(xs);
        QS(0);
        if (w == 0 && n1 >= 0 && ((u32)__builtin_amdgcn_ballot_w64(fv <= (u32)n1) & 255u) != 0u) wait_flags(rs, h2flag, (u32)n1 + 1, dead);
        BAR();
        QS(1);
        f32x16 a1;
        u32 ss, sv, hb, oa, qfo, qfv;
        v4u pp, pq;
        SCHED();
        auto slot = [&](auto zc) __attribute__((always_inline)) {
            constexpr int z = decltype(zc)::value, ks = (z + 20) % 40;
            if constexpr (z < 20) { if constexpr (V) M32B(false, a0, x[ks & 7], wt[ks]); else M32A(false, a0, wt[ks], x[ks & 7]); }
            else if constexpr (z < 60) { if constexpr (V) M32B(z == 20, a1, x[ks & 7], wt[ks]); else M32A(z == 20, a1, wt[ks], x[ks & 7]); }
            else if constexpr (z == 60) {
                if constexpr (V) asm volatile("v_mfma_f32_32x32x16_bf16 %0, %1, %2, 0" : "+v"(a0) : "v"(x[ks & 7]), "a"(wt[ks]));
                else asm volatile("v_mfma_f32_32x32x16_bf16 %0, %1, %2, 0" : "+v"(a0) : "a"(wt[ks]), "v"(x[ks & 7]));
            } else { if constexpr (V) M32B(false, a0, x[ks & 7], wt[ks]); else M32A(false, a0, wt[ks], x[ks & 7]); }
            if constexpr (z == 18) { QS(2); asm volatile("s_waitcnt vmcnt(6) lgkmcnt(0)\n\ts_barrier" ::: "memory"); QS(3); }
            if constexpr (z < 15 || z > 20) x[(ks + 5) & 7] = XRD((ks + 5) % 40);
            if constexpr (z >= 18 && z <= 20) { x[2 * (z - 18)] = XRD(2 * (z - 18)); x[2 * (z - 18) + 1] = XRD(2 * (z - 18) + 1); }
            SCHED();
            if constexpr (z < 4) QDMA(z, dx, t16, xs);
            if constexpr (z >= 5 && z < 17 && (z & 1)) QDMA((z + 3) >> 1, dx, t16, xs);
            if constexpr ((z >= 7 && z < 15) || (z >= 47 && z < 55)) asm volatile("v_xor_b32 %0, 0x10000, %0" : "+v"(ar[(ks + 5) & 7]));
            if constexpr (z == 16) { ss = W_H1 + (u32)nxt * H_TILE; PINS(ss); }
            if constexpr (z == 17) { sv = t16 | (nxt >= NSEG ? OOB : 0u); PIN(sv); }
            if constexpr (z >= 21 && z < 41 && (z & 1)) QDMA((z - 21) >> 1, ds, sv, ss);
            if constexpr (z == 22 || z == 24) QPK(pp, a0, 0, z - 22);
            if constexpr (z == 26 || z == 28) QPK(pq, a0, 1, z - 26);
            if constexpr (z == 30) { QS(4); asm volatile("s_waitcnt vmcnt(5)\n\ts_barrier" ::: "memory"); QS(5); }
            if constexpr (z == 32 || z == 44) { hb = (m == 2 ? W_VO : W_KQ) + (u32)((z == 32 ? n0 : cur) * NH + h) * 16384u; PINS(hb); }
            if constexpr (z == 34 || z == 45) { hb = near_(hb, m == 1 ? 0u : 1u, pol); PINS(hb); }
            if constexpr (z == 36) { hb = n0 < NSEG && !fresh ? hb : OOB; PINS(hb); }
            if constexpr (z == 38 || z == 46) { oa = ol + hb; PIN(oa); }
            if constexpr (z == 41) st16(rs, oa, pp);
            if constexpr (z == 43) st16(rs, oa + 512, pq);
            if constexpr (z == 54) { QS(6); asm volatile("s_waitcnt vmcnt(4)\n\ts_barrier" ::: "memory"); QS(7); }
            if constexpr (z == 56) { if (__builtin_expect(n1 == QFIRST - QLEAD - 1, 0)) { filt_load<3, 0>(p, grp, flt); brk = ld16s(rs, bfo, 0); } }
            if constexpr (z == 60) NOPS(5);
            if constexpr (z == 63) st16(rs, oa, pp);
            if constexpr (z == 65) st16(rs, oa + 512, pq);
            if constexpr (z == 61 || z == 62) QPK(pp, a1, 0, 2 * (z - 61));
            if constexpr (z == 63 || z == 64) QPK(pq, a1, 1, 2 * (z - 63));
            if constexpr (z == 67) { qfo = n1 >= 0 ? fo1 : fo0; PIN(qfo); }
            if constexpr (z == 68) { qfv = tid == 193 ? (u32)n1 + 1u : (u32)min(nxt, NSEG); PIN(qfv); }
            if constexpr (z == QSAMP) LD4A(fv, h2flag, 0);
            SCHED();
        };
        if (__builtin_expect(!fresh, 1)) static_for<0, 16>(slot);
        static_for<16, 80>(slot);
        QS(8);
        asm volatile("s_waitcnt vmcnt(1)\n\ts_barrier" ::: "memory");
        st4s(rs, qfo, 0, qfv);
        QS(9);
        asm volatile("s_waitcnt vmcnt(1)" : "+v"(fv) :: "memory");
        n0 = nxt; fresh = false;
      }
      if (n1 < 0) {
        SFOR(z, 20)
            if constexpr (V) M32B(false, a0, x[(z + 20) & 7], wt[z + 20]); else M32A(false, a0, wt[z + 20], x[(z + 20) & 7]);
            if constexpr (z < 15) x[(z + 25) & 7] = XRD(z + 25);
            if constexpr (z >= 7 && z < 15) asm volatile("v_xor_b32 %0, 0x10000, %0" : "+v"(ar[(z + 25) & 7]));
            SCHED();
        SEND
        u32 oa = ol + near_((m == 2 ? W_VO : W_KQ) + (u32)(n0 * NH + h) * 16384u, m == 1 ? 0u : 1u, pol);
        v4u pp, pq;
        asm volatile("s_nop 11" : "+v"(oa));
        QPK(pp, a0, 0, 0); QPK(pp, a0, 0, 2); st16(rs, oa, pp);
        QPK(pq, a0, 1, 0); QPK(pq, a0, 1, 2); st16(rs, oa + 512, pq);
      }
    };
#pragma unroll 1
    for (;;) {
        if (m == 2) run(IC<1>{}); else run(IC<0>{});
        if (n1 > 0) break;
        if (!boot_ok(p, bfo, brk, 2u)) boot_wait(p, rs, bfo, 2u, dead);
        BAR();
        conv_job<1>(p, rs, lds, grp, flt, 0, tid == 1 ? fo : OOB, (u32)n0 + 1u);
        const u32 so = uu(W_H1 + (u32)(n0 + 1) * H_TILE);
        SFOR(i, 10) QDMA(i, dx, t16, so); SEND
    }
}

constexpr int OU_SEL = 131072;
#define OSAMP 46

DEV void role_out(const Params& p, lptr lds, int L, int kq, int ng) {
    const int tid = threadIdx.x, lane = tid & 63, w = uni(tid >> 6), hh = lane >> 5, r32 = lane & 31;
    const rsrc_t rs = mk_rsrc(p.ws, W_END);
    v4u wo[64];
    Filt flt;
    bool dead = false;
    {
        const v4u sw = mk_srd(p.Wo, DM * NQ * 2);
        const u32 vo = wst_src(lane, DM) + (ng == 4 ? 1024u : (u32)((ng >> 1) * 512 + (ng & 1) * 128 + ((lane ^ (lane >> 2)) & 8) * 16));
        auto fill = [&](int b) {
#pragma unroll 1
            for (int i = 0; i < 8; ++i) DMAIN(sw, uu((b % 5) * 32768 + (4 * i + w) * 1024), vo, uu((kq * 1024 + b * 128 + 16 * i + 4 * w) * (DM * 2)));
        };
#pragma unroll 1
        for (int b = 0; b < 5; ++b) fill(b);
        lptr tb = lds + hh * 1024 + wst_rd(lane, w);
        SFOR(b, 8)
            VMW(b < 3 ? 32 : 62 - 8 * b); BAR();
            lptr q = tb + (b % 5) * 32768;
#pragma unroll
            for (int k = 0; k < 8; ++k) { lptr a = q + (k >> 1) * 8192 + (k & 1) * 2048; wo[8 * b + k] = tr2(a, a + 4096); if (b < 7) asm volatile("" : "+a"(wo[8 * b + k])); else PIN(wo[8 * b + k]); }
            if constexpr (b < 3) { BAR(); fill(b + 5); }
            if constexpr (b == 2) filt_load<3, 0>(p, L == 0 ? 44 + kq * 5 + ng : 1 << 12, flt);
        SEND
    }
    EV(6);
    const u32 pol = get_pol(p, rs, dead);
    if (L == 0) {
        BAR();
        conv_job<3>(p, rs, lds, 44 + kq * 5 + ng, flt, 0);
        DRAIN(); BAR();
        st4s(rs, tid == 0 ? foff(F_CV + 44 + kq * 5 + ng) : OOB, 0, 2u);
    }
    EV(15);
    MK_SRD();
    const u32 ofl = (lane < 8 && w == 0) ? near_(foff(F_O + L * 32 + kq * 8 + lane), 0u, pol) : OOB;
    const u32 l16 = (u32)lane * 16u;
    const u32 fo = (tid == 192) ? near_(foff(F_P + L * 32 + kq * 5 + ng), 1u, pol) : OOB;
    auto osrc = [&](int s) { return uu(hblk(W_VO, L * NSEG + s, kq * 8, 0u, pol) + w * 1024); };
#define ODMA(i, dst, vo, so) DMA16I(dst, (i) * 4096, vo, so, ((i) >> 1) * 16384 + ((i) & 1) * 4096)
    const u32 pvo = (u32)(kq * 5 + ng) * 16384u + (u32)w * 2048u + l16;
    lptr tw = lds + OU_SEL + 256 + r32 * 272 + (ng == 4 ? hh * 32 + w * 4 : (w & 1) * 128 + hh * 16 + (w >> 1) * 8), tr = lds + OU_SEL + 256 + (8 * w + (lane >> 4)) * 272 + (lane & 15) * 16;
    lptr selw = lds + OU_SEL + (w == 0 ? 0 : 64);
    u32 pfo = OOB;
    u32 buf = 0;
    if (w == 0) wait_flags(rs, ofl, 1u, dead);
    BAR();
    for (int s = 0; s < NSEG; ++s) {
        u32 src = osrc(s), dst = uu(buf + w * 1024);
        lptr A = lds + buf + l16;
        asm volatile("" : "+s"(src), "+s"(dst));
#define OS(k) do {} while (0)
        OS(0);
        OS(1);
        SFOR(i, 10) ODMA(i, dst, l16, src); SEND
        OS(2);
        asm volatile("s_waitcnt vmcnt(7)\n\ts_barrier" ::: "memory");
        OS(3);
        LS2(L == 0 && w == 1, 96 + kq * 5 + ng);
        v4u o[64];
#pragma unroll
        for (int k = 0; k < 3; ++k) o[k] = lds_r16(A + k * 1024);
        st4s(rs, pfo, 0, (u32)s);
        u32 fv, pa; int rdy;
        f32x16 acc;
        SCHED();
        SFOR(k, 64)
            if constexpr (k < 56) M32A(k == 0, acc, wo[k], o[k]); else asm volatile("v_mfma_f32_32x32x16_bf16 %0, %1, %2, %0" : "+v"(acc) : "v"(wo[k]), "v"(o[k]));
            if constexpr (k < 61) o[k + 3] = lds_r16(A + (k + 3) * 1024);
            SCHED();
            if constexpr (k < 12 && (k & 1) == 0) ODMA(10 + (k >> 1), dst, l16, src);
            if constexpr (k == 7) OS(4);
            if constexpr (k == 8) OS(5);
            if constexpr (k == 23) OS(6);
            if constexpr (k == 24) OS(7);
            if constexpr (k == 43) OS(8);
            if constexpr (k == 44) OS(9);
            if constexpr (k == 59) OS(10);
            if constexpr (k == 63) OS(11);
            if constexpr (k == 7) asm volatile("s_waitcnt vmcnt(8)\n\ts_barrier" ::: "memory");
            if constexpr (k == 23) asm volatile("s_waitcnt vmcnt(4)\n\ts_barrier" ::: "memory");
            if constexpr (k == 30) { pa = pvo + near_(p_item(s), (u32)L, pol); PIN(pa); }
            if constexpr (k == 43) asm volatile("s_waitcnt vmcnt(0)\n\ts_barrier" ::: "memory");
            if constexpr (k == OSAMP) LD4A(fv, ofl, 0);
            if constexpr (k == 62) {
                asm volatile("s_waitcnt vmcnt(0)" : "+v"(fv) :: "memory");
                *(LAS int*)selw = (int)((u32)__builtin_amdgcn_ballot_w64(fv < (u32)s + 2) & 255u);
            }
            SCHED();
        SEND
        asm volatile("s_nop 11" : "+v"(acc));
        u32 pc[8];
#pragma unroll
        for (int i = 0; i < 8; ++i) pc[i] = pk(acc[2 * i], acc[2 * i + 1]);
        if (ng != 4) {
#pragma unroll
            for (int i = 0; i < 4; ++i) *(LAS v2u*)(tw + i * 32) = (v2u){pc[2 * i], pc[2 * i + 1]};
        } else {
#pragma unroll
            for (int i = 0; i < 8; ++i) *(LAS u32*)(tw + (i >> 1) * 64 + (i & 1) * 16) = pc[i];
        }
        BAR();
        const v4u p0 = lds_r16(tr), p1 = lds_r16(tr + 4 * 272);
        rdy = *(LAS int*)(lds + OU_SEL);
        st16(rs, pa, p0); st16(rs, pa + 1024, p1);
        const bool ready = uni(rdy) == 0;
        SCHED();
        OS(12);
        if (ready) pfo = fo;
        else {
            LD4A(fv, ofl, 0);
            asm volatile("s_waitcnt vmcnt(1)\n\ts_barrier" ::: "memory");
            st4s(rs, fo, 0, (u32)s + 1); pfo = OOB;
            OS(14);
            LS2(L == 0 && w == 1, 160 + kq * 5 + ng);
            asm volatile("s_waitcnt vmcnt(1)" : "+v"(fv) :: "memory");
            if (w == 0 && s + 1 < NSEG && ((u32)__builtin_amdgcn_ballot_w64(fv < (u32)s + 2) & 255u) != 0u) wait_flags(rs, ofl, (u32)s + 2, dead);
            BAR();
        }
        OS(13);
        buf ^= 65536u;
    }
}

template <int L> DEV void role_row(const Params& p, lptr lds, int ri) {
    const int lane = threadIdx.x & 63, w = uni(threadIdx.x >> 6);
    const rsrc_t rs = mk_rsrc(p.ws, W_END);
    bool dead = false;
    const u32 pol = get_pol(p, rs, dead);
    const u32 fp = threadIdx.x < 20 ? near_(foff(F_P + L * 32 + lane), 1u, pol) : OOB, fo = threadIdx.x == 192 && L == 0 ? near_(foff(F_H2 + (ri >> 2)), 1u, pol) : OOB;
    u32 fv = 0;
    MK_SRD();
    auto yfill = [&](int b, int i) { DMA32(uu((b & 1) * 40960 + w * 10240 + i * 2048), (u32)lane * 16u, uu(W_Y + (u32)((ri >> 1) * 8 + b) * 20480u + (w & 1) * 10240 + i * 2048)); };
    auto ywait = [&](u32 v) {
        wait_flags(rs, threadIdx.x < (v == 3u ? 36u : 160u) ? foff(F_CV + threadIdx.x) : OOB, v, dead);
    };
    v4u y8[10] = {};
    v2u xa, xb; u32 xc;
    const v4u sx = L == 0 ? mk_srd(p.x, ACT) : mk_srd(p.ws + W_X2, ACT);
    auto xload = [&](int s) {
        asm volatile("buffer_load_dwordx2 %0, %3, %5, %6 offen sc1\n\tbuffer_load_dwordx2 %1, %3, %5, %6 offen offset:512 sc1\n\tbuffer_load_dword %2, %4, %5, %6 offen offset:1024 sc1"
                     : "=&v"(xa), "=&v"(xb), "=&v"(xc) : "v"((u32)lane * 8u), "v"((u32)lane * 4u), "s"(sx), "s"(uu((u32)(s * 32 + ri) * (DM * 2))) : "memory");
        SCHED();
    };
    ywait(1u);
    BAR();
    EV(15);
#pragma unroll 1
    for (int i = 0; i < 5; ++i) yfill(0, i);
    if (L == 1) wait_flags(rs, lane == 0 ? near_(foff(F_H2 + (ri >> 2)), 1u, pol) : OOB, 1u, dead);
    xload(0);
    LD4A(fv, fp, 0);
    VMW(0); SCHED();
    asm volatile("" : "+v"(xa), "+v"(xb), "+v"(xc), "+v"(fv));
    BAR();
    for (int s = 0; s < NSEG; ++s) {
        u32 xi[5] = {xa.x, xa.y, xb.x, xb.y, xc};
        u32 pa = near_(p_item(s), (u32)L, pol) + (u32)ri * 256u, pc = pa + 65536u + (u32)(lane >> 4) * 81920u + (u32)(lane & 15) * 16u;
        pa += (u32)(lane >> 5) * 81920u + (u32)((lane >> 4) & 1) * 16384u + (u32)(lane & 15) * 16u;
        u32 yf = s == 1 ? foff(F_CV + min(threadIdx.x, 159u)) : s == 25 ? foff(F_CV + min(lane, 35)) : OOB;
        asm volatile("" : "+v"(xi[0]), "+v"(xi[1]), "+v"(xi[2]), "+v"(xi[3]), "+v"(xi[4]), "+v"(pa), "+v"(pc), "+v"(yf));
#define RS(k) do {} while (0)
        RS(0);
        if (w == 0 && ((u32)__builtin_amdgcn_ballot_w64(fv <= (u32)s) & 0xFFFFFu) != 0u) wait_flags(rs, fp, (u32)s + 1, dead);
        BAR();
        RS(1);
        v4u d[5];
        asm volatile("buffer_load_dwordx4 %0, %5, %7, 0 offen sc1\n\tbuffer_load_dwordx4 %1, %5, %7, %8 offen sc1\n\tbuffer_load_dwordx4 %2, %5, %7, %9 offen sc1\n\t"
                     "buffer_load_dwordx4 %3, %5, %7, %10 offen sc1\n\tbuffer_load_dwordx4 %4, %6, %7, 0 offen sc1"
                     : "=&v"(d[0]), "=&v"(d[1]), "=&v"(d[2]), "=&v"(d[3]), "=&v"(d[4]) : "v"(pa), "v"(pc), "s"(srd), "s"(163840), "s"(32768), "s"(196608) : "memory");
        RS(2);
        LS2(L == 0, 192 + ri);
        SCHED();
        f32x2 y[5];
        {
            if ((s & 7) == 0) {
                lptr ya = lds + ((s >> 3) & 1) * 40960 + (w >> 1) * 20480 + (w & 1) * 16, yc = ya + 4096 + (lane >> 1) * 32 + (lane & 1) * 10240;
#pragma unroll
                for (int i = 0; i < 8; ++i) y8[i] = lds_r16(ya + lane * 32 + (i >> 2) * 2048 + (i & 3) * 5120);
                y8[8] = lds_r16(yc); y8[9] = lds_r16(yc + 5120);
            }
#pragma unroll
            for (int i = 0; i < 10; ++i) {
                y[i >> 1][i & 1] = lo_f(y8[i].x);
                y8[i] = (v4u){__builtin_amdgcn_alignbit(y8[i].y, y8[i].x, 16), __builtin_amdgcn_alignbit(y8[i].z, y8[i].y, 16), __builtin_amdgcn_alignbit(y8[i].w, y8[i].z, 16), y8[i].w >> 16};
                PIN(y8[i]);
            }
            asm volatile("" : "+v"(y[0]), "+v"(y[1]), "+v"(y[2]), "+v"(y[3]), "+v"(y[4]));
        }
        u32 yv;
        {
            xload(s + 1);
            const bool yy = (s & 7) - 2u < 5u && s < NSEG - 8;
            u32 yo = yy ? (u32)lane * 16u : OOB;
            DMA32(uu(yy ? (((s >> 3) + 1) & 1) * 40960 + w * 10240 + ((s & 7) - 2) * 2048 : 81920), yo, uu(W_Y + (u32)((ri >> 1) * 8 + (s >> 3) + 1) * 20480u + (w & 1) * 10240 + ((s & 7) - 2) * 2048));
            LD4A(yv, yf, 0);
            asm volatile("s_waitcnt vmcnt(6)" : "+v"(d[0]), "+v"(d[1]), "+v"(d[2]), "+v"(d[3]), "+v"(d[4]));
        }
        f32x2 o[5];
        {
            f32x2 t[5][2];
#pragma unroll
            for (int i = 0; i < 5; ++i) {
                const auto e = __builtin_amdgcn_permlane32_swap(d[i].x, d[i].z, false, false), f = __builtin_amdgcn_permlane32_swap(d[i].y, d[i].w, false, false);
                t[i][0] = up2(e[0]) + up2(e[1]); t[i][1] = up2(f[0]) + up2(f[1]);
            }
            o[0] = t[0][0] + t[1][0]; o[1] = t[0][1] + t[1][1]; o[2] = t[2][0] + t[3][0]; o[3] = t[2][1] + t[3][1];
            const auto e = __builtin_amdgcn_permlane16_swap(u_(t[4][0].x), u_(t[4][1].x), false, false), f = __builtin_amdgcn_permlane16_swap(u_(t[4][0].y), u_(t[4][1].y), false, false);
            o[4] = (f32x2){f_(e[0]) + f_(e[1]), f_(f[0]) + f_(f[1])};
        }
        f32x2 x1[5];
#pragma unroll
        for (int i = 0; i < 5; ++i) x1[i] = up2(pk2(up2(xi[i]) + up2(pk2(o[i]))));
        asm volatile("" : "+v"(x1[0]), "+v"(x1[1]), "+v"(x1[2]), "+v"(x1[3]), "+v"(x1[4]));
        RS(3);
        const f32x2 st1 = ln_stats(x1);
        u32 xp[5];
#pragma unroll
        for (int i = 0; i < 5; ++i) xp[i] = pk2(x1[i] + ((x1[i] - st1.x) * st1.y) * y[i]);
        const int row = s * 32 + ri;
        RS(4);
        if (L == 0) {
            const u32 ro = (u32)row * (DM * 2);
            f32x2 x2[5];
#pragma unroll
            for (int i = 0; i < 5; ++i) x2[i] = up2(xp[i]);
            st8s(rs, (u32)lane * 8, W_X2 + ro, (v2u){xp[0], xp[1]});
            st8s(rs, (u32)lane * 8 + 512, W_X2 + ro, (v2u){xp[2], xp[3]});
            st4s(rs, (u32)lane * 4 + 1024, W_X2 + ro, xp[4]);
            SCHED();
            const f32x2 st2 = ln_stats(x2);
            u32 hp[5];
#pragma unroll
            for (int i = 0; i < 5; ++i) hp[i] = pk2((x2[i] - st2.x) * st2.y);
            store_h(rs, near_(W_H2 + (u32)s * H_TILE, 1u, pol), row, hp);
        } else {
            const rsrc_t ro = mk_rsrc(p.out, ACT);
            __builtin_amdgcn_raw_buffer_store_b64((v2u){xp[0], xp[1]}, ro, (u32)lane * 8u, (u32)row * (DM * 2), OUTAUX);
            __builtin_amdgcn_raw_buffer_store_b64((v2u){xp[2], xp[3]}, ro, (u32)lane * 8u + 512u, (u32)row * (DM * 2), OUTAUX);
            __builtin_amdgcn_raw_buffer_store_b32(xp[4], ro, (u32)lane * 4u + 1024u, (u32)row * (DM * 2), OUTAUX);
        }
        RS(5);
        LD4A(fv, fp, 0);
        if (L == 0) asm volatile("s_waitcnt vmcnt(1)\n\ts_barrier" ::: "memory");
        st4s(rs, fo, 0, (u32)s + 1);
        RS(6);
        VMW(1); SCHED();
        asm volatile("" : "+v"(fv), "+v"(yv), "+v"(xa), "+v"(xb), "+v"(xc));
        if (__builtin_expect(s == 1 || s == 25, 0) && !all_(yv >= (s == 1 ? 2u : 3u))) ywait(s == 1 ? 2u : 3u);
        RS(7);
        if (ri == 0) LST(320 + L * 64 + s, rt32());
        LS2(L == 0, 224 + ri);
    }
}

constexpr float SCALE = 0.08838834764831845f, LOG2E = 1.4426950408889634f;
constexpr int C_K = 0, C_V = 8192, C_QX = 16384, C_MEM = 24576, C_Q = C_MEM + 10240, C_KM = C_Q + 2304, C_VM = C_KM + 2304, C_DUMP = C_VM + 2048,
              C_ZERO = C_DUMP + 1024, C_WL = C_ZERO + 128;
constexpr int NLF = 14;
constexpr int C_MASK = C_WL + 4 * NLF * 2048;
static_assert(C_WL % 16 == 0 && C_MASK + 3072 <= LDS_BYTES, "head lds");
#define CH_H0 98
#define CH_W0 80
#define CH_MH 11
#define CH_MW 18
#define CH_REC 12
#define CH_LATE 30
#define CH_SOLO 6
#define CH_KV 12
#define CH_KV0 60
#define CH_M0 3
#define CH_DEC 1u
#define CH_EARLY 10
#define CH_NEAR 4
#define CH_UP 3
#define CH_WX 6
DEV void wait_until(u32 t) {
    unsigned long long c;
    asm volatile("1:\n\ts_memrealtime %0\n\ts_waitcnt lgkmcnt(0)\n\ts_mov_b64 vcc, %0\n\ts_sub_u32 vcc_lo, %1, vcc_lo\n\ts_cmpk_lt_u32 vcc_lo, 400\n\ts_cbranch_scc1 1b" : "=&s"(c) : "s"(uu(t - 1u)) : "vcc", "scc", "memory");
}
#define CH_FD 14
#define CH_FC 34
#define FINE_T1(t) uu((t) - (CH_FD + 1u))
#define FINE_T4(t) uu(4u * (t) - CH_FC)
#define NOP8 "s_nop 0\n\ts_nop 0\n\ts_nop 0\n\ts_nop 0\n\ts_nop 0\n\ts_nop 0\n\ts_nop 0\n\ts_nop 0\n\t"
#define LOOP_TXT(tmp, t1) "s_memrealtime vcc\n1:\n\ts_waitcnt lgkmcnt(0)\n\ts_sub_u32 " tmp ", " t1 ", vcc_lo\n\ts_memrealtime vcc\n\ts_cmpk_lt_u32 " tmp ", 400\n\ts_cbranch_scc1 1b\n\t"
#define WAIT_TXT(tmp, t1, t4) LOOP_TXT(tmp, t1) \
    "s_nop 1\n\ts_memrealtime s[90:91]\n\ts_nop 3\n\ts_memrealtime s[92:93]\n\ts_nop 3\n\ts_memrealtime s[94:95]\n\ts_getpc_b64 s[96:97]\n2:\n\ts_waitcnt lgkmcnt(0)\n\t" \
    "s_add_u32 s90, s90, vcc_lo\n\ts_add_u32 s92, s92, s94\n\ts_sub_u32 s90, " t4 ", s90\n\ts_sub_u32 s90, s90, s92\n\ts_max_i32 s90, s90, 0\n\ts_min_i32 s90, s90, 29\n\t" \
    "s_mul_i32 s90, s90, 87\n\ts_lshr_b32 s90, s90, 4\n\ts_andn2_b32 s90, s90, 3\n\ts_sub_u32 s90, 3f-2b, s90\n\ts_add_u32 s96, s96, s90\n\ts_addc_u32 s97, s97, 0\n\t" \
    "s_setpc_b64 s[96:97]\n\t" NOP8 NOP8 NOP8 NOP8 NOP8 "3:\n\t"
#define WAIT_CLOB "vcc", "scc", "s90", "s91", "s92", "s93", "s94", "s95", "s96", "s97", "memory"
#define SMF_A(acc, a, b) asm volatile("v_smfmac_f32_16x16x64_bf16 %0, %1, %2, %3" : "+v"(acc) : "v"(a), "a"(b), "v"(idx))
#define SMF_V(acc, a, b) asm volatile("v_smfmac_f32_16x16x64_bf16 %0, %1, %2, %3" : "+v"(acc) : "v"(a), "v"(b), "v"(idx))
#define MF0(d, a, b) asm volatile("v_mfma_f32_16x16x32_bf16 %0, %1, %2, 0" : "=&v"(d) : "v"(a), "v"(b))
#define MF1(d, a, b) asm volatile("v_mfma_f32_16x16x32_bf16 %0, %1, %2, %0" : "+v"(d) : "v"(a), "v"(b))
#define MF0H(d, a, b) asm volatile("v_mfma_f32_16x16x16_bf16 %0, %1, %2, 0" : "=&v"(d) : "v"(a), "v"(b))
#define MF1H(d, a, b) asm volatile("v_mfma_f32_16x16x16_bf16 %0, %1, %2, %0" : "+v"(d) : "v"(a), "v"(b))
DEV v8u tr_frag(lptr p0, lptr p1, lptr p2, lptr p3) { return cat8(tr2(p0, p1), tr2(p2, p3)); }
constexpr int qslot(int ks) { return (ks & 1) && ks < 8 ? ks >> 1 : -1; }
constexpr int kslot(int ks) { return ks < 4 ? 4 + ks : -1; }
constexpr int vslot(int ks) { return ks >= 4 ? 4 + ks : -1; }
DEV u32 ring_map(u32 off, u32 half) { return ((off >> 13) << 14) + (half << 13) + (off & 8191u); }
#define ZRD(x, i) x = __builtin_bit_cast(f32x4, lds_r16(ZP + 16 * (i)))
#define VMAX(d, a, b) asm volatile("v_max_f32 %0, %1, %2" : "=v"(d) : "v"(a), "v"(b))
#define VMAX3(d, a, b, c_) asm volatile("v_max3_f32 %0, %1, %2, %3" : "=v"(d) : "v"(a), "v"(b), "v"(c_))
#define VADD(d, a, b) asm volatile("v_add_f32 %0, %1, %2" : "=v"(d) : "v"(a), "v"(b))
#define VEXP(d, a) asm volatile("v_exp_f32 %0, %1" : "=v"(d) : "v"(a))
#define VFMC(d, a, b) asm volatile("v_fmamk_f32 %0, %1, 0x3e0293ee, %2" : "=v"(d) : "v"(a), "v"(b))
#define VMULC(d, a) asm volatile("v_mul_f32 %0, 0x3e0293ee, %1" : "=v"(d) : "v"(a))
#define SEL(d, a, b) asm volatile("v_cndmask_b32 %0, %1, %2, %3" : "=v"(d) : "v"(a), "v"(b), "s"(himask))
#define QPERM(d, a, q) asm volatile("v_mov_b32_dpp %0, %1 quad_perm:" q " row_mask:0xf bank_mask:0xf" : "=v"(d) : "v"(a))

#define SUMRANGE 0x28000000u
#define HSAMP 71
DEV void role_head(const Params& p, lptr lds, int L, int h) {
    const int tid = threadIdx.x, lane = tid & 63, w = uni(tid >> 6), g = lane >> 4, r = lane & 15;
    const rsrc_t rs = mk_rsrc(p.ws, W_END);
    v8u wq0[10], wq1[10], wk0[10], wk1[10], wv0[10], wv1[10];
    lptr WL = lds + C_WL + w * (NLF * 2048) + lane * 16;
    {
        constexpr int GS = 1056, CS = 48 * GS;
        const v4u sq = mk_srd(p.Wq, DM * NQ * 2), sk = mk_srd(p.Wk, DM * NQ * 2), sv = mk_srd(p.Wv, DM * NQ * 2);
        const u32 vo = (u32)(g * NQ + h * DH + 8 * (r ^ ((g & 2) << 2))) * 2u;
        auto fill = [&](int ch) {
#pragma unroll 1
            for (int i = 0; i < 4; ++i) {
                const u32 dst = uu((ch % 3) * CS + (4 * i + w) * GS), so = uu((ch * 64 + 16 * i + 4 * w) * (NQ * 2));
                DMAIN(sq, dst, vo, so); DMAIN(sk, dst + 16 * GS, vo, so); DMAIN(sv, dst + 32 * GS, vo, so);
            }
        };
        int vote = 0;
        if (L == 1) {
            if (w == 0) vote = probe(rs, h);
            BAR();
            EV(2);
            pre_rings(p, rs, h);
        }
        fill(0); fill(1); fill(2);
        const int q4 = r >> 2;
        lptr tb = lds + (4 * (g >> 1) + q4) * GS + (g & 1) * 512 + ((w * 64 + (r & 3) * 8) ^ ((g & 1) << 7));
        SFOR(ch, 10)
            VMW(ch == 1 || ch == 2 ? 25 : ch < 8 ? 24 : 108 - 12 * ch);
            BAR();
            if constexpr (ch == 0) boot_set(p, rs, tid == 0 && L == 1 ? foff(F_RG + h) : OOB, (u32)((1 << 20) + vote));
            auto frag = [&](int m, int t) { lptr b = tb + (ch % 3) * CS + m * 16 * GS + t * 32; return tr_frag(b, b + 256, b + 8 * GS, b + 8 * GS + 256); };
            wq0[ch] = frag(0, 0); wq1[ch] = frag(0, 1); wk0[ch] = frag(1, 0); wk1[ch] = frag(1, 1); wv0[ch] = frag(2, 0); wv1[ch] = frag(2, 1);
            asm volatile("" : "+a"(wq0[ch]), "+a"(wk0[ch]), "+v"(wv0[ch]), "+v"(wv1[ch]));
            if constexpr (qslot(ch) < 0) asm volatile("" : "+a"(wq1[ch])); else asm volatile("" : "+v"(wq1[ch]));
            if constexpr (kslot(ch) < 0) asm volatile("" : "+a"(wk1[ch])); else asm volatile("" : "+v"(wk1[ch]));
            BAR();
            if constexpr (ch < 7) fill(ch + 3);
        SEND
#pragma unroll
        for (int ch = 0; ch < 10; ++ch) {
            if (qslot(ch) >= 0) { lds_w16(WL + qslot(ch) * 2048, wq1[ch].lo); lds_w16(WL + qslot(ch) * 2048 + 1024, wq1[ch].hi); }
            if (kslot(ch) >= 0) { lds_w16(WL + kslot(ch) * 2048, wk1[ch].lo); lds_w16(WL + kslot(ch) * 2048 + 1024, wk1[ch].hi); }
            if (vslot(ch) >= 0) { lds_w16(WL + vslot(ch) * 2048, wv1[ch].lo); lds_w16(WL + vslot(ch) * 2048 + 1024, wv1[ch].hi); }
        }
        CFENCE();
    }
    const u32 idx = (r & 2) ? 0xEEEEu : 0x4444u;
    const unsigned long long himask = 0xCCCCCCCCCCCCCCCCull;
    bool dead = false;
    EV(6);
    const u32 pol = get_pol(p, rs, dead);
    const u32 pol0 = pol & 1u;
    const u32 l16 = (u32)lane * 16u;
    const u32 mring = W_MRING + (u32)L * (MB / 4) + pol0 * 8192u, hint = mring + 4 * 32768u;
    const u32 fkh = lane < 3 ? near_(foff(F_QKV + lane * 64 + L * 32 + h), 0u, pol) : lane < 6 ? hint + (u32)(lane - 3) * 4u : OOB;
    u32 tm = 0;
    MK_SRD();
    const unsigned long long kmask = __builtin_amdgcn_ballot_w64((hblk(W_KQ, L * NSEG + lane, h, 0u, pol) & 8192u) != 0u),
                             vmask = __builtin_amdgcn_ballot_w64((hblk(W_VO, L * NSEG + lane, h, 1u, pol) & 8192u) != 0u);
    u32 kb = uu(W_KQ + (u32)(L * NSEG * NH + h) * 16384u + w * 2048);
    u32 dk = uu(C_K + w * 2048), dv = dk + (C_V - C_K), dq = dk + (C_QX - C_K), dm = uu(C_MEM + w * 2048), dm2 = uu(w < 2 ? C_MEM + 8192 + w * 1024 : C_DUMP);
    asm volatile("" : "+s"(dk), "+s"(dv), "+s"(dq), "+s"(dm), "+s"(dm2));
    u32 ksrc = kb + ((u32)kmask & 1u) * 8192u, vsrc = kb + (W_VO - W_KQ) + ((u32)vmask & 1u) * 8192u;
    lptr qw = lds + C_Q + (2 * g) * 288 + (32 * w + 2 * r) * 2;
    lptr kw = qw + (C_KM - C_Q);
    lptr qr = lds + C_Q + (2 * (r >> 2) + (r & 1)) * 288 + g * 16;
    lptr LB = lds + l16, VX = lds + C_V + w * 2048 + l16;
    if (tid >= 64 && tid < 70) { const u32 z = tid == 68 ? 0x3F803F80u : tid == 69 ? PSENT : 0u; lds_w16(lds + C_ZERO + (tid - 64) * 16, (v4u){z, z, z, z}); }
    if (w < 3) {
        const u32 ng = u_(-1000000000.0f * LOG2E);
        v4u mk;
#pragma unroll
        for (int i = 0; i < 4; ++i) mk[i] = w == 0 || (w == 1 && 4 * g + i > r) ? ng : 0u;
        lds_w16(lds + C_MASK + w * 1024 + l16, mk);
    }
    u32 zaddr = 0; PIN(zaddr);
    lptr ZP = lds + zaddr + C_ZERO;
#define LF_RD(slot, hf) lf[slot][hf] = lds_r16(WL + (slot) * 2048 + (hf) * 1024)
    BAR();
    const u32 ofl = tid == 192 ? near_(foff(F_O + L * 32 + h), 0u, pol) : tid == 193 && (h & 15) == 0 ? hint + 128u + (u32)h * 8u : OOB;
    u32 oso = (g & 1) ? OOB : W_ORING + (u32)L * (MB / 2) + ring_map((u32)((r >> 3) * 32768 + h * 1024 + w * 256 + (g >> 1) * 128 + (r & 7) * 16), pol0);
    u32 missed = 0, dirty = 1, rep = PSENT;
    asm volatile("" : "+v"(oso), "+v"(qw), "+v"(kw), "+v"(qr), "+v"(VX));
    asm volatile("" : "+s"(ksrc), "+s"(vsrc));
    wait_flags(rs, lane < 3 ? fkh : OOB, 1u, dead);
    EV(7);
    DMA32(dv, l16, vsrc); DMA32(dk, l16, ksrc);
    EV(8);
    auto next_slot = [&](u32 now) {
        const u32 hv = ld4(rs, fkh), per = rdl(hv, 4);
        if (per < 400u) { tm = rdl(hv, 5) - per; for (int n = 0; n < 3 && (int)(tm - now) < 0; ++n) tm += per; }
    };
    if (L == 1) { next_slot(rt32() + CH_KV0); tm -= 24u; }
    for (int s = 0; s < NSEG;) {
        {
            *(LAS u32*)(lds + C_DUMP + 128 + w * 4) = tm;
            VMW(5); BAR();
            st4s(rs, ofl, 0, (l16 & 16u) ? rep : (u32)s);
            const v4u c = lds_r16(lds + C_DUMP + 128) - tm;
            const u32 add = uu((u32)max(max((int)c.x, (int)c.y), max((int)c.z, (int)c.w)));
            tm += add;
            if (__builtin_expect((dirty | add) != 0u, 0)) {
                wait_until(tm - 44u);
                st16(rs, oso, lds_r16(ZP + 80));
            }
        }
        u32 ms = mring + (u32)(s & 3) * 32768u + w * 2048, ms2 = mring + (u32)(s & 3) * 32768u + 16384u + (w & 1) * 1024;
        u32 l3 = w < 2 ? l16 : OOB;
        asm volatile("" : "+s"(ms), "+s"(ms2)); PIN(l3);
        f32x4 aq0, aq1, ak0, ak1;
        v4u lf[NLF][2];
        ZRD(aq0, 0); ZRD(aq1, 1); ZRD(ak0, 2); ZRD(ak1, 3);
#pragma unroll
        for (int i = 0; i < 4; ++i) { LF_RD(i, 0); LF_RD(i, 1); }
        SCHED();
        v4u A[10];
#define HT(k) do {} while (0)
        v4u kx[8], qf[4], kmf[4], vx[2], ones, P;
        float fa, fb, fc, fd, sx[4], t[4], sm0, sm1, tm0, tm1, inv; u32 pa, pb, fl, pmm, tmp = 0; unsigned long long odd;
        f32x4 av0, av1, s0, s1, sm, oa, ob, rsum;
        v2u vm, pm, vh, vl, on[2];
        f32x2 zz[4];
#define LW(sl) cat8(lf[sl][0], lf[sl][1])
#define PQ(ks, tl) do { if constexpr (!(tl)) SMF_A(aq0, A[ks], wq0[ks]); else if constexpr (qslot(ks) < 0) SMF_A(aq1, A[ks], wq1[ks]); else { const v8u lw_ = LW(qslot(ks)); SMF_V(aq1, A[ks], lw_); } } while (0)
#define PK(ks, tl) do { if constexpr (!(tl)) SMF_A(ak0, A[ks], wk0[ks]); else if constexpr (kslot(ks) < 0) SMF_A(ak1, A[ks], wk1[ks]); else { const v8u lw_ = LW(kslot(ks)); SMF_V(ak1, A[ks], lw_); } } while (0)
#define PV(ks, tl) do { if constexpr (!(tl)) SMF_V(av0, A[ks], wv0[ks]); else if constexpr (vslot(ks) < 0) SMF_V(av1, A[ks], wv1[ks]); else { const v8u lw_ = LW(vslot(ks)); SMF_V(av1, A[ks], lw_); } } while (0)
#define KXR(i) kx[i] = lds_r16(LB + C_K + ((i) & 1) * 4096 + ((i) >> 1) * 1024)
#define XW(q_) do { *(LAS u32*)(q_) = pa; *(LAS u32*)((q_) + 288) = pb; } while (0)
        auto slot = [&](auto zc) __attribute__((always_inline)) {
            constexpr int z = decltype(zc)::value;
            constexpr int vs[] = {0, 8, 1, 10, 9, 2, 11, -1, -1, -1, -1, 4, 6, 12, -1, -1, -1, 13, 14, -1, 16, 18, 15, 3, 5, 17, 7, 19};
            if constexpr (z < 12) PQ(z >> 1, z & 1);
            else if constexpr (z < 20) PQ(6 + (z & 3), z >> 4);
            else if constexpr (z < 32) PK((z - 20) >> 1, z & 1);
            else if constexpr (z < 40) PK(6 + (z & 3), (z >> 2) & 1);
            else if constexpr (z < 42) { if constexpr (z == 40) MF0(s0, kx[0], qf[0]); else MF0(s1, kx[1], qf[0]); }
            else if constexpr (z < 48) { if constexpr (z & 1) MF1(s1, kx[z - 40], qf[(z - 40) >> 1]); else MF1(s0, kx[z - 40], qf[(z - 40) >> 1]); }
            else if constexpr (z == 55) MF0(sm, kmf[0], qf[0]);
            else if constexpr (z > 55 && z < 59) MF1(sm, kmf[z - 55], qf[z - 55]);
            else if constexpr (z == 62) MF0(rsum, ones, P);
            else if constexpr (z == 63) MF0(oa, vx[0], P);
            else if constexpr (z == 64) MF0(ob, vx[1], P);
            else if constexpr (z == 67) { const v2u ones2 = ones.lo; MF1H(rsum, ones2, pm); }
            else PV(vs[z - 48] >> 1, vs[z - 48] & 1);
#define USE(x) asm volatile("" :: "v"(x))
            if constexpr (z == 20 || z == 24 || z == 26) USE(lf[4 + ((z - 20) >> 1)][1]);
            if constexpr (z == 41) USE(qf[3]);
            if constexpr (z == 45 || z == 46) USE(kx[z - 39]);
            if constexpr (z == 53) USE(lf[9][1]);
            if constexpr (z >= 54 && z < 58) USE(kmf[z - 54]);
            if constexpr (z == 61) USE(vl);
            if constexpr (z == 64) USE(lf[10][1]);
            if constexpr (z == 69) USE(lf[11][1]);
            if constexpr (z == 72) USE(lf[12][1]);
            if constexpr (z == 74) USE(lf[13][1]);
            if constexpr (z < 8) A[2 + z] = lds_r16(LB + C_MEM + (2 + z) * 1024);
            if constexpr (z >= 15 && z < 23) LF_RD(4 + ((z - 15) >> 1), (z - 15) & 1);
            if constexpr (z >= 24 && z < 27) KXR(z - 24);
            if constexpr (z == 27) XW(qw);
            if constexpr (z >= 28 && z < 31) KXR(z - 25);
            if constexpr (z == 32) asm volatile("s_waitcnt lgkmcnt(3)\n\ts_barrier" ::: "memory");
            if constexpr (z >= 33 && z < 37) qf[z - 33] = lds_r16(qr + (z - 33) * 64);
            if constexpr (z == 37 || z == 38) LF_RD(8, z - 37);
            if constexpr (z == 39 || z == 41) KXR(6 + (z == 41));
            if constexpr (z == 44) XW(kw);
            if constexpr (z == 45 || z == 46) LF_RD(9, z - 45);
            if constexpr (z == 47) vx[0] = lds_r16(VX);
            if constexpr (z == 48) { VMW(0); vh = *(LAS v2u*)(VX + 1032); }
            if constexpr (z == 54) { vl = *(LAS v2u*)(VX + 1024); vx[1] = (v4u){vh.x, vh.y, vl.x, vl.y}; }
            if constexpr (z == 49) { USE(lf[9][0]); asm volatile("s_barrier" ::: "memory"); }
            if constexpr (z >= 50 && z < 54) kmf[z - 50] = lds_r16(qr + (C_KM - C_Q) + (z - 50) * 64);
            if constexpr (z == 59 || z == 60) LF_RD(10, z - 59);
            if constexpr (z == 61 || z == 65) LF_RD(11, z == 65);
            if constexpr (z == 66 || z == 68) LF_RD(12, z == 68);
            if constexpr (z == 69 || z == 70) LF_RD(13, z - 69);
            if constexpr (z == HSAMP) fl = ld4(rs, fkh);
            SCHED();
            if constexpr (z == 19) HT(2);
            if constexpr (z == 39) HT(3);
            if constexpr (z == 51) HT(4);
            if constexpr (z == 64) HT(5);
            if constexpr (z == 20) VADD(fa, aq0[0], aq0[2]);
            if constexpr (z == 21) VADD(fb, aq0[1], aq0[3]);
            if constexpr (z == 23) VADD(fc, aq1[0], aq1[2]);
            if constexpr (z == 24) VADD(fd, aq1[1], aq1[3]);
            if constexpr (z == 25) VCVT(pa, fa, fc);
            if constexpr (z == 26) VCVT(pb, fb, fd);
            if constexpr (z >= 44 && z < 48) asm volatile("v_mov_b64 %0, 0" : "=v"(zz[z - 44]));
            if constexpr (z == 47) { av0 = __builtin_shufflevector(zz[0], zz[1], 0, 1, 2, 3); av1 = __builtin_shufflevector(zz[2], zz[3], 0, 1, 2, 3); }
            if constexpr (z == 48 || z == 49) asm volatile("v_mov_b64 %0, %1" : "=v"(on[z - 48]) : "s"(0x3F803F803F803F80ull));
            if constexpr (z == 49) ones = __builtin_shufflevector(on[0], on[1], 0, 1, 2, 3);
            if constexpr (z == 40) { VADD(fa, ak0[0], ak0[2]); VADD(fb, ak0[1], ak0[3]); }
            if constexpr (z == 42) { VADD(fd, ak1[1], ak1[3]); VADD(fc, ak1[0], ak1[2]); }
            if constexpr (z == 43) { VCVT(pb, fb, fd); VCVT(pa, fa, fc); }
            if constexpr (z >= 50 && z < 54) SEL(sx[z - 50], s0[z - 50], s1[z - 50]);
            if constexpr (z >= 54 && z < 58) VEXP(t[z - 54], sx[z - 54]);
            if constexpr (z == 58) asm volatile("v_cvt_pk_bf16_f32 %0, %2, %3\n\tv_cvt_pk_bf16_f32 %1, %4, %5" : "=&v"(P.x), "=v"(P.y) : "v"(t[0]), "v"(t[1]), "v"(t[2]), "v"(t[3]));
            if constexpr (z == 59) QPERM(P.z, P.x, "[2,3,0,1]");
            if constexpr (z == 60) QPERM(P.w, P.y, "[2,3,0,1]");
            if constexpr (z == 61) { USE(sm); VMULC(sm0, sm[0]); }
            if constexpr (z == 62) { VEXP(tm0, sm0); VMULC(sm1, sm[1]); }
            if constexpr (z == 63) VEXP(tm1, sm1);
            if constexpr (z == 64) asm volatile("v_cvt_pk_bf16_f32 %0, %2, %3\n\tv_cndmask_b32 %1, %0, 0, %4" : "=&v"(pmm), "=v"(pm.x) : "v"(tm0), "v"(tm1), "s"(himask));
            if constexpr (z == 65) asm volatile("v_cndmask_b32 %0, 0, %1, %2" : "=v"(pm.y) : "v"(pmm), "s"(himask));
            if constexpr (z == 67) { SEL(oa[0], oa[0], ob[0]); SEL(oa[1], oa[1], ob[1]); }
            if constexpr (z == 68 || z == 69) SEL(oa[z - 66], oa[z - 66], ob[z - 66]);
            if constexpr (z == 70) asm volatile("v_sub_u32 %0, %1, %2" : "=v"(pa) : "v"(rsum[0]), "s"(0x2B800000u), "v"(rsum));
            if constexpr (z == 71) asm volatile("v_cmp_le_u32 %0, %1, %2" : "=s"(odd) : "s"(SUMRANGE), "v"(pa));
            if constexpr (z == 72) VADD(fa, av0[0], av0[2]);
            if constexpr (z == 73) { NOPS(0); VADD(fb, av0[1], av0[3]); }
            if constexpr (z == 74) VCVT(vm.x, fa, fb);
            SCHED();
        };
        asm volatile("s_mov_b32 m0, %2\n\t" WAIT_TXT("%0", "%1", "%11") "buffer_load_dwordx4 %4, %5, %6 offen sc1 lds\n\tbuffer_load_dwordx4 %4, %5, %6 offen offset:1024 sc1 lds\n\t"
                     "s_mov_b32 m0, %3\n\ts_nop 0\n\tbuffer_load_dwordx4 %8, %5, %7 offen sc1 lds\n\ts_sleep 3\n\ts_mov_b32 m0, %9\n\ts_nop 0\n\t"
                     "buffer_load_dwordx4 %4, %5, %10 offen sc1 lds\n\tbuffer_load_dwordx4 %4, %5, %10 offen offset:1024 sc1 lds\n\ts_waitcnt vmcnt(2)\n\ts_barrier"
                     : "=&s"(tmp) : "s"(FINE_T1(tm)), "s"(dm), "s"(dm2), "v"(l16), "s"(srd), "s"(ms), "s"(ms2), "v"(l3), "s"(dq), "s"(ksrc ^ 8192u), "s"(FINE_T4(tm)) : WAIT_CLOB);
#pragma unroll
        for (int ks = 0; ks < 2; ++ks) A[ks] = lds_r16(LB + C_MEM + ks * 1024);
        SCHED();
        static_for<0, 76>(slot);
        u32 fail = 0;
        if (__builtin_expect(odd != 0ull, 0)) {
            u32 mn = ~0u;
#pragma unroll 5
            for (int i = 0; i < 10; ++i) { const v4u e = lds_r16(LB + C_MEM + i * 1024); mn = min(min(min(mn, e.x ^ PSENT), e.y ^ PSENT), min(e.z ^ PSENT, e.w ^ PSENT)); }
            fail = missed < SPIN_CAP && !all_(mn != 0u) ? OOB : 0u;
            float mx = fmaxf(fmaxf(fmaxf(sx[0], sx[1]), fmaxf(sx[2], sx[3])), fmaxf(sm0, sm1));
            mx = max32(max16(fmaxf(mx, dpp<0x4E>(mx))));
#pragma unroll
            for (int i = 0; i < 4; ++i) t[i] = __builtin_amdgcn_exp2f(sx[i] - mx);
            P.x = pk(t[0], t[1]); P.y = pk(t[2], t[3]); P.z = u_(dpp<0x4E>(f_(P.x))); P.w = u_(dpp<0x4E>(f_(P.y)));
            pmm = pk(__builtin_amdgcn_exp2f(sm0 - mx), __builtin_amdgcn_exp2f(sm1 - mx));
            pm = (r & 2) ? (v2u){0u, pmm} : (v2u){pmm, 0u};
            asm volatile("s_nop 1" : "+v"(P), "+v"(pm));
            MF0(rsum, ones, P); MF0(oa, vx[0], P); MF0(ob, vx[1], P);
            NOPS(7);
            { const v2u ones2 = ones.lo; MF1H(rsum, ones2, pm); }
#pragma unroll
            for (int i = 0; i < 4; ++i) SEL(oa[i], oa[i], ob[i]);
            asm volatile("s_nop 4" :: "v"(rsum));
            oso |= fail;
        }
        asm volatile("v_rcp_f32 %0, %3\n\ts_nop 4\n\tv_add_f32 %1, %4, %6\n\tv_add_f32 %2, %5, %7\n\tv_cvt_pk_bf16_f32 %1, %1, %2\n\ts_nop 0"
                     : "=&v"(inv), "=&v"(vm.y), "=&v"(fd) : "v"(rsum[0]), "v"(av1[0]), "v"(av1[1]), "v"(av1[2]), "v"(av1[3]));
        asm volatile("v_mfma_f32_16x16x16_bf16 %0, %1, %2, %0\n\ts_nop 6" : "+v"(oa) : "v"(vm), "v"(pm));
        {
            v4u oc;
            const f32x2 i2 = {inv, inv};
            const f32x2 u0 = oa.lo * i2, u1 = oa.hi * i2;
            asm volatile("v_cvt_pk_bf16_f32 %0, %4, %5\n\tv_cvt_pk_bf16_f32 %2, %4, %5\n\tv_cvt_pk_bf16_f32 %1, %6, %7\n\tv_cvt_pk_bf16_f32 %3, %6, %7\n\t"
                         "v_permlane16_swap_b32 %0, %2\n\ts_nop 0\n\tv_permlane16_swap_b32 %1, %3"
                         : "=&v"(oc.x), "=&v"(oc.y), "=&v"(oc.z), "=&v"(oc.w) : "v"(u0.x), "v"(u0.y), "v"(u1.x), "v"(u1.y));
            st16(rs, oso, oc);
        }
        SCHED();
        if (__builtin_expect(fail == 0u, 1)) {
        unsigned long long clk;
        asm volatile("s_memrealtime %0" : "=s"(clk));
        HT(6);
        v4u tq[4], tk[8], tv[2], px, pv;
        f32x4 c0, c1, cm, o[4], sum;
        float ex[10], cs0, cs1;
        v2u p0, p1, tvm;
        u32 oo, kvo, oz; bool kvok;
        u32 ln = l16; PIN(ln);
        lptr QX = lds + C_QX + (w & 1) * 4096 + ln, VO = lds + C_V + (w ^ 1) * 2048 + ln, VMX = lds + C_VM + w * 512 + (ln >> 1), MK = lds + C_MASK + (w & 1) * 1024 + ln;
#define TKR(i) tk[i] = lds_r16(LB + C_K + ((i) >> 2) * 4096 + ((i) & 3) * 1024)
#pragma unroll
        for (int i = 0; i < 4; ++i) tq[i] = lds_r16(QX + i * 1024);
        c0 = __builtin_bit_cast(f32x4, lds_r16(MK + 1024));
        TKR(0);
        *(LAS v2u*)VMX = vm;
        SCHED();
        SFOR(i, 12)
            if constexpr (i == 0) MF0(cm, kmf[0], tq[0]); else if constexpr (i < 4) MF1(cm, kmf[i], tq[i]);
            else if constexpr (i < 8) MF1(c0, tk[i - 4], tq[i - 4]); else MF1(c1, tk[i - 4], tq[i - 8]);
            if constexpr (i < 3) TKR(i + 1);
            if constexpr (i == 2) c1 = __builtin_bit_cast(f32x4, lds_r16(MK));
            if constexpr (i >= 3 && i < 7) TKR(i + 1);
            if constexpr (i == 7) { vx[1] = lds_r16(VX + 1024); tv[0] = lds_r16(VO); }
            if constexpr (i == 8) tv[1] = lds_r16(VO + 1024);
            SCHED();
            if constexpr (i == 0) { oo = ln + (ln & 0x300u) + ((vsrc ^ 8192u) + (u32)((w & 1) * 256)); PIN(oo); kb += NH * 16384; PINS(kb); }
            if constexpr (i == 1) { ksrc = kb + (((kmask >> ((s + 1) & 63)) & 1ull) ? 8192u : 0u); PINS(ksrc); }
            if constexpr (i == 3) { vsrc = kb + (((vmask >> ((s + 1) & 63)) & 1ull) ? W_VO - W_KQ + 8192u : W_VO - W_KQ); PINS(vsrc); }
            if constexpr (i == 6) { USE(cm); VMULC(cs0, cm[0]); }
            if constexpr (i == 8) VMULC(cs1, cm[1]);
            if constexpr (i == 9) VEXP(ex[8], cs0);
            if constexpr (i == 10) { VEXP(ex[9], cs1); VEXP(ex[0], c0[0]); }
            if constexpr (i == 11) { VEXP(ex[1], c0[1]); VEXP(ex[2], c0[2]); VEXP(ex[3], c0[3]); }
            SCHED();
        SEND
        asm volatile("v_cvt_pk_bf16_f32 %0, %6, %7\n\tv_mov_b32 %1, 0\n\tv_mov_b32 %2, 0\n\tv_cvt_pk_bf16_f32 %3, %6, %7\n\tv_cvt_pk_bf16_f32 %4, %8, %9\n\tv_cvt_pk_bf16_f32 %5, %10, %11"
                     : "=&v"(p0.x), "=&v"(p0.y), "=&v"(p1.x), "=&v"(p1.y), "=&v"(px.x), "=&v"(px.y) : "v"(ex[8]), "v"(ex[9]), "v"(ex[0]), "v"(ex[1]), "v"(ex[2]), "v"(ex[3]));
        asm volatile("s_waitcnt lgkmcnt(0)\n\ts_barrier" : "+s"(clk) :: "memory");
        tvm = *(LAS v2u*)(VMX + (512 - (w & 1) * 1024));
        pv = lds_r16(ZP + 80);
        SCHED();
#pragma unroll
        for (int i = 0; i < 4; ++i) VEXP(ex[4 + i], c1[i]);
        asm volatile("v_cvt_pk_bf16_f32 %0, %2, %3\n\tv_cvt_pk_bf16_f32 %1, %4, %5\n\ts_nop 1" : "=&v"(px.z), "=&v"(px.w) : "v"(ex[4]), "v"(ex[5]), "v"(ex[6]), "v"(ex[7]));
        {
            const v2u ones2 = ones.lo;
            const u32 now = (u32)clk;
            auto pvm = [&](auto first) __attribute__((always_inline)) {
                constexpr bool F = decltype(first)::value;
                SCHED();
                MF0(sum, ones, px); if constexpr (F) { oz = (oso & ~(3u << 17)) | (u32)((s + 3) & 3) << 17; PIN(oz); } SCHED();
                MF0(o[0], vx[0], px); if constexpr (F) { oso = (oso & ~(3u << 17)) | (u32)((s + 1) & 3) << 17; PIN(oso); } SCHED();
                MF0(o[1], vx[1], px); NOPS(1);
                MF0(o[2], tv[0], px); NOPS(0);
                MF1H(sum, ones2, p0); NOPS(1);
                MF0(o[3], tv[1], px); NOPS(1);
                MF1H(o[0], vm, p0);
                MF1H(o[1], vm, p1);
                MF1H(o[2], tvm, p0);
                asm volatile("v_sub_u32 %0, %2, %3\n\tv_rcp_f32 %1, %2" : "=&v"(pa), "=&v"(inv) : "v"(sum[0]), "s"(0x2B800000u), "v"(sum));
                MF1H(o[3], tvm, p1);
                SCHED();
            };
            pvm(IC<1>{});
            if (__builtin_expect(__builtin_amdgcn_ballot_w64(pa >= SUMRANGE) != 0ull, 0)) {
                const f32x4 m0 = __builtin_bit_cast(f32x4, lds_r16(MK + 1024)), m1 = __builtin_bit_cast(f32x4, lds_r16(MK));
                float mx = fmaxf(cs0, cs1);
#pragma unroll
                for (int i = 0; i < 4; ++i) { ex[i] = m0[i] == 0.f ? c0[i] : m0[i]; ex[4 + i] = m1[i] == 0.f ? c1[i] : m1[i]; mx = fmaxf(mx, fmaxf(ex[i], ex[4 + i])); }
                mx = max32(max16(mx));
#pragma unroll
                for (int i = 0; i < 4; ++i) px[i] = pk(__builtin_amdgcn_exp2f(ex[2 * i] - mx), __builtin_amdgcn_exp2f(ex[2 * i + 1] - mx));
                p0.x = p1.y = pk(__builtin_amdgcn_exp2f(cs0 - mx), __builtin_amdgcn_exp2f(cs1 - mx));
                asm volatile("s_nop 7" : "+v"(px), "+v"(p0), "+v"(p1));
                pvm(IC<0>{});
                NOPS(3);
            }
            asm volatile("s_nop 1" : "+v"(o[0]), "+v"(o[1]));
            st16(rs, oo, (v4u){pk2(o[0].lo * inv), pk2(o[0].hi * inv), pk2(o[1].lo * inv), pk2(o[1].hi * inv)});
            asm volatile("" : "+v"(o[2]), "+v"(o[3]));
            st16(rs, oo ^ 2048u, (v4u){pk2(o[2].lo * inv), pk2(o[2].hi * inv), pk2(o[3].lo * inv), pk2(o[3].hi * inv)});
            SCHED();
            PIN(fl); kvok = ((u32)__builtin_amdgcn_ballot_w64(fl < (u32)s + 2) & 7u) == 0u;
            kvo = l16 | (kvok ? 0u : OOB);
            DMA32(dv, kvo, vsrc); DMA32(dk, kvo, ksrc);
            st16(rs, oz, pv);
            SCHED();
            rep = (u32)s << 16 | min(missed, 1u) << 14 | min(now - tm, 0x3FFFu);
            if (h == 0 && w == 3) { LST(L * 64 + s, now); LST(128 + L * 64 + s, ((int)(rdl(fl, 3) - now) >= 4) | ((int)kvok << 1) | ((int)min(missed, 3u) << 2)); LST(640 + L * 64 + s, tm); LST(1728 + L * 64 + s, __builtin_amdgcn_s_memtime()); }
            tm = rdl(fl, 5);
            missed = missed == SPIN_CAP ? missed : 0u;
            dirty = 0;
            if (__builtin_expect(rdl(fl, 3) - now + CH_LATE > 4000u && s + 1 < NSEG, 0)) {
                dirty = 1;
#pragma unroll 1
                for (int n = 0; n < 64; ++n) {
                    CFENCE();
                    const u32 hv = ld4(rs, fkh);
                    tm = rdl(hv, 5);
                    if (rdl(hv, 3) - now + CH_LATE <= 4000u) break;
                }
            }
        }
        LS2(L == 0 && w == 0, h);
        if (__builtin_expect(!kvok, 0) && s + 1 < NSEG) {
            dirty = 1;
            wait_flags(rs, ln < 48u ? fkh : OOB, (u32)s + 2, dead);
            DMA32(dv, l16, vsrc); DMA32(dk, l16, ksrc);
            const u32 now = rt32() + CH_KV;
            if ((int)(tm - now) < 0) next_slot(now);
        }
        ++s;
        } else {
            u32 ln = l16; PIN(ln);
            oso = (ln & 256u) ? oso : oso & ~OOB;
            const u32 was = tm - tmp;
            tm = rdl(fl, 5); dirty = 1;
            if ((int)(tm - rdl(fl, 4) - was) > 24) tm -= rdl(fl, 4);
            if ((++missed & 7u) == 0u) {
                *(LAS u32*)(lds + C_DUMP + (w == 0 ? 0 : 64)) = ld4(rs, foff(F_ERR));
                BAR();
                missed = uu(*(LAS u32*)(lds + C_DUMP)) != 0u ? SPIN_CAP : missed;
                st4s(rs, missed == SPIN_CAP ? foff(F_ERR) : OOB, 0, 1u);
            }
        }
    }
    DRAIN(); BAR();
    st4s(rs, ofl, 0, (l16 & 16u) ? rep : NSEG);
}

#define WO_NI 4
#define WROT 0
DEV void role_wo(const Params& p, lptr lds, int j) {
    const int tid = threadIdx.x, lane = tid & 63, w = uni(tid >> 6), g = lane >> 4, r = lane & 15;
    const int rg = j & 1, cg = j >> 1, rot = cg & WROT;
    const rsrc_t rs = mk_rsrc(p.ws, W_END);
    v8u wo[8][5];
    {
        const v4u so_ = mk_srd(p.Wo, DM * NQ * 2);
        const int one = (0x3F2FF1F0u >> (4 * (cg & 7))) & 15;
        u32 vo[3];
#pragma unroll
        for (int i = 0; i < 3; ++i) { const int n = 64 * i + lane; vo[i] = n % 6 < 5 ? (u32)((n / 6) * (DM * 2) + cg * 80 + (n % 6) * 16) : ((n / 6) & 3) == one ? (u32)((n / 6) * (DM * 2) + (cg >> 3) * 640 + 304 + rg * 16) : OOB; }
        auto fill = [&](int b) {
            const u32 dst = uu(w * 36864 + (b % 3) * 12288), src = uu((8 * w + ((b + rot) & 7)) * (DH * DM * 2));
#pragma unroll
            for (int i = 0; i < 12; ++i) DMAIN(so_, dst + i * 1024, vo[i % 3], src + (i / 3) * (32 * DM * 2));
        };
#pragma unroll 1
        for (int b = 0; b < 3; ++b) fill(b);
        const int q4 = r >> 2;
        lptr tb = lds + w * 36864 + (32 * (g >> 1) + 16 * (q4 >> 1) + 8 * ((r >> 1) & 1) + 4 * (g & 1) + (q4 & 1)) * 96 + (r & 1) * 8;
        SFOR(b, 8)
            VMW(b < 6 ? 24 : 84 - 12 * b);
#pragma unroll
            for (int t = 0; t < 5; ++t) {
                lptr q = tb + (b % 3) * 12288 + (t == 0 ? 4 : (w + t) & 3) * 16;
                wo[b][t] = tr_frag(q, q + 192, q + 64 * 96, q + 64 * 96 + 192);
                if (t < 4) asm volatile("" : "+a"(wo[b][t])); else asm volatile("" : "+v"(wo[b][t]));
            }
            CFENCE();
            if constexpr (b < 5) fill(b + 3);
        SEND
    }
    BAR();
    EV(6);
    const u32 idx = (r & 2) ? 0xEEEEu : 0x4444u;
    bool dead = false;
    const u32 pol0 = get_pol(p, rs, dead) & 1u;
    const u32 l16 = (u32)lane * 16u;
    u32 oo[8];
#pragma unroll
    for (int i = 0; i < 8; ++i) oo[i] = uu(W_ORING + ring_map((u32)(rg * 32768 + (8 * w + ((i + rot) & 7)) * 1024), pol0));
    const u32 hint = W_MRING + 4 * 32768u + pol0 * 8192u;
    u32 hvo[2];
#pragma unroll
    for (int l = 0; l < 2; ++l) hvo[l] = lane < 2 ? hint + (u32)(l ^ 1) * (MB / 4) + (u32)lane * 4u : lane == 2 ? hint + (u32)l * (MB / 4) + 128u : lane == 3 ? foff(F_ERR) : lane == 4 ? hint + (u32)l * (MB / 4) + 256u : OOB;
    const int xo = g * 128 + (r & 7) * 16, O = 5 * cg + (r < 8 ? w : 4), q2 = 2 + (r >> 3);
    lptr xw0 = lds + (r < 8 ? 2048 + w * 4 : ((w + 1) & 3) * 512 + 4) + xo, xw1 = lds + ((w + q2) & 3) * 512 + q2 * 4 + xo;
    lptr xr = lds + (r < 8 ? w : 4) * 512 + xo;
    if (r < 8) { *(LAS u32*)xr = 0u; *(LAS u32*)(xr + 8192) = 0u; }
    const u32 mpo = (r & 4) == 0 && (r < 8 || w == 0) ? W_MRING + ring_map((u32)((O >> 1) * 256 + rg * 128 + (4 * (g & 1) + 2 * (O & 1) + (g >> 1)) * 16 + (r & 3) * 4), pol0) : OOB;
    constexpr u32 PER0 = CH_W0 + CH_MW + CH_H0 + CH_MH;
    const bool lead = j == 0 && w == 3;
    u32 hso[2] = {OOB, OOB};
    MK_SRD();
    u32 t = 0, tn = 0;
    int sn[2] = {0, 0};
    u32 hav = 4 * CH_H0, wav = 4 * CH_W0, mx = 8 * CH_M0, ok[2] = {0u, 0u}, dw[2] = {0u, 0u};
    u32 zero = 0, rep = 0xFFFFu, failed = 1, pmp = OOB, mp = OOB, mq = OOB, cw = 0, vo = l16, tp = 0, tw1 = 0, stop = 0;
    f32x4 acc[5] = {};
    if (tid < 20) *(LAS u32*)(lds + 5632 + tid * 4) = 0u;
    unsigned long long clk = 0;
    const u32 psent = PSENT;
    lptr sw = lds + 5120 + w * 4;
    asm volatile("" : "+v"(zero), "+v"(sw), "+v"(xw0), "+v"(xw1), "+v"(xr));
    auto turn = [&](auto lc) __attribute__((always_inline)) {
        constexpr int L = decltype(lc)::value, M = L ^ 1;
        u32 done, rd, dh, dn, nt, za, zn, pdur, hn; v3u hw;
        t = uu(t); tn = uu(tn); tp = uu(tp); dw[L] = uu(dw[L]); failed = uu(failed); rep = uu(rep);
        const int sc = sn[L];
        auto settle = [&](auto ic) __attribute__((always_inline)) {
            constexpr int i = decltype(ic)::value;
            if constexpr (i == 0) { pdur = (u32)clk - tp; dw[L] += max((int)((u32)clk + CH_WX - t), 0); PINS(pdur); PINS(dw[L]); }
            if constexpr (i == 1) {
                asm volatile("v_or_b32 %0, %2, %3\n\tv_cmp_ne_u32 vcc, %4, %5\n\tv_or_b32 %1, %2, %5\n\tv_cndmask_b32 %1, %6, %1, vcc" : "=&v"(za), "=&v"(zn) : "s"(failed << 31), "v"(pmp), "v"(mq), "v"(mp), "v"(OOB) : "vcc");
                done = failed ^ 1u; PINS(done);
            }
            SCHED();
        };
        auto ahead = [&]() __attribute__((always_inline)) {
            u32 h0 = rdl(hn, 0), h1 = rdl(hn, 1), h2 = rdl(hn, 2), h3 = rdl(hn, 3), h4 = rdl(hn, 4);
            asm volatile("" : "+s"(h0), "+s"(h1), "+s"(h2), "+s"(h3), "+s"(h4));
            tp = t; t = lead ? tn : (int)(h0 - t) < 0 ? h0 + h1 : h0;
            tn = nt; stop |= h3; rep = max(h2, h4); tw1 = t - 1u;
            asm volatile("" : "+s"(tp), "+s"(t), "+s"(tn), "+s"(stop), "+s"(rep), "+s"(tw1));
            SCHED();
        };
        auto books = [&](auto ic) __attribute__((always_inline)) {
            constexpr int i = decltype(ic)::value;
            if constexpr (i == 0) { rd = rep & 0xFFFFu; PINS(rd); }
            if constexpr (i == 1) { dn = wav >> 2; PINS(dn); pdur = min(pdur, dn + 8u); PINS(pdur); }
            if constexpr (i == 2) { pdur = (done & ok[M]) ? pdur : dn; PINS(pdur); }
            if constexpr (i == 3) { wav += pdur - dn; PINS(wav); }
            if constexpr (i == 4) { dh = hav >> 2; PINS(dh); nt = min(rd, dh + 8u); PINS(nt); }
            if constexpr (i == 5) { nt = rd < 0x3FFFu ? nt : dh; PINS(nt); }
            if constexpr (i == 6) { hav += nt - dh; PINS(hav); }
            if constexpr (i == 7) { mx = (u32)max((int)(mx - CH_DEC), 0) + (ok[M] & ~done) * (CH_UP * 8u); ok[M] = done; PINS(mx); }
            if constexpr (i == 8) { dh = (2u * hav + mx) / 8u + (sn[1] < 2 ? CH_MH + CH_SOLO : CH_MH); PINS(dh); }
            if constexpr (i == 9) { dn = (2u * wav + mx) / 8u + (sn[0] < CH_NEAR ? CH_MW + CH_EARLY : CH_MW); PINS(dn); }
            if constexpr (i == 10) { nt = dw[L] + dh; dh += dn; dw[L] = dn; sn[M] += (int)done; PINS(nt); PINS(dh); PINS(sn[M]); }
            if constexpr (i == 11) { rd = tn - t + (dh >> 1); PINS(rd); }
            if constexpr (i == 12) { rd = sn[M] + 1 < NSEG ? rd : 0u; PINS(rd); }
            if constexpr (i == 13) { nt = t + (u32)max((int)nt, (int)rd); PINS(nt); }
            if constexpr (i == 14) asm volatile("v_mov_b32 %0, %1" : "=v"(hw.x) : "s"(nt));
            if constexpr (i == 15) asm volatile("v_mov_b32 %0, %1" : "=v"(hw.y) : "s"(dh));
            if constexpr (i == 16) asm volatile("v_mov_b32 %0, %1" : "=v"(hw.z) : "s"(nt + dn));
            SCHED();
        };
        if (__builtin_expect(sc + 1 < NSEG, 1)) {
            u32 zb;
#define WT(k) do {} while (0)
            v4u d[8];
            {
                u32 tmp;
                asm volatile(LOOP_TXT("%1", "%2") "buffer_load_dwordx4 %0, %3, %4, %5 offen sc1\n\ts_waitcnt lgkmcnt(0)"
                             : "=&v"(d[0]), "=&s"(tmp) : "s"(uu(tw1)), "v"(vo), "s"(srd), "s"(oo[0]) : "vcc", "scc", "memory");
            }
            WT(0);
            SFOR(i, 7)
                asm volatile("buffer_load_dwordx4 %0, %1, %2, %3 offen sc1" : "=v"(d[i + 1]) : "v"(vo), "s"(srd), "s"(oo[i + 1]) : "memory");
                SCHED();
                if constexpr (i == 0) { settle(IC<0>{}); static_for<0, 2>(books); }
                if constexpr (i == 1) { settle(IC<1>{}); books(IC<2>{}); }
                if constexpr (i == 2) {
                    mp = mpo + (L * (MB / 4) + (u32)((sc + 1) & 3) * 32768u); pmp = mpo + (L * (MB / 4) + (u32)(sc & 3) * 32768u);
                    zb = mp | ok[L] << 31;
                    PIN(mp); PIN(pmp); PIN(zb);
                }
                if constexpr (i == 3) static_for<3, 7>(books);
                if constexpr (i == 4) { books(IC<7>{}); books(IC<9>{}); }
                if constexpr (i == 5) { books(IC<8>{}); books(IC<10>{}); }
                SCHED();
            SEND
            LD4A(hn, hvo[L], 0);
            *(LAS u32*)(sw + L * 8192) = zero;
            SCHED();
            WT(1);
            float f0[5], f1[5], xs; unsigned long long bad; v4u sv;
#define WSW(o_) asm volatile("v_permlane32_swap_b32 %0, %1" : "+v"(f0[o_]), "+v"(f1[o_]))
#define WAD(o_) asm volatile("v_add_f32_dpp %0, %1, %0 row_ror:8 row_mask:0xf bank_mask:0xf\n\tv_add_f32_dpp %0, %2, %3 row_ror:8 row_mask:0xf bank_mask:0xc" : "+v"(f0[o_]) : "v"(f1[o_]), "v"(f0[o_ + 1]), "v"(f1[o_ + 1]))
#define WFO(o_) do { VADD(f0[o_], acc[o_][0], acc[o_][2]); VADD(f1[o_], acc[o_][1], acc[o_][3]); } while (0)
#define WOWN(k) do { if constexpr (WO_NI + (k) < 8) SMF_V(acc[4], d[(WO_NI + (k)) & 7], wo[(WO_NI + (k)) & 7][4]); SCHED(); } while (0)
            SFOR(i, 40)
                constexpr int pc = i / 5, oc = i % 5;
                if constexpr (oc == 0) VMW((pc == 0 ? 8 : 11) - pc);
                if constexpr (oc < 4) SMF_A(acc[oc], d[pc], wo[pc][oc]); else if constexpr (pc < WO_NI) SMF_V(acc[4], d[pc], wo[pc][4]);
                SCHED();
                if constexpr (i == 0) WT(2);
                if constexpr (i == 20) WT(3);
                if constexpr (i == 35) WT(4);
                if constexpr (i == 0) st4s(rs, zb, 0, psent);
                if constexpr (i == 1) st4s(rs, za, 0, psent);
                if constexpr (i == 2) st4s(rs, zn, 0, cw);
                if constexpr (i >= 5 && i < 8) books(IC<6 + i>{});
                if constexpr (i >= 10 && i < 13) books(IC<4 + i>{});
                if constexpr (i == 13) { xs = 0.f; PIN(xs); }
                if constexpr (i == 15) { vo = l16 + (M * (MB / 2) + (u32)(sn[M] & 3) * 131072u); PIN(vo); }
                if constexpr (i == 36) NOPS(1);
                if constexpr (i == 37) NOPS(2);
                SCHED();
            SEND
            WFO(0); WFO(1);
            asm volatile("v_cmp_u_f32 %0, %1, %2" : "=s"(bad) : "v"(f0[0]), "v"(f1[0]));
            WSW(0); WSW(1); WFO(2); WAD(0); WFO(3);
            *(LAS float*)(xw0 + L * 8192) = f0[0]; SCHED();
            WSW(2); WSW(3);
            if (__builtin_expect(bad != 0ull, 0)) {
                u32 mn = ~0u;
#pragma unroll
                for (int i = 0; i < 8; ++i) { u32 e = d[i].x; PIN(e); mn = min(mn, e ^ PSENT); }
                if (all_(mn != 0u)) {
#pragma unroll
                    for (int i = 0; i < 8; ++i) { v4u e = d[i]; PIN(e); mn = min(min(mn, e.y ^ PSENT), min(e.z ^ PSENT, e.w ^ PSENT)); }
                }
                *(LAS u32*)(sw + L * 8192) = all_(mn != 0u) ? 0u : 1u;
            }
            WAD(2);
            *(LAS float*)(xw1 + L * 8192) = f0[2]; SCHED();
            WOWN(0); WOWN(1); WOWN(2); WOWN(3);
            WT(5);
            f32x4 xa; float nb;
            asm volatile("s_waitcnt lgkmcnt(0)\n\ts_barrier\n\tds_read_b128 %0, %2 offset:%4\n\tds_read_b128 %1, %3 offset:%4+5120\n\ts_nop 3"
                         : "=&v"(xa), "=&v"(sv) : "v"(xr), "v"(zero), "i"(L * 8192) : "memory");
            WT(6);
            WFO(4);
            asm volatile("s_nop 0\n\tv_permlane32_swap_b32 %1, %2\n\ts_nop 1\n\tv_add_f32_dpp %0, %2, %1 row_ror:8 row_mask:0xf bank_mask:0x3\n\ts_waitcnt lgkmcnt(1)" : "+v"(xs), "+v"(f0[4]), "+v"(f1[4]));
            asm volatile("v_add_f32 %0, %0, %4\n\tv_add_f32 %1, %5, %6\n\tv_add_f32 %0, %0, %7\n\tv_add_f32 %0, %0, %1\n\tv_cmp_u_f32 vcc, %0, %0\n\ts_nop 0\n\t"
                         "v_mov_b32_dpp %1, %0 row_shl:4 row_mask:0xf bank_mask:0xf\n\tv_cndmask_b32 %3, %8, %9, vcc\n\tv_cvt_pk_bf16_f32 %2, %0, %1"
                         : "+v"(xs), "=&v"(nb), "=&v"(cw), "=&v"(mq) : "v"(xa[0]), "v"(xa[1]), "v"(xa[2]), "v"(xa[3]), "v"(mp), "v"(OOB) : "vcc");
            st4s(rs, mq, 0, cw);
            SCHED();
            WT(7);
            asm volatile("s_memrealtime %0" : "=s"(clk));
            __builtin_amdgcn_raw_buffer_store_b96(hw, rs, hso[L], 0, 16);
#pragma unroll
            for (int q = 0; q < 5; ++q) acc[q] = __builtin_bit_cast(f32x4, lds_r16(lds + zero + (5632 + 16 * q)));
            VMW(5); SCHED(); PIN(hn);
            asm volatile("s_waitcnt lgkmcnt(5)" ::: "memory");
            SCHED(); PIN(sv);
            failed = uu(sv.x | sv.y | sv.z | sv.w);
            ahead();
            asm volatile("s_waitcnt lgkmcnt(0)" : "+s"(clk) :: "memory");
        } else {
            BAR();
            static_for<0, 2>(settle);
            st4s(rs, za, 0, psent);
            st4s(rs, zn, 0, cw);
            static_for<0, 14>(books);
            failed = 1u;
            hn = ld4(rs, hvo[L]);
            vo = l16 + (M * (MB / 2) + (u32)(sn[M] & 3) * 131072u);
            ahead();
            clk = __builtin_amdgcn_s_memrealtime();
        }
    };
#pragma unroll 1
    for (int k = 0; k < 2 * SPIN_CAP && !stop && (sn[0] + 1 < NSEG || sn[1] + 1 < NSEG); ++k) {
        turn(IC<0>{}); turn(IC<1>{});
        if (__builtin_expect(k != 0, 1)) continue;
        if (j == 0) {
            for (int n = 0; n < SPIN_CAP && !dead && sn[0] == 0; ++n) {
                CFENCE();
                if (all_(ld4(rs, W_ORING + pol0 * 8192u + (u32)w * 16384u + (u32)lane * 128u) != PSENT)) break;
                __builtin_amdgcn_s_sleep(1);
            }
            BAR();
        }
        if (lead) {
            t = rt32() + 56u; tn = t + PER0 / 2 + CH_REC;
            st16(rs, lane < 2 ? hint + (u32)lane * (MB / 4) : OOB, (v4u){lane == 0 ? t : tn, PER0, (lane == 0 ? t : tn) + (CH_W0 + CH_MW + CH_REC), 0u});
        } else {
            for (int n = 0; n < SPIN_CAP && !dead; ++n) {
                CFENCE();
                const u32 hv = ld4(rs, hvo[1]);
                t = rdl(hv, 0);
                if (rdl(hv, 1) < 400u) break;
                __builtin_amdgcn_s_sleep(1);
            }
        }
        hso[0] = (lead && lane == 0) ? hint : OOB; hso[1] = hso[0] + MB / 4;
        rep = 0xFFFFu; dw[0] = dw[1] = CH_W0 + CH_MW + CH_REC;
        tw1 = t - 1u;
    }
    if (sn[0] + 1 < NSEG || sn[1] + 1 < NSEG) st4s(rs, tid == 0 ? foff(F_ERR) : OOB, 0, 1u);
}

__global__ void __launch_bounds__(256, 1) k_main(Args args) {
    Params p;
    (Args&)p = args;
    lptr lds = (lptr)(u32)16 - 16;
    if (blockIdx.x == 0) LST(256, rt32());
    unsigned long long pc; int len;
    asm volatile("s_getpc_b64 %0\n.Lk_main_here:\n\ts_mov_b32 %1, .Lk_end_here-.Lk_main_here" : "=s"(pc), "=s"(len));
    const v4u code = ldin16(mk_rsrc((const void*)(pc & ~15ull), (u32)max(len, 0)), (((blockIdx.x >> 3) & 31u) * 256u + threadIdx.x) * 16u, 0);
    CFENCE();
    if (threadIdx.x == 0) {
        const v4u e = ld16s(mk_rsrc(p.ws, W_END), foff(F_EP), 0);
        const unsigned long long tag = (unsigned long long)e.y << 32 | (e.x + 1u);
        u32 role = ((u32)__builtin_amdgcn_s_getreg(20 | (3 << 11)) & 7u) * 32u + ((blockIdx.x >> 3) & 31u);
#pragma unroll 1
        for (int n = 0; n < 256; ++n, role = (role + 1u) & 255u)
            if (__hip_atomic_exchange((GAS unsigned long long*)(p.ws + W_SLOT) + role * 8u, tag, __ATOMIC_RELAXED, __HIP_MEMORY_SCOPE_AGENT) != tag) break;
        lds_w16(lds, (v4u){e.x + 1u, e.y, e.z, role});
    }
    __syncthreads();
    const v4u boot = lds_r16(lds);
    p.e0 = uu(boot.x); p.e1 = uu(boot.y); p.e2 = uu(boot.z);
    const int role = uni((int)boot.w), x = role >> 5, i = role & 31;
    asm volatile("" :: "v"(code));
    __syncthreads();
    LST(8192 + blockIdx.x * 16, role);
    if (x < 2) { role_head(p, lds, i >> 4, x * 16 + (i & 15)); __builtin_amdgcn_endpgm(); }
    if (x == 2) { role_wo(p, lds, i); __builtin_amdgcn_endpgm(); }
    const int no = x == 3 ? 30 : x < 6 ? 5 : 0;
    if (x > 3 && i >= 8) { role_qkv(p, lds, (x - 4) * 24 + i - 8); __builtin_amdgcn_endpgm(); }
    const bool isout = i < no;
    const int og = x == 3 ? i / 5 : x + 2, ng = i % 5, L = og >> 2, kq = og & 3;
    const int k = i - no, w = uni(threadIdx.x >> 6);
    const int j = isout ? (L == 1 ? 0 : 44) + kq * 5 + ng : x == 3 ? 36 + k : k < 2 ? 12 + 2 * x + k : x < 6 ? 24 + x : k < 5 ? 10 + 3 * x + k : 15 + 3 * x + k;
    const rsrc_t rs = mk_rsrc(p.ws, W_END);
    wait_until(rt32() + (j < NKV ? ST_KV : ST_OUT));
    if (j < NKV) {
        const int nt = j < 320 - 7 * NKV ? 8 : 7;
#pragma unroll 1
        for (int q = 0; q < 4; ++q) kv_fetch(p, j + NKV * q, uu(q * KT_BYTES));
        Filt f;
        u32 warm[3];
#pragma unroll 1
        for (int q = 0; q < nt; ++q) {
            VMW(15); BAR();
            if (q == 0) EV(2);
            kv_fetch(p, j + NKV * (q + 4), uu(((q + 4) & 7) * KT_BYTES));
            kv_tile(p, rs, lds + q * KT_BYTES, lds + (q == 0 ? 8 : q - 1) * KT_BYTES, j + NKV * q);
        }
        const int g = j < 20 ? j : j < 28 ? j + 16 : j - 8;
        filt_load<5, 0>(p, g, f);
        VMW(10); BAR();
        boot_set(p, rs, threadIdx.x == 0 ? foff(F_KV + j) : OOB, 2u);
        EV(4);
        { bool dead = false; boot_wait(p, rs, pol_off(), 0u, dead); }
#pragma unroll
        for (int q = 0; q < 3; ++q)
            warm[q] = __builtin_amdgcn_raw_buffer_load_b32(mk_rsrc(p.ssmk, ACT), 176u * 4096u + ((u32)((q * NKV + j) * 256) + threadIdx.x) * 64u, 0, 0);
        if (g >= 36) conv_job<3>(p, rs, lds, g, f, 0); else conv_job<0>(p, rs, lds, g, f, 0);
        asm volatile("" :: "v"(warm[0]), "v"(warm[1]), "v"(warm[2]));
        const int g0 = j < 20 ? 36 + j : j < 36 ? 28 + j : 44 + j, g1 = j < 20 ? g0 + 1 : j < 36 ? 80 : 160;
        if (g < 36) filt_load<5, 0>(p, g0, f);
        DRAIN(); BAR();
        st4s(rs, threadIdx.x == 0 ? foff(F_CV + g) : OOB, 0, 2u);
        if (g < 36) {
#pragma unroll 1
            for (int n = g0; n < g1; n += 8) conv_job<2>(p, rs, lds, n, f, min(n + 8, 159));
            DRAIN(); BAR();
            st4s(rs, threadIdx.x == 0 ? foff(F_CV + g) : OOB, 0, 3u);
            if (j >= 36) { EV(15); __builtin_amdgcn_endpgm(); }
        }
    }
    if (!isout) { if (j < 28) role_row<0>(p, lds, (j - 20) * 4 + w); else role_row<1>(p, lds, (j - 28) * 4 + w);
        boot_set(p, rs, j == 28 && threadIdx.x == 0 ? foff(F_EP) : OOB, 0u);
        __builtin_amdgcn_endpgm();
    }
    role_out(p, lds, L, kq, ng);
}

__global__ void k_end() { asm volatile(".Lk_end_here:"); }

extern "C" void kernel_launch(void* const* d_in, const int* in_sizes, int n_in, void* d_out, int out_size, void* d_ws, size_t ws_size,
                              hipStream_t stream) {
    Args p;
    p.x = (const u16*)d_in[0]; p.mem0 = (const u16*)d_in[1]; p.Wq = (const u16*)d_in[2]; p.Wk = (const u16*)d_in[3];
    p.Wv = (const u16*)d_in[4]; p.Wo = (const u16*)d_in[5];
    p.bk = (const u16*)d_in[8]; p.bv = (const u16*)d_in[9]; p.shift = (const u16*)d_in[10]; p.ssmk = (const u16*)d_in[11]; p.ssmD = (const u16*)d_in[12];
    p.out = (u16*)d_out;
    p.ws = (unsigned char*)(((size_t)d_ws + 2 * MB - 1) & ~(size_t)(2 * MB - 1));
    (void)hipFuncSetAttribute((const void*)k_main, hipFuncAttributeMaxDynamicSharedMemorySize, LDS_BYTES);
    hipLaunchKernelGGL(k_main, dim3(256), dim3(256), LDS_BYTES, stream, p);
}
```

```cpp
#include <hip/hip_runtime.h>
#include <hip/hip_bf16.h>

typedef unsigned short u16;
typedef unsigned int u32;
typedef u32 v2u __attribute__((ext_vector_type(2)));
typedef u32 v3u __attribute__((ext_vector_type(3)));
typedef u32 v4u __attribute__((ext_vector_type(4)));
typedef u32 v8u __attribute__((ext_vector_type(8)));
typedef float f32x2 __attribute__((ext_vector_type(2)));
typedef float f32x4 __attribute__((ext_vector_type(4)));
typedef float f32x16 __attribute__((ext_vector_type(16)));
typedef __bf16 bf16x2 __attribute__((ext_vector_type(2)));
typedef __bf16 bf16x8 __attribute__((ext_vector_type(8)));
typedef short s16x4 __attribute__((ext_vector_type(4)));
#define LAS __attribute__((address_space(3)))
#define GAS __attribute__((address_space(1)))
typedef LAS unsigned char* lptr;
#define DEV __device__ __forceinline__

constexpr int SEQ = 2048, DM = 640, NH = 32, DH = 128, NSEG = 64, NQ = NH * DH;

constexpr u32 MB = 1u << 20;
constexpr u32 W_ORING = 0;
constexpr u32 W_MRING = MB;
constexpr u32 W_CTRL = 4 * MB;
constexpr u32 ACT = SEQ * DM * 2;
constexpr u32 H_TILE = 128 * 1024;
constexpr u32 W_H2 = 8 * MB;
constexpr u32 W_KQ = 16 * MB;
constexpr u32 W_VO = W_KQ + 64 * MB;
constexpr u32 W_P = W_VO + 64 * MB;
constexpr u32 P_ITEM = 512 * 1024;
constexpr u32 W_H1 = W_P + 44 * MB, W_Y = W_H1 + 8 * MB, W_X2 = W_Y + ACT;
constexpr u32 W_KVT = W_X2 + ACT;
constexpr u32 W_END = W_KVT + 3 * MB;
constexpr u32 NREG = 12;
static_assert((size_t)W_END + 2 * MB <= 256ull * MB && W_H1 >= (NREG - 1) * 16 * MB + 2 * MB && W_H1 <= NREG * 16 * MB, "workspace");
constexpr u32 F_EP = 0, F_RG = 32, F_KV = 64, F_LN = 128,
              F_QKV = 320, F_O = F_QKV + 192, F_P = F_O + 64, F_H2 = F_P + 64, F_GO = F_H2 + 32, F_CV = F_H2 + 64, F_ERR = F_CV + 191, F_N = F_CV + 192;
constexpr u32 W_SLOT = 3 * MB;
DEV u32 foff(u32 idx) { return W_CTRL + (idx >> 6) * 16384u + (idx & 63u) * 128u; }
DEV u32 near_(u32 o, u32 side, u32 pol) { return o + ((side ^ (pol >> (o >> 24)) ^ (o >> 21)) & 1u) * 8192u; }
DEV u32 p_item(int s) { return W_P + (u32)s * P_ITEM; }

constexpr u32 OOB = 0x80000000u;
constexpr u32 PSENT = 0x7F817F81u;
constexpr int SPIN_CAP = 40000;
constexpr int LDS_BYTES = 160 * 1024;
#define CALM 8
#define CALM_L 1
#define ST_KV 0
#define ST_OUT 0

#define EV(k) do {} while (0)
#define LST(idx, v) do {} while (0)
#define LS2(cond, idx) do {} while (0)
struct Args {
    const u16 *x, *mem0, *Wq, *Wk, *Wv, *Wo, *bk, *bv, *shift, *ssmk, *ssmD;
    u16* out;
    unsigned char* ws;
};
struct Params : Args { u32 e0, e1, e2; };

typedef __amdgpu_buffer_rsrc_t rsrc_t;
DEV rsrc_t mk_rsrc(const void* p, u32 bytes) { return __builtin_amdgcn_make_buffer_rsrc((void*)p, 0, bytes, 0x00020000); }
DEV v4u ld16s(rsrc_t r, u32 off, u32 so) { return __builtin_amdgcn_raw_buffer_load_b128(r, off, so, 16); }
DEV v2u ld8s(rsrc_t r, u32 off, u32 so) { return __builtin_amdgcn_raw_buffer_load_b64(r, off, so, 16); }
DEV v4u ldin16(rsrc_t r, u32 off, u32 so) { return __builtin_amdgcn_raw_buffer_load_b128(r, off, so, 0); }
DEV v2u ldin8(rsrc_t r, u32 off, u32 so) { return __builtin_amdgcn_raw_buffer_load_b64(r, off, so, 0); }
DEV u32 ld4(rsrc_t r, u32 off) { return __builtin_amdgcn_raw_buffer_load_b32(r, off, 0, 16); }
DEV void st16(rsrc_t r, u32 off, v4u v) { __builtin_amdgcn_raw_buffer_store_b128(v, r, off, 0, 16); }
DEV void st8s(rsrc_t r, u32 off, u32 so, v2u v) { __builtin_amdgcn_raw_buffer_store_b64(v, r, off, so, 16); }
DEV void st4s(rsrc_t r, u32 off, u32 so, u32 v) { __builtin_amdgcn_raw_buffer_store_b32(v, r, off, so, 16); }
#define DRAIN() asm volatile("s_waitcnt vmcnt(0)" ::: "memory")
#define VMW(n) asm volatile("s_waitcnt vmcnt(%0)" :: "i"(n) : "memory")
#define BAR() asm volatile("s_waitcnt lgkmcnt(0)\n\ts_barrier" ::: "memory")
#define SCHED() __builtin_amdgcn_sched_barrier(0)
#define CFENCE() asm volatile("" ::: "memory")
#define NOPS(n) asm volatile("s_nop %0" :: "i"(n))
#define PIN(x) asm volatile("" : "+v"(x))
#define PINS(x) asm volatile("" : "+s"(x))
#define USE(x) asm volatile("" :: "v"(x))

template <int V> struct IC { static constexpr int value = V; };
template <int I, int N, class F> DEV void static_for(F&& f) { if constexpr (I < N) { f(IC<I>{}); static_for<I + 1, N>(f); } }
#define SFOR(var, n) static_for<0, n>([&](auto ic_) __attribute__((always_inline)) { constexpr int var = decltype(ic_)::value;
#define SEND });

DEV u32 pk(float a, float b) { bf16x2 v = {(__bf16)a, (__bf16)b}; return __builtin_bit_cast(u32, v); }
DEV float lo_f(u32 u) { return __builtin_bit_cast(float, u << 16); }
DEV float hi_f(u32 u) { return __builtin_bit_cast(float, u & 0xffff0000u); }
DEV f32x2 up2(u32 u) { return (f32x2){lo_f(u), hi_f(u)}; }
DEV u32 pk2(f32x2 v) { return pk(v.x, v.y); }
DEV f32x2 rb2(f32x2 a) { f32x2 r; asm("v_cvt_pk_bf16_f32 %0, 0, %1" : "=v"(r.x) : "v"(a.x)); asm("v_cvt_pk_bf16_f32 %0, 0, %1" : "=v"(r.y) : "v"(a.y)); return r; }
DEV float rbf(float a) { return (float)(__bf16)a; }
DEV v4u lds_r16(lptr p) { return *(LAS v4u*)p; }
DEV void lds_w16(lptr p, v4u v) { *(LAS v4u*)p = v; }
DEV v2u lds_tr(lptr p) { return __builtin_bit_cast(v2u, __builtin_amdgcn_ds_read_tr16_b64_v4i16((LAS s16x4*)p)); }
DEV v4u tr2(lptr p0, lptr p1) { const v2u a = lds_tr(p0), b = lds_tr(p1); return (v4u){a.x, a.y, b.x, b.y}; }
DEV v8u cat8(v4u a, v4u b) { return __builtin_shufflevector(a, b, 0, 1, 2, 3, 4, 5, 6, 7); }
DEV f32x4 mfma16(v4u a, v4u b, f32x4 c) { return __builtin_amdgcn_mfma_f32_16x16x32_bf16(__builtin_bit_cast(bf16x8, a), __builtin_bit_cast(bf16x8, b), c, 0, 0, 0); }
DEV f32x16 mfma32(v4u a, v4u b, f32x16 c) { return __builtin_amdgcn_mfma_f32_32x32x16_bf16(__builtin_bit_cast(bf16x8, a), __builtin_bit_cast(bf16x8, b), c, 0, 0, 0); }

DEV float f_(u32 u) { return __builtin_bit_cast(float, u); }
DEV u32 u_(float f) { return __builtin_bit_cast(u32, f); }
template <int CTRL> DEV float dpp(float v) { return f_((u32)__builtin_amdgcn_update_dpp(0, (int)u_(v), CTRL, 0xf, 0xf, false)); }
DEV float max16(float v) { auto r = __builtin_amdgcn_permlane16_swap(u_(v), u_(v), false, false); return fmaxf(f_(r[0]), f_(r[1])); }
DEV float max32(float v) { auto r = __builtin_amdgcn_permlane32_swap(u_(v), u_(v), false, false); return fmaxf(f_(r[0]), f_(r[1])); }
DEV float add16(float v) { auto r = __builtin_amdgcn_permlane16_swap(u_(v), u_(v), false, false); return f_(r[0]) + f_(r[1]); }
DEV float add32(float v) { auto r = __builtin_amdgcn_permlane32_swap(u_(v), u_(v), false, false); return f_(r[0]) + f_(r[1]); }
DEV float wave_sum(float v) {
    v += dpp<0xB1>(v);
    v += dpp<0x4E>(v);
    v += dpp<0x124>(v);
    v += dpp<0x128>(v);
    return add32(add16(v));
}
DEV bool all_(bool c) { return __builtin_amdgcn_ballot_w64(!c) == 0ull; }
DEV int uni(int v) { return __builtin_amdgcn_readfirstlane(v); }
DEV u32 uu(u32 v) { return (u32)__builtin_amdgcn_readfirstlane((int)v); }
DEV u32 rdl(u32 v, int l) { return (u32)__builtin_amdgcn_readlane((int)v, l); }
DEV u32 rt32() { return (u32)__builtin_amdgcn_s_memrealtime(); }

DEV void wait_flags(rsrc_t rs, u32 off, u32 want, bool& dead, bool calm = false) {
    if (dead) return;
#pragma unroll 1
    for (int n = 0;; ++n) {
        CFENCE();
        const u32 v = ld4(rs, off);
        if (all_(off >= OOB || v >= want)) break;
        __builtin_amdgcn_s_sleep(1);
        if (calm) __builtin_amdgcn_s_sleep(CALM);
        if ((n & 63) == 63) { if (uni((int)ld4(rs, foff(F_ERR))) != 0) { dead = true; break; } }
        if (n > SPIN_CAP) { st4s(rs, foff(F_ERR), 0, 1u); dead = true; break; }
    }
    CFENCE();
}

DEV bool boot_ok(const Params& p, u32 off, v4u v, u32 want) { return all_(off >= OOB || (v.x == p.e0 && v.y == p.e1 && v.z == p.e2 && v.w >= want)); }
DEV void boot_set(const Params& p, rsrc_t rs, u32 off, u32 v) { st16(rs, off, (v4u){p.e0, p.e1, p.e2, v}); }
DEV u32 boot_wait(const Params& p, rsrc_t rs, u32 off, u32 want, bool& dead) {
    v4u v;
#pragma unroll 1
    for (int n = 0;; ++n) {
        CFENCE();
        v = ld16s(rs, off, 0);
        if (boot_ok(p, off, v, want) || dead) break;
        __builtin_amdgcn_s_sleep(1);
        if (n > SPIN_CAP) { st4s(rs, foff(F_ERR), 0, 1u); dead = true; }
    }
    CFENCE();
    return v.w;
}

DEV u32 pol_off() { const int lane = threadIdx.x & 63; return lane < 32 ? foff(F_RG + lane) : OOB; }
DEV u32 pol_of(u32 raw) {
    const int lane = threadIdx.x & 63;
    const float v = add16(__builtin_amdgcn_fmed3f((float)((int)raw - (1 << 20)), -250.f, 250.f));
    const float lo = dpp<0x121>(v), hi = dpp<0x12F>(v), ex = dpp<0x122>(v) + dpp<0x123>(v) + dpp<0x124>(v);
    const float sum = v + lo + ((lane & 15) == 11 ? 0.f : hi) + ((lane & 15) == 0 ? ex : 0.f);
    return (u32)__builtin_amdgcn_ballot_w64(sum >= 0.f) & 0xFFFFu;
}
DEV u32 get_pol(const Params& p, rsrc_t rs, bool& dead) { return pol_of(boot_wait(p, rs, pol_off(), 0u, dead)); }
DEV u32 hblk(u32 base, int ls, int h, u32 side, u32 pol) { return near_(base + (u32)(ls * NH + h) * 16384u, side, pol); }
DEV f32x2 ln_stats(const f32x2 (&x)[5]) {
    const float c = f_(uu(u_(x[0].x)));
    f32x2 d[5];
#pragma unroll
    for (int i = 0; i < 5; ++i) d[i] = x[i] - c;
    const f32x2 s2 = (d[0] + d[1]) + (d[2] + d[3]) + d[4], q = (d[0] * d[0] + d[1] * d[1]) + (d[2] * d[2] + (d[3] * d[3] + d[4] * d[4]));
    const float dm = wave_sum(s2.x + s2.y) * (1.0f / DM), mean = c + dm;
    const float var = fmaxf(wave_sum(q.x + q.y) * (1.0f / DM) - dm * dm, 0.f);
    const float sd = rbf(__builtin_amdgcn_sqrtf(rbf(rbf(var) + 1e-5f)));
    return (f32x2){rbf(mean), __builtin_amdgcn_rcpf(sd)};
}
DEV void store_h(rsrc_t rs, u32 tile, int row, const u32 (&hp)[5]) {
    const u32 lane = threadIdx.x & 63;
    const u32 sw = (u32)(row & 15) * 16u, o8 = (lane >> 5) * 16384u + (((lane & 31u) * 8u) ^ sw), hb = tile + (u32)(row & 31) * 256u;
    st8s(rs, o8, hb, (v2u){hp[0], hp[1]});
    st8s(rs, o8, hb + 32768, (v2u){hp[2], hp[3]});
    st4s(rs, (lane * 4) ^ sw, hb + 65536, hp[4]);
}

DEV void ln_row(rsrc_t rs, int row, v2u a, v2u b, u32 c) {
    const f32x2 x[5] = {up2(a.x), up2(a.y), up2(b.x), up2(b.y), up2(c)};
    const f32x2 st = ln_stats(x);
    u32 hp[5];
#pragma unroll
    for (int q = 0; q < 5; ++q) hp[q] = pk2(up2(pk2(x[q] - st.x)) * st.y);
    store_h(rs, W_H1 + (u32)(row >> 5) * H_TILE, row, hp);
}

#define DMA16(ldsb, vo, so) asm volatile("s_mov_b32 m0, %0\n\ts_nop 0\n\tbuffer_load_dwordx4 %1, %2, %3 offen sc1 lds" :: "s"(ldsb), "v"(vo), "s"(srd), "s"(so) : "memory")
#define DMA16I(ldsb, lo, vo, so, go) do { u32 so_; asm volatile("s_add_u32 m0, %1, %5\n\ts_add_u32 %0, %4, %6\n\tbuffer_load_dwordx4 %2, %3, %0 offen sc1 lds" \
                         : "=&s"(so_) : "s"(ldsb), "v"(vo), "s"(srd), "s"(so), "i"(lo), "i"(go) : "memory", "scc"); } while (0)
#define DMA32(ldsb, vo, so) asm volatile("s_mov_b32 m0, %0\n\ts_nop 0\n\tbuffer_load_dwordx4 %1, %2, %3 offen sc1 lds\n\tbuffer_load_dwordx4 %1, %2, %3 offen offset:1024 sc1 lds" \
                         :: "s"(ldsb), "v"(vo), "s"(srd), "s"(so) : "memory")
#define INPOL "nt"
#define OUTAUX 16
#define INAUX 2
#define DMANT(sr, ldsb, vo, so) asm volatile("s_mov_b32 m0, %0\n\ts_nop 0\n\tbuffer_load_dwordx4 %1, %2, %3 offen " INPOL " lds" :: "s"(ldsb), "v"(vo), "s"(sr), "s"(so) : "memory")
#define DMAIN(sr, ldsb, vo, so) asm volatile("s_mov_b32 m0, %0\n\ts_nop 0\n\tbuffer_load_dwordx4 %1, %2, %3 offen lds" :: "s"(ldsb), "v"(vo), "s"(sr), "s"(so) : "memory")
DEV const u16* sel(bool c, const u16* a, const u16* b) { PINS(a); PINS(b); return c ? a : b; }
DEV v4u mk_srd(const void* q, u32 bytes) { return (v4u){uu((u32)(size_t)q), uu((u32)((size_t)q >> 32) & 0xffffu), bytes, 0x00020000u}; }
#define MK_SRD() const v4u srd = mk_srd(p.ws, W_END)

constexpr int CV_PAD = 40;
constexpr int CV_PL = 4416;
constexpr int CV_F = 8 * CV_PL;
constexpr int CV_KB = 80;
constexpr int CV_KV = 65 * CV_KB;
constexpr int CV_CH = CV_F + CV_KV;
static_assert(CV_CH % 16 == 0 && 4 * CV_CH <= LDS_BYTES, "conv lds");

struct Filt { v4u a[5], b[5]; };
DEV int chan(int grp, int w) { return 16 * (grp >> 2) + (grp & 3) + 4 * w; }
template <int N = 5, int AUX = INAUX> DEV void filt_load(const Params& p, int grp, Filt& f) {
    const int tid = threadIdx.x, lane = tid & 63, d = chan(grp, tid >> 6);
    const rsrc_t rf = mk_rsrc(p.ssmk, ACT);
#pragma unroll
    for (int i = 0; i < N; ++i) {
        const u32 s0 = (u32)(8 * (lane + 64 * i) - CV_PAD);
        f.a[i] = __builtin_amdgcn_raw_buffer_load_b128(rf, s0 < SEQ ? (u32)(d * SEQ + s0) * 2u : OOB, 0, AUX);
        f.b[i] = __builtin_amdgcn_raw_buffer_load_b128(rf, s0 + 8 < SEQ ? (u32)(d * SEQ + s0 + 8) * 2u : OOB, 0, AUX);
    }
}
constexpr int KT_BYTES = 18 * 1024, KT_X = 64 * 144, NKV = 44, KT_DUMP = 8 * KT_BYTES + KT_X;
DEV void kv_fetch(const Params& p, int tile, u32 dst) {
    const int lane = threadIdx.x & 63, w = uni(threadIdx.x >> 6);
    const v4u sk = mk_srd(p.bk, ACT), sv = mk_srd(p.bv, ACT), ss = mk_srd(sel(w == 0, p.bk, p.shift), w == 0 ? ACT : DM * 8);
    const u32 vo = tile < 320 ? (u32)((lane >> 3) * DM + (tile % 10) * 64 + (lane & 7) * 8) * 2u : OOB, r0 = uu((u32)((tile / 10) * 64 + 8 * w) * (DM * 2)), d = uu(dst + w * 1024);
    DMANT(sk, d + 1024, vo, r0); DMANT(sk, d + 5120, vo, r0 + 32 * DM * 2); DMANT(sv, d + 9216, vo, r0); DMANT(sv, d + 13312, vo, r0 + 32 * DM * 2);
    DMAIN(ss, uu(w == 0 ? dst : w == 1 ? dst + 17408 : KT_DUMP), w == 0 ? (tile >= 10 ? vo : OOB) : w == 1 && lane < 32 ? (u32)lane * 16u : OOB,
          uu(w == 0 ? (tile >= 10 ? r0 - 8 * DM * 2 : 0u) : (u32)(tile % 10) * 512u));
}
DEV void kv_tile(const Params& p, rsrc_t rs, lptr src, lptr xt, int tile) {
    const int tid = threadIdx.x;
    const v4u sv = lds_r16(src + 17408 + (tid & 31) * 16);
    lptr kp = src + ((tid >> 5) * 8 + 5) * 128 + (tid & 31) * 4;
    f32x2 kf[11];
#pragma unroll
    for (int i = 0; i < 11; ++i) kf[i] = up2(*(LAS u32*)(kp + i * 128));
    const f32x2 sh[4] = {{lo_f(sv.x), lo_f(sv.z)}, {hi_f(sv.x), hi_f(sv.z)}, {lo_f(sv.y), lo_f(sv.w)}, {hi_f(sv.y), hi_f(sv.w)}};
    u32 kv[8];
#pragma unroll
    for (int i = 0; i < 8; ++i) {
        const f32x2 acc = rb2(rb2(rb2(rb2(kf[i] * sh[3]) + rb2(kf[i + 1] * sh[2])) + rb2(kf[i + 2] * sh[1])) + rb2(kf[i + 3] * sh[0]));
        kv[i] = pk2(acc * up2(*(LAS u32*)(kp + 9216 + (i - 5) * 128)));
    }
    lds_w16(xt + (2 * (tid & 31)) * 144 + (tid >> 5) * 16, (v4u){__builtin_amdgcn_perm(kv[6], kv[7], 0x05040100u), __builtin_amdgcn_perm(kv[4], kv[5], 0x05040100u),
                                                             __builtin_amdgcn_perm(kv[2], kv[3], 0x05040100u), __builtin_amdgcn_perm(kv[0], kv[1], 0x05040100u)});
    lds_w16(xt + (2 * (tid & 31) + 1) * 144 + (tid >> 5) * 16, (v4u){__builtin_amdgcn_perm(kv[6], kv[7], 0x07060302u), __builtin_amdgcn_perm(kv[4], kv[5], 0x07060302u),
                                                                 __builtin_amdgcn_perm(kv[2], kv[3], 0x07060302u), __builtin_amdgcn_perm(kv[0], kv[1], 0x07060302u)});
    BAR();
#pragma unroll
    for (int i = 0; i < 2; ++i) {
        const int ch = i * 32 + (tid >> 3);
        st16(rs, W_KVT + (u32)(((tile % 10) * 64 + ch) * SEQ + (tile / 10) * 64 + (tid & 7) * 8) * 2u, lds_r16(xt + ch * 144 + (tid & 7) * 16));
    }
}
template <int MODE> DEV void conv_job(const Params& p, rsrc_t rs, lptr lds, int grp, Filt& f, int next, u32 fo2 = OOB, u32 fv2 = 0) {
    const int tid = threadIdx.x, lane = tid & 63, w = uni(tid >> 6), bq = lane & 31, hh = lane >> 5;
    lptr F = lds + w * CV_CH;
    lptr fa = F + (bq & 7) * CV_PL + ((bq >> 3) - hh + 4) * 16;
    lptr kb = F + CV_F + CV_KB + hh * 16, kh = kb + (bq + 32) * CV_KB;
    constexpr bool LO = MODE & 1, EARLY = MODE == 0 || MODE == 3;
    bool dead = false;
    v4u kvr[4];
    const u32 D = p.ssmD[chan(grp, w)];
    auto kv_load = [&]() {
        if (EARLY) boot_wait(p, rs, lane < NKV ? foff(F_KV + lane) : OOB, 2u, dead);
#pragma unroll
        for (int i = 0; i < (LO ? 2 : 4); ++i) kvr[i] = ld16s(rs, (u32)lane * 16u, W_KVT + (u32)(chan(grp, w) * SEQ + i * 512) * 2u);
    };
    auto windows = [&](int i) {
        if (i < 4 || lane < 16) {
            const u32 e[8] = {f.a[i].x, f.a[i].y, f.a[i].z, f.a[i].w, f.b[i].x, f.b[i].y, f.b[i].z, f.b[i].w};
            u32 o[7];
#pragma unroll
            for (int q = 0; q < 7; ++q) o[q] = __builtin_amdgcn_alignbit(e[q + 1], e[q], 16);
#pragma unroll
            for (int q = 0; q < 4; ++q) {
                lds_w16(F + (2 * q) * CV_PL + (64 * i + lane) * 16, (v4u){o[q], o[q + 1], o[q + 2], o[q + 3]});
                lds_w16(F + (2 * q + 1) * CV_PL + (64 * i + lane) * 16, (v4u){e[q + 1], e[q + 2], e[q + 3], e[q + 4]});
            }
        }
    };
    lptr yw = lds + (lane >> 4) * CV_CH + 3072 + (lane & 14) * 64 + w * 32 + (lane & 1) * 16, yr = F + 3072 + lane * 16;
    const u32 yo = W_Y + (u32)((((w & 1) * 8 + (lane >> 3)) * 8 + (w >> 1)) * DM + (grp & 3) * 160 + 4 * (grp >> 2)) * 32u + (u32)(lane & 7) * 16u;
    v4u dg[2];
    auto skip = [&](lptr k) { const f32x16 z = {}; return mfma32(lds_r16(k + 32), dg[1], mfma32(lds_r16(k), dg[0], z)); };
    auto pack = [&](const f32x16& ac, const f32x16& sk, int q) {
        const int r = 4 * (q & 3);
        u32 v[4];
#pragma unroll
        for (int i = 0; i < 4; ++i) v[i] = pk2(rb2((f32x2){ac[r + 2 * i], ac[r + 2 * i + 1]}) + rb2((f32x2){sk[r + 2 * i], sk[r + 2 * i + 1]}));
        const auto x = __builtin_amdgcn_permlane32_swap(v[0], v[2], false, false), y = __builtin_amdgcn_permlane32_swap(v[1], v[3], false, false);
        return (v4u){x[0], y[0], x[1], y[1]};
    };
    if (MODE != 2) EV(8);
    if (!EARLY) kv_load();
    if (lane < CV_KB / 16) lds_w16(F + CV_F + lane * 16, (v4u){0u, 0u, 0u, 0u});
    windows(0);
    if (MODE != 1) { windows(1); windows(2); }
    if (!LO) windows(4);
    if (EARLY) kv_load();
    if (MODE == 2) windows(3);
    if (MODE != 2) EV(9);
#pragma unroll
    for (int i = 0; i < (LO ? 2 : 4); ++i) lds_w16(F + CV_F + (16 * i + (lane >> 2) + 1) * CV_KB + (lane & 3) * 16, kvr[i]);
#pragma unroll
    for (int ks = 0; ks < 2; ++ks)
#pragma unroll
        for (int q = 0; q < 4; ++q) { const int j = 16 * ks + 8 * hh + 7 - bq; dg[ks][q] = j == 2 * q ? D : j == 2 * q + 1 ? D << 16 : 0u; }
    f32x16 acc0 = {}, acc1 = {};
    auto first = [&]() { asm volatile("s_waitcnt vmcnt(0)\n\ts_barrier" ::: "memory"); st4s(rs, tid == 0 ? foff(F_CV + grp) : fo2, 0, tid == 0 ? 1u : fv2); EV(12); };
    if (MODE != 2) {
        EV(10);
        v4u fr[2][4];
        auto rd = [&](int m) { lptr kl = kb + max(bq - m, -1) * CV_KB; fr[m & 1][0] = lds_r16(kl); fr[m & 1][1] = lds_r16(fa + m * 64); fr[m & 1][2] = lds_r16(kl + 32); fr[m & 1][3] = lds_r16(fa + m * 64 - 32); };
        rd(0); rd(1);
        const f32x16 sk = skip(kb + bq * CV_KB);
#pragma unroll
        for (int m = 0; m < 8; ++m) {
            acc0 = mfma32(fr[m & 1][0], fr[m & 1][1], acc0); acc0 = mfma32(fr[m & 1][2], fr[m & 1][3], acc0);
            if (m < 6) rd(m + 2);
            SCHED();
        }
        lds_w16(yw, pack(acc0, sk, 0));
        BAR();
        st16(rs, w < 2 ? yo : OOB, lds_r16(yr));
        EV(11);
    }
    if (LO) {
        if (MODE == 1) { windows(1); windows(2); first(); }
        v4u a[2][2], l[2][2];
#pragma unroll
        for (int u = 0; u < 2; ++u) { lptr kl = kb + max(bq - 8 - u, -1) * CV_KB; a[u][0] = lds_r16(fa + (8 + u) * 64); l[u][0] = lds_r16(kl); a[u][1] = lds_r16(fa + (8 + u) * 64 - 32); l[u][1] = lds_r16(kl + 32); }
#pragma unroll 1
        for (int m = 8; m < 32; m += 2) {
            if (MODE == 3 && m == 20) first();
#pragma unroll
            for (int u = 0; u < 2; ++u) {
                lptr kl = kb + max(bq - m - u - 2, -1) * CV_KB;
                acc0 = mfma32(l[u][0], a[u][0], acc0); a[u][0] = lds_r16(fa + (m + u + 2) * 64); l[u][0] = lds_r16(kl); SCHED();
                acc0 = mfma32(l[u][1], a[u][1], acc0); a[u][1] = lds_r16(fa + (m + u + 2) * 64 - 32); l[u][1] = lds_r16(kl + 32); SCHED();
            }
        }
    } else {
        if (MODE == 0) windows(3); else filt_load<5, 0>(p, next, f);
        v4u a0 = lds_r16(fa), a1 = lds_r16(fa - 32), c0 = lds_r16(fa + 2048), c1 = lds_r16(fa + 2048 - 32);
        v4u h0 = lds_r16(kh), h1 = lds_r16(kh + 32), l0 = lds_r16(kb + bq * CV_KB), l1 = lds_r16(kb + bq * CV_KB + 32);
#pragma unroll 2
        for (int m = 0; m < (MODE == 0 ? 8 : 32); ++m) {
            lptr kl = kb + max(bq - m - 1, -1) * CV_KB;
            if (MODE == 0 && m == 4) first();
            acc1 = mfma32(h0, a0, acc1); h0 = lds_r16(kh - (m + 1) * CV_KB); a0 = lds_r16(fa + (m + 1) * 64); SCHED();
            acc1 = mfma32(l0, c0, acc1); c0 = lds_r16(fa + (m + 1) * 64 + 2048); l0 = lds_r16(kl); SCHED();
            acc1 = mfma32(h1, a1, acc1); h1 = lds_r16(kh - (m + 1) * CV_KB + 32); a1 = lds_r16(fa + (m + 1) * 64 - 32); SCHED();
            acc1 = mfma32(l1, c1, acc1); c1 = lds_r16(fa + (m + 1) * 64 + 2048 - 32); l1 = lds_r16(kl + 32); SCHED();
        }
        if (MODE == 0) {
#pragma unroll 2
            for (int m = 8; m < 32; ++m) {
                lptr kl = kb + max(bq - m - 1, -1) * CV_KB;
                acc1 = mfma32(h0, a0, acc1); h0 = lds_r16(kh - (m + 1) * CV_KB); SCHED();
                acc0 = mfma32(l0, a0, acc0); a0 = lds_r16(fa + (m + 1) * 64); SCHED();
                acc1 = mfma32(l0, c0, acc1); c0 = lds_r16(fa + (m + 1) * 64 + 2048); l0 = lds_r16(kl); SCHED();
                acc1 = mfma32(h1, a1, acc1); h1 = lds_r16(kh - (m + 1) * CV_KB + 32); SCHED();
                acc0 = mfma32(l1, a1, acc0); a1 = lds_r16(fa + (m + 1) * 64 - 32); SCHED();
                acc1 = mfma32(l1, c1, acc1); c1 = lds_r16(fa + (m + 1) * 64 + 2048 - 32); l1 = lds_r16(kl + 32); SCHED();
            }
        }
    }
    if (MODE != 2) EV(13);
    v4u yy[4];
    if (MODE != 2) { const f32x16 sk = skip(kb + bq * CV_KB); yy[0] = pack(acc0, sk, 0); yy[1] = pack(acc0, sk, 2); }
    if (!LO) { const f32x16 sk = skip(kh); yy[2] = pack(acc1, sk, 0); yy[3] = pack(acc1, sk, 2); BAR(); }
#pragma unroll
    for (int q = (MODE == 2 ? 2 : 0); q < (LO ? 2 : 4); ++q) lds_w16(yw + q * CV_PL, yy[q]);
    BAR();
#pragma unroll
    for (int q = (MODE == 2 ? 2 : 0); q < (LO ? 2 : 4); ++q) st16(rs, q == 0 && w < 2 ? OOB : yo + (u32)q * (2 * DM * 32), lds_r16(yr + q * CV_PL));
    BAR();
    if (MODE != 2) EV(14);
}

#define NPROBE 6
DEV int probe(rsrc_t rs, int c) {
    const int lane = threadIdx.x & 63;
    const u32 k = (u32)(c & 15) < NREG ? (u32)(c & 15) : 0u;
    const u32 off0 = k * (16 * MB) + 2 * MB - 65536 + ((u32)(c & 15) >= NREG ? (u32)(c & 3) + 1u : 0u) * 256 + (u32)(c >> 4) * 128;
    st4s(rs, lane == 0 ? off0 : OOB, 0, 0u); DRAIN();
    int d0 = 1 << 16, d1 = 1 << 16;
#pragma unroll 1
    for (int q = 0; q < NPROBE; ++q) {
        const u32 o = lane == 0 ? off0 + (u32)(q & 1) * 8192 + (u32)(q >> 1) * 18432 : OOB;
        const unsigned long long t0 = __builtin_amdgcn_s_memtime();
        st4s(rs, o, 0, 0u); DRAIN();
        const int dt = (int)(__builtin_amdgcn_s_memtime() - t0);
        if (q & 1) d1 = min(d1, dt); else d0 = min(d0, dt);
    }
    const u32 xcc = (u32)__builtin_amdgcn_s_getreg(20 | (3 << 11)) & 0xfu;
    return (d0 - d1) * ((xcc & 4u) ? -1 : 1);
}
DEV void pre_rings(const Params& p, rsrc_t rs, int j) {
    const u32 gt = (u32)(j * 256 + threadIdx.x) * 16u, n = ((gt >> 14) << 9) + ((gt & 8191u) >> 4);
    const v4u ps = {PSENT, PSENT, PSENT, PSENT};
    v4u m = ps;
    if (n < 640u) m = *(const v4u*)(p.mem0 + (2 * ((n >> 2) & 3) + (n & 1)) * DM + (n >> 4) * 16 + (n & 2) * 4);
    int vote = 0;
    if (uni(threadIdx.x >> 6) == 0) vote = probe(rs, j);
    BAR();
    EV(2);
#pragma unroll
    for (u32 i = 0; i < 8; ++i) st16(rs, W_ORING + gt + i * 131072u, ps);
#pragma unroll
    for (u32 i = 0; i < 2; ++i) st16(rs, gt < 16384u ? W_MRING + i * (MB / 4) + 131072u + gt : OOB, ps);
    if (n < 640u) {
        m = (v4u){__builtin_amdgcn_perm(m.z, m.x, 0x05040100u), __builtin_amdgcn_perm(m.z, m.x, 0x07060302u), __builtin_amdgcn_perm(m.w, m.y, 0x05040100u), __builtin_amdgcn_perm(m.w, m.y, 0x07060302u)};
#pragma unroll
        for (int i = 0; i < 4; ++i) m[i] = m[i] == PSENT ? 0x7FC17FC1u : m[i];
    }
    st16(rs, W_MRING + gt, m);
    st16(rs, W_MRING + MB / 4 + gt, m);
    static_assert(2 * (F_N - F_QKV) == 32 * 36, "clear");
    st4s(rs, threadIdx.x < 36 ? foff(F_QKV) + (u32)(j * 36 + threadIdx.x) * 128u : OOB, 0, 0u);
    DRAIN(); BAR();
    boot_set(p, rs, threadIdx.x == 0 ? foff(F_RG + j) : OOB, (u32)((1 << 20) + vote));
    EV(3);
}

#define M32A(first, d, a, b) do { if (first) asm volatile("v_mfma_f32_32x32x16_bf16 %0, %1, %2, 0" : "=&v"(d) : "a"(a), "v"(b)); \
                                  else asm volatile("v_mfma_f32_32x32x16_bf16 %0, %1, %2, %0" : "+v"(d) : "a"(a), "v"(b)); } while (0)
#define M32B(first, d, a, b) do { if (first) asm volatile("v_mfma_f32_32x32x16_bf16 %0, %1, %2, 0" : "=&v"(d) : "v"(a), "a"(b)); \
                                  else asm volatile("v_mfma_f32_32x32x16_bf16 %0, %1, %2, %0" : "+v"(d) : "v"(a), "a"(b)); } while (0)
#define VCVT(d, a, b) asm volatile("v_cvt_pk_bf16_f32 %0, %1, %2" : "=v"(d) : "v"(a), "v"(b))
#define LD4A(d, vo, so) asm volatile("buffer_load_dword %0, %1, %2, %3 offen sc1" : "=v"(d) : "v"(vo), "s"(srd), "s"(so) : "memory")
DEV u32 wst_src(int lane, int ld) { return (u32)((lane >> 4) * ld + 8 * ((lane & 15) ^ ((lane >> 4) << 2))) * 2u; }
DEV u32 wst_rd(int lane, int w) { const int q4 = (lane >> 2) & 3; return (u32)(q4 * 256 + ((w ^ q4) * 64) + ((lane >> 4) & 1) * 32 + (lane & 3) * 8); }

constexpr int QK_A = 65536;
#define QLEAD 4
#define QFIRST 2
static_assert(2 * QFIRST <= 12, "the first rounds stay within the first LN rows");
static_assert(QFIRST < QLEAD, "a round of the lead carries the flag of the convolution");
#define QW0 10
#define QW1 3
#define QSAMP 72
DEV void role_qkv(const Params& p, lptr lds, int c) {
    const int grp = 64 + c;
    const int tid = threadIdx.x, lane = tid & 63, w = uni(tid >> 6), hh = lane >> 5, r32 = lane & 31;
    const int m = c >> 5, h = c & 31;
    const rsrc_t rs = mk_rsrc(p.ws, W_END);
    v4u wt[40];
    Filt flt;
    bool dead = false;
    u32 pol, h2flag, fo, ol, ar[8];
    const u32 ds = uu(w * 1024), dx = ds + QK_A;
#define XRD(ks) lds_r16(lds + ar[(ks) & 7] + ((ks) >> 3) * 8192)
    const u32 t16 = (u32)tid * 16u, lnf = tid < 96 ? foff(F_LN + tid) : OOB;
    MK_SRD();
#define QDMA(i, dst, vo, so) DMA16I(dst, (i) * 4096, vo, so, ((i) >> 1) * 16384 + ((i) & 1) * 4096)
    {
        const v4u sw = mk_srd(sel(m == 0, p.Wq, sel(m == 1, p.Wk, p.Wv)), DM * NQ * 2), sx = mk_srd(p.x, ACT);
        const u32 vo = wst_src(lane, NQ) + (u32)h * (DH * 2), x8 = (u32)lane * 8u, x4 = (u32)lane * 4u;
        v2u xa[6], xb[6]; u32 xc[6];
#pragma unroll
        for (int i = 0; i < 6; ++i)
            asm volatile("buffer_load_dwordx2 %0, %3, %5, %6 offen\n\tbuffer_load_dwordx2 %1, %3, %5, %6 offen offset:512\n\tbuffer_load_dword %2, %4, %5, %6 offen offset:1024"
                         : "=&v"(xa[i]), "=&v"(xb[i]), "=&v"(xc[i]) : "v"(x8), "v"(x4), "s"(sx), "s"(uu((4 * c + w + 384 * i) * (DM * 2))) : "memory");
        EV(1);
        auto wfill = [&](int i0, int n) {
#pragma unroll 1
            for (int i = i0; i < i0 + n; ++i) DMAIN(sw, uu((4 * i + w) * 1024), vo, uu((16 * i + 4 * w) * (NQ * 2)));
        };
        wfill(0, QW0);
        asm volatile("s_waitcnt vmcnt(%0) ; HZCLR" :: "i"(QW0) : "memory");
        EV(2);
#pragma unroll
        for (int i = 0; i < 6; ++i) {
            wfill(QW0 + QW1 * i, QW1);
            if (i == 2) { VMW(2 * QW1 + 3); BAR(); boot_set(p, rs, tid == 0 ? foff(F_LN + c) : OOB, 1u); }
            asm volatile("" : "+v"(xa[i]), "+v"(xb[i]), "+v"(xc[i]));
            const int row = 4 * c + w + 384 * i;
            if (i < 5 || row < SEQ) {
                ln_row(rs, row, xa[i], xb[i], xc[i]);
            }
        }
        EV(3);
        constexpr int K0 = QW0 + 6 * QW1;
        static_assert(K0 * 4096 >= QK_A + 40960, "the first tile's place is free");
        v4u fv, praw;
        asm volatile("buffer_load_dwordx4 %0, %2, %4, 0 offen sc1\n\tbuffer_load_dwordx4 %1, %3, %4, 0 offen sc1" : "=&v"(praw), "=&v"(fv) : "v"(pol_off()), "v"(lnf), "s"(srd) : "memory");
        wfill(K0, 40 - K0);
        asm volatile("s_waitcnt vmcnt(%2) ; HZCLR\n\ts_barrier" : "+v"(praw), "+v"(fv) : "i"(40 - K0) : "memory");
        boot_set(p, rs, tid == 0 ? foff(F_LN + c) : OOB, 2u);
        EV(4);
        lptr tb = lds + hh * 2048 + wst_rd(lane, w);
        SFOR(k, 40)
            if constexpr (k == K0) {
                if (!boot_ok(p, lnf, fv, 1u)) boot_wait(p, rs, lnf, 1u, dead);
                if (!boot_ok(p, pol_off(), praw, 0u)) praw.w = boot_wait(p, rs, pol_off(), 0u, dead);
                BAR();
                EV(5);
                const u32 so = W_H1;
                SFOR(i, 10) QDMA(i, dx, t16, so); SEND
                pol = pol_of(praw.w);
                h2flag = tid < 8 ? near_(foff(F_H2 + lane), 1u, pol) : OOB;
                ol = (m == 2 ? (u32)(2 * w + (r32 >> 4)) * 1024u : (u32)((r32 >> 4) * 4 + w) * 1024u) + (u32)(hh * 16 + (r32 & 15)) * 16u;
                fo = near_(foff(F_QKV + m * 64 + h), 0u, pol);
#pragma unroll
                for (int j = 0; j < 8; ++j) { ar[j] = (u32)(QK_A + r32 * 256 + ((2 * j + hh) ^ (r32 & 15)) * 16); PIN(ar[j]); }
                PIN(h2flag); PIN(ol); PINS(fo); PINS(pol);
                VMW(10); BAR();
            }
            lptr q = tb + (k >> 3) * 32768 + (k & 7) * 4096; wt[k] = tr2(q, q + 1024); asm volatile("" : "+a"(wt[k]));
        SEND
    }
    EV(6);
    int n0 = -1, n1 = -QLEAD;
    const u32 ksc = m == 1 ? 0x3e0293eeu : 0x3f800000u;
    const u32 bfo = tid < 192 ? lnf : tid - 192u < NKV ? foff(F_KV - 192 + tid) : OOB;
    v4u brk = {};
    auto run = [&](auto isv) __attribute__((always_inline)) {
      constexpr bool V = decltype(isv)::value;
      v4u x[8] = {};
      f32x16 a0 = {};
      bool fresh = true;
      u32 fv = 0, fo0 = tid == 192 ? fo : n1 > -QLEAD && tid == 194 ? foff(F_CV + grp) : OOB, fo1 = tid == 192 ? fo : tid == 193 ? fo + 32 * 128 : OOB;
      PIN(fo0); PIN(fo1);
#define QS(k) do {} while (0)
#define QPK1(d, a, a_, q_) do { if constexpr (V) VCVT(d[q_], a[4 * (a_) + 8 * ((q_) >> 1) + 2 * ((q_) & 1)], a[4 * (a_) + 8 * ((q_) >> 1) + 2 * ((q_) & 1) + 1]); else VCVT(d[q_], a[4 * (a_) + (q_)], a[4 * (a_) + (q_) + 8]); } while (0)
#define QSC(a, i) asm volatile("v_mul_f32 %0, %2, %0\n\tv_mul_f32 %1, %2, %1" : "+v"(a[i]), "+v"(a[(i) + 8]) : "s"(ksc))
#define QPK(d, a, a_, q_) do { if constexpr (!V) { QSC(a, 4 * (a_) + (q_)); QSC(a, 4 * (a_) + (q_) + 1); } QPK1(d, a, a_, q_); QPK1(d, a, a_, (q_) + 1); } while (0)
#pragma unroll 1
      for (const int end = n1 == -QLEAD ? QFIRST - QLEAD : NSEG; n1 < end; ++n1) {
        const int cur = n1 < 0 ? n0 + 1 : 64 | n1, nxt = n1 < 0 ? n0 + 2 : n0 + 1;
        u32 xs = n1 < 0 ? W_H1 + (u32)cur * H_TILE : near_(W_H2 + (u32)n1 * H_TILE, 1u, pol);
        PINS(xs);
        QS(0);
        if (w == 0 && n1 >= 0 && ((u32)__builtin_amdgcn_ballot_w64(fv <= (u32)n1) & 255u) != 0u) wait_flags(rs, h2flag, (u32)n1 + 1, dead);
        BAR();
        QS(1);
        f32x16 a1;
        u32 ss, sv, hb, oa, qfo, qfv;
        v4u pp, pq;
        SCHED();
        auto slot = [&](auto zc) __attribute__((always_inline)) {
            constexpr int z = decltype(zc)::value, ks = (z + 20) % 40;
            if constexpr (z < 20) { if constexpr (V) M32B(false, a0, x[ks & 7], wt[ks]); else M32A(false, a0, wt[ks], x[ks & 7]); }
            else if constexpr (z < 60) { if constexpr (V) M32B(z == 20, a1, x[ks & 7], wt[ks]); else M32A(z == 20, a1, wt[ks], x[ks & 7]); }
            else if constexpr (z == 60) {
                if constexpr (V) asm volatile("v_mfma_f32_32x32x16_bf16 %0, %1, %2, 0" : "+v"(a0) : "v"(x[ks & 7]), "a"(wt[ks]));
                else asm volatile("v_mfma_f32_32x32x16_bf16 %0, %1, %2, 0" : "+v"(a0) : "a"(wt[ks]), "v"(x[ks & 7]));
            } else { if constexpr (V) M32B(false, a0, x[ks & 7], wt[ks]); else M32A(false, a0, wt[ks], x[ks & 7]); }
            if constexpr (z == 18) { QS(2); asm volatile("s_waitcnt vmcnt(6) lgkmcnt(0)\n\ts_barrier" ::: "memory"); QS(3); }
            if constexpr (z < 15 || z > 20) x[(ks + 5) & 7] = XRD((ks + 5) % 40);
            if constexpr (z >= 18 && z <= 20) { x[2 * (z - 18)] = XRD(2 * (z - 18)); x[2 * (z - 18) + 1] = XRD(2 * (z - 18) + 1); }
            SCHED();
            if constexpr (z < 4) QDMA(z, dx, t16, xs);
            if constexpr (z >= 5 && z < 17 && (z & 1)) QDMA((z + 3) >> 1, dx, t16, xs);
            if constexpr ((z >= 7 && z < 15) || (z >= 47 && z < 55)) asm volatile("v_xor_b32 %0, 0x10000, %0" : "+v"(ar[(ks + 5) & 7]));
            if constexpr (z == 16) { ss = W_H1 + (u32)nxt * H_TILE; PINS(ss); }
            if constexpr (z == 17) { sv = t16 | (nxt >= NSEG ? OOB : 0u); PIN(sv); }
            if constexpr (z >= 21 && z < 41 && (z & 1)) QDMA((z - 21) >> 1, ds, sv, ss);
            if constexpr (z == 22 || z == 24) QPK(pp, a0, 0, z - 22);
            if constexpr (z == 26 || z == 28) QPK(pq, a0, 1, z - 26);
            if constexpr (z == 30) { QS(4); asm volatile("s_waitcnt vmcnt(5)\n\ts_barrier" ::: "memory"); QS(5); }
            if constexpr (z == 32 || z == 44) { hb = (m == 2 ? W_VO : W_KQ) + (u32)((z == 32 ? n0 : cur) * NH + h) * 16384u; PINS(hb); }
            if constexpr (z == 34 || z == 45) { hb = near_(hb, m == 1 ? 0u : 1u, pol); PINS(hb); }
            if constexpr (z == 36) { hb = n0 < NSEG && !fresh ? hb : OOB; PINS(hb); }
            if constexpr (z == 38 || z == 46) { oa = ol + hb; PIN(oa); }
            if constexpr (z == 41) st16(rs, oa, pp);
            if constexpr (z == 43) st16(rs, oa + 512, pq);
            if constexpr (z == 54) { QS(6); asm volatile("s_waitcnt vmcnt(4)\n\ts_barrier" ::: "memory"); QS(7); }
            if constexpr (z == 56) { if (__builtin_expect(n1 == QFIRST - QLEAD - 1, 0)) { filt_load<3, 0>(p, grp, flt); brk = ld16s(rs, bfo, 0); } }
            if constexpr (z == 60) NOPS(5);
            if constexpr (z == 63) st16(rs, oa, pp);
            if constexpr (z == 65) st16(rs, oa + 512, pq);
            if constexpr (z == 61 || z == 62) QPK(pp, a1, 0, 2 * (z - 61));
            if constexpr (z == 63 || z == 64) QPK(pq, a1, 1, 2 * (z - 63));
            if constexpr (z == 67) { qfo = n1 >= 0 ? fo1 : fo0; PIN(qfo); }
            if constexpr (z == 68) { qfv = tid == 193 ? (u32)n1 + 1u : (u32)min(nxt, NSEG); PIN(qfv); }
            if constexpr (z == QSAMP) LD4A(fv, h2flag, 0);
            SCHED();
        };
        if (__builtin_expect(!fresh, 1)) static_for<0, 16>(slot);
        static_for<16, 80>(slot);
        QS(8);
        asm volatile("s_waitcnt vmcnt(1)\n\ts_barrier" ::: "memory");
        st4s(rs, qfo, 0, qfv);
        QS(9);
        asm volatile("s_waitcnt vmcnt(1)" : "+v"(fv) :: "memory");
        n0 = nxt; fresh = false;
      }
      if (n1 < 0) {
        SFOR(z, 20)
            if constexpr (V) M32B(false, a0, x[(z + 20) & 7], wt[z + 20]); else M32A(false, a0, wt[z + 20], x[(z + 20) & 7]);
            if constexpr (z < 15) x[(z + 25) & 7] = XRD(z + 25);
            if constexpr (z >= 7 && z < 15) asm volatile("v_xor_b32 %0, 0x10000, %0" : "+v"(ar[(z + 25) & 7]));
            SCHED();
        SEND
        u32 oa = ol + near_((m == 2 ? W_VO : W_KQ) + (u32)(n0 * NH + h) * 16384u, m == 1 ? 0u : 1u, pol);
        v4u pp, pq;
        asm volatile("s_nop 11" : "+v"(oa));
        QPK(pp, a0, 0, 0); QPK(pp, a0, 0, 2); st16(rs, oa, pp);
        QPK(pq, a0, 1, 0); QPK(pq, a0, 1, 2); st16(rs, oa + 512, pq);
      }
    };
#pragma unroll 1
    for (;;) {
        if (m == 2) run(IC<1>{}); else run(IC<0>{});
        if (n1 > 0) break;
        if (!boot_ok(p, bfo, brk, 2u)) boot_wait(p, rs, bfo, 2u, dead);
        BAR();
        conv_job<1>(p, rs, lds, grp, flt, 0, tid == 1 ? fo : OOB, (u32)n0 + 1u);
        const u32 so = uu(W_H1 + (u32)(n0 + 1) * H_TILE);
        SFOR(i, 10) QDMA(i, dx, t16, so); SEND
    }
}

constexpr int OU_SEL = 131072;
#define OSAMP 46

DEV void role_out(const Params& p, lptr lds, int L, int kq, int ng) {
    const int tid = threadIdx.x, lane = tid & 63, w = uni(tid >> 6), hh = lane >> 5, r32 = lane & 31;
    const rsrc_t rs = mk_rsrc(p.ws, W_END);
    v4u wo[64];
    Filt flt;
    bool dead = false;
    {
        const v4u sw = mk_srd(p.Wo, DM * NQ * 2);
        const u32 vo = wst_src(lane, DM) + (ng == 4 ? 1024u : (u32)((ng >> 1) * 512 + (ng & 1) * 128 + ((lane ^ (lane >> 2)) & 8) * 16));
        auto fill = [&](int b) {
#pragma unroll 1
            for (int i = 0; i < 8; ++i) DMAIN(sw, uu((b % 5) * 32768 + (4 * i + w) * 1024), vo, uu((kq * 1024 + b * 128 + 16 * i + 4 * w) * (DM * 2)));
        };
#pragma unroll 1
        for (int b = 0; b < 5; ++b) fill(b);
        lptr tb = lds + hh * 1024 + wst_rd(lane, w);
        SFOR(b, 8)
            VMW(b < 3 ? 32 : 62 - 8 * b); BAR();
            lptr q = tb + (b % 5) * 32768;
#pragma unroll
            for (int k = 0; k < 8; ++k) { lptr a = q + (k >> 1) * 8192 + (k & 1) * 2048; wo[8 * b + k] = tr2(a, a + 4096); if (b < 7) asm volatile("" : "+a"(wo[8 * b + k])); else PIN(wo[8 * b + k]); }
            if constexpr (b < 3) { BAR(); fill(b + 5); }
            if constexpr (b == 2) filt_load<3, 0>(p, L == 0 ? 44 + kq * 5 + ng : 1 << 12, flt);
        SEND
    }
    EV(6);
    const u32 pol = get_pol(p, rs, dead);
    if (L == 0) {
        BAR();
        conv_job<3>(p, rs, lds, 44 + kq * 5 + ng, flt, 0);
        DRAIN(); BAR();
        st4s(rs, tid == 0 ? foff(F_CV + 44 + kq * 5 + ng) : OOB, 0, 2u);
    }
    EV(15);
    MK_SRD();
    const u32 ofl = (lane < 8 && w == 0) ? near_(foff(F_O + L * 32 + kq * 8 + lane), 0u, pol) : OOB;
    const u32 l16 = (u32)lane * 16u;
    const u32 fo = (tid == 192) ? near_(foff(F_P + L * 32 + kq * 5 + ng), 1u, pol) : OOB;
    auto osrc = [&](int s) { return uu(hblk(W_VO, L * NSEG + s, kq * 8, 0u, pol) + w * 1024); };
#define ODMA(i, dst, vo, so) DMA16I(dst, (i) * 4096, vo, so, ((i) >> 1) * 16384 + ((i) & 1) * 4096)
    const u32 pvo = (u32)(kq * 5 + ng) * 16384u + (u32)w * 2048u + l16;
    lptr tw = lds + OU_SEL + 256 + r32 * 272 + (ng == 4 ? hh * 32 + w * 4 : (w & 1) * 128 + hh * 16 + (w >> 1) * 8), tr = lds + OU_SEL + 256 + (8 * w + (lane >> 4)) * 272 + (lane & 15) * 16;
    lptr selw = lds + OU_SEL + (w == 0 ? 0 : 64);
    u32 pfo = OOB;
    u32 buf = 0;
    if (w == 0) wait_flags(rs, ofl, 1u, dead);
    BAR();
    for (int s = 0; s < NSEG; ++s) {
        u32 src = osrc(s), dst = uu(buf + w * 1024);
        lptr A = lds + buf + l16;
        asm volatile("" : "+s"(src), "+s"(dst));
#define OS(k) do {} while (0)
        OS(0);
        OS(1);
        SFOR(i, 10) ODMA(i, dst, l16, src); SEND
        OS(2);
        asm volatile("s_waitcnt vmcnt(7)\n\ts_barrier" ::: "memory");
        OS(3);
        LS2(L == 0 && w == 1, 96 + kq * 5 + ng);
        v4u o[64];
#pragma unroll
        for (int k = 0; k < 3; ++k) o[k] = lds_r16(A + k * 1024);
        st4s(rs, pfo, 0, (u32)s);
        u32 fv, pa; int rdy;
        f32x16 acc;
        SCHED();
        SFOR(k, 64)
            if constexpr (k < 56) M32A(k == 0, acc, wo[k], o[k]); else asm volatile("v_mfma_f32_32x32x16_bf16 %0, %1, %2, %0" : "+v"(acc) : "v"(wo[k]), "v"(o[k]));
            if constexpr (k < 61) o[k + 3] = lds_r16(A + (k + 3) * 1024);
            SCHED();
            if constexpr (k < 12 && (k & 1) == 0) ODMA(10 + (k >> 1), dst, l16, src);
            if constexpr (k == 7) OS(4);
            if constexpr (k == 8) OS(5);
            if constexpr (k == 23) OS(6);
            if constexpr (k == 24) OS(7);
            if constexpr (k == 43) OS(8);
            if constexpr (k == 44) OS(9);
            if constexpr (k == 59) OS(10);
            if constexpr (k == 63) OS(11);
            if constexpr (k == 7) asm volatile("s_waitcnt vmcnt(8)\n\ts_barrier" ::: "memory");
            if constexpr (k == 23) asm volatile("s_waitcnt vmcnt(4)\n\ts_barrier" ::: "memory");
            if constexpr (k == 30) { pa = pvo + near_(p_item(s), (u32)L, pol); PIN(pa); }
            if constexpr (k == 43) asm volatile("s_waitcnt vmcnt(0)\n\ts_barrier" ::: "memory");
            if constexpr (k == OSAMP) LD4A(fv, ofl, 0);
            if constexpr (k == 62) {
                asm volatile("s_waitcnt vmcnt(0)" : "+v"(fv) :: "memory");
                *(LAS int*)selw = (int)((u32)__builtin_amdgcn_ballot_w64(fv < (u32)s + 2) & 255u);
            }
            SCHED();
        SEND
        asm volatile("s_nop 11" : "+v"(acc));
        u32 pc[8];
#pragma unroll
        for (int i = 0; i < 8; ++i) pc[i] = pk(acc[2 * i], acc[2 * i + 1]);
        if (ng != 4) {
#pragma unroll
            for (int i = 0; i < 4; ++i) *(LAS v2u*)(tw + i * 32) = (v2u){pc[2 * i], pc[2 * i + 1]};
        } else {
#pragma unroll
            for (int i = 0; i < 8; ++i) *(LAS u32*)(tw + (i >> 1) * 64 + (i & 1) * 16) = pc[i];
        }
        BAR();
        const v4u p0 = lds_r16(tr), p1 = lds_r16(tr + 4 * 272);
        rdy = *(LAS int*)(lds + OU_SEL);
        st16(rs, pa, p0); st16(rs, pa + 1024, p1);
        const bool ready = uni(rdy) == 0;
        SCHED();
        OS(12);
        if (ready) pfo = fo;
        else {
            LD4A(fv, ofl, 0);
            asm volatile("s_waitcnt vmcnt(1)\n\ts_barrier" ::: "memory");
            st4s(rs, fo, 0, (u32)s + 1); pfo = OOB;
            OS(14);
            LS2(L == 0 && w == 1, 160 + kq * 5 + ng);
            asm volatile("s_waitcnt vmcnt(1)" : "+v"(fv) :: "memory");
            if (w == 0 && s + 1 < NSEG && ((u32)__builtin_amdgcn_ballot_w64(fv < (u32)s + 2) & 255u) != 0u) wait_flags(rs, ofl, (u32)s + 2, dead, L >= CALM_L && s + 2 < NSEG);
            BAR();
        }
        OS(13);
        buf ^= 65536u;
    }
}

template <int L> DEV void role_row(const Params& p, lptr lds, int ri) {
    const int lane = threadIdx.x & 63, w = uni(threadIdx.x >> 6);
    const rsrc_t rs = mk_rsrc(p.ws, W_END);
    bool dead = false;
    const u32 pol = get_pol(p, rs, dead);
    const u32 fp = threadIdx.x < 20 ? near_(foff(F_P + L * 32 + lane), 1u, pol) : OOB, fo = threadIdx.x != 192 ? OOB : L == 0 ? near_(foff(F_H2 + (ri >> 2)), 1u, pol) : ri == 3 ? foff(F_EP) : OOB;
    u32 fv = 0;
    MK_SRD();
    auto yfill = [&](int b, int i) { DMA32(uu((b & 1) * 40960 + w * 10240 + i * 2048), (u32)lane * 16u, uu(W_Y + (u32)((ri >> 1) * 8 + b) * 20480u + (w & 1) * 10240 + i * 2048)); };
    auto ywait = [&](u32 v) {
        wait_flags(rs, threadIdx.x < (v == 3u ? 36u : 160u) ? foff(F_CV + threadIdx.x) : OOB, v, dead);
    };
    v4u y8[10] = {};
    v2u xa, xb; u32 xc;
    const v4u sx = L == 0 ? mk_srd(p.x, ACT) : mk_srd(p.ws + W_X2, ACT);
    auto xload = [&](int s) {
        asm volatile("buffer_load_dwordx2 %0, %3, %5, %6 offen sc1\n\tbuffer_load_dwordx2 %1, %3, %5, %6 offen offset:512 sc1\n\tbuffer_load_dword %2, %4, %5, %6 offen offset:1024 sc1"
                     : "=&v"(xa), "=&v"(xb), "=&v"(xc) : "v"((u32)lane * 8u), "v"((u32)lane * 4u), "s"(sx), "s"(uu((u32)(s * 32 + ri) * (DM * 2))) : "memory");
        SCHED();
    };
    ywait(1u);
    BAR();
    EV(15);
#pragma unroll 1
    for (int i = 0; i < 5; ++i) yfill(0, i);
    if (L == 1) wait_flags(rs, lane == 0 ? near_(foff(F_H2 + (ri >> 2)), 1u, pol) : OOB, 1u, dead);
    xload(0);
    LD4A(fv, fp, 0);
    VMW(0); SCHED();
    asm volatile("" : "+v"(xa), "+v"(xb), "+v"(xc), "+v"(fv));
    BAR();
    for (int s = 0; s < NSEG; ++s) {
        u32 xi[5] = {xa.x, xa.y, xb.x, xb.y, xc};
        u32 pa = near_(p_item(s), (u32)L, pol) + (u32)ri * 256u, pc = pa + 65536u + (u32)(lane >> 4) * 81920u + (u32)(lane & 15) * 16u;
        pa += (u32)(lane >> 5) * 81920u + (u32)((lane >> 4) & 1) * 16384u + (u32)(lane & 15) * 16u;
        u32 yf = s == 1 ? foff(F_CV + min(threadIdx.x, 159u)) : s == 25 ? foff(F_CV + min(lane, 35)) : OOB;
        asm volatile("" : "+v"(xi[0]), "+v"(xi[1]), "+v"(xi[2]), "+v"(xi[3]), "+v"(xi[4]), "+v"(pa), "+v"(pc), "+v"(yf));
#define RS(k) do {} while (0)
        RS(0);
        if (w == 0 && ((u32)__builtin_amdgcn_ballot_w64(fv <= (u32)s) & 0xFFFFFu) != 0u) wait_flags(rs, fp, (u32)s + 1, dead);
        BAR();
        RS(1);
        v4u d[5];
        asm volatile("buffer_load_dwordx4 %0, %5, %7, 0 offen sc1\n\tbuffer_load_dwordx4 %1, %5, %7, %8 offen sc1\n\tbuffer_load_dwordx4 %2, %5, %7, %9 offen sc1\n\t"
                     "buffer_load_dwordx4 %3, %5, %7, %10 offen sc1\n\tbuffer_load_dwordx4 %4, %6, %7, 0 offen sc1"
                     : "=&v"(d[0]), "=&v"(d[1]), "=&v"(d[2]), "=&v"(d[3]), "=&v"(d[4]) : "v"(pa), "v"(pc), "s"(srd), "s"(163840), "s"(32768), "s"(196608) : "memory");
        RS(2);
        LS2(L == 0, 192 + ri);
        SCHED();
        f32x2 y[5];
        {
            if ((s & 7) == 0) {
                lptr ya = lds + ((s >> 3) & 1) * 40960 + (w >> 1) * 20480 + (w & 1) * 16, yc = ya + 4096 + (lane >> 1) * 32 + (lane & 1) * 10240;
#pragma unroll
                for (int i = 0; i < 8; ++i) y8[i] = lds_r16(ya + lane * 32 + (i >> 2) * 2048 + (i & 3) * 5120);
                y8[8] = lds_r16(yc); y8[9] = lds_r16(yc + 5120);
            }
#pragma unroll
            for (int i = 0; i < 10; ++i) {
                y[i >> 1][i & 1] = lo_f(y8[i].x);
                y8[i] = (v4u){__builtin_amdgcn_alignbit(y8[i].y, y8[i].x, 16), __builtin_amdgcn_alignbit(y8[i].z, y8[i].y, 16), __builtin_amdgcn_alignbit(y8[i].w, y8[i].z, 16), y8[i].w >> 16};
                PIN(y8[i]);
            }
            asm volatile("" : "+v"(y[0]), "+v"(y[1]), "+v"(y[2]), "+v"(y[3]), "+v"(y[4]));
        }
        u32 yv;
        {
            xload(s + 1);
            const bool yy = (s & 7) - 2u < 5u && s < NSEG - 8;
            u32 yo = yy ? (u32)lane * 16u : OOB;
            DMA32(uu(yy ? (((s >> 3) + 1) & 1) * 40960 + w * 10240 + ((s & 7) - 2) * 2048 : 81920), yo, uu(W_Y + (u32)((ri >> 1) * 8 + (s >> 3) + 1) * 20480u + (w & 1) * 10240 + ((s & 7) - 2) * 2048));
            LD4A(yv, yf, 0);
            asm volatile("s_waitcnt vmcnt(6)" : "+v"(d[0]), "+v"(d[1]), "+v"(d[2]), "+v"(d[3]), "+v"(d[4]));
        }
        f32x2 o[5];
        {
            f32x2 t[5][2];
#pragma unroll
            for (int i = 0; i < 5; ++i) {
                const auto e = __builtin_amdgcn_permlane32_swap(d[i].x, d[i].z, false, false), f = __builtin_amdgcn_permlane32_swap(d[i].y, d[i].w, false, false);
                t[i][0] = up2(e[0]) + up2(e[1]); t[i][1] = up2(f[0]) + up2(f[1]);
            }
            o[0] = t[0][0] + t[1][0]; o[1] = t[0][1] + t[1][1]; o[2] = t[2][0] + t[3][0]; o[3] = t[2][1] + t[3][1];
            const auto e = __builtin_amdgcn_permlane16_swap(u_(t[4][0].x), u_(t[4][1].x), false, false), f = __builtin_amdgcn_permlane16_swap(u_(t[4][0].y), u_(t[4][1].y), false, false);
            o[4] = (f32x2){f_(e[0]) + f_(e[1]), f_(f[0]) + f_(f[1])};
        }
        f32x2 x1[5];
#pragma unroll
        for (int i = 0; i < 5; ++i) x1[i] = up2(pk2(up2(xi[i]) + up2(pk2(o[i]))));
        asm volatile("" : "+v"(x1[0]), "+v"(x1[1]), "+v"(x1[2]), "+v"(x1[3]), "+v"(x1[4]));
        RS(3);
        const f32x2 st1 = ln_stats(x1);
        u32 xp[5];
#pragma unroll
        for (int i = 0; i < 5; ++i) xp[i] = pk2(x1[i] + ((x1[i] - st1.x) * st1.y) * y[i]);
        const int row = s * 32 + ri;
        RS(4);
        if (L == 0) {
            const u32 ro = (u32)row * (DM * 2);
            f32x2 x2[5];
#pragma unroll
            for (int i = 0; i < 5; ++i) x2[i] = up2(xp[i]);
            st8s(rs, (u32)lane * 8, W_X2 + ro, (v2u){xp[0], xp[1]});
            st8s(rs, (u32)lane * 8 + 512, W_X2 + ro, (v2u){xp[2], xp[3]});
            st4s(rs, (u32)lane * 4 + 1024, W_X2 + ro, xp[4]);
            SCHED();
            const f32x2 st2 = ln_stats(x2);
            u32 hp[5];
#pragma unroll
            for (int i = 0; i < 5; ++i) hp[i] = pk2((x2[i] - st2.x) * st2.y);
            store_h(rs, near_(W_H2 + (u32)s * H_TILE, 1u, pol), row, hp);
        } else {
            const rsrc_t ro = mk_rsrc(p.out, ACT);
            __builtin_amdgcn_raw_buffer_store_b64((v2u){xp[0], xp[1]}, ro, (u32)lane * 8u, (u32)row * (DM * 2), OUTAUX);
            __builtin_amdgcn_raw_buffer_store_b64((v2u){xp[2], xp[3]}, ro, (u32)lane * 8u + 512u, (u32)row * (DM * 2), OUTAUX);
            __builtin_amdgcn_raw_buffer_store_b32(xp[4], ro, (u32)lane * 4u + 1024u, (u32)row * (DM * 2), OUTAUX);
        }
        RS(5);
        if (L == 1 && s == NSEG - 1) __builtin_amdgcn_endpgm();
        LD4A(fv, fp, 0);
        if (L == 0) asm volatile("s_waitcnt vmcnt(1)\n\ts_barrier" ::: "memory");
        st4s(rs, L == 0 || s == 40 ? fo : OOB, 0, L == 0 ? (u32)s + 1 : p.e0);
        RS(6);
        VMW(1); SCHED();
        asm volatile("" : "+v"(fv), "+v"(yv), "+v"(xa), "+v"(xb), "+v"(xc));
        if (__builtin_expect(s == 1 || s == 25, 0) && !all_(yv >= (s == 1 ? 2u : 3u))) ywait(s == 1 ? 2u : 3u);
        RS(7);
        if (ri == 0) LST(320 + L * 64 + s, rt32());
        LS2(L == 0, 224 + ri);
    }
}

constexpr float SCALE = 0.08838834764831845f, LOG2E = 1.4426950408889634f;
constexpr int C_K = 0, C_V = 8192, C_QX = 16384, C_MEM = 24576, C_MASK = C_MEM + 10240,
              C_Q = C_MASK + 1024, C_ZERO = C_Q + 2304, C_KM = C_Q + 4096, C_VM = C_KM + 4096, C_DUMP = C_VM + 2048, C_WL = C_DUMP + 1024;
constexpr int NLF = 14;
static_assert(C_WL % 16 == 0 && C_WL + 4 * NLF * 2048 <= LDS_BYTES, "head lds");
#define CH_H0 98
#define CH_W0 80
#define CH_MH 11
#define CH_MW 16
#define CH_REC 16
#define CH_LATE 30
#define CH_SOLO 6
#define CH_KV 12
#define CH_KV0 44
#define CH_M0 3
#define CH_DEC 2u
#define CH_EARLY 10
#define CH_NEAR 4
#define CH_FIRST 112
#define CH_ATT 2
#define CH_CLIP 12u
#define CH_UP 3
#define CH_WX 6
DEV void wait_until(u32 t) {
    unsigned long long c;
    asm volatile("1:\n\ts_memrealtime %0\n\ts_waitcnt lgkmcnt(0)\n\ts_mov_b64 vcc, %0\n\ts_sub_u32 vcc_lo, %1, vcc_lo\n\ts_cmpk_lt_u32 vcc_lo, 400\n\ts_cbranch_scc1 1b" : "=&s"(c) : "s"(uu(t - 1u)) : "vcc", "scc", "memory");
}
#define CH_FD 14
#define CH_FC 34
#define FINE_T1(t) uu((t) - (CH_FD + 1u))
#define FINE_T4(t) uu(4u * (t) - CH_FC)
#define NOP8 "s_nop 0\n\ts_nop 0\n\ts_nop 0\n\ts_nop 0\n\ts_nop 0\n\ts_nop 0\n\ts_nop 0\n\ts_nop 0\n\t"
#define LOOP_TXT(tmp, t1) "s_memrealtime vcc\n1:\n\ts_waitcnt lgkmcnt(0)\n\ts_sub_u32 " tmp ", " t1 ", vcc_lo\n\ts_memrealtime vcc\n\ts_cmpk_lt_u32 " tmp ", 400\n\ts_cbranch_scc1 1b\n\t"
#define WAIT_TXT(tmp, t1, t4) LOOP_TXT(tmp, t1) \
    "s_nop 1\n\ts_memrealtime s[90:91]\n\ts_nop 3\n\ts_memrealtime s[92:93]\n\ts_nop 3\n\ts_memrealtime s[94:95]\n\ts_getpc_b64 s[96:97]\n2:\n\ts_waitcnt lgkmcnt(0)\n\t" \
    "s_add_u32 s90, s90, vcc_lo\n\ts_add_u32 s92, s92, s94\n\ts_sub_u32 s90, " t4 ", s90\n\ts_sub_u32 s90, s90, s92\n\ts_max_i32 s90, s90, 0\n\ts_min_i32 s90, s90, 29\n\t" \
    "s_mul_i32 s90, s90, 87\n\ts_lshr_b32 s90, s90, 4\n\ts_andn2_b32 s90, s90, 3\n\ts_sub_u32 s90, 3f-2b, s90\n\ts_add_u32 s96, s96, s90\n\ts_addc_u32 s97, s97, 0\n\t" \
    "s_setpc_b64 s[96:97]\n\t" NOP8 NOP8 NOP8 NOP8 NOP8 "3:\n\t"
#define WAIT_CLOB "vcc", "scc", "s90", "s91", "s92", "s93", "s94", "s95", "s96", "s97", "memory"
#define SMF_A(acc, a, b) asm volatile("v_smfmac_f32_16x16x64_bf16 %0, %1, %2, %3" : "+v"(acc) : "v"(a), "a"(b), "v"(idx))
#define SMF_V(acc, a, b) asm volatile("v_smfmac_f32_16x16x64_bf16 %0, %1, %2, %3" : "+v"(acc) : "v"(a), "v"(b), "v"(idx))
#define MF0(d, a, b) asm volatile("v_mfma_f32_16x16x32_bf16 %0, %1, %2, 0" : "=&v"(d) : "v"(a), "v"(b))
#define MF1(d, a, b) asm volatile("v_mfma_f32_16x16x32_bf16 %0, %1, %2, %0" : "+v"(d) : "v"(a), "v"(b))
#define MF0H(d, a, b) asm volatile("v_mfma_f32_16x16x16_bf16 %0, %1, %2, 0" : "=&v"(d) : "v"(a), "v"(b))
#define MF1H(d, a, b) asm volatile("v_mfma_f32_16x16x16_bf16 %0, %1, %2, %0" : "+v"(d) : "v"(a), "v"(b))
DEV v8u tr_frag(lptr p0, lptr p1, lptr p2, lptr p3) { return cat8(tr2(p0, p1), tr2(p2, p3)); }
constexpr int qslot(int ks) { return (ks & 1) && ks < 8 ? ks >> 1 : -1; }
constexpr int kslot(int ks) { return ks < 4 ? 4 + ks : -1; }
constexpr int vslot(int ks) { return ks >= 4 ? 4 + ks : -1; }
DEV u32 ring_map(u32 off, u32 half) { return ((off >> 13) << 14) + (half << 13) + (off & 8191u); }
#define ZRD(x, i) x = __builtin_bit_cast(f32x4, lds_r16(ZP + 16 * (i)))
#define VMAX(d, a, b) asm volatile("v_max_f32 %0, %1, %2" : "=v"(d) : "v"(a), "v"(b))
#define VMAX3(d, a, b, c_) asm volatile("v_max3_f32 %0, %1, %2, %3" : "=v"(d) : "v"(a), "v"(b), "v"(c_))
#define VADD(d, a, b) asm volatile("v_add_f32 %0, %1, %2" : "=v"(d) : "v"(a), "v"(b))
#define VEXP(d, a) asm volatile("v_exp_f32 %0, %1" : "=v"(d) : "v"(a))
#define VFMC(d, a, b) asm volatile("v_fmamk_f32 %0, %1, 0x3e0293ee, %2" : "=v"(d) : "v"(a), "v"(b))
#define VMULC(d, a) asm volatile("v_mul_f32 %0, 0x3e0293ee, %1" : "=v"(d) : "v"(a))
#define SEL(d, a, b) asm volatile("v_cndmask_b32 %0, %1, %2, %3" : "=v"(d) : "v"(a), "v"(b), "s"(himask))
#define QPERM(d, a, q) asm volatile("v_mov_b32_dpp %0, %1 quad_perm:" q " row_mask:0xf bank_mask:0xf" : "=v"(d) : "v"(a))

#define SUMRANGE 0x28000000u
#define FL_EARLY 1
#define HSAMP 71
DEV void role_head(const Params& p, lptr lds, int L, int h) {
    const int tid = threadIdx.x, lane = tid & 63, w = uni(tid >> 6), g = lane >> 4, r = lane & 15;
    const rsrc_t rs = mk_rsrc(p.ws, W_END);
    v8u wq0[10], wq1[10], wk0[10], wk1[10], wv0[10], wv1[10];
    lptr WL = lds + C_WL + w * (NLF * 2048) + lane * 16;
    MK_SRD();
    bool dead = false, have;
    const u32 l16 = (u32)lane * 16u;
    u32 pol, pol0, mring, hint, fkh, ksrc, vsrc, kb = uu(W_KQ + (u32)(L * NSEG * NH + h) * 16384u + w * 2048);
    unsigned long long kmask, vmask;
    u32 dk = uu(C_K + w * 2048), dv = dk + (C_V - C_K), dq = dk + (C_QX - C_K), dm = uu(C_MEM + w * 2048), dm2 = uu(w < 2 ? C_MEM + 8192 + w * 1024 : C_DUMP);
    {
        constexpr int GS = 1056, CS = 48 * GS;
        const v4u sq = mk_srd(p.Wq, DM * NQ * 2), sk = mk_srd(p.Wk, DM * NQ * 2), sv = mk_srd(p.Wv, DM * NQ * 2);
        const u32 vo = (u32)(g * NQ + h * DH + 8 * (r ^ ((g & 2) << 2))) * 2u;
        auto fill = [&](int ch) {
#pragma unroll 1
            for (int i = 0; i < 4; ++i) {
                const u32 dst = uu((ch % 3) * CS + (4 * i + w) * GS), so = uu((ch * 64 + 16 * i + 4 * w) * (NQ * 2));
                DMAIN(sq, dst, vo, so); DMAIN(sk, dst + 16 * GS, vo, so); DMAIN(sv, dst + 32 * GS, vo, so);
            }
        };
        if (L == 1) pre_rings(p, rs, h);
        fill(0); fill(1); fill(2);
        const int q4 = r >> 2;
        lptr tb = lds + (4 * (g >> 1) + q4) * GS + (g & 1) * 512 + ((w * 64 + (r & 3) * 8) ^ ((g & 1) << 7));
        v4u praw; u32 f0;
        SFOR(ch, 10)
            asm volatile("s_waitcnt vmcnt(%0) ; HZCLR" :: "i"(ch >= 4 && ch < 7 ? 25 : ch < 8 ? 24 : ch == 8 ? 12 : 1) : "memory");
            BAR();
            auto frag = [&](int m, int t) { lptr b = tb + (ch % 3) * CS + m * 16 * GS + t * 32; return tr_frag(b, b + 256, b + 8 * GS, b + 8 * GS + 256); };
            wq0[ch] = frag(0, 0); wq1[ch] = frag(0, 1); wk0[ch] = frag(1, 0); wk1[ch] = frag(1, 1); wv0[ch] = frag(2, 0); wv1[ch] = frag(2, 1);
            asm volatile("" : "+a"(wq0[ch]), "+a"(wk0[ch]), "+v"(wv0[ch]), "+v"(wv1[ch]));
            if constexpr (qslot(ch) < 0) asm volatile("" : "+a"(wq1[ch])); else asm volatile("" : "+v"(wq1[ch]));
            if constexpr (kslot(ch) < 0) asm volatile("" : "+a"(wk1[ch])); else asm volatile("" : "+v"(wk1[ch]));
            BAR();
            if constexpr (ch < 7) fill(ch + 3);
            if constexpr (ch == 3) asm volatile("buffer_load_dwordx4 %0, %1, %2, 0 offen sc1" : "=v"(praw) : "v"(pol_off()), "s"(srd) : "memory");
            if constexpr (ch == 7) {
                PIN(praw);
                if (!boot_ok(p, pol_off(), praw, 0u)) praw.w = boot_wait(p, rs, pol_off(), 0u, dead);
                pol = pol_of(praw.w); pol0 = pol & 1u;
                mring = W_MRING + (u32)L * (MB / 4) + pol0 * 8192u; hint = mring + 4 * 32768u;
                fkh = lane < 3 ? near_(foff(F_QKV + lane * 64 + L * 32 + h), 0u, pol) : lane < 6 ? hint + (u32)(lane - 3) * 4u : OOB;
                kmask = __builtin_amdgcn_ballot_w64((hblk(W_KQ, L * NSEG + lane, h, 0u, pol) & 8192u) != 0u);
                vmask = __builtin_amdgcn_ballot_w64((hblk(W_VO, L * NSEG + lane, h, 1u, pol) & 8192u) != 0u);
                ksrc = kb + ((u32)kmask & 1u) * 8192u; vsrc = kb + (W_VO - W_KQ) + ((u32)vmask & 1u) * 8192u;
                PIN(fkh);
                asm volatile("" : "+s"(pol), "+s"(kmask), "+s"(vmask), "+s"(ksrc), "+s"(vsrc), "+s"(dk), "+s"(dv), "+s"(dq), "+s"(dm), "+s"(dm2));
            }
            if constexpr (ch == 8) LD4A(f0, fkh, 0);
            SCHED();
        SEND
        VMW(0); SCHED(); PIN(f0);
        have = ((u32)__builtin_amdgcn_ballot_w64(f0 == 0u) & 7u) == 0u;
        if (have) { DMA32(dv, l16, vsrc); DMA32(dk, l16, ksrc); }
#pragma unroll
        for (int ch = 0; ch < 10; ++ch) {
            if (qslot(ch) >= 0) { lds_w16(WL + qslot(ch) * 2048, wq1[ch].lo); lds_w16(WL + qslot(ch) * 2048 + 1024, wq1[ch].hi); }
            if (kslot(ch) >= 0) { lds_w16(WL + kslot(ch) * 2048, wk1[ch].lo); lds_w16(WL + kslot(ch) * 2048 + 1024, wk1[ch].hi); }
            if (vslot(ch) >= 0) { lds_w16(WL + vslot(ch) * 2048, wv1[ch].lo); lds_w16(WL + vslot(ch) * 2048 + 1024, wv1[ch].hi); }
        }
        CFENCE();
    }
    const u32 idx = (r & 2) ? 0xEEEEu : 0x4444u;
    const unsigned long long himask = 0xCCCCCCCCCCCCCCCCull;
    EV(6);
    u32 tm = 0;
    lptr qw = lds + C_Q + (2 * g) * 288 + (32 * w + 2 * r) * 2;
    lptr kw = qw + (C_KM - C_Q);
    lptr qr = lds + C_Q + (2 * (r >> 2) + (r & 1)) * 288 + g * 16;
    lptr LB = lds + l16, VX = lds + C_V + w * 2048 + l16, QX = lds + C_QX + (w & 1) * 4096 + l16;
    if (tid >= 64 && tid < 70) { const u32 z = tid == 68 ? 0x3F803F80u : tid == 69 ? PSENT : 0u; lds_w16(lds + C_ZERO + (tid - 64) * 16, (v4u){z, z, z, z}); }
    if (w < 3) {
        const u32 ng = u_(-1000000000.0f * LOG2E);
        v4u mk;
#pragma unroll
        for (int i = 0; i < 4; ++i) mk[i] = w == 0 || (w == 1 && 4 * g + i > r) ? ng : 0u;
        lds_w16(lds + C_MASK + w * 4096 + l16, mk);
    }
    u32 zaddr = 0; PIN(zaddr);
    lptr ZP = lds + zaddr + C_ZERO;
#define LF_RD(slot, hf) lf[slot][hf] = lds_r16(WL + (slot) * 2048 + (hf) * 1024)
    const u32 ofl = tid == 192 ? near_(foff(F_O + L * 32 + h), 0u, pol) : tid == 193 && (h & 15) == 0 ? hint + 128u + (u32)h * 8u : OOB;
    u32 oso = (g & 1) ? OOB : W_ORING + (u32)L * (MB / 2) + ring_map((u32)((r >> 3) * 32768 + h * 1024 + w * 256 + (g >> 1) * 128 + (r & 7) * 16), pol0);
    u32 missed = 0, dirty = 1, rep = PSENT;
    const u32 early = L < FL_EARLY ? 0u : OOB;
    asm volatile("" : "+v"(oso), "+v"(qw), "+v"(kw), "+v"(qr), "+v"(VX), "+v"(QX));
    if (!have) { wait_flags(rs, lane < 3 ? fkh : OOB, 1u, dead); DMA32(dv, l16, vsrc); DMA32(dk, l16, ksrc); }
    EV(7);
    st4s(rs, L == 0 && tid == 192 ? foff(F_GO + h) : OOB, 0, 1u);
    EV(8);
    auto next_slot = [&](u32 now) {
        const u32 hv = ld4(rs, fkh), per = rdl(hv, 4);
        if (per < 400u) { tm = rdl(hv, 5) - per; for (int n = 0; n < 3 && (int)(tm - now) < 0; ++n) tm += per; }
    };
    if (L == 1) { next_slot(rt32() + CH_KV0); tm -= 24u; }
#define C2(k) do {} while (0)
    for (int s = 0; s < NSEG;) {
        {
            *(LAS u32*)(lds + C_DUMP + 128 + w * 4) = tm;
            BAR();
            const v4u c = lds_r16(lds + C_DUMP + 128) - tm;
            const u32 add = uu((u32)max(max((int)c.x, (int)c.y), max((int)c.z, (int)c.w)));
            tm += add; dirty |= add;
        }
        u32 ms = mring + (u32)(s & 3) * 32768u + w * 2048, ms2 = mring + (u32)(s & 3) * 32768u + 16384u + (w & 1) * 1024;
        u32 l3 = w < 2 ? l16 : OOB;
        asm volatile("" : "+s"(ms), "+s"(ms2)); PIN(l3);
        f32x4 aq0, aq1, ak0, ak1;
        v4u lf[NLF][2];
        u32 tw1 = FINE_T1(tm), tw4 = FINE_T4(tm), fv = (l16 & 16u) ? rep : (u32)s;
        asm volatile("" : "+s"(tw1), "+s"(tw4)); PIN(fv);
        SCHED();
        C2(4);
        asm volatile("s_waitcnt vmcnt(5)\n\ts_barrier" ::: "memory");
        C2(5);
        st4s(rs, ofl | early, 0, fv);
        if (__builtin_expect(dirty != 0u, 0)) {
            wait_until(tm - 44u);
            st16(rs, oso, lds_r16(ZP + 80));
        }
        SCHED();
        v4u A[10];
#define HT(k) do {} while (0)
        v4u kx[8], qf[4], kmf[4], vx[2], ones, P, tq[4];
        float fa, fb, fc, fd, sx[4], t[4], sm0, sm1, tm0, tm1, inv; u32 pa, pb, fl, pmm, tmp = 0; unsigned long long odd;
        f32x4 av0, av1, s0, s1, sm, oa, ob, rsum;
        v2u vm, pm, vh, vl, on[2];
        f32x2 zz[4];
#define LW(sl) cat8(lf[sl][0], lf[sl][1])
#define PQ(ks, tl) do { if constexpr (!(tl)) SMF_A(aq0, A[ks], wq0[ks]); else if constexpr (qslot(ks) < 0) SMF_A(aq1, A[ks], wq1[ks]); else { const v8u lw_ = LW(qslot(ks)); SMF_V(aq1, A[ks], lw_); } } while (0)
#define PK(ks, tl) do { if constexpr (!(tl)) SMF_A(ak0, A[ks], wk0[ks]); else if constexpr (kslot(ks) < 0) SMF_A(ak1, A[ks], wk1[ks]); else { const v8u lw_ = LW(kslot(ks)); SMF_V(ak1, A[ks], lw_); } } while (0)
#define PV(ks, tl) do { if constexpr (!(tl)) SMF_V(av0, A[ks], wv0[ks]); else if constexpr (vslot(ks) < 0) SMF_V(av1, A[ks], wv1[ks]); else { const v8u lw_ = LW(vslot(ks)); SMF_V(av1, A[ks], lw_); } } while (0)
#define KXR(i) kx[i] = lds_r16(LB + C_K + ((i) & 1) * 4096 + ((i) >> 1) * 1024)
#define XW(q_) do { *(LAS u32*)(q_) = pa; *(LAS u32*)((q_) + 288) = pb; } while (0)
        auto slot = [&](auto zc) __attribute__((always_inline)) {
            constexpr int z = decltype(zc)::value;
            constexpr int vs[] = {0, 8, 1, 10, 9, 2, 11, -1, -1, -1, -1, 4, 6, 12, -1, -1, -1, 13, 14, -1, 16, 18, 15, 3, 5, 17, 7, 19};
            if constexpr (z < 12) PQ(z >> 1, z & 1);
            else if constexpr (z < 20) PQ(6 + (z & 3), z >> 4);
            else if constexpr (z < 32) PK((z - 20) >> 1, z & 1);
            else if constexpr (z < 40) PK(6 + (z & 3), (z >> 2) & 1);
            else if constexpr (z < 42) { if constexpr (z == 40) MF0(s0, kx[0], qf[0]); else MF0(s1, kx[1], qf[0]); }
            else if constexpr (z < 48) { if constexpr (z & 1) MF1(s1, kx[z - 40], qf[(z - 40) >> 1]); else MF1(s0, kx[z - 40], qf[(z - 40) >> 1]); }
            else if constexpr (z == 55) MF0(sm, kmf[0], qf[0]);
            else if constexpr (z > 55 && z < 59) MF1(sm, kmf[z - 55], qf[z - 55]);
            else if constexpr (z == 62) MF0(rsum, ones, P);
            else if constexpr (z == 63) MF0(oa, vx[0], P);
            else if constexpr (z == 64) MF0(ob, vx[1], P);
            else if constexpr (z == 67) { const v2u ones2 = ones.lo; MF1H(rsum, ones2, pm); }
            else PV(vs[z - 48] >> 1, vs[z - 48] & 1);
            if constexpr (z == 20 || z == 24 || z == 26) USE(lf[4 + ((z - 20) >> 1)][1]);
            if constexpr (z == 41) USE(qf[3]);
            if constexpr (z == 45 || z == 46) USE(kx[z - 39]);
            if constexpr (z == 53) USE(lf[9][1]);
            if constexpr (z >= 54 && z < 58) USE(kmf[z - 54]);
            if constexpr (z == 61) USE(vl);
            if constexpr (z == 64) USE(lf[10][1]);
            if constexpr (z == 69) USE(lf[11][1]);
            if constexpr (z == 72) USE(lf[12][1]);
            if constexpr (z == 74) USE(lf[13][1]);
            if constexpr (z < 8) A[2 + z] = lds_r16(LB + C_MEM + (2 + z) * 1024);
            if constexpr (z >= 15 && z < 23) LF_RD(4 + ((z - 15) >> 1), (z - 15) & 1);
            if constexpr (z >= 24 && z < 27) KXR(z - 24);
            if constexpr (z == 27) XW(qw);
            if constexpr (z >= 28 && z < 31) KXR(z - 25);
            if constexpr (z == 32) asm volatile("s_waitcnt lgkmcnt(3)\n\ts_barrier" ::: "memory");
            if constexpr (z >= 33 && z < 37) qf[z - 33] = lds_r16(qr + (z - 33) * 64);
            if constexpr (z == 37 || z == 38) LF_RD(8, z - 37);
            if constexpr (z == 39 || z == 41) KXR(6 + (z == 41));
            if constexpr (z == 44) XW(kw);
            if constexpr (z == 45 || z == 46) LF_RD(9, z - 45);
            if constexpr (z == 47) vx[0] = lds_r16(VX);
            if constexpr (z == 48) { VMW(1); vh = *(LAS v2u*)(VX + 1032); }
            if constexpr (z == 54) { vl = *(LAS v2u*)(VX + 1024); vx[1] = (v4u){vh.x, vh.y, vl.x, vl.y}; }
            if constexpr (z == 49) { USE(lf[9][0]); asm volatile("s_barrier" ::: "memory"); }
            if constexpr (z >= 50 && z < 54) kmf[z - 50] = lds_r16(qr + (C_KM - C_Q) + (z - 50) * 64);
            if constexpr (z == 59 || z == 60) LF_RD(10, z - 59);
            if constexpr (z == 61 || z == 65) LF_RD(11, z == 65);
            if constexpr (z == 66 || z == 68) LF_RD(12, z == 68);
            if constexpr (z == 69 || z == 70) LF_RD(13, z - 69);
            if constexpr (z >= 72) tq[z - 72] = lds_r16(QX + (z - 72) * 1024);
            if constexpr (z == HSAMP) LD4A(fl, fkh, 0);
            SCHED();
            if constexpr (z == 19) HT(2);
            if constexpr (z == 39) HT(3);
            if constexpr (z == 51) HT(4);
            if constexpr (z == 64) HT(5);
            if constexpr (z == 20) VADD(fa, aq0[0], aq0[2]);
            if constexpr (z == 21) VADD(fb, aq0[1], aq0[3]);
            if constexpr (z == 23) VADD(fc, aq1[0], aq1[2]);
            if constexpr (z == 24) VADD(fd, aq1[1], aq1[3]);
            if constexpr (z == 25) VCVT(pa, fa, fc);
            if constexpr (z == 26) VCVT(pb, fb, fd);
            if constexpr (z >= 44 && z < 48) asm volatile("v_mov_b64 %0, 0" : "=v"(zz[z - 44]));
            if constexpr (z == 47) { av0 = __builtin_shufflevector(zz[0], zz[1], 0, 1, 2, 3); av1 = __builtin_shufflevector(zz[2], zz[3], 0, 1, 2, 3); }
            if constexpr (z == 48 || z == 49) asm volatile("v_mov_b64 %0, %1" : "=v"(on[z - 48]) : "s"(0x3F803F803F803F80ull));
            if constexpr (z == 49) ones = __builtin_shufflevector(on[0], on[1], 0, 1, 2, 3);
            if constexpr (z == 40) { VADD(fa, ak0[0], ak0[2]); VADD(fb, ak0[1], ak0[3]); }
            if constexpr (z == 42) { VADD(fd, ak1[1], ak1[3]); VADD(fc, ak1[0], ak1[2]); }
            if constexpr (z == 43) { VCVT(pb, fb, fd); VCVT(pa, fa, fc); }
            if constexpr (z >= 50 && z < 54) SEL(sx[z - 50], s0[z - 50], s1[z - 50]);
            if constexpr (z >= 54 && z < 58) VEXP(t[z - 54], sx[z - 54]);
            if constexpr (z == 58) asm volatile("v_cvt_pk_bf16_f32 %0, %2, %3\n\tv_cvt_pk_bf16_f32 %1, %4, %5" : "=&v"(P.x), "=v"(P.y) : "v"(t[0]), "v"(t[1]), "v"(t[2]), "v"(t[3]));
            if constexpr (z == 59) QPERM(P.z, P.x, "[2,3,0,1]");
            if constexpr (z == 60) QPERM(P.w, P.y, "[2,3,0,1]");
            if constexpr (z == 61) { USE(sm); VMULC(sm0, sm[0]); }
            if constexpr (z == 62) { VEXP(tm0, sm0); VMULC(sm1, sm[1]); }
            if constexpr (z == 63) VEXP(tm1, sm1);
            if constexpr (z == 64) asm volatile("v_cvt_pk_bf16_f32 %0, %2, %3\n\tv_cndmask_b32 %1, %0, 0, %4" : "=&v"(pmm), "=v"(pm.x) : "v"(tm0), "v"(tm1), "s"(himask));
            if constexpr (z == 65) asm volatile("v_cndmask_b32 %0, 0, %1, %2" : "=v"(pm.y) : "v"(pmm), "s"(himask));
            if constexpr (z == 67) { SEL(oa[0], oa[0], ob[0]); SEL(oa[1], oa[1], ob[1]); }
            if constexpr (z == 68 || z == 69) SEL(oa[z - 66], oa[z - 66], ob[z - 66]);
            if constexpr (z == 70) asm volatile("v_sub_u32 %0, %1, %2" : "=v"(pa) : "v"(rsum[0]), "s"(0x2B800000u), "v"(rsum));
            if constexpr (z == 71) asm volatile("v_cmp_le_u32 %0, %1, %2" : "=s"(odd) : "s"(SUMRANGE), "v"(pa));
            if constexpr (z == 72) VADD(fa, av0[0], av0[2]);
            if constexpr (z == 73) VADD(fb, av0[1], av0[3]);
            if constexpr (z == 74) VCVT(vm.x, fa, fb);
            SCHED();
        };
        asm volatile("s_mov_b32 m0, %2\n\t" WAIT_TXT("%0", "%1", "%9") "buffer_load_dwordx4 %4, %5, %6 offen sc1 lds\n\tbuffer_load_dwordx4 %4, %5, %6 offen offset:1024 sc1 lds\n\t"
                     "s_mov_b32 m0, %3\n\ts_nop 0\n\tbuffer_load_dwordx4 %8, %5, %7 offen sc1 lds"
                     : "=&s"(tmp) : "s"(tw1), "s"(dm), "s"(dm2), "v"(l16), "s"(srd), "s"(ms), "s"(ms2), "v"(l3), "s"(tw4) : WAIT_CLOB);
        HT(0);
        ZRD(aq0, 0); ZRD(aq1, 1); ZRD(ak0, 2); ZRD(ak1, 3);
#pragma unroll
        for (int i = 0; i < 4; ++i) { LF_RD(i, 0); LF_RD(i, 1); }
        u32 ob_ = (vsrc ^ 8192u) + (u32)((w & 1) * 256); PINS(ob_);
        USE(lf[3][1]);
        DMA32(dq, l16, ksrc ^ 8192u);
        st4s(rs, ofl | (early ^ OOB), 0, fv);
        asm volatile("s_waitcnt vmcnt(3)\n\ts_barrier" ::: "memory");
        HT(1);
#pragma unroll
        for (int ks = 0; ks < 2; ++ks) A[ks] = lds_r16(LB + C_MEM + ks * 1024);
        SCHED();
        static_for<0, 76>(slot);
        u32 fail = 0;
        if (__builtin_expect(odd != 0ull, 0)) {
            u32 mn = ~0u;
#pragma unroll 5
            for (int i = 0; i < 10; ++i) { const v4u e = lds_r16(LB + C_MEM + i * 1024); mn = min(min(min(mn, e.x ^ PSENT), e.y ^ PSENT), min(e.z ^ PSENT, e.w ^ PSENT)); }
            fail = missed < SPIN_CAP && !all_(mn != 0u) ? OOB : 0u;
            float mx = fmaxf(fmaxf(fmaxf(sx[0], sx[1]), fmaxf(sx[2], sx[3])), fmaxf(sm0, sm1));
            mx = max32(max16(fmaxf(mx, dpp<0x4E>(mx))));
#pragma unroll
            for (int i = 0; i < 4; ++i) t[i] = __builtin_amdgcn_exp2f(sx[i] - mx);
            P.x = pk(t[0], t[1]); P.y = pk(t[2], t[3]); P.z = u_(dpp<0x4E>(f_(P.x))); P.w = u_(dpp<0x4E>(f_(P.y)));
            pmm = pk(__builtin_amdgcn_exp2f(sm0 - mx), __builtin_amdgcn_exp2f(sm1 - mx));
            pm = (r & 2) ? (v2u){0u, pmm} : (v2u){pmm, 0u};
            asm volatile("s_nop 1" : "+v"(P), "+v"(pm));
            MF0(rsum, ones, P); MF0(oa, vx[0], P); MF0(ob, vx[1], P);
            NOPS(7);
            { const v2u ones2 = ones.lo; MF1H(rsum, ones2, pm); }
#pragma unroll
            for (int i = 0; i < 4; ++i) SEL(oa[i], oa[i], ob[i]);
            asm volatile("s_nop 4" :: "v"(rsum));
            oso |= fail;
        }
        v4u tk[8];
        f32x4 c0, c1;
#define TKR(i) tk[i] = lds_r16(LB + C_K + ((i) >> 2) * 4096 + ((i) & 3) * 1024)
        asm volatile("v_rcp_f32 %0, %3\n\ts_nop 2\n\tv_add_f32 %1, %4, %6\n\tv_add_f32 %2, %5, %7\n\tv_cvt_pk_bf16_f32 %1, %1, %2\n\ts_nop 0"
                     : "=&v"(inv), "=&v"(vm.y), "=&v"(fd) : "v"(rsum[0]), "v"(av1[0]), "v"(av1[1]), "v"(av1[2]), "v"(av1[3]));
        MF1H(oa, vm, pm);
        SCHED();
        c0 = __builtin_bit_cast(f32x4, lds_r16(QX + (C_MASK + 4096 - C_QX)));
        SCHED();
        {
            v4u oc; float u0, u1, u2, u3;
            asm volatile("s_nop 6\n\tv_mul_f32 %4, %8, %12\n\tv_mul_f32 %5, %9, %12\n\tv_mul_f32 %6, %10, %12\n\tv_mul_f32 %7, %11, %12\n\t"
                         "v_cvt_pk_bf16_f32 %0, %4, %5\n\tv_cvt_pk_bf16_f32 %2, %4, %5\n\tv_cvt_pk_bf16_f32 %1, %6, %7\n\tv_cvt_pk_bf16_f32 %3, %6, %7\n\t"
                         "v_permlane16_swap_b32 %0, %2\n\ts_nop 0\n\tv_permlane16_swap_b32 %1, %3"
                         : "=&v"(oc.x), "=&v"(oc.y), "=&v"(oc.z), "=&v"(oc.w), "=&v"(u0), "=&v"(u1), "=&v"(u2), "=&v"(u3) : "v"(oa[0]), "v"(oa[1]), "v"(oa[2]), "v"(oa[3]), "v"(inv));
            st16(rs, oso, oc);
        }
        SCHED();
        unsigned long long clk;
        asm volatile("s_memrealtime %0" : "=s"(clk));
        if (__builtin_expect(fail == 0u, 1)) {
        HT(6);
        C2(0);
        v4u tv[2], px, pv;
        f32x4 cm, o[4], sum;
        float ex[10], cs0, cs1;
        v2u p0, p1, tvm;
        u32 oo, kvo, oz; bool kvok;
        u32 ln = l16; PIN(ln);
        lptr VO, VMX, MK = QX + (C_MASK - C_QX);
        TKR(0);
        SCHED();
        SFOR(i, 12)
            if constexpr (i == 0) MF0(cm, kmf[0], tq[0]); else if constexpr (i < 4) MF1(cm, kmf[i], tq[i]);
            else if constexpr (i < 8) MF1(c0, tk[i - 4], tq[i - 4]); else MF1(c1, tk[i - 4], tq[i - 8]);
            if constexpr (i == 0) USE(tq[3]);
            if constexpr (i == 3) USE(tk[0]);
            if constexpr (i == 4) USE(tk[1]);
            if constexpr (i == 6) USE(tk[3]);
            if constexpr (i >= 8 && i < 11) USE(tk[i - 3]);
            if constexpr (i < 3) TKR(i + 1);
            if constexpr (i == 3) { c1 = __builtin_bit_cast(f32x4, lds_r16(MK)); TKR(4); }
            if constexpr (i >= 4 && i < 7) TKR(i + 1);
            if constexpr (i == 5) *(LAS v2u*)VMX = vm;
            if constexpr (i == 7) { vx[1] = lds_r16(VX + 1024); tv[0] = lds_r16(VO); }
            if constexpr (i == 8) tv[1] = lds_r16(VO + 1024);
            SCHED();
            if constexpr (i == 0) { VMX = lds + (ln >> 1); PIN(VMX); }
            if constexpr (i == 1) { VMX += C_VM + w * 512; PIN(VMX); }
            if constexpr (i == 2) { VO = lds + C_V + (w ^ 1) * 2048 + ln; PIN(VO); }
            if constexpr (i == 6) { USE(cm); VMULC(cs0, cm[0]); }
            if constexpr (i == 8) VMULC(cs1, cm[1]);
            if constexpr (i == 9) VEXP(ex[8], cs0);
            if constexpr (i == 10) { VEXP(ex[9], cs1); VEXP(ex[0], c0[0]); }
            if constexpr (i == 11) { VEXP(ex[1], c0[1]); VEXP(ex[2], c0[2]); VEXP(ex[3], c0[3]); }
            SCHED();
        SEND
        asm volatile("v_cvt_pk_bf16_f32 %0, %6, %7\n\tv_mov_b32 %1, 0\n\tv_mov_b32 %2, 0\n\tv_cvt_pk_bf16_f32 %3, %6, %7\n\tv_cvt_pk_bf16_f32 %4, %8, %9\n\tv_cvt_pk_bf16_f32 %5, %10, %11"
                     : "=&v"(p0.x), "=&v"(p0.y), "=&v"(p1.x), "=&v"(p1.y), "=&v"(px.x), "=&v"(px.y) : "v"(ex[8]), "v"(ex[9]), "v"(ex[0]), "v"(ex[1]), "v"(ex[2]), "v"(ex[3]));
        asm volatile("s_waitcnt lgkmcnt(0)\n\ts_barrier" : "+s"(clk) :: "memory");
        tvm = *(LAS v2u*)(VMX + (512 - (w & 1) * 1024));
        pv = lds_r16(ZP + 80);
        SCHED();
#pragma unroll
        for (int i = 0; i < 4; ++i) VEXP(ex[4 + i], c1[i]);
        asm volatile("v_cvt_pk_bf16_f32 %0, %2, %3\n\tv_cvt_pk_bf16_f32 %1, %4, %5\n\ts_nop 1" : "=&v"(px.z), "=&v"(px.w) : "v"(ex[4]), "v"(ex[5]), "v"(ex[6]), "v"(ex[7]));
        {
            const v2u ones2 = ones.lo;
            const u32 now = (u32)clk;
            auto pvm = [&](auto first) __attribute__((always_inline)) {
                SCHED();
                MF0(sum, ones, px); SCHED(); if constexpr (decltype(first)::value) { oo = ln + (ln & 0x300u) + ob_; PIN(oo); } SCHED();
                MF0(o[0], vx[0], px); NOPS(1);
                MF0(o[1], vx[1], px); NOPS(1);
                MF0(o[2], tv[0], px);
                MF1H(sum, ones2, p0); NOPS(0);
                MF1H(o[0], vm, p0); NOPS(1);
                MF1H(o[1], vm, p1); NOPS(1);
                MF0(o[3], tv[1], px);
                asm volatile("v_sub_u32 %0, %2, %3\n\tv_rcp_f32 %1, %2" : "=&v"(pa), "=&v"(inv) : "v"(sum[0]), "s"(0x2B800000u), "v"(sum));
                MF1H(o[2], tvm, p0); NOPS(2);
                MF1H(o[3], tvm, p1);
                SCHED();
            };
            pvm(IC<1>{});
            if (__builtin_expect(__builtin_amdgcn_ballot_w64(pa >= SUMRANGE) != 0ull, 0)) {
                const f32x4 m0 = __builtin_bit_cast(f32x4, lds_r16(MK + 4096)), m1 = __builtin_bit_cast(f32x4, lds_r16(MK));
                float mx = fmaxf(cs0, cs1);
#pragma unroll
                for (int i = 0; i < 4; ++i) { ex[i] = m0[i] == 0.f ? c0[i] : m0[i]; ex[4 + i] = m1[i] == 0.f ? c1[i] : m1[i]; mx = fmaxf(mx, fmaxf(ex[i], ex[4 + i])); }
                mx = max32(max16(mx));
#pragma unroll
                for (int i = 0; i < 4; ++i) px[i] = pk(__builtin_amdgcn_exp2f(ex[2 * i] - mx), __builtin_amdgcn_exp2f(ex[2 * i + 1] - mx));
                p0.x = p1.y = pk(__builtin_amdgcn_exp2f(cs0 - mx), __builtin_amdgcn_exp2f(cs1 - mx));
                asm volatile("s_nop 7" : "+v"(px), "+v"(p0), "+v"(p1));
                pvm(IC<0>{});
                NOPS(3);
            }
            asm volatile("" : "+v"(o[0]), "+v"(o[1]));
            st16(rs, oo, (v4u){pk2(o[0].lo * inv), pk2(o[0].hi * inv), pk2(o[1].lo * inv), pk2(o[1].hi * inv)});
            asm volatile("" : "+v"(o[2]), "+v"(o[3]));
            st16(rs, oo ^ 2048u, (v4u){pk2(o[2].lo * inv), pk2(o[2].hi * inv), pk2(o[3].lo * inv), pk2(o[3].hi * inv)});
            SCHED();
            C2(1);
            kb += NH * 16384;
            ksrc = kb + (((kmask >> ((s + 1) & 63)) & 1ull) ? 8192u : 0u);
            vsrc = kb + (((vmask >> ((s + 1) & 63)) & 1ull) ? W_VO - W_KQ + 8192u : W_VO - W_KQ);
            oz = (oso & ~(3u << 17)) | (u32)((s + 3) & 3) << 17; oso = (oso & ~(3u << 17)) | (u32)((s + 1) & 3) << 17;
            VMW(3); SCHED();
            C2(2);
            PIN(fl); kvok = ((u32)__builtin_amdgcn_ballot_w64(fl < (u32)s + 2) & 7u) == 0u;
            kvo = l16 | (kvok ? 0u : OOB);
            DMA32(dv, kvo, vsrc); DMA32(dk, kvo, ksrc);
            st16(rs, oz, pv);
            SCHED();
            C2(3);
            rep = (u32)s << 16 | min(missed, 1u) << 14 | min(now - tm, 0x3FFFu);
            if (h == 0 && w == 3) { LST(L * 64 + s, now); LST(128 + L * 64 + s, ((int)(rdl(fl, 3) - now) >= 4) | ((int)kvok << 1) | ((int)min(missed, 3u) << 2)); LST(640 + L * 64 + s, tm); LST(1728 + L * 64 + s, __builtin_amdgcn_s_memtime()); }
            tm = rdl(fl, 5);
            missed = missed == SPIN_CAP ? missed : 0u;
            dirty = 0;
            if (__builtin_expect(rdl(fl, 3) - now + CH_LATE > 4000u && s + 1 < NSEG, 0)) {
                dirty = 1;
#pragma unroll 1
                for (int n = 0; n < 64; ++n) {
                    CFENCE();
                    const u32 hv = ld4(rs, fkh);
                    tm = rdl(hv, 5);
                    if (rdl(hv, 3) - now + CH_LATE <= 4000u) break;
                }
            }
        }
        LS2(L == 0 && w == 0, h);
        if (__builtin_expect(!kvok, 0) && s + 1 < NSEG) {
            dirty = 1;
            wait_flags(rs, ln < 48u ? fkh : OOB, (u32)s + 2, dead);
            DMA32(dv, l16, vsrc); DMA32(dk, l16, ksrc);
            const u32 now = rt32() + CH_KV;
            if ((int)(tm - now) < 0) next_slot(now);
        }
        ++s;
        } else {
            u32 ln = l16; PIN(ln);
            oso = (ln & 256u) ? oso : oso & ~OOB;
            asm volatile("s_waitcnt vmcnt(1) lgkmcnt(0)" :: "s"(clk) : "memory"); SCHED(); PIN(fl);
            const u32 was = tm - tmp;
            tm = rdl(fl, 5); dirty = 1;
            if ((int)(tm - rdl(fl, 4) - was) > 24) tm -= rdl(fl, 4);
            if ((++missed & 7u) == 0u) {
                *(LAS u32*)(lds + C_DUMP + (w == 0 ? 0 : 64)) = ld4(rs, foff(F_ERR));
                BAR();
                missed = uu(*(LAS u32*)(lds + C_DUMP)) != 0u ? SPIN_CAP : missed;
                st4s(rs, missed == SPIN_CAP ? foff(F_ERR) : OOB, 0, 1u);
            }
        }
    }
    DRAIN(); BAR();
    st4s(rs, ofl, 0, (l16 & 16u) ? rep : NSEG);
}

#define WO_NI 4
#define WROT 0
DEV void role_wo(const Params& p, lptr lds, int j) {
    const int tid = threadIdx.x, lane = tid & 63, w = uni(tid >> 6), g = lane >> 4, r = lane & 15;
    const int rg = j & 1, cg = j >> 1, rot = cg & WROT;
    const rsrc_t rs = mk_rsrc(p.ws, W_END);
    v8u wo[8][5];
    {
        const v4u so_ = mk_srd(p.Wo, DM * NQ * 2);
        const int one = (0x3F2FF1F0u >> (4 * (cg & 7))) & 15;
        u32 vo[3];
#pragma unroll
        for (int i = 0; i < 3; ++i) { const int n = 64 * i + lane; vo[i] = n % 6 < 5 ? (u32)((n / 6) * (DM * 2) + cg * 80 + (n % 6) * 16) : ((n / 6) & 3) == one ? (u32)((n / 6) * (DM * 2) + (cg >> 3) * 640 + 304 + rg * 16) : OOB; }
        auto fill = [&](int b) {
            const u32 dst = uu(w * 36864 + (b % 3) * 12288), src = uu((8 * w + ((b + rot) & 7)) * (DH * DM * 2));
#pragma unroll
            for (int i = 0; i < 12; ++i) DMAIN(so_, dst + i * 1024, vo[i % 3], src + (i / 3) * (32 * DM * 2));
        };
#pragma unroll 1
        for (int b = 0; b < 3; ++b) fill(b);
        const int q4 = r >> 2;
        lptr tb = lds + w * 36864 + (32 * (g >> 1) + 16 * (q4 >> 1) + 8 * ((r >> 1) & 1) + 4 * (g & 1) + (q4 & 1)) * 96 + (r & 1) * 8;
        SFOR(b, 8)
            VMW(b < 6 ? 24 : 84 - 12 * b);
#pragma unroll
            for (int t = 0; t < 5; ++t) {
                lptr q = tb + (b % 3) * 12288 + (t == 0 ? 4 : (w + t) & 3) * 16;
                wo[b][t] = tr_frag(q, q + 192, q + 64 * 96, q + 64 * 96 + 192);
                if (t < 4) asm volatile("" : "+a"(wo[b][t])); else asm volatile("" : "+v"(wo[b][t]));
            }
            CFENCE();
            if constexpr (b < 5) fill(b + 3);
        SEND
    }
    BAR();
    EV(6);
    const u32 idx = (r & 2) ? 0xEEEEu : 0x4444u;
    bool dead = false;
    const u32 pol0 = get_pol(p, rs, dead) & 1u;
    const u32 l16 = (u32)lane * 16u;
    u32 oo[8];
#pragma unroll
    for (int i = 0; i < 8; ++i) oo[i] = uu(W_ORING + ring_map((u32)(rg * 32768 + (8 * w + ((i + rot) & 7)) * 1024), pol0));
    const u32 hint = W_MRING + 4 * 32768u + pol0 * 8192u;
    u32 hvo[2];
#pragma unroll
    for (int l = 0; l < 2; ++l) hvo[l] = lane < 2 ? hint + (u32)(l ^ 1) * (MB / 4) + (u32)lane * 4u : lane == 2 ? hint + (u32)l * (MB / 4) + 128u : lane == 3 ? foff(F_ERR) : lane == 4 ? hint + (u32)l * (MB / 4) + 256u : OOB;
    const int xo = g * 128 + (r & 7) * 16, O = 5 * cg + (r < 8 ? w : 4), q2 = 2 + (r >> 3);
    lptr xw0 = lds + (r < 8 ? 2048 + w * 4 : ((w + 1) & 3) * 512 + 4) + xo, xw1 = lds + ((w + q2) & 3) * 512 + q2 * 4 + xo;
    lptr xr = lds + (r < 8 ? w : 4) * 512 + xo;
    if (r < 8) { *(LAS u32*)xr = 0u; *(LAS u32*)(xr + 8192) = 0u; }
    const u32 mpo = (r & 4) == 0 && (r < 8 || w == 0) ? W_MRING + ring_map((u32)((O >> 1) * 256 + rg * 128 + (4 * (g & 1) + 2 * (O & 1) + (g >> 1)) * 16 + (r & 3) * 4), pol0) : OOB;
    constexpr u32 PER0 = CH_W0 + CH_MW + CH_H0 + CH_MH;
    const bool lead = j == 0 && w == 3;
    u32 hso[2] = {OOB, OOB};
    MK_SRD();
    u32 t = 0, tn = 0;
    int sn[2] = {0, 0};
    u32 hav = 16 * CH_H0, wav = 16 * CH_W0, mx = 16 * CH_M0, ok[2] = {0u, 0u}, dw[2] = {0u, 0u};
    u32 zero = 0, rep = 0xFFFFu, failed = 1, pmp = OOB, mp = OOB, mq = OOB, cw = 0, vo = l16, tp = 0, tw1 = 0, stop = 0;
    f32x4 acc[5] = {};
    if (tid < 20) *(LAS u32*)(lds + 5632 + tid * 4) = 0u;
    unsigned long long clk = 0;
    const u32 psent = PSENT;
    lptr sw = lds + 5120 + w * 4;
    asm volatile("" : "+v"(zero), "+v"(sw), "+v"(xw0), "+v"(xw1), "+v"(xr));
    auto turn = [&](auto lc) __attribute__((always_inline)) {
        constexpr int L = decltype(lc)::value, M = L ^ 1;
        u32 done, rd, dh, dn, nt, za, zn, pdur, hn; v3u hw;
        t = uu(t); tn = uu(tn); tp = uu(tp); dw[L] = uu(dw[L]); failed = uu(failed); rep = uu(rep);
        const int sc = sn[L];
        auto settle = [&](auto ic) __attribute__((always_inline)) {
            constexpr int i = decltype(ic)::value;
            if constexpr (i == 0) { pdur = (u32)clk - tp; dw[L] += max((int)((u32)clk + CH_WX - t), 0); PINS(pdur); PINS(dw[L]); }
            if constexpr (i == 1) {
                asm volatile("v_or_b32 %0, %2, %3\n\tv_cmp_ne_u32 vcc, %4, %5\n\tv_or_b32 %1, %2, %5\n\tv_cndmask_b32 %1, %6, %1, vcc" : "=&v"(za), "=&v"(zn) : "s"(failed << 31), "v"(pmp), "v"(mq), "v"(mp), "v"(OOB) : "vcc");
                done = failed ^ 1u; PINS(done);
            }
            SCHED();
        };
        auto ahead = [&]() __attribute__((always_inline)) {
            u32 h0 = rdl(hn, 0), h1 = rdl(hn, 1), h2 = rdl(hn, 2), h3 = rdl(hn, 3), h4 = rdl(hn, 4);
            asm volatile("" : "+s"(h0), "+s"(h1), "+s"(h2), "+s"(h3), "+s"(h4));
            tp = t; t = lead ? tn : (int)(h0 - t) < 0 ? h0 + h1 : h0;
            tn = nt; stop |= h3; rep = max(h2, h4); tw1 = t - 1u;
            asm volatile("" : "+s"(tp), "+s"(t), "+s"(tn), "+s"(stop), "+s"(rep), "+s"(tw1));
            SCHED();
        };
        auto books = [&](auto ic) __attribute__((always_inline)) {
            constexpr int i = decltype(ic)::value;
            if constexpr (i == 0) { rd = rep & 0xFFFFu; PINS(rd); }
            if constexpr (i == 1) { dn = wav >> 4; PINS(dn); pdur = min(pdur, dn + CH_CLIP); PINS(pdur); }
            if constexpr (i == 2) { pdur = (done & ok[M]) ? pdur - dn : 0u; PINS(pdur); }
            if constexpr (i == 3) { wav += (u32)max((int)pdur, (int)(pdur << CH_ATT)); PINS(wav); }
            if constexpr (i == 4) { dh = hav >> 4; PINS(dh); nt = min(rd, dh + CH_CLIP); PINS(nt); }
            if constexpr (i == 5) { nt = rd < 0x3FFFu ? nt - dh : 0u; PINS(nt); }
            if constexpr (i == 6) { hav += (u32)max((int)nt, (int)(nt << CH_ATT)); PINS(hav); }
            if constexpr (i == 7) { rd = (ok[M] & ~done) * (CH_UP * 16u); PINS(rd); mx = (u32)max((int)(mx - CH_DEC), (int)rd); ok[M] = done; PINS(mx); }
            if constexpr (i == 8) { dh = (hav + mx) / 16u + (sn[1] < 2 && sn[0] >= CH_NEAR ? CH_MH + CH_SOLO : CH_MH); PINS(dh); }
            if constexpr (i == 9) { dn = (wav + mx) / 16u + (sn[0] < CH_NEAR ? CH_MW + CH_EARLY : CH_MW); PINS(dn); }
            if constexpr (i == 10) { nt = dw[L] + dh; dh += dn; dw[L] = dn; sn[M] += (int)done; PINS(nt); PINS(dh); PINS(sn[M]); }
            if constexpr (i == 11) { rd = tn - t + (dh >> 1); PINS(rd); }
            if constexpr (i == 12) { rd = sn[M] + 1 < NSEG ? rd : 0u; PINS(rd); }
            if constexpr (i == 13) { nt = t + (u32)max((int)nt, (int)rd); PINS(nt); }
            if constexpr (i == 14) asm volatile("v_mov_b32 %0, %1" : "=v"(hw.x) : "s"(nt));
            if constexpr (i == 15) asm volatile("v_mov_b32 %0, %1" : "=v"(hw.y) : "s"(dh));
            if constexpr (i == 16) asm volatile("v_mov_b32 %0, %1" : "=v"(hw.z) : "s"(nt + dn));
            SCHED();
        };
        if (__builtin_expect(sc + 1 < NSEG, 1)) {
            u32 zb;
#define WT(k) do {} while (0)
            v4u d[8];
            {
                u32 tmp;
                asm volatile(LOOP_TXT("%1", "%2") "buffer_load_dwordx4 %0, %3, %4, %5 offen sc1\n\ts_waitcnt lgkmcnt(0)"
                             : "=&v"(d[0]), "=&s"(tmp) : "s"(uu(tw1)), "v"(vo), "s"(srd), "s"(oo[0]) : "vcc", "scc", "memory");
            }
            WT(0);
            SFOR(i, 7)
                asm volatile("buffer_load_dwordx4 %0, %1, %2, %3 offen sc1" : "=v"(d[i + 1]) : "v"(vo), "s"(srd), "s"(oo[i + 1]) : "memory");
                SCHED();
                if constexpr (i == 0) { settle(IC<0>{}); static_for<0, 2>(books); }
                if constexpr (i == 1) { settle(IC<1>{}); books(IC<2>{}); }
                if constexpr (i == 2) {
                    mp = mpo + (L * (MB / 4) + (u32)((sc + 1) & 3) * 32768u); pmp = mpo + (L * (MB / 4) + (u32)(sc & 3) * 32768u);
                    zb = mp | ok[L] << 31;
                    PIN(mp); PIN(pmp); PIN(zb);
                }
                if constexpr (i == 3) static_for<3, 7>(books);
                if constexpr (i == 4) { books(IC<7>{}); books(IC<9>{}); }
                if constexpr (i == 5) { books(IC<8>{}); books(IC<10>{}); }
                SCHED();
            SEND
            LD4A(hn, hvo[L], 0);
            *(LAS u32*)(sw + L * 8192) = zero;
            SCHED();
            WT(1);
            float f0[5], f1[5], xs; unsigned long long bad; v4u sv;
#define WSW(o_) asm volatile("v_permlane32_swap_b32 %0, %1" : "+v"(f0[o_]), "+v"(f1[o_]))
#define WAD(o_) asm volatile("v_add_f32_dpp %0, %1, %0 row_ror:8 row_mask:0xf bank_mask:0xf\n\tv_add_f32_dpp %0, %2, %3 row_ror:8 row_mask:0xf bank_mask:0xc" : "+v"(f0[o_]) : "v"(f1[o_]), "v"(f0[o_ + 1]), "v"(f1[o_ + 1]))
#define WFO(o_) do { VADD(f0[o_], acc[o_][0], acc[o_][2]); VADD(f1[o_], acc[o_][1], acc[o_][3]); } while (0)
#define WOWN(k) do { if constexpr (WO_NI + (k) < 8) SMF_V(acc[4], d[(WO_NI + (k)) & 7], wo[(WO_NI + (k)) & 7][4]); SCHED(); } while (0)
            SFOR(i, 40)
                constexpr int pc = i / 5, oc = i % 5;
                if constexpr (oc == 0) VMW((pc == 0 ? 8 : 11) - pc);
                if constexpr (oc < 4) SMF_A(acc[oc], d[pc], wo[pc][oc]); else if constexpr (pc < WO_NI) SMF_V(acc[4], d[pc], wo[pc][4]);
                SCHED();
                if constexpr (i == 0) WT(2);
                if constexpr (i == 20) WT(3);
                if constexpr (i == 35) WT(4);
                if constexpr (i == 0) st4s(rs, zb, 0, psent);
                if constexpr (i == 1) st4s(rs, za, 0, psent);
                if constexpr (i == 2) st4s(rs, zn, 0, cw);
                if constexpr (i >= 5 && i < 8) books(IC<6 + i>{});
                if constexpr (i >= 10 && i < 13) books(IC<4 + i>{});
                if constexpr (i == 13) { xs = 0.f; PIN(xs); }
                if constexpr (i == 15) { vo = l16 + (M * (MB / 2) + (u32)(sn[M] & 3) * 131072u); PIN(vo); }
                if constexpr (i == 36) NOPS(1);
                if constexpr (i == 37) NOPS(2);
                SCHED();
            SEND
            WFO(0); WFO(1);
            asm volatile("v_cmp_u_f32 %0, %1, %2" : "=s"(bad) : "v"(f0[0]), "v"(f1[0]));
            WSW(0); WSW(1); WFO(2); WAD(0); WFO(3);
            *(LAS float*)(xw0 + L * 8192) = f0[0]; SCHED();
            WSW(2); WSW(3);
            if (__builtin_expect(bad != 0ull, 0)) {
                u32 mn = ~0u;
#pragma unroll
                for (int i = 0; i < 8; ++i) { u32 e = d[i].x; PIN(e); mn = min(mn, e ^ PSENT); }
                if (all_(mn != 0u)) {
#pragma unroll
                    for (int i = 0; i < 8; ++i) { v4u e = d[i]; PIN(e); mn = min(min(mn, e.y ^ PSENT), min(e.z ^ PSENT, e.w ^ PSENT)); }
                }
                *(LAS u32*)(sw + L * 8192) = all_(mn != 0u) ? 0u : 1u;
            }
            WAD(2);
            *(LAS float*)(xw1 + L * 8192) = f0[2]; SCHED();
            WOWN(0); WOWN(1); WOWN(2); WOWN(3);
            WT(5);
            f32x4 xa; float nb;
            asm volatile("s_waitcnt lgkmcnt(0)\n\ts_barrier\n\tds_read_b128 %0, %2 offset:%4\n\tds_read_b128 %1, %3 offset:%4+5120\n\ts_nop 3"
                         : "=&v"(xa), "=&v"(sv) : "v"(xr), "v"(zero), "i"(L * 8192) : "memory");
            WT(6);
            WFO(4);
            asm volatile("s_nop 0\n\tv_permlane32_swap_b32 %1, %2\n\ts_nop 1\n\tv_add_f32_dpp %0, %2, %1 row_ror:8 row_mask:0xf bank_mask:0x3\n\ts_waitcnt lgkmcnt(1)" : "+v"(xs), "+v"(f0[4]), "+v"(f1[4]));
            asm volatile("v_add_f32 %0, %0, %4\n\tv_add_f32 %1, %5, %6\n\tv_add_f32 %0, %0, %7\n\tv_add_f32 %0, %0, %1\n\tv_cmp_u_f32 vcc, %0, %0\n\ts_nop 0\n\t"
                         "v_mov_b32_dpp %1, %0 row_shl:4 row_mask:0xf bank_mask:0xf\n\tv_cndmask_b32 %3, %8, %9, vcc\n\tv_cvt_pk_bf16_f32 %2, %0, %1"
                         : "+v"(xs), "=&v"(nb), "=&v"(cw), "=&v"(mq) : "v"(xa[0]), "v"(xa[1]), "v"(xa[2]), "v"(xa[3]), "v"(mp), "v"(OOB) : "vcc");
            st4s(rs, mq, 0, cw);
            SCHED();
            WT(7);
            asm volatile("s_memrealtime %0" : "=s"(clk));
            __builtin_amdgcn_raw_buffer_store_b96(hw, rs, hso[L], 0, 16);
#pragma unroll
            for (int q = 0; q < 5; ++q) acc[q] = __builtin_bit_cast(f32x4, lds_r16(lds + zero + (5632 + 16 * q)));
            VMW(5); SCHED(); PIN(hn);
            asm volatile("s_waitcnt lgkmcnt(5)" ::: "memory");
            SCHED(); PIN(sv);
            failed = uu(sv.x | sv.y | sv.z | sv.w);
            ahead();
            asm volatile("s_waitcnt lgkmcnt(0)" : "+s"(clk) :: "memory");
        } else {
            BAR();
            static_for<0, 2>(settle);
            st4s(rs, za, 0, psent);
            st4s(rs, zn, 0, cw);
            static_for<0, 14>(books);
            failed = 1u;
            hn = ld4(rs, hvo[L]);
            vo = l16 + (M * (MB / 2) + (u32)(sn[M] & 3) * 131072u);
            ahead();
            clk = __builtin_amdgcn_s_memrealtime();
        }
    };
#pragma unroll 1
    for (int k = 0; k < 2 * SPIN_CAP && !stop && (sn[0] + 1 < NSEG || sn[1] + 1 < NSEG); ++k) {
        turn(IC<0>{}); turn(IC<1>{});
        if (__builtin_expect(k != 0, 1)) continue;
        if (lead) {
            wait_flags(rs, lane < 32 ? foff(F_GO + lane) : OOB, 1u, dead);
            t = rt32() + CH_FIRST; tn = t + PER0 / 2 + CH_REC;
            st16(rs, lane < 2 ? hint + (u32)lane * (MB / 4) : OOB, (v4u){lane == 0 ? t : tn, PER0, (lane == 0 ? t : tn) + (CH_W0 + CH_MW + CH_REC), 0u});
        } else {
            for (int n = 0; n < SPIN_CAP && !dead; ++n) {
                CFENCE();
                const u32 hv = ld4(rs, hvo[1]);
                t = rdl(hv, 0);
                if (rdl(hv, 1) < 400u) break;
                __builtin_amdgcn_s_sleep(1);
            }
        }
        hso[0] = (lead && lane == 0) ? hint : OOB; hso[1] = hso[0] + MB / 4;
        rep = 0xFFFFu; dw[0] = dw[1] = CH_W0 + CH_MW + CH_REC;
        tw1 = t - 1u;
    }
    if (sn[0] + 1 < NSEG || sn[1] + 1 < NSEG) st4s(rs, tid == 0 ? foff(F_ERR) : OOB, 0, 1u);
}

__global__ void __launch_bounds__(256, 1) k_main(Args args) {
    Params p;
    (Args&)p = args;
    lptr lds = (lptr)(u32)16 - 16;
    if (blockIdx.x == 0) LST(256, rt32());
    unsigned long long pc; int len;
    asm volatile("s_getpc_b64 %0\n.Lk_main_here:\n\ts_mov_b32 %1, .Lk_end_here-.Lk_main_here" : "=s"(pc), "=s"(len));
    const v4u code = ldin16(mk_rsrc((const void*)(pc & ~15ull), (u32)max(len, 0)), (((blockIdx.x >> 3) & 31u) * 256u + threadIdx.x) * 16u, 0);
    asm volatile("" :: "s"(p.x), "s"(p.Wo), "s"(p.ws) : "memory");
    if (threadIdx.x == 0) {
        const v4u e = ld16s(mk_rsrc(p.ws, W_END), foff(F_EP), 0);
        const unsigned long long tag = (unsigned long long)e.y << 32 | (e.x + 1u);
        u32 role = ((u32)__builtin_amdgcn_s_getreg(20 | (3 << 11)) & 7u) * 32u + ((blockIdx.x >> 3) & 31u);
#pragma unroll 1
        for (int n = 0; n < 256; ++n, role = (role + 1u) & 255u)
            if (__hip_atomic_exchange((GAS unsigned long long*)(p.ws + W_SLOT) + role * 8u, tag, __ATOMIC_RELAXED, __HIP_MEMORY_SCOPE_AGENT) != tag) break;
        lds_w16(lds, (v4u){e.x + 1u, e.y, e.z, role});
    }
    __syncthreads();
    const v4u boot = lds_r16(lds);
    p.e0 = uu(boot.x); p.e1 = uu(boot.y); p.e2 = uu(boot.z);
    const int role = uni((int)boot.w), x = role >> 5, i = role & 31;
    asm volatile("" :: "v"(code));
    __syncthreads();
    LST(8192 + blockIdx.x * 16, role);
    if (x < 2) { role_head(p, lds, i >> 4, x * 16 + (i & 15)); __builtin_amdgcn_endpgm(); }
    if (x == 2) { role_wo(p, lds, i); __builtin_amdgcn_endpgm(); }
    const int no = x == 3 ? 30 : x < 6 ? 5 : 0;
    if (x > 3 && i >= 8) { role_qkv(p, lds, (x - 4) * 24 + i - 8); __builtin_amdgcn_endpgm(); }
    const bool isout = i < no;
    const int og = x == 3 ? i / 5 : x + 2, ng = i % 5, L = og >> 2, kq = og & 3;
    const int k = i - no, w = uni(threadIdx.x >> 6);
    const int j = isout ? (L == 1 ? 0 : 44) + kq * 5 + ng : x == 3 ? 36 + k : k < 2 ? 12 + 2 * x + k : x < 6 ? 24 + x : k < 5 ? 10 + 3 * x + k : 15 + 3 * x + k;
    const rsrc_t rs = mk_rsrc(p.ws, W_END);
    wait_until(rt32() + (j < NKV ? ST_KV : ST_OUT));
    if (j < NKV) {
        const int nt = j < 320 - 7 * NKV ? 8 : 7;
#pragma unroll 1
        for (int q = 0; q < 4; ++q) kv_fetch(p, j + NKV * q, uu(q * KT_BYTES));
        Filt f;
        u32 warm[3];
#pragma unroll 1
        for (int q = 0; q < nt; ++q) {
            VMW(15); BAR();
            if (q == 0) EV(2);
            kv_fetch(p, j + NKV * (q + 4), uu(((q + 4) & 7) * KT_BYTES));
            kv_tile(p, rs, lds + q * KT_BYTES, lds + (q == 0 ? 8 : q - 1) * KT_BYTES, j + NKV * q);
        }
        const int g = j < 20 ? j : j < 28 ? j + 16 : j - 8;
        filt_load<5, 0>(p, g, f);
        VMW(10); BAR();
        boot_set(p, rs, threadIdx.x == 0 ? foff(F_KV + j) : OOB, 2u);
        EV(4);
        { bool dead = false; boot_wait(p, rs, pol_off(), 0u, dead); }
#pragma unroll
        for (int q = 0; q < 3; ++q)
            warm[q] = __builtin_amdgcn_raw_buffer_load_b32(mk_rsrc(p.ssmk, ACT), 176u * 4096u + ((u32)((q * NKV + j) * 256) + threadIdx.x) * 64u, 0, 0);
        if (g >= 36) conv_job<3>(p, rs, lds, g, f, 0); else conv_job<0>(p, rs, lds, g, f, 0);
        asm volatile("" :: "v"(warm[0]), "v"(warm[1]), "v"(warm[2]));
        const int g0 = j < 20 ? 36 + j : j < 36 ? 28 + j : 44 + j, g1 = j < 20 ? g0 + 1 : j < 36 ? 80 : 160;
        if (g < 36) filt_load<5, 0>(p, g0, f);
        DRAIN(); BAR();
        st4s(rs, threadIdx.x == 0 ? foff(F_CV + g) : OOB, 0, 2u);
        if (g < 36) {
#pragma unroll 1
            for (int n = g0; n < g1; n += 8) conv_job<2>(p, rs, lds, n, f, min(n + 8, 159));
            DRAIN(); BAR();
            st4s(rs, threadIdx.x == 0 ? foff(F_CV + g) : OOB, 0, 3u);
            if (j >= 36) { EV(15); __builtin_amdgcn_endpgm(); }
        }
    }
    if (!isout) { if (j < 28) role_row<0>(p, lds, (j - 20) * 4 + w); else role_row<1>(p, lds, (j - 28) * 4 + w); __builtin_amdgcn_endpgm(); }
    role_out(p, lds, L, kq, ng);
}

__global__ void k_end() { asm volatile(".Lk_end_here:"); }

extern "C" void kernel_launch(void* const* d_in, const int* in_sizes, int n_in, void* d_out, int out_size, void* d_ws, size_t ws_size,
                              hipStream_t stream) {
    Args p;
    p.x = (const u16*)d_in[0]; p.mem0 = (const u16*)d_in[1]; p.Wq = (const u16*)d_in[2]; p.Wk = (const u16*)d_in[3];
    p.Wv = (const u16*)d_in[4]; p.Wo = (const u16*)d_in[5];
    p.bk = (const u16*)d_in[8]; p.bv = (const u16*)d_in[9]; p.shift = (const u16*)d_in[10]; p.ssmk = (const u16*)d_in[11]; p.ssmD = (const u16*)d_in[12];
    p.out = (u16*)d_out;
    p.ws = (unsigned char*)(((size_t)d_ws + 2 * MB - 1) & ~(size_t)(2 * MB - 1));
    (void)hipFuncSetAttribute((const void*)k_main, hipFuncAttributeMaxDynamicSharedMemorySize, LDS_BYTES);
    hipLaunchKernelGGL(k_main, dim3(256), dim3(256), LDS_BYTES, stream, p);
}
```

```cpp
#include <hip/hip_runtime.h>
#include <hip/hip_bf16.h>

typedef unsigned short u16;
typedef unsigned int u32;
typedef u32 v2u __attribute__((ext_vector_type(2)));
typedef u32 v3u __attribute__((ext_vector_type(3)));
typedef u32 v4u __attribute__((ext_vector_type(4)));
typedef u32 v8u __attribute__((ext_vector_type(8)));
typedef float f32x2 __attribute__((ext_vector_type(2)));
typedef float f32x4 __attribute__((ext_vector_type(4)));
typedef float f32x16 __attribute__((ext_vector_type(16)));
typedef __bf16 bf16x2 __attribute__((ext_vector_type(2)));
typedef __bf16 bf16x8 __attribute__((ext_vector_type(8)));
typedef short s16x4 __attribute__((ext_vector_type(4)));
#define LAS __attribute__((address_space(3)))
#define GAS __attribute__((address_space(1)))
typedef LAS unsigned char* lptr;
#define DEV __device__ __forceinline__

constexpr int SEQ = 2048, DM = 640, NH = 32, DH = 128, NSEG = 64, NQ = NH * DH;

constexpr u32 MB = 1u << 20;
constexpr u32 W_ORING = 0;
constexpr u32 W_MRING = MB;
constexpr u32 W_CTRL = 4 * MB;
constexpr u32 ACT = SEQ * DM * 2;
constexpr u32 H_TILE = 128 * 1024;
constexpr u32 W_H2 = 8 * MB;
constexpr u32 W_KQ = 16 * MB;
constexpr u32 W_VO = W_KQ + 64 * MB;
constexpr u32 W_P = W_VO + 64 * MB;
constexpr u32 P_ITEM = 512 * 1024;
constexpr u32 W_H1 = W_P + 44 * MB, W_Y = W_H1 + 8 * MB, W_X2 = W_Y + ACT;
constexpr u32 W_KVT = W_X2 + ACT;
constexpr u32 W_END = W_KVT + 3 * MB;
constexpr u32 NREG = 12;
static_assert((size_t)W_END + 2 * MB <= 256ull * MB && W_H1 >= (NREG - 1) * 16 * MB + 2 * MB && W_H1 <= NREG * 16 * MB, "workspace");
constexpr u32 F_EP = 0, F_RG = 32, F_KV = 64, F_LN = 128,
              F_QKV = 320, F_O = F_QKV + 192, F_P = F_O + 64, F_H2 = F_P + 64, F_GO = F_H2 + 32, F_CV = F_H2 + 64, F_ERR = F_CV + 191, F_N = F_CV + 192;
constexpr u32 W_SLOT = 3 * MB;
DEV u32 foff(u32 idx) { return W_CTRL + (idx >> 6) * 16384u + (idx & 63u) * 128u; }
DEV u32 near_(u32 o, u32 side, u32 pol) { return o + ((side ^ (pol >> (o >> 24)) ^ (o >> 21)) & 1u) * 8192u; }
DEV u32 p_item(int s) { return W_P + (u32)s * P_ITEM; }

constexpr u32 OOB = 0x80000000u;
constexpr u32 PSENT = 0x7F817F81u;
constexpr int SPIN_CAP = 40000;
constexpr int LDS_BYTES = 160 * 1024;
#define CALM 8
#define CALM_L 1
#define ST_KV 0
#define ST_OUT 0

#define EV(k) do {} while (0)
#define LST(idx, v) do {} while (0)
#define LS2(cond, idx) do {} while (0)
struct Args {
    const u16 *x, *mem0, *Wq, *Wk, *Wv, *Wo, *bk, *bv, *shift, *ssmk, *ssmD;
    u16* out;
    unsigned char* ws;
};
struct Params : Args { u32 e0, e1, e2; };

typedef __amdgpu_buffer_rsrc_t rsrc_t;
DEV rsrc_t mk_rsrc(const void* p, u32 bytes) { return __builtin_amdgcn_make_buffer_rsrc((void*)p, 0, bytes, 0x00020000); }
DEV v4u ld16s(rsrc_t r, u32 off, u32 so) { return __builtin_amdgcn_raw_buffer_load_b128(r, off, so, 16); }
DEV v2u ld8s(rsrc_t r, u32 off, u32 so) { return __builtin_amdgcn_raw_buffer_load_b64(r, off, so, 16); }
DEV v4u ldin16(rsrc_t r, u32 off, u32 so) { return __builtin_amdgcn_raw_buffer_load_b128(r, off, so, 0); }
DEV v2u ldin8(rsrc_t r, u32 off, u32 so) { return __builtin_amdgcn_raw_buffer_load_b64(r, off, so, 0); }
DEV u32 ld4(rsrc_t r, u32 off) { return __builtin_amdgcn_raw_buffer_load_b32(r, off, 0, 16); }
DEV void st16(rsrc_t r, u32 off, v4u v) { __builtin_amdgcn_raw_buffer_store_b128(v, r, off, 0, 16); }
DEV void st8s(rsrc_t r, u32 off, u32 so, v2u v) { __builtin_amdgcn_raw_buffer_store_b64(v, r, off, so, 16); }
DEV void st4s(rsrc_t r, u32 off, u32 so, u32 v) { __builtin_amdgcn_raw_buffer_store_b32(v, r, off, so, 16); }
#define DRAIN() asm volatile("s_waitcnt vmcnt(0)" ::: "memory")
#define VMW(n) asm volatile("s_waitcnt vmcnt(%0)" :: "i"(n) : "memory")
#define BAR() asm volatile("s_waitcnt lgkmcnt(0)\n\ts_barrier" ::: "memory")
#define SCHED() __builtin_amdgcn_sched_barrier(0)
#define CFENCE() asm volatile("" ::: "memory")
#define NOPS(n) asm volatile("s_nop %0" :: "i"(n))
#define PIN(x) asm volatile("" : "+v"(x))
#define PINS(x) asm volatile("" : "+s"(x))
#define USE(x) asm volatile("" :: "v"(x))

template <int V> struct IC { static constexpr int value = V; };
template <int I, int N, class F> DEV void static_for(F&& f) { if constexpr (I < N) { f(IC<I>{}); static_for<I + 1, N>(f); } }
#define SFOR(var, n) static_for<0, n>([&](auto ic_) __attribute__((always_inline)) { constexpr int var = decltype(ic_)::value;
#define SEND });

DEV u32 pk(float a, float b) { bf16x2 v = {(__bf16)a, (__bf16)b}; return __builtin_bit_cast(u32, v); }
DEV float lo_f(u32 u) { return __builtin_bit_cast(float, u << 16); }
DEV float hi_f(u32 u) { return __builtin_bit_cast(float, u & 0xffff0000u); }
DEV f32x2 up2(u32 u) { return (f32x2){lo_f(u), hi_f(u)}; }
DEV u32 pk2(f32x2 v) { return pk(v.x, v.y); }
DEV f32x2 rb2(f32x2 a) { f32x2 r; asm("v_cvt_pk_bf16_f32 %0, 0, %1" : "=v"(r.x) : "v"(a.x)); asm("v_cvt_pk_bf16_f32 %0, 0, %1" : "=v"(r.y) : "v"(a.y)); return r; }
DEV float rbf(float a) { return (float)(__bf16)a; }
DEV v4u lds_r16(lptr p) { return *(LAS v4u*)p; }
DEV void lds_w16(lptr p, v4u v) { *(LAS v4u*)p = v; }
DEV v2u lds_tr(lptr p) { return __builtin_bit_cast(v2u, __builtin_amdgcn_ds_read_tr16_b64_v4i16((LAS s16x4*)p)); }
DEV v4u tr2(lptr p0, lptr p1) { const v2u a = lds_tr(p0), b = lds_tr(p1); return (v4u){a.x, a.y, b.x, b.y}; }
DEV v8u cat8(v4u a, v4u b) { return __builtin_shufflevector(a, b, 0, 1, 2, 3, 4, 5, 6, 7); }
DEV f32x4 mfma16(v4u a, v4u b, f32x4 c) { return __builtin_amdgcn_mfma_f32_16x16x32_bf16(__builtin_bit_cast(bf16x8, a), __builtin_bit_cast(bf16x8, b), c, 0, 0, 0); }
DEV f32x16 mfma32(v4u a, v4u b, f32x16 c) { return __builtin_amdgcn_mfma_f32_32x32x16_bf16(__builtin_bit_cast(bf16x8, a), __builtin_bit_cast(bf16x8, b), c, 0, 0, 0); }

DEV float f_(u32 u) { return __builtin_bit_cast(float, u); }
DEV u32 u_(float f) { return __builtin_bit_cast(u32, f); }
template <int CTRL> DEV float dpp(float v) { return f_((u32)__builtin_amdgcn_update_dpp(0, (int)u_(v), CTRL, 0xf, 0xf, false)); }
DEV float max16(float v) { auto r = __builtin_amdgcn_permlane16_swap(u_(v), u_(v), false, false); return fmaxf(f_(r[0]), f_(r[1])); }
DEV float max32(float v) { auto r = __builtin_amdgcn_permlane32_swap(u_(v), u_(v), false, false); return fmaxf(f_(r[0]), f_(r[1])); }
DEV float add16(float v) { auto r = __builtin_amdgcn_permlane16_swap(u_(v), u_(v), false, false); return f_(r[0]) + f_(r[1]); }
DEV float add32(float v) { auto r = __builtin_amdgcn_permlane32_swap(u_(v), u_(v), false, false); return f_(r[0]) + f_(r[1]); }
DEV float wave_sum(float v) {
    v += dpp<0xB1>(v);
    v += dpp<0x4E>(v);
    v += dpp<0x124>(v);
    v += dpp<0x128>(v);
    return add32(add16(v));
}
DEV bool all_(bool c) { return __builtin_amdgcn_ballot_w64(!c) == 0ull; }
DEV int uni(int v) { return __builtin_amdgcn_readfirstlane(v); }
DEV u32 uu(u32 v) { return (u32)__builtin_amdgcn_readfirstlane((int)v); }
DEV u32 rdl(u32 v, int l) { return (u32)__builtin_amdgcn_readlane((int)v, l); }
DEV u32 rt32() { return (u32)__builtin_amdgcn_s_memrealtime(); }

DEV void wait_flags(rsrc_t rs, u32 off, u32 want, bool& dead, bool calm = false) {
    if (dead) return;
#pragma unroll 1
    for (int n = 0;; ++n) {
        CFENCE();
        const u32 v = ld4(rs, off);
        if (all_(off >= OOB || v >= want)) break;
        __builtin_amdgcn_s_sleep(1);
        if (calm) __builtin_amdgcn_s_sleep(CALM);
        if ((n & 63) == 63) { if (uni((int)ld4(rs, foff(F_ERR))) != 0) { dead = true; break; } }
        if (n > SPIN_CAP) { st4s(rs, foff(F_ERR), 0, 1u); dead = true; break; }
    }
    CFENCE();
}

DEV bool boot_ok(const Params& p, u32 off, v4u v, u32 want) { return all_(off >= OOB || (v.x == p.e0 && v.y == p.e1 && v.z == p.e2 && v.w >= want)); }
DEV void boot_set(const Params& p, rsrc_t rs, u32 off, u32 v) { st16(rs, off, (v4u){p.e0, p.e1, p.e2, v}); }
DEV u32 boot_wait(const Params& p, rsrc_t rs, u32 off, u32 want, bool& dead) {
    v4u v;
#pragma unroll 1
    for (int n = 0;; ++n) {
        CFENCE();
        v = ld16s(rs, off, 0);
        if (boot_ok(p, off, v, want) || dead) break;
        __builtin_amdgcn_s_sleep(1);
        if (n > SPIN_CAP) { st4s(rs, foff(F_ERR), 0, 1u); dead = true; }
    }
    CFENCE();
    return v.w;
}

DEV u32 pol_off() { const int lane = threadIdx.x & 63; return lane < 32 ? foff(F_RG + lane) : OOB; }
DEV u32 pol_of(u32 raw) {
    const int lane = threadIdx.x & 63;
    const float v = add16(__builtin_amdgcn_fmed3f((float)((int)raw - (1 << 20)), -250.f, 250.f));
    const float lo = dpp<0x121>(v), hi = dpp<0x12F>(v), ex = dpp<0x122>(v) + dpp<0x123>(v) + dpp<0x124>(v);
    const float sum = v + lo + ((lane & 15) == 11 ? 0.f : hi) + ((lane & 15) == 0 ? ex : 0.f);
    return (u32)__builtin_amdgcn_ballot_w64(sum >= 0.f) & 0xFFFFu;
}
DEV u32 get_pol(const Params& p, rsrc_t rs, bool& dead) { return pol_of(boot_wait(p, rs, pol_off(), 0u, dead)); }
DEV u32 hblk(u32 base, int ls, int h, u32 side, u32 pol) { return near_(base + (u32)(ls * NH + h) * 16384u, side, pol); }
DEV f32x2 ln_stats(const f32x2 (&x)[5]) {
    const float c = f_(uu(u_(x[0].x)));
    f32x2 d[5];
#pragma unroll
    for (int i = 0; i < 5; ++i) d[i] = x[i] - c;
    const f32x2 s2 = (d[0] + d[1]) + (d[2] + d[3]) + d[4], q = (d[0] * d[0] + d[1] * d[1]) + (d[2] * d[2] + (d[3] * d[3] + d[4] * d[4]));
    const float dm = wave_sum(s2.x + s2.y) * (1.0f / DM), mean = c + dm;
    const float var = fmaxf(wave_sum(q.x + q.y) * (1.0f / DM) - dm * dm, 0.f);
    const float sd = rbf(__builtin_amdgcn_sqrtf(rbf(rbf(var) + 1e-5f)));
    return (f32x2){rbf(mean), __builtin_amdgcn_rcpf(sd)};
}
DEV void store_h(rsrc_t rs, u32 tile, int row, const u32 (&hp)[5]) {
    const u32 lane = threadIdx.x & 63;
    const u32 sw = (u32)(row & 15) * 16u, o8 = (lane >> 5) * 16384u + (((lane & 31u) * 8u) ^ sw), hb = tile + (u32)(row & 31) * 256u;
    st8s(rs, o8, hb, (v2u){hp[0], hp[1]});
    st8s(rs, o8, hb + 32768, (v2u){hp[2], hp[3]});
    st4s(rs, (lane * 4) ^ sw, hb + 65536, hp[4]);
}

DEV void ln_row(rsrc_t rs, int row, v2u a, v2u b, u32 c) {
    const f32x2 x[5] = {up2(a.x), up2(a.y), up2(b.x), up2(b.y), up2(c)};
    const f32x2 st = ln_stats(x);
    u32 hp[5];
#pragma unroll
    for (int q = 0; q < 5; ++q) hp[q] = pk2(up2(pk2(x[q] - st.x)) * st.y);
    store_h(rs, W_H1 + (u32)(row >> 5) * H_TILE, row, hp);
}

#define DMA16(ldsb, vo, so) asm volatile("s_mov_b32 m0, %0\n\ts_nop 0\n\tbuffer_load_dwordx4 %1, %2, %3 offen sc1 lds" :: "s"(ldsb), "v"(vo), "s"(srd), "s"(so) : "memory")
#define DMA16I(ldsb, lo, vo, so, go) do { u32 so_; asm volatile("s_add_u32 m0, %1, %5\n\ts_add_u32 %0, %4, %6\n\tbuffer_load_dwordx4 %2, %3, %0 offen sc1 lds" \
                         : "=&s"(so_) : "s"(ldsb), "v"(vo), "s"(srd), "s"(so), "i"(lo), "i"(go) : "memory", "scc"); } while (0)
#define DMA32(ldsb, vo, so) asm volatile("s_mov_b32 m0, %0\n\ts_nop 0\n\tbuffer_load_dwordx4 %1, %2, %3 offen sc1 lds\n\tbuffer_load_dwordx4 %1, %2, %3 offen offset:1024 sc1 lds" \
                         :: "s"(ldsb), "v"(vo), "s"(srd), "s"(so) : "memory")
#define INPOL "nt"
#define OUTAUX 16
#define INAUX 2
#define DMANT(sr, ldsb, vo, so) asm volatile("s_mov_b32 m0, %0\n\ts_nop 0\n\tbuffer_load_dwordx4 %1, %2, %3 offen " INPOL " lds" :: "s"(ldsb), "v"(vo), "s"(sr), "s"(so) : "memory")
#define DMAIN(sr, ldsb, vo, so) asm volatile("s_mov_b32 m0, %0\n\ts_nop 0\n\tbuffer_load_dwordx4 %1, %2, %3 offen lds" :: "s"(ldsb), "v"(vo), "s"(sr), "s"(so) : "memory")
DEV const u16* sel(bool c, const u16* a, const u16* b) { PINS(a); PINS(b); return c ? a : b; }
DEV v4u mk_srd(const void* q, u32 bytes) { return (v4u){uu((u32)(size_t)q), uu((u32)((size_t)q >> 32) & 0xffffu), bytes, 0x00020000u}; }
#define MK_SRD() const v4u srd = mk_srd(p.ws, W_END)

constexpr int CV_PAD = 40;
constexpr int CV_PL = 4416;
constexpr int CV_F = 8 * CV_PL;
constexpr int CV_KB = 80;
constexpr int CV_KV = 65 * CV_KB;
constexpr int CV_CH = CV_F + CV_KV;
static_assert(CV_CH % 16 == 0 && 4 * CV_CH <= LDS_BYTES, "conv lds");

struct Filt { v4u a[5], b[5]; };
DEV int chan(int grp, int w) { return 16 * (grp >> 2) + (grp & 3) + 4 * w; }
template <int N = 5, int AUX = INAUX> DEV void filt_load(const Params& p, int grp, Filt& f) {
    const int tid = threadIdx.x, lane = tid & 63, d = chan(grp, tid >> 6);
    const rsrc_t rf = mk_rsrc(p.ssmk, ACT);
#pragma unroll
    for (int i = 0; i < N; ++i) {
        const u32 s0 = (u32)(8 * (lane + 64 * i) - CV_PAD);
        f.a[i] = __builtin_amdgcn_raw_buffer_load_b128(rf, s0 < SEQ ? (u32)(d * SEQ + s0) * 2u : OOB, 0, AUX);
        f.b[i] = __builtin_amdgcn_raw_buffer_load_b128(rf, s0 + 8 < SEQ ? (u32)(d * SEQ + s0 + 8) * 2u : OOB, 0, AUX);
    }
}
constexpr int KT_BYTES = 18 * 1024, KT_X = 64 * 144, NKV = 44, KT_DUMP = 8 * KT_BYTES + KT_X;
DEV void kv_fetch(const Params& p, int tile, u32 dst) {
    const int lane = threadIdx.x & 63, w = uni(threadIdx.x >> 6);
    const v4u sk = mk_srd(p.bk, ACT), sv = mk_srd(p.bv, ACT), ss = mk_srd(sel(w == 0, p.bk, p.shift), w == 0 ? ACT : DM * 8);
    const u32 vo = tile < 320 ? (u32)((lane >> 3) * DM + (tile % 10) * 64 + (lane & 7) * 8) * 2u : OOB, r0 = uu((u32)((tile / 10) * 64 + 8 * w) * (DM * 2)), d = uu(dst + w * 1024);
    DMANT(sk, d + 1024, vo, r0); DMANT(sk, d + 5120, vo, r0 + 32 * DM * 2); DMANT(sv, d + 9216, vo, r0); DMANT(sv, d + 13312, vo, r0 + 32 * DM * 2);
    DMAIN(ss, uu(w == 0 ? dst : w == 1 ? dst + 17408 : KT_DUMP), w == 0 ? (tile >= 10 ? vo : OOB) : w == 1 && lane < 32 ? (u32)lane * 16u : OOB,
          uu(w == 0 ? (tile >= 10 ? r0 - 8 * DM * 2 : 0u) : (u32)(tile % 10) * 512u));
}
DEV void kv_tile(const Params& p, rsrc_t rs, lptr src, lptr xt, int tile) {
    const int tid = threadIdx.x;
    const v4u sv = lds_r16(src + 17408 + (tid & 31) * 16);
    lptr kp = src + ((tid >> 5) * 8 + 5) * 128 + (tid & 31) * 4;
    f32x2 kf[11];
#pragma unroll
    for (int i = 0; i < 11; ++i) kf[i] = up2(*(LAS u32*)(kp + i * 128));
    const f32x2 sh[4] = {{lo_f(sv.x), lo_f(sv.z)}, {hi_f(sv.x), hi_f(sv.z)}, {lo_f(sv.y), lo_f(sv.w)}, {hi_f(sv.y), hi_f(sv.w)}};
    u32 kv[8];
#pragma unroll
    for (int i = 0; i < 8; ++i) {
        const f32x2 acc = rb2(rb2(rb2(rb2(kf[i] * sh[3]) + rb2(kf[i + 1] * sh[2])) + rb2(kf[i + 2] * sh[1])) + rb2(kf[i + 3] * sh[0]));
        kv[i] = pk2(acc * up2(*(LAS u32*)(kp + 9216 + (i - 5) * 128)));
    }
    lds_w16(xt + (2 * (tid & 31)) * 144 + (tid >> 5) * 16, (v4u){__builtin_amdgcn_perm(kv[6], kv[7], 0x05040100u), __builtin_amdgcn_perm(kv[4], kv[5], 0x05040100u),
                                                             __builtin_amdgcn_perm(kv[2], kv[3], 0x05040100u), __builtin_amdgcn_perm(kv[0], kv[1], 0x05040100u)});
    lds_w16(xt + (2 * (tid & 31) + 1) * 144 + (tid >> 5) * 16, (v4u){__builtin_amdgcn_perm(kv[6], kv[7], 0x07060302u), __builtin_amdgcn_perm(kv[4], kv[5], 0x07060302u),
                                                                 __builtin_amdgcn_perm(kv[2], kv[3], 0x07060302u), __builtin_amdgcn_perm(kv[0], kv[1], 0x07060302u)});
    BAR();
#pragma unroll
    for (int i = 0; i < 2; ++i) {
        const int ch = i * 32 + (tid >> 3);
        st16(rs, W_KVT + (u32)(((tile % 10) * 64 + ch) * SEQ + (tile / 10) * 64 + (tid & 7) * 8) * 2u, lds_r16(xt + ch * 144 + (tid & 7) * 16));
    }
}
template <int MODE> DEV void conv_job(const Params& p, rsrc_t rs, lptr lds, int grp, Filt& f, int next, u32 fo2 = OOB, u32 fv2 = 0) {
    const int tid = threadIdx.x, lane = tid & 63, w = uni(tid >> 6), bq = lane & 31, hh = lane >> 5;
    lptr F = lds + w * CV_CH;
    lptr fa = F + (bq & 7) * CV_PL + ((bq >> 3) - hh + 4) * 16;
    lptr kb = F + CV_F + CV_KB + hh * 16, kh = kb + (bq + 32) * CV_KB;
    constexpr bool LO = MODE & 1, EARLY = MODE == 0 || MODE == 3;
    bool dead = false;
    v4u kvr[4];
    const u32 D = p.ssmD[chan(grp, w)];
    auto kv_load = [&]() {
        if (EARLY) boot_wait(p, rs, lane < NKV ? foff(F_KV + lane) : OOB, 2u, dead);
#pragma unroll
        for (int i = 0; i < (LO ? 2 : 4); ++i) kvr[i] = ld16s(rs, (u32)lane * 16u, W_KVT + (u32)(chan(grp, w) * SEQ + i * 512) * 2u);
    };
    auto windows = [&](int i) {
        if (i < 4 || lane < 16) {
            const u32 e[8] = {f.a[i].x, f.a[i].y, f.a[i].z, f.a[i].w, f.b[i].x, f.b[i].y, f.b[i].z, f.b[i].w};
            u32 o[7];
#pragma unroll
            for (int q = 0; q < 7; ++q) o[q] = __builtin_amdgcn_alignbit(e[q + 1], e[q], 16);
#pragma unroll
            for (int q = 0; q < 4; ++q) {
                lds_w16(F + (2 * q) * CV_PL + (64 * i + lane) * 16, (v4u){o[q], o[q + 1], o[q + 2], o[q + 3]});
                lds_w16(F + (2 * q + 1) * CV_PL + (64 * i + lane) * 16, (v4u){e[q + 1], e[q + 2], e[q + 3], e[q + 4]});
            }
        }
    };
    lptr yw = lds + (lane >> 4) * CV_CH + 3072 + (lane & 14) * 64 + w * 32 + (lane & 1) * 16, yr = F + 3072 + lane * 16;
    const u32 yo = W_Y + (u32)((((w & 1) * 8 + (lane >> 3)) * 8 + (w >> 1)) * DM + (grp & 3) * 160 + 4 * (grp >> 2)) * 32u + (u32)(lane & 7) * 16u;
    v4u dg[2];
    auto skip = [&](lptr k) { const f32x16 z = {}; return mfma32(lds_r16(k + 32), dg[1], mfma32(lds_r16(k), dg[0], z)); };
    auto pack = [&](const f32x16& ac, const f32x16& sk, int q) {
        const int r = 4 * (q & 3);
        u32 v[4];
#pragma unroll
        for (int i = 0; i < 4; ++i) v[i] = pk2(rb2((f32x2){ac[r + 2 * i], ac[r + 2 * i + 1]}) + rb2((f32x2){sk[r + 2 * i], sk[r + 2 * i + 1]}));
        const auto x = __builtin_amdgcn_permlane32_swap(v[0], v[2], false, false), y = __builtin_amdgcn_permlane32_swap(v[1], v[3], false, false);
        return (v4u){x[0], y[0], x[1], y[1]};
    };
    if (MODE != 2) EV(8);
    if (!EARLY) kv_load();
    if (lane < CV_KB / 16) lds_w16(F + CV_F + lane * 16, (v4u){0u, 0u, 0u, 0u});
    windows(0);
    if (MODE != 1) { windows(1); windows(2); }
    if (!LO) windows(4);
    if (EARLY) kv_load();
    if (MODE == 2) windows(3);
    if (MODE != 2) EV(9);
#pragma unroll
    for (int i = 0; i < (LO ? 2 : 4); ++i) lds_w16(F + CV_F + (16 * i + (lane >> 2) + 1) * CV_KB + (lane & 3) * 16, kvr[i]);
#pragma unroll
    for (int ks = 0; ks < 2; ++ks)
#pragma unroll
        for (int q = 0; q < 4; ++q) { const int j = 16 * ks + 8 * hh + 7 - bq; dg[ks][q] = j == 2 * q ? D : j == 2 * q + 1 ? D << 16 : 0u; }
    f32x16 acc0 = {}, acc1 = {};
    auto first = [&]() { asm volatile("s_waitcnt vmcnt(0)\n\ts_barrier" ::: "memory"); st4s(rs, tid == 0 ? foff(F_CV + grp) : fo2, 0, tid == 0 ? 1u : fv2); EV(12); };
    if (MODE != 2) {
        EV(10);
        v4u fr[2][4];
        auto rd = [&](int m) { lptr kl = kb + max(bq - m, -1) * CV_KB; fr[m & 1][0] = lds_r16(kl); fr[m & 1][1] = lds_r16(fa + m * 64); fr[m & 1][2] = lds_r16(kl + 32); fr[m & 1][3] = lds_r16(fa + m * 64 - 32); };
        rd(0); rd(1);
        const f32x16 sk = skip(kb + bq * CV_KB);
#pragma unroll
        for (int m = 0; m < 8; ++m) {
            acc0 = mfma32(fr[m & 1][0], fr[m & 1][1], acc0); acc0 = mfma32(fr[m & 1][2], fr[m & 1][3], acc0);
            if (m < 6) rd(m + 2);
            SCHED();
        }
        lds_w16(yw, pack(acc0, sk, 0));
        BAR();
        st16(rs, w < 2 ? yo : OOB, lds_r16(yr));
        EV(11);
    }
    if (LO) {
        if (MODE == 1) { windows(1); windows(2); first(); }
        v4u a[2][2], l[2][2];
#pragma unroll
        for (int u = 0; u < 2; ++u) { lptr kl = kb + max(bq - 8 - u, -1) * CV_KB; a[u][0] = lds_r16(fa + (8 + u) * 64); l[u][0] = lds_r16(kl); a[u][1] = lds_r16(fa + (8 + u) * 64 - 32); l[u][1] = lds_r16(kl + 32); }
#pragma unroll 1
        for (int m = 8; m < 32; m += 2) {
            if (MODE == 3 && m == 20) first();
#pragma unroll
            for (int u = 0; u < 2; ++u) {
                lptr kl = kb + max(bq - m - u - 2, -1) * CV_KB;
                acc0 = mfma32(l[u][0], a[u][0], acc0); a[u][0] = lds_r16(fa + (m + u + 2) * 64); l[u][0] = lds_r16(kl); SCHED();
                acc0 = mfma32(l[u][1], a[u][1], acc0); a[u][1] = lds_r16(fa + (m + u + 2) * 64 - 32); l[u][1] = lds_r16(kl + 32); SCHED();
            }
        }
    } else {
        if (MODE == 0) windows(3); else filt_load<5, 0>(p, next, f);
        v4u a0 = lds_r16(fa), a1 = lds_r16(fa - 32), c0 = lds_r16(fa + 2048), c1 = lds_r16(fa + 2048 - 32);
        v4u h0 = lds_r16(kh), h1 = lds_r16(kh + 32), l0 = lds_r16(kb + bq * CV_KB), l1 = lds_r16(kb + bq * CV_KB + 32);
#pragma unroll 2
        for (int m = 0; m < (MODE == 0 ? 8 : 32); ++m) {
            lptr kl = kb + max(bq - m - 1, -1) * CV_KB;
            if (MODE == 0 && m == 4) first();
            acc1 = mfma32(h0, a0, acc1); h0 = lds_r16(kh - (m + 1) * CV_KB); a0 = lds_r16(fa + (m + 1) * 64); SCHED();
            acc1 = mfma32(l0, c0, acc1); c0 = lds_r16(fa + (m + 1) * 64 + 2048); l0 = lds_r16(kl); SCHED();
            acc1 = mfma32(h1, a1, acc1); h1 = lds_r16(kh - (m + 1) * CV_KB + 32); a1 = lds_r16(fa + (m + 1) * 64 - 32); SCHED();
            acc1 = mfma32(l1, c1, acc1); c1 = lds_r16(fa + (m + 1) * 64 + 2048 - 32); l1 = lds_r16(kl + 32); SCHED();
        }
        if (MODE == 0) {
#pragma unroll 2
            for (int m = 8; m < 32; ++m) {
                lptr kl = kb + max(bq - m - 1, -1) * CV_KB;
                acc1 = mfma32(h0, a0, acc1); h0 = lds_r16(kh - (m + 1) * CV_KB); SCHED();
                acc0 = mfma32(l0, a0, acc0); a0 = lds_r16(fa + (m + 1) * 64); SCHED();
                acc1 = mfma32(l0, c0, acc1); c0 = lds_r16(fa + (m + 1) * 64 + 2048); l0 = lds_r16(kl); SCHED();
                acc1 = mfma32(h1, a1, acc1); h1 = lds_r16(kh - (m + 1) * CV_KB + 32); SCHED();
                acc0 = mfma32(l1, a1, acc0); a1 = lds_r16(fa + (m + 1) * 64 - 32); SCHED();
                acc1 = mfma32(l1, c1, acc1); c1 = lds_r16(fa + (m + 1) * 64 + 2048 - 32); l1 = lds_r16(kl + 32); SCHED();
            }
        }
    }
    if (MODE != 2) EV(13);
    v4u yy[4];
    if (MODE != 2) { const f32x16 sk = skip(kb + bq * CV_KB); yy[0] = pack(acc0, sk, 0); yy[1] = pack(acc0, sk, 2); }
    if (!LO) { const f32x16 sk = skip(kh); yy[2] = pack(acc1, sk, 0); yy[3] = pack(acc1, sk, 2); BAR(); }
#pragma unroll
    for (int q = (MODE == 2 ? 2 : 0); q < (LO ? 2 : 4); ++q) lds_w16(yw + q * CV_PL, yy[q]);
    BAR();
#pragma unroll
    for (int q = (MODE == 2 ? 2 : 0); q < (LO ? 2 : 4); ++q) st16(rs, q == 0 && w < 2 ? OOB : yo + (u32)q * (2 * DM * 32), lds_r16(yr + q * CV_PL));
    BAR();
    if (MODE != 2) EV(14);
}

#define NPROBE 6
DEV int probe(rsrc_t rs, int c) {
    const int lane = threadIdx.x & 63;
    const u32 k = (u32)(c & 15) < NREG ? (u32)(c & 15) : 0u;
    const u32 off0 = k * (16 * MB) + 2 * MB - 65536 + ((u32)(c & 15) >= NREG ? (u32)(c & 3) + 1u : 0u) * 256 + (u32)(c >> 4) * 128;
    st4s(rs, lane == 0 ? off0 : OOB, 0, 0u); DRAIN();
    int d0 = 1 << 16, d1 = 1 << 16;
#pragma unroll 1
    for (int q = 0; q < NPROBE; ++q) {
        const u32 o = lane == 0 ? off0 + (u32)(q & 1) * 8192 + (u32)(q >> 1) * 18432 : OOB;
        const unsigned long long t0 = __builtin_amdgcn_s_memtime();
        st4s(rs, o, 0, 0u); DRAIN();
        const int dt = (int)(__builtin_amdgcn_s_memtime() - t0);
        if (q & 1) d1 = min(d1, dt); else d0 = min(d0, dt);
    }
    const u32 xcc = (u32)__builtin_amdgcn_s_getreg(20 | (3 << 11)) & 0xfu;
    return (d0 - d1) * ((xcc & 4u) ? -1 : 1);
}
DEV int pre_rings(const Params& p, rsrc_t rs, int j) {
    const u32 gt = (u32)(j * 256 + threadIdx.x) * 16u, n = ((gt >> 14) << 9) + ((gt & 8191u) >> 4);
    const v4u ps = {PSENT, PSENT, PSENT, PSENT};
    v4u m = ps;
    if (n < 640u) m = *(const v4u*)(p.mem0 + (2 * ((n >> 2) & 3) + (n & 1)) * DM + (n >> 4) * 16 + (n & 2) * 4);
    int vote = 0;
    if (uni(threadIdx.x >> 6) == 0) vote = probe(rs, j);
    BAR();
    EV(2);
#pragma unroll
    for (u32 i = 0; i < 8; ++i) st16(rs, W_ORING + gt + i * 131072u, ps);
#pragma unroll
    for (u32 i = 0; i < 2; ++i) st16(rs, gt < 16384u ? W_MRING + i * (MB / 4) + 131072u + gt : OOB, ps);
    if (n < 640u) {
        m = (v4u){__builtin_amdgcn_perm(m.z, m.x, 0x05040100u), __builtin_amdgcn_perm(m.z, m.x, 0x07060302u), __builtin_amdgcn_perm(m.w, m.y, 0x05040100u), __builtin_amdgcn_perm(m.w, m.y, 0x07060302u)};
#pragma unroll
        for (int i = 0; i < 4; ++i) m[i] = m[i] == PSENT ? 0x7FC17FC1u : m[i];
    }
    st16(rs, W_MRING + gt, m);
    st16(rs, W_MRING + MB / 4 + gt, m);
    static_assert(2 * (F_N - F_QKV) == 32 * 36, "clear");
    st4s(rs, threadIdx.x < 36 ? foff(F_QKV) + (u32)(j * 36 + threadIdx.x) * 128u : OOB, 0, 0u);
    return vote;
}

#define M32A(first, d, a, b) do { if (first) asm volatile("v_mfma_f32_32x32x16_bf16 %0, %1, %2, 0" : "=&v"(d) : "a"(a), "v"(b)); \
                                  else asm volatile("v_mfma_f32_32x32x16_bf16 %0, %1, %2, %0" : "+v"(d) : "a"(a), "v"(b)); } while (0)
#define M32B(first, d, a, b) do { if (first) asm volatile("v_mfma_f32_32x32x16_bf16 %0, %1, %2, 0" : "=&v"(d) : "v"(a), "a"(b)); \
                                  else asm volatile("v_mfma_f32_32x32x16_bf16 %0, %1, %2, %0" : "+v"(d) : "v"(a), "a"(b)); } while (0)
#define VCVT(d, a, b) asm volatile("v_cvt_pk_bf16_f32 %0, %1, %2" : "=v"(d) : "v"(a), "v"(b))
#define LD4A(d, vo, so) asm volatile("buffer_load_dword %0, %1, %2, %3 offen sc1" : "=v"(d) : "v"(vo), "s"(srd), "s"(so) : "memory")
DEV u32 wst_src(int lane, int ld) { return (u32)((lane >> 4) * ld + 8 * ((lane & 15) ^ ((lane >> 4) << 2))) * 2u; }
DEV u32 wst_rd(int lane, int w) { const int q4 = (lane >> 2) & 3; return (u32)(q4 * 256 + ((w ^ q4) * 64) + ((lane >> 4) & 1) * 32 + (lane & 3) * 8); }

constexpr int QK_A = 65536;
#define QLEAD 3
#define QFIRST 2
static_assert(2 * QFIRST <= 12, "the first rounds stay within the first LN rows");
static_assert(QFIRST < QLEAD, "a round of the lead carries the flag of the convolution");
#define QW0 10
#define QW1 3
#define QSAMP 72
#define QPK1(d, a, a_, q_) do { if constexpr (V) VCVT(d[q_], a[4 * (a_) + 8 * ((q_) >> 1) + 2 * ((q_) & 1)], a[4 * (a_) + 8 * ((q_) >> 1) + 2 * ((q_) & 1) + 1]); else VCVT(d[q_], a[4 * (a_) + (q_)], a[4 * (a_) + (q_) + 8]); } while (0)
#define QSC(a, i) asm volatile("v_mul_f32 %0, %2, %0\n\tv_mul_f32 %1, %2, %1" : "+v"(a[i]), "+v"(a[(i) + 8]) : "s"(ksc))
#define QPK(d, a, a_, q_) do { if constexpr (!V) { QSC(a, 4 * (a_) + (q_)); QSC(a, 4 * (a_) + (q_) + 1); } QPK1(d, a, a_, q_); QPK1(d, a, a_, (q_) + 1); } while (0)
DEV void role_qkv(const Params& p, lptr lds, int c) {
    const int grp = 64 + c;
    const int tid = threadIdx.x, lane = tid & 63, w = uni(tid >> 6), hh = lane >> 5, r32 = lane & 31;
    const int m = c >> 5, h = c & 31;
    const rsrc_t rs = mk_rsrc(p.ws, W_END);
    v4u wt[40];
    Filt flt;
    bool dead = false;
    const u32 ksc = m == 1 ? 0x3e0293eeu : 0x3f800000u;
    u32 pol, h2flag, fo, ol, ar[8];
    const u32 ds = uu(w * 1024), dx = ds + QK_A;
#define XRD(ks) lds_r16(lds + ar[(ks) & 7] + ((ks) >> 3) * 8192)
    const u32 t16 = (u32)tid * 16u, lnf = tid < 96 ? foff(F_LN + tid) : OOB;
    MK_SRD();
#define QDMA(i, dst, vo, so) DMA16I(dst, (i) * 4096, vo, so, ((i) >> 1) * 16384 + ((i) & 1) * 4096)
    {
        const v4u sw = mk_srd(sel(m == 0, p.Wq, sel(m == 1, p.Wk, p.Wv)), DM * NQ * 2), sx = mk_srd(p.x, ACT);
        const u32 vo = wst_src(lane, NQ) + (u32)h * (DH * 2), x8 = (u32)lane * 8u, x4 = (u32)lane * 4u;
        v2u xa[6], xb[6]; u32 xc[6];
#pragma unroll
        for (int i = 0; i < 6; ++i)
            asm volatile("buffer_load_dwordx2 %0, %3, %5, %6 offen\n\tbuffer_load_dwordx2 %1, %3, %5, %6 offen offset:512\n\tbuffer_load_dword %2, %4, %5, %6 offen offset:1024"
                         : "=&v"(xa[i]), "=&v"(xb[i]), "=&v"(xc[i]) : "v"(x8), "v"(x4), "s"(sx), "s"(uu((4 * c + w + 384 * i) * (DM * 2))) : "memory");
        EV(1);
        auto wfill = [&](int i0, int n) {
#pragma unroll 1
            for (int i = i0; i < i0 + n; ++i) DMAIN(sw, uu((4 * i + w) * 1024), vo, uu((16 * i + 4 * w) * (NQ * 2)));
        };
        const int bl = (int)((sw.x >> 8) + h) & 31, hn = (bl + 2) & 3;
        const u32 va = hn == 3 || bl == 31 ? OOB : vo + (u32)((((h + 3 - hn) & 3) - (h & 3)) * 256), vb = hn == 3 || (bl >> 2) != 6 ? OOB : vo + (u32)((31 - bl) * 256);
        wfill(0, QW0);
        asm volatile("s_waitcnt vmcnt(%0) ; HZCLR" :: "i"(QW0) : "memory");
        EV(2);
#pragma unroll
        for (int i = 0; i < 6; ++i) {
            wfill(QW0 + QW1 * i, QW1);
            DMAIN(sw, uu((4 * 39 + w) * 1024), i < 4 ? va : vb, uu((16 * (39 - 3 * (i & 3) - hn) + 4 * w) * (NQ * 2)));
            if (i == 2) { VMW(2 * QW1 + 5); BAR(); boot_set(p, rs, tid == 0 ? foff(F_LN + c) : OOB, 1u); }
            asm volatile("" : "+v"(xa[i]), "+v"(xb[i]), "+v"(xc[i]));
            const int row = 4 * c + w + 384 * i;
            if (i < 5 || row < SEQ) {
                ln_row(rs, row, xa[i], xb[i], xc[i]);
            }
        }
        EV(3);
        constexpr int K0 = QW0 + 6 * QW1;
        static_assert(K0 * 4096 >= QK_A + 40960, "the first tile's place is free");
        v4u fv, praw;
        asm volatile("buffer_load_dwordx4 %0, %2, %4, 0 offen sc1\n\tbuffer_load_dwordx4 %1, %3, %4, 0 offen sc1" : "=&v"(praw), "=&v"(fv) : "v"(pol_off()), "v"(lnf), "s"(srd) : "memory");
        wfill(K0, 40 - K0);
        asm volatile("s_waitcnt vmcnt(%2) ; HZCLR\n\ts_barrier" : "+v"(praw), "+v"(fv) : "i"(40 - K0) : "memory");
        boot_set(p, rs, tid == 0 ? foff(F_LN + c) : OOB, 2u);
        EV(4);
        lptr tb = lds + hh * 2048 + wst_rd(lane, w);
#define WCV(k) do { lptr q_ = tb + ((k) >> 3) * 32768 + ((k) & 7) * 4096; wt[k] = tr2(q_, q_ + 1024); } while (0)
        SFOR(k, K0) WCV(k); asm volatile("" : "+a"(wt[k])); SCHED(); SEND
        if (!boot_ok(p, lnf, fv, 1u)) boot_wait(p, rs, lnf, 1u, dead);
        if (!boot_ok(p, pol_off(), praw, 0u)) praw.w = boot_wait(p, rs, pol_off(), 0u, dead);
        BAR();
        EV(5);
        { const u32 so = W_H1; SFOR(i, 10) QDMA(i, ds, t16, so); SEND }
        { const u32 so = W_H1 + H_TILE; SFOR(i, 10) QDMA(i, dx, t16, so); SEND }
        pol = pol_of(praw.w);
        h2flag = tid < 8 ? near_(foff(F_H2 + lane), 1u, pol) : OOB;
        ol = (m == 2 ? (u32)(2 * w + (r32 >> 4)) * 1024u : (u32)((r32 >> 4) * 4 + w) * 1024u) + (u32)(hh * 16 + (r32 & 15)) * 16u;
        fo = near_(foff(F_QKV + m * 64 + h), 0u, pol);
        u32 oa = ol + near_((m == 2 ? W_VO : W_KQ) + (u32)h * 16384u, m == 1 ? 0u : 1u, pol);
#pragma unroll
        for (int j = 0; j < 8; ++j) { ar[j] = (u32)(r32 * 256 + ((2 * j + hh) ^ (r32 & 15)) * 16); PIN(ar[j]); }
        PIN(h2flag); PIN(ol); PIN(oa); PINS(fo); PINS(pol);
        VMW(10); BAR();
        EV(6);
        auto first = [&](auto isv) __attribute__((always_inline)) {
            constexpr bool V = decltype(isv)::value;
            v4u x[8], pp, pq; f32x16 a;
#pragma unroll
            for (int ks = 0; ks < 5; ++ks) x[ks] = XRD(ks);
            SFOR(ks, 40)
                if constexpr (V) M32B(ks == 0, a, x[ks & 7], wt[ks]); else M32A(ks == 0, a, wt[ks], x[ks & 7]);
                if constexpr (ks < 35) x[(ks + 5) & 7] = XRD(ks + 5);
                if constexpr (ks % 2 == 0 && K0 + ks / 2 < 40) WCV(K0 + ks / 2);
                SCHED();
                if constexpr (ks >= 3 && ks % 2 == 1 && K0 + (ks - 3) / 2 < 40) asm volatile("" : "+a"(wt[K0 + (ks - 3) / 2]));
                if constexpr (ks >= 27 && ks < 35) asm volatile("v_xor_b32 %0, 0x10000, %0" : "+v"(ar[(ks + 5) & 7]));
                SCHED();
            SEND
            NOPS(11);
            QPK(pp, a, 0, 0); QPK(pp, a, 0, 2); st16(rs, oa, pp);
            QPK(pq, a, 1, 0); QPK(pq, a, 1, 2); st16(rs, oa + 512, pq);
        };
        if (m == 2) first(IC<1>{}); else first(IC<0>{});
    }
    int n0 = 0, n1 = -QLEAD;
    const u32 bfo = tid < 192 ? lnf : tid - 192u < NKV ? foff(F_KV - 192 + tid) : OOB;
    v4u brk = {};
    auto run = [&](auto isv) __attribute__((always_inline)) {
      constexpr bool V = decltype(isv)::value;
      v4u x[8] = {};
      f32x16 a0 = {};
      bool fresh = true;
      u32 fv = 0, fo0 = tid == 192 ? fo : n1 > -QLEAD && tid == 194 ? foff(F_CV + grp) : OOB, fo1 = tid == 192 ? fo : tid == 193 ? fo + 32 * 128 : OOB;
      PIN(fo0); PIN(fo1);
      if (n1 == -QLEAD) { DRAIN(); BAR(); st4s(rs, fo0, 0, 1u); EV(7); }
#define QS(k) do {} while (0)
#pragma unroll 1
      for (const int end = n1 == -QLEAD ? QFIRST - QLEAD : NSEG; n1 < end; ++n1) {
        const int cur = n1 < 0 ? n0 + 1 : 64 | n1, nxt = n1 < 0 ? n0 + 2 : n0 + 1;
        u32 xs = n1 < 0 ? W_H1 + (u32)cur * H_TILE : near_(W_H2 + (u32)n1 * H_TILE, 1u, pol);
        PINS(xs);
        QS(0);
        if (w == 0 && n1 >= 0 && ((u32)__builtin_amdgcn_ballot_w64(fv <= (u32)n1) & 255u) != 0u) wait_flags(rs, h2flag, (u32)n1 + 1, dead);
        BAR();
        QS(1);
        f32x16 a1;
        u32 ss, sv, hb, oa, qfo, qfv;
        v4u pp, pq;
        SCHED();
        auto slot = [&](auto zc) __attribute__((always_inline)) {
            constexpr int z = decltype(zc)::value, ks = (z + 20) % 40;
            if constexpr (z < 20) { if constexpr (V) M32B(false, a0, x[ks & 7], wt[ks]); else M32A(false, a0, wt[ks], x[ks & 7]); }
            else if constexpr (z < 60) { if constexpr (V) M32B(z == 20, a1, x[ks & 7], wt[ks]); else M32A(z == 20, a1, wt[ks], x[ks & 7]); }
            else if constexpr (z == 60) {
                if constexpr (V) asm volatile("v_mfma_f32_32x32x16_bf16 %0, %1, %2, 0" : "+v"(a0) : "v"(x[ks & 7]), "a"(wt[ks]));
                else asm volatile("v_mfma_f32_32x32x16_bf16 %0, %1, %2, 0" : "+v"(a0) : "a"(wt[ks]), "v"(x[ks & 7]));
            } else { if constexpr (V) M32B(false, a0, x[ks & 7], wt[ks]); else M32A(false, a0, wt[ks], x[ks & 7]); }
            if constexpr (z == 18) { QS(2); asm volatile("s_waitcnt vmcnt(6) lgkmcnt(0)\n\ts_barrier" ::: "memory"); QS(3); }
            if constexpr (z < 15 || z > 20) x[(ks + 5) & 7] = XRD((ks + 5) % 40);
            if constexpr (z >= 18 && z <= 20) { x[2 * (z - 18)] = XRD(2 * (z - 18)); x[2 * (z - 18) + 1] = XRD(2 * (z - 18) + 1); }
            SCHED();
            if constexpr (z < 4) QDMA(z, dx, t16, xs);
            if constexpr (z >= 5 && z < 17 && (z & 1)) QDMA((z + 3) >> 1, dx, t16, xs);
            if constexpr ((z >= 7 && z < 15) || (z >= 47 && z < 55)) asm volatile("v_xor_b32 %0, 0x10000, %0" : "+v"(ar[(ks + 5) & 7]));
            if constexpr (z == 16) { ss = W_H1 + (u32)nxt * H_TILE; PINS(ss); }
            if constexpr (z == 17) { sv = t16 | (nxt >= NSEG ? OOB : 0u); PIN(sv); }
            if constexpr (z >= 21 && z < 41 && (z & 1)) QDMA((z - 21) >> 1, ds, sv, ss);
            if constexpr (z == 22 || z == 24) QPK(pp, a0, 0, z - 22);
            if constexpr (z == 26 || z == 28) QPK(pq, a0, 1, z - 26);
            if constexpr (z == 30) { QS(4); asm volatile("s_waitcnt vmcnt(5)\n\ts_barrier" ::: "memory"); QS(5); }
            if constexpr (z == 32 || z == 44) { hb = (m == 2 ? W_VO : W_KQ) + (u32)((z == 32 ? n0 : cur) * NH + h) * 16384u; PINS(hb); }
            if constexpr (z == 34 || z == 45) { hb = near_(hb, m == 1 ? 0u : 1u, pol); PINS(hb); }
            if constexpr (z == 36) { hb = n0 < NSEG && !fresh ? hb : OOB; PINS(hb); }
            if constexpr (z == 38 || z == 46) { oa = ol + hb; PIN(oa); }
            if constexpr (z == 41) st16(rs, oa, pp);
            if constexpr (z == 43) st16(rs, oa + 512, pq);
            if constexpr (z == 54) { QS(6); asm volatile("s_waitcnt vmcnt(4)\n\ts_barrier" ::: "memory"); QS(7); }
            if constexpr (z == 56) { if (__builtin_expect(n1 == QFIRST - QLEAD - 1, 0)) { filt_load<3, 0>(p, grp, flt); brk = ld16s(rs, bfo, 0); } }
            if constexpr (z == 60) NOPS(5);
            if constexpr (z == 63) st16(rs, oa, pp);
            if constexpr (z == 65) st16(rs, oa + 512, pq);
            if constexpr (z == 61 || z == 62) QPK(pp, a1, 0, 2 * (z - 61));
            if constexpr (z == 63 || z == 64) QPK(pq, a1, 1, 2 * (z - 63));
            if constexpr (z == 67) { qfo = n1 >= 0 ? fo1 : fo0; PIN(qfo); }
            if constexpr (z == 68) { qfv = tid == 193 ? (u32)n1 + 1u : (u32)min(nxt, NSEG); PIN(qfv); }
            if constexpr (z == QSAMP) LD4A(fv, h2flag, 0);
            SCHED();
        };
        if (__builtin_expect(!fresh, 1)) static_for<0, 16>(slot);
        static_for<16, 80>(slot);
        QS(8);
        asm volatile("s_waitcnt vmcnt(1)\n\ts_barrier" ::: "memory");
        st4s(rs, qfo, 0, qfv);
        QS(9);
        asm volatile("s_waitcnt vmcnt(1)" : "+v"(fv) :: "memory");
        n0 = nxt; fresh = false;
      }
      if (n1 < 0) {
        SFOR(z, 20)
            if constexpr (V) M32B(false, a0, x[(z + 20) & 7], wt[z + 20]); else M32A(false, a0, wt[z + 20], x[(z + 20) & 7]);
            if constexpr (z < 15) x[(z + 25) & 7] = XRD(z + 25);
            if constexpr (z >= 7 && z < 15) asm volatile("v_xor_b32 %0, 0x10000, %0" : "+v"(ar[(z + 25) & 7]));
            SCHED();
        SEND
        u32 oa = ol + near_((m == 2 ? W_VO : W_KQ) + (u32)(n0 * NH + h) * 16384u, m == 1 ? 0u : 1u, pol);
        v4u pp, pq;
        asm volatile("s_nop 11" : "+v"(oa));
        QPK(pp, a0, 0, 0); QPK(pp, a0, 0, 2); st16(rs, oa, pp);
        QPK(pq, a0, 1, 0); QPK(pq, a0, 1, 2); st16(rs, oa + 512, pq);
      }
    };
#pragma unroll 1
    for (;;) {
        if (m == 2) run(IC<1>{}); else run(IC<0>{});
        if (n1 > 0) break;
        if (!boot_ok(p, bfo, brk, 2u)) boot_wait(p, rs, bfo, 2u, dead);
        BAR();
        conv_job<1>(p, rs, lds, grp, flt, 0, tid == 1 ? fo : OOB, (u32)n0 + 1u);
        const u32 so = uu(W_H1 + (u32)(n0 + 1) * H_TILE);
        SFOR(i, 10) QDMA(i, dx, t16, so); SEND
    }
}

constexpr int OU_SEL = 131072;
#define OSAMP 46

DEV void role_out(const Params& p, lptr lds, int L, int kq, int ng) {
    const int tid = threadIdx.x, lane = tid & 63, w = uni(tid >> 6), hh = lane >> 5, r32 = lane & 31;
    const rsrc_t rs = mk_rsrc(p.ws, W_END);
    v4u wo[64];
    Filt flt;
    bool dead = false;
    {
        const v4u sw = mk_srd(p.Wo, DM * NQ * 2);
        const u32 vo = wst_src(lane, DM) + (ng == 4 ? 1024u : (u32)((ng >> 1) * 512 + (ng & 1) * 128 + ((lane ^ (lane >> 2)) & 8) * 16));
        auto fill = [&](int b) {
#pragma unroll 1
            for (int i = 0; i < 8; ++i) DMAIN(sw, uu((b % 5) * 32768 + (4 * i + w) * 1024), vo, uu((kq * 1024 + b * 128 + 16 * i + 4 * w) * (DM * 2)));
        };
#pragma unroll 1
        for (int b = 0; b < 5; ++b) fill(b);
        lptr tb = lds + hh * 1024 + wst_rd(lane, w);
        SFOR(b, 8)
            VMW(b < 3 ? 32 : 62 - 8 * b); BAR();
            lptr q = tb + (b % 5) * 32768;
#pragma unroll
            for (int k = 0; k < 8; ++k) { lptr a = q + (k >> 1) * 8192 + (k & 1) * 2048; wo[8 * b + k] = tr2(a, a + 4096); if (b < 7) asm volatile("" : "+a"(wo[8 * b + k])); else PIN(wo[8 * b + k]); }
            if constexpr (b < 3) { BAR(); fill(b + 5); }
            if constexpr (b == 2) filt_load<3, 0>(p, L == 0 ? 44 + kq * 5 + ng : 1 << 12, flt);
        SEND
    }
    EV(6);
    const u32 pol = get_pol(p, rs, dead);
    if (L == 0) {
        BAR();
        conv_job<3>(p, rs, lds, 44 + kq * 5 + ng, flt, 0);
        DRAIN(); BAR();
        st4s(rs, tid == 0 ? foff(F_CV + 44 + kq * 5 + ng) : OOB, 0, 2u);
    }
    EV(15);
    MK_SRD();
    const u32 ofl = (lane < 8 && w == 0) ? near_(foff(F_O + L * 32 + kq * 8 + lane), 0u, pol) : OOB;
    const u32 l16 = (u32)lane * 16u;
    const u32 fo = (tid == 192) ? near_(foff(F_P + L * 32 + kq * 5 + ng), 1u, pol) : OOB;
    auto osrc = [&](int s) { return uu(hblk(W_VO, L * NSEG + s, kq * 8, 0u, pol) + w * 1024); };
#define ODMA(i, dst, vo, so) DMA16I(dst, (i) * 4096, vo, so, ((i) >> 1) * 16384 + ((i) & 1) * 4096)
    const u32 pvo = (u32)(kq * 5 + ng) * 16384u + (u32)w * 2048u + l16;
    lptr tw = lds + OU_SEL + 256 + r32 * 272 + (ng == 4 ? hh * 32 + w * 4 : (w & 1) * 128 + hh * 16 + (w >> 1) * 8), tr = lds + OU_SEL + 256 + (8 * w + (lane >> 4)) * 272 + (lane & 15) * 16;
    lptr selw = lds + OU_SEL + (w == 0 ? 0 : 64);
    u32 pfo = OOB;
    u32 buf = 0;
    if (w == 0) wait_flags(rs, ofl, 1u, dead);
    BAR();
    for (int s = 0; s < NSEG; ++s) {
        u32 src = osrc(s), dst = uu(buf + w * 1024);
        lptr A = lds + buf + l16;
        asm volatile("" : "+s"(src), "+s"(dst));
#define OS(k) do {} while (0)
        OS(0);
        OS(1);
        SFOR(i, 10) ODMA(i, dst, l16, src); SEND
        OS(2);
        asm volatile("s_waitcnt vmcnt(7)\n\ts_barrier" ::: "memory");
        OS(3);
        LS2(L == 0 && w == 1, 96 + kq * 5 + ng);
        v4u o[64];
#pragma unroll
        for (int k = 0; k < 3; ++k) o[k] = lds_r16(A + k * 1024);
        st4s(rs, pfo, 0, (u32)s);
        u32 fv, pa; int rdy;
        f32x16 acc;
        SCHED();
        SFOR(k, 64)
            if constexpr (k < 56) M32A(k == 0, acc, wo[k], o[k]); else asm volatile("v_mfma_f32_32x32x16_bf16 %0, %1, %2, %0" : "+v"(acc) : "v"(wo[k]), "v"(o[k]));
            if constexpr (k < 61) o[k + 3] = lds_r16(A + (k + 3) * 1024);
            SCHED();
            if constexpr (k < 12 && (k & 1) == 0) ODMA(10 + (k >> 1), dst, l16, src);
            if constexpr (k == 7) OS(4);
            if constexpr (k == 8) OS(5);
            if constexpr (k == 23) OS(6);
            if constexpr (k == 24) OS(7);
            if constexpr (k == 43) OS(8);
            if constexpr (k == 44) OS(9);
            if constexpr (k == 59) OS(10);
            if constexpr (k == 63) OS(11);
            if constexpr (k == 7) asm volatile("s_waitcnt vmcnt(8)\n\ts_barrier" ::: "memory");
            if constexpr (k == 23) asm volatile("s_waitcnt vmcnt(4)\n\ts_barrier" ::: "memory");
            if constexpr (k == 30) { pa = pvo + near_(p_item(s), (u32)L, pol); PIN(pa); }
            if constexpr (k == 43) asm volatile("s_waitcnt vmcnt(0)\n\ts_barrier" ::: "memory");
            if constexpr (k == OSAMP) LD4A(fv, ofl, 0);
            if constexpr (k == 62) {
                asm volatile("s_waitcnt vmcnt(0)" : "+v"(fv) :: "memory");
                *(LAS int*)selw = (int)((u32)__builtin_amdgcn_ballot_w64(fv < (u32)s + 2) & 255u);
            }
            SCHED();
        SEND
        asm volatile("s_nop 11" : "+v"(acc));
        u32 pc[8];
#pragma unroll
        for (int i = 0; i < 8; ++i) pc[i] = pk(acc[2 * i], acc[2 * i + 1]);
        if (ng != 4) {
#pragma unroll
            for (int i = 0; i < 4; ++i) *(LAS v2u*)(tw + i * 32) = (v2u){pc[2 * i], pc[2 * i + 1]};
        } else {
#pragma unroll
            for (int i = 0; i < 8; ++i) *(LAS u32*)(tw + (i >> 1) * 64 + (i & 1) * 16) = pc[i];
        }
        BAR();
        const v4u p0 = lds_r16(tr), p1 = lds_r16(tr + 4 * 272);
        rdy = *(LAS int*)(lds + OU_SEL);
        st16(rs, pa, p0); st16(rs, pa + 1024, p1);
        const bool ready = uni(rdy) == 0;
        SCHED();
        OS(12);
        if (ready) pfo = fo;
        else {
            LD4A(fv, ofl, 0);
            asm volatile("s_waitcnt vmcnt(1)\n\ts_barrier" ::: "memory");
            st4s(rs, fo, 0, (u32)s + 1); pfo = OOB;
            OS(14);
            LS2(L == 0 && w == 1, 160 + kq * 5 + ng);
            asm volatile("s_waitcnt vmcnt(1)" : "+v"(fv) :: "memory");
            if (w == 0 && s + 1 < NSEG && ((u32)__builtin_amdgcn_ballot_w64(fv < (u32)s + 2) & 255u) != 0u) wait_flags(rs, ofl, (u32)s + 2, dead, L >= CALM_L && s + 2 < NSEG);
            BAR();
        }
        OS(13);
        buf ^= 65536u;
    }
}

template <int L> DEV void role_row(const Params& p, lptr lds, int ri) {
    const int lane = threadIdx.x & 63, w = uni(threadIdx.x >> 6);
    const rsrc_t rs = mk_rsrc(p.ws, W_END);
    bool dead = false;
    const u32 pol = get_pol(p, rs, dead);
    const u32 fp = threadIdx.x < 20 ? near_(foff(F_P + L * 32 + lane), 1u, pol) : OOB, fo = threadIdx.x != 192 ? OOB : L == 0 ? near_(foff(F_H2 + (ri >> 2)), 1u, pol) : ri == 3 ? foff(F_EP) : OOB;
    u32 fv = 0;
    MK_SRD();
    auto yfill = [&](int b, int i) { DMA32(uu((b & 1) * 40960 + w * 10240 + i * 2048), (u32)lane * 16u, uu(W_Y + (u32)((ri >> 1) * 8 + b) * 20480u + (w & 1) * 10240 + i * 2048)); };
    auto ywait = [&](u32 v) {
        wait_flags(rs, threadIdx.x < (v == 3u ? 36u : 160u) ? foff(F_CV + threadIdx.x) : OOB, v, dead);
    };
    v4u y8[10] = {};
    v2u xa, xb; u32 xc;
    const v4u sx = L == 0 ? mk_srd(p.x, ACT) : mk_srd(p.ws + W_X2, ACT);
    auto xload = [&](int s) {
        asm volatile("buffer_load_dwordx2 %0, %3, %5, %6 offen sc1\n\tbuffer_load_dwordx2 %1, %3, %5, %6 offen offset:512 sc1\n\tbuffer_load_dword %2, %4, %5, %6 offen offset:1024 sc1"
                     : "=&v"(xa), "=&v"(xb), "=&v"(xc) : "v"((u32)lane * 8u), "v"((u32)lane * 4u), "s"(sx), "s"(uu((u32)(s * 32 + ri) * (DM * 2))) : "memory");
        SCHED();
    };
    ywait(1u);
    BAR();
    EV(15);
#pragma unroll 1
    for (int i = 0; i < 5; ++i) yfill(0, i);
    if (L == 1) wait_flags(rs, lane == 0 ? near_(foff(F_H2 + (ri >> 2)), 1u, pol) : OOB, 1u, dead);
    xload(0);
    LD4A(fv, fp, 0);
    VMW(0); SCHED();
    asm volatile("" : "+v"(xa), "+v"(xb), "+v"(xc), "+v"(fv));
    BAR();
    for (int s = 0; s < NSEG; ++s) {
        u32 xi[5] = {xa.x, xa.y, xb.x, xb.y, xc};
        u32 pa = near_(p_item(s), (u32)L, pol) + (u32)ri * 256u, pc = pa + 65536u + (u32)(lane >> 4) * 81920u + (u32)(lane & 15) * 16u;
        pa += (u32)(lane >> 5) * 81920u + (u32)((lane >> 4) & 1) * 16384u + (u32)(lane & 15) * 16u;
        u32 yf = s == 1 ? foff(F_CV + min(threadIdx.x, 159u)) : s == 25 ? foff(F_CV + min(lane, 35)) : OOB;
        asm volatile("" : "+v"(xi[0]), "+v"(xi[1]), "+v"(xi[2]), "+v"(xi[3]), "+v"(xi[4]), "+v"(pa), "+v"(pc), "+v"(yf));
#define RS(k) do {} while (0)
        RS(0);
        if (w == 0 && ((u32)__builtin_amdgcn_ballot_w64(fv <= (u32)s) & 0xFFFFFu) != 0u) wait_flags(rs, fp, (u32)s + 1, dead);
        BAR();
        RS(1);
        v4u d[5];
        asm volatile("buffer_load_dwordx4 %0, %5, %7, 0 offen sc1\n\tbuffer_load_dwordx4 %1, %5, %7, %8 offen sc1\n\tbuffer_load_dwordx4 %2, %5, %7, %9 offen sc1\n\t"
                     "buffer_load_dwordx4 %3, %5, %7, %10 offen sc1\n\tbuffer_load_dwordx4 %4, %6, %7, 0 offen sc1"
                     : "=&v"(d[0]), "=&v"(d[1]), "=&v"(d[2]), "=&v"(d[3]), "=&v"(d[4]) : "v"(pa), "v"(pc), "s"(srd), "s"(163840), "s"(32768), "s"(196608) : "memory");
        RS(2);
        LS2(L == 0, 192 + ri);
        SCHED();
        f32x2 y[5];
        {
            if ((s & 7) == 0) {
                lptr ya = lds + ((s >> 3) & 1) * 40960 + (w >> 1) * 20480 + (w & 1) * 16, yc = ya + 4096 + (lane >> 1) * 32 + (lane & 1) * 10240;
#pragma unroll
                for (int i = 0; i < 8; ++i) y8[i] = lds_r16(ya + lane * 32 + (i >> 2) * 2048 + (i & 3) * 5120);
                y8[8] = lds_r16(yc); y8[9] = lds_r16(yc + 5120);
            }
#pragma unroll
            for (int i = 0; i < 10; ++i) {
                y[i >> 1][i & 1] = lo_f(y8[i].x);
                y8[i] = (v4u){__builtin_amdgcn_alignbit(y8[i].y, y8[i].x, 16), __builtin_amdgcn_alignbit(y8[i].z, y8[i].y, 16), __builtin_amdgcn_alignbit(y8[i].w, y8[i].z, 16), y8[i].w >> 16};
                PIN(y8[i]);
            }
            asm volatile("" : "+v"(y[0]), "+v"(y[1]), "+v"(y[2]), "+v"(y[3]), "+v"(y[4]));
        }
        u32 yv;
        {
            xload(s + 1);
            const bool yy = (s & 7) - 2u < 5u && s < NSEG - 8;
            u32 yo = yy ? (u32)lane * 16u : OOB;
            DMA32(uu(yy ? (((s >> 3) + 1) & 1) * 40960 + w * 10240 + ((s & 7) - 2) * 2048 : 81920), yo, uu(W_Y + (u32)((ri >> 1) * 8 + (s >> 3) + 1) * 20480u + (w & 1) * 10240 + ((s & 7) - 2) * 2048));
            LD4A(yv, yf, 0);
            asm volatile("s_waitcnt vmcnt(6)" : "+v"(d[0]), "+v"(d[1]), "+v"(d[2]), "+v"(d[3]), "+v"(d[4]));
        }
        f32x2 o[5];
        {
            f32x2 t[5][2];
#pragma unroll
            for (int i = 0; i < 5; ++i) {
                const auto e = __builtin_amdgcn_permlane32_swap(d[i].x, d[i].z, false, false), f = __builtin_amdgcn_permlane32_swap(d[i].y, d[i].w, false, false);
                t[i][0] = up2(e[0]) + up2(e[1]); t[i][1] = up2(f[0]) + up2(f[1]);
            }
            o[0] = t[0][0] + t[1][0]; o[1] = t[0][1] + t[1][1]; o[2] = t[2][0] + t[3][0]; o[3] = t[2][1] + t[3][1];
            const auto e = __builtin_amdgcn_permlane16_swap(u_(t[4][0].x), u_(t[4][1].x), false, false), f = __builtin_amdgcn_permlane16_swap(u_(t[4][0].y), u_(t[4][1].y), false, false);
            o[4] = (f32x2){f_(e[0]) + f_(e[1]), f_(f[0]) + f_(f[1])};
        }
        f32x2 x1[5];
#pragma unroll
        for (int i = 0; i < 5; ++i) x1[i] = up2(pk2(up2(xi[i]) + up2(pk2(o[i]))));
        asm volatile("" : "+v"(x1[0]), "+v"(x1[1]), "+v"(x1[2]), "+v"(x1[3]), "+v"(x1[4]));
        RS(3);
        const f32x2 st1 = ln_stats(x1);
        u32 xp[5];
#pragma unroll
        for (int i = 0; i < 5; ++i) xp[i] = pk2(x1[i] + ((x1[i] - st1.x) * st1.y) * y[i]);
        const int row = s * 32 + ri;
        RS(4);
        if (L == 0) {
            const u32 ro = (u32)row * (DM * 2);
            f32x2 x2[5];
#pragma unroll
            for (int i = 0; i < 5; ++i) x2[i] = up2(xp[i]);
            st8s(rs, (u32)lane * 8, W_X2 + ro, (v2u){xp[0], xp[1]});
            st8s(rs, (u32)lane * 8 + 512, W_X2 + ro, (v2u){xp[2], xp[3]});
            st4s(rs, (u32)lane * 4 + 1024, W_X2 + ro, xp[4]);
            SCHED();
            const f32x2 st2 = ln_stats(x2);
            u32 hp[5];
#pragma unroll
            for (int i = 0; i < 5; ++i) hp[i] = pk2((x2[i] - st2.x) * st2.y);
            store_h(rs, near_(W_H2 + (u32)s * H_TILE, 1u, pol), row, hp);
        } else {
            const rsrc_t ro = mk_rsrc(p.out, ACT);
            __builtin_amdgcn_raw_buffer_store_b64((v2u){xp[0], xp[1]}, ro, (u32)lane * 8u, (u32)row * (DM * 2), OUTAUX);
            __builtin_amdgcn_raw_buffer_store_b64((v2u){xp[2], xp[3]}, ro, (u32)lane * 8u + 512u, (u32)row * (DM * 2), OUTAUX);
            __builtin_amdgcn_raw_buffer_store_b32(xp[4], ro, (u32)lane * 4u + 1024u, (u32)row * (DM * 2), OUTAUX);
        }
        RS(5);
        if (L == 1 && s == NSEG - 1) __builtin_amdgcn_endpgm();
        LD4A(fv, fp, 0);
        if (L == 0) asm volatile("s_waitcnt vmcnt(1)\n\ts_barrier" ::: "memory");
        st4s(rs, L == 0 || s == 40 ? fo : OOB, 0, L == 0 ? (u32)s + 1 : p.e0);
        RS(6);
        VMW(1); SCHED();
        asm volatile("" : "+v"(fv), "+v"(yv), "+v"(xa), "+v"(xb), "+v"(xc));
        if (__builtin_expect(s == 1 || s == 25, 0) && !all_(yv >= (s == 1 ? 2u : 3u))) ywait(s == 1 ? 2u : 3u);
        RS(7);
        if (ri == 0) LST(320 + L * 64 + s, rt32());
        LS2(L == 0, 224 + ri);
    }
}

constexpr float SCALE = 0.08838834764831845f, LOG2E = 1.4426950408889634f;
constexpr int C_K = 0, C_V = 8192, C_QX = 16384, C_MEM = 24576, C_MASK = C_MEM + 10240,
              C_Q = C_MASK + 1024, C_ZERO = C_Q + 2304, C_KM = C_Q + 4096, C_VM = C_KM + 4096, C_DUMP = C_VM + 2048, C_WL = C_DUMP + 1024;
constexpr int NLF = 14;
static_assert(C_WL % 16 == 0 && C_WL + 4 * NLF * 2048 <= LDS_BYTES, "head lds");
#define CH_H0 98
#define CH_W0 80
#define CH_MH 11
#define CH_MW 16
#define CH_REC 16
#define CH_LATE 30
#define CH_SOLO 6
#define CH_KV 12
#define CH_KV0 44
#define CH_M0 3
#define CH_DEC 2u
#define CH_EARLY 16
#define CH_NEAR 4
#define CH_FIRST 150
#define CH_ATT 2
#define CH_CLIP 12u
#define CH_LEAD 70
#define CH_UP 3
#define CH_WX 6
DEV void wait_until(u32 t) {
    unsigned long long c;
    asm volatile("1:\n\ts_memrealtime %0\n\ts_waitcnt lgkmcnt(0)\n\ts_mov_b64 vcc, %0\n\ts_sub_u32 vcc_lo, %1, vcc_lo\n\ts_cmpk_lt_u32 vcc_lo, 400\n\ts_cbranch_scc1 1b" : "=&s"(c) : "s"(uu(t - 1u)) : "vcc", "scc", "memory");
}
#define CH_FD 14
#define CH_FC 34
#define FINE_T1(t) uu((t) - (CH_FD + 1u))
#define FINE_T4(t) uu(4u * (t) - CH_FC)
#define NOP8 "s_nop 0\n\ts_nop 0\n\ts_nop 0\n\ts_nop 0\n\ts_nop 0\n\ts_nop 0\n\ts_nop 0\n\ts_nop 0\n\t"
#define LOOP_TXT(tmp, t1) "s_memrealtime vcc\n1:\n\ts_waitcnt lgkmcnt(0)\n\ts_sub_u32 " tmp ", " t1 ", vcc_lo\n\ts_memrealtime vcc\n\ts_cmpk_lt_u32 " tmp ", 400\n\ts_cbranch_scc1 1b\n\t"
#define WAIT_TXT(tmp, t1, t4) LOOP_TXT(tmp, t1) \
    "s_nop 1\n\ts_memrealtime s[90:91]\n\ts_nop 3\n\ts_memrealtime s[92:93]\n\ts_nop 3\n\ts_memrealtime s[94:95]\n\ts_getpc_b64 s[96:97]\n2:\n\ts_waitcnt lgkmcnt(0)\n\t" \
    "s_add_u32 s90, s90, vcc_lo\n\ts_add_u32 s92, s92, s94\n\ts_sub_u32 s90, " t4 ", s90\n\ts_sub_u32 s90, s90, s92\n\ts_max_i32 s90, s90, 0\n\ts_min_i32 s90, s90, 29\n\t" \
    "s_mul_i32 s90, s90, 87\n\ts_lshr_b32 s90, s90, 4\n\ts_andn2_b32 s90, s90, 3\n\ts_sub_u32 s90, 3f-2b, s90\n\ts_add_u32 s96, s96, s90\n\ts_addc_u32 s97, s97, 0\n\t" \
    "s_setpc_b64 s[96:97]\n\t" NOP8 NOP8 NOP8 NOP8 NOP8 "3:\n\t"
#define WAIT_CLOB "vcc", "scc", "s90", "s91", "s92", "s93", "s94", "s95", "s96", "s97", "memory"
#define SMF_A(acc, a, b) asm volatile("v_smfmac_f32_16x16x64_bf16 %0, %1, %2, %3" : "+v"(acc) : "v"(a), "a"(b), "v"(idx))
#define SMF_V(acc, a, b) asm volatile("v_smfmac_f32_16x16x64_bf16 %0, %1, %2, %3" : "+v"(acc) : "v"(a), "v"(b), "v"(idx))
#define MF0(d, a, b) asm volatile("v_mfma_f32_16x16x32_bf16 %0, %1, %2, 0" : "=&v"(d) : "v"(a), "v"(b))
#define MF1(d, a, b) asm volatile("v_mfma_f32_16x16x32_bf16 %0, %1, %2, %0" : "+v"(d) : "v"(a), "v"(b))
#define MF0H(d, a, b) asm volatile("v_mfma_f32_16x16x16_bf16 %0, %1, %2, 0" : "=&v"(d) : "v"(a), "v"(b))
#define MF1H(d, a, b) asm volatile("v_mfma_f32_16x16x16_bf16 %0, %1, %2, %0" : "+v"(d) : "v"(a), "v"(b))
DEV v8u tr_frag(lptr p0, lptr p1, lptr p2, lptr p3) { return cat8(tr2(p0, p1), tr2(p2, p3)); }
constexpr int qslot(int ks) { return (ks & 1) && ks < 8 ? ks >> 1 : -1; }
constexpr int kslot(int ks) { return ks < 4 ? 4 + ks : -1; }
constexpr int vslot(int ks) { return ks >= 4 ? 4 + ks : -1; }
DEV u32 ring_map(u32 off, u32 half) { return ((off >> 13) << 14) + (half << 13) + (off & 8191u); }
#define ZRD(x, i) x = __builtin_bit_cast(f32x4, lds_r16(ZP + 16 * (i)))
#define VMAX(d, a, b) asm volatile("v_max_f32 %0, %1, %2" : "=v"(d) : "v"(a), "v"(b))
#define VMAX3(d, a, b, c_) asm volatile("v_max3_f32 %0, %1, %2, %3" : "=v"(d) : "v"(a), "v"(b), "v"(c_))
#define VADD(d, a, b) asm volatile("v_add_f32 %0, %1, %2" : "=v"(d) : "v"(a), "v"(b))
#define VEXP(d, a) asm volatile("v_exp_f32 %0, %1" : "=v"(d) : "v"(a))
#define VFMC(d, a, b) asm volatile("v_fmamk_f32 %0, %1, 0x3e0293ee, %2" : "=v"(d) : "v"(a), "v"(b))
#define VMULC(d, a) asm volatile("v_mul_f32 %0, 0x3e0293ee, %1" : "=v"(d) : "v"(a))
#define SEL(d, a, b) asm volatile("v_cndmask_b32 %0, %1, %2, %3" : "=v"(d) : "v"(a), "v"(b), "s"(himask))
#define QPERM(d, a, q) asm volatile("v_mov_b32_dpp %0, %1 quad_perm:" q " row_mask:0xf bank_mask:0xf" : "=v"(d) : "v"(a))

#define SUMRANGE 0x28000000u
#define FL_EARLY 1
#define HSAMP 71
DEV void role_head(const Params& p, lptr lds, int L, int h) {
    const int tid = threadIdx.x, lane = tid & 63, w = uni(tid >> 6), g = lane >> 4, r = lane & 15;
    const rsrc_t rs = mk_rsrc(p.ws, W_END);
    v8u wq0[10], wq1[10], wk0[10], wk1[10], wv0[10], wv1[10];
    lptr WL = lds + C_WL + w * (NLF * 2048) + lane * 16;
    MK_SRD();
    bool dead = false, have;
    const u32 l16 = (u32)lane * 16u;
    u32 pol, pol0, mring, hint, fkh, ksrc, vsrc, kb = uu(W_KQ + (u32)(L * NSEG * NH + h) * 16384u + w * 2048);
    unsigned long long kmask, vmask;
    u32 dk = uu(C_K + w * 2048), dv = dk + (C_V - C_K), dq = dk + (C_QX - C_K), dm = uu(C_MEM + w * 2048), dm2 = uu(w < 2 ? C_MEM + 8192 + w * 1024 : C_DUMP);
    {
        constexpr int GS = 1056, CS = 48 * GS;
        const v4u sq = mk_srd(p.Wq, DM * NQ * 2), sk = mk_srd(p.Wk, DM * NQ * 2), sv = mk_srd(p.Wv, DM * NQ * 2);
        const u32 vo = (u32)(g * NQ + h * DH + 8 * (r ^ ((g & 2) << 2))) * 2u;
        auto fill = [&](int ch) {
#pragma unroll 1
            for (int i = 0; i < 4; ++i) {
                const u32 dst = uu((ch % 3) * CS + (4 * i + w) * GS), so = uu((ch * 64 + 16 * i + 4 * w) * (NQ * 2));
                DMAIN(sq, dst, vo, so); DMAIN(sk, dst + 16 * GS, vo, so); DMAIN(sv, dst + 32 * GS, vo, so);
            }
        };
        const int vote = L == 1 ? pre_rings(p, rs, h) : 0;
        fill(0);
        if (L == 1) { VMW(12); BAR(); EV(3); }
        boot_set(p, rs, tid == 0 && L == 1 ? foff(F_RG + h) : OOB, (u32)((1 << 20) + vote));
        fill(1); fill(2);
        const int q4 = r >> 2;
        lptr tb = lds + (4 * (g >> 1) + q4) * GS + (g & 1) * 512 + ((w * 64 + (r & 3) * 8) ^ ((g & 1) << 7));
        v4u praw; u32 f0;
        SFOR(ch, 10)
            asm volatile("s_waitcnt vmcnt(%0) ; HZCLR" :: "i"(ch == 0 || (ch >= 4 && ch < 7) ? 25 : ch < 8 ? 24 : ch == 8 ? 12 : 1) : "memory");
            BAR();
            auto frag = [&](int m, int t) { lptr b = tb + (ch % 3) * CS + m * 16 * GS + t * 32; return tr_frag(b, b + 256, b + 8 * GS, b + 8 * GS + 256); };
            wq0[ch] = frag(0, 0); wq1[ch] = frag(0, 1); wk0[ch] = frag(1, 0); wk1[ch] = frag(1, 1); wv0[ch] = frag(2, 0); wv1[ch] = frag(2, 1);
            asm volatile("" : "+a"(wq0[ch]), "+a"(wk0[ch]), "+v"(wv0[ch]), "+v"(wv1[ch]));
            if constexpr (qslot(ch) < 0) asm volatile("" : "+a"(wq1[ch])); else asm volatile("" : "+v"(wq1[ch]));
            if constexpr (kslot(ch) < 0) asm volatile("" : "+a"(wk1[ch])); else asm volatile("" : "+v"(wk1[ch]));
            BAR();
            if constexpr (ch < 7) fill(ch + 3);
            if constexpr (ch == 3) asm volatile("buffer_load_dwordx4 %0, %1, %2, 0 offen sc1" : "=v"(praw) : "v"(pol_off()), "s"(srd) : "memory");
            if constexpr (ch == 7) {
                PIN(praw);
                if (!boot_ok(p, pol_off(), praw, 0u)) praw.w = boot_wait(p, rs, pol_off(), 0u, dead);
                pol = pol_of(praw.w); pol0 = pol & 1u;
                mring = W_MRING + (u32)L * (MB / 4) + pol0 * 8192u; hint = mring + 4 * 32768u;
                fkh = lane < 3 ? near_(foff(F_QKV + lane * 64 + L * 32 + h), 0u, pol) : lane < 6 ? hint + (u32)(lane - 3) * 4u : OOB;
                kmask = __builtin_amdgcn_ballot_w64((hblk(W_KQ, L * NSEG + lane, h, 0u, pol) & 8192u) != 0u);
                vmask = __builtin_amdgcn_ballot_w64((hblk(W_VO, L * NSEG + lane, h, 1u, pol) & 8192u) != 0u);
                ksrc = kb + ((u32)kmask & 1u) * 8192u; vsrc = kb + (W_VO - W_KQ) + ((u32)vmask & 1u) * 8192u;
                PIN(fkh);
                asm volatile("" : "+s"(pol), "+s"(kmask), "+s"(vmask), "+s"(ksrc), "+s"(vsrc), "+s"(dk), "+s"(dv), "+s"(dq), "+s"(dm), "+s"(dm2));
            }
            if constexpr (ch == 8) LD4A(f0, fkh, 0);
            SCHED();
        SEND
        VMW(0); SCHED(); PIN(f0);
        have = ((u32)__builtin_amdgcn_ballot_w64(f0 == 0u) & 7u) == 0u;
        if (have) { DMA32(dv, l16, vsrc); DMA32(dk, l16, ksrc); }
#pragma unroll
        for (int ch = 0; ch < 10; ++ch) {
            if (qslot(ch) >= 0) { lds_w16(WL + qslot(ch) * 2048, wq1[ch].lo); lds_w16(WL + qslot(ch) * 2048 + 1024, wq1[ch].hi); }
            if (kslot(ch) >= 0) { lds_w16(WL + kslot(ch) * 2048, wk1[ch].lo); lds_w16(WL + kslot(ch) * 2048 + 1024, wk1[ch].hi); }
            if (vslot(ch) >= 0) { lds_w16(WL + vslot(ch) * 2048, wv1[ch].lo); lds_w16(WL + vslot(ch) * 2048 + 1024, wv1[ch].hi); }
        }
        CFENCE();
    }
    const u32 idx = (r & 2) ? 0xEEEEu : 0x4444u;
    const unsigned long long himask = 0xCCCCCCCCCCCCCCCCull;
    EV(6);
    u32 tm = 0;
    lptr qw = lds + C_Q + (2 * g) * 288 + (32 * w + 2 * r) * 2;
    lptr kw = qw + (C_KM - C_Q);
    lptr qr = lds + C_Q + (2 * (r >> 2) + (r & 1)) * 288 + g * 16;
    lptr LB = lds + l16, VX = lds + C_V + w * 2048 + l16, QX = lds + C_QX + (w & 1) * 4096 + l16;
    if (tid >= 64 && tid < 70) { const u32 z = tid == 68 ? 0x3F803F80u : tid == 69 ? PSENT : 0u; lds_w16(lds + C_ZERO + (tid - 64) * 16, (v4u){z, z, z, z}); }
    if (w < 3) {
        const u32 ng = u_(-1000000000.0f * LOG2E);
        v4u mk;
#pragma unroll
        for (int i = 0; i < 4; ++i) mk[i] = w == 0 || (w == 1 && 4 * g + i > r) ? ng : 0u;
        lds_w16(lds + C_MASK + w * 4096 + l16, mk);
    }
    u32 zaddr = 0; PIN(zaddr);
    lptr ZP = lds + zaddr + C_ZERO;
#define LF_RD(slot, hf) lf[slot][hf] = lds_r16(WL + (slot) * 2048 + (hf) * 1024)
    const u32 ofl = tid == 192 ? near_(foff(F_O + L * 32 + h), 0u, pol) : tid == 193 && (h & 15) == 0 ? hint + 128u + (u32)h * 8u : OOB;
    u32 oso = (g & 1) ? OOB : W_ORING + (u32)L * (MB / 2) + ring_map((u32)((r >> 3) * 32768 + h * 1024 + w * 256 + (g >> 1) * 128 + (r & 7) * 16), pol0);
    u32 missed = 0, dirty = 1, rep = PSENT;
    const u32 early = L < FL_EARLY ? 0u : OOB;
    asm volatile("" : "+v"(oso), "+v"(qw), "+v"(kw), "+v"(qr), "+v"(VX), "+v"(QX));
    if (!have) { wait_flags(rs, lane < 3 ? fkh : OOB, 1u, dead); DMA32(dv, l16, vsrc); DMA32(dk, l16, ksrc); }
    EV(7);
    st4s(rs, L == 0 && tid == 192 ? foff(F_GO + h) : OOB, 0, rt32() | 1u);
    EV(8);
    auto next_slot = [&](u32 now) {
        const u32 hv = ld4(rs, fkh), per = rdl(hv, 4);
        if (per < 400u) { tm = rdl(hv, 5) - per; for (int n = 0; n < 3 && (int)(tm - now) < 0; ++n) tm += per; }
    };
    if (L == 1) { next_slot(rt32() + CH_KV0); tm -= 24u; }
#define C2(k) do {} while (0)
    for (int s = 0; s < NSEG;) {
        {
            *(LAS u32*)(lds + C_DUMP + 128 + w * 4) = tm;
            BAR();
            const v4u c = lds_r16(lds + C_DUMP + 128) - tm;
            const u32 add = uu((u32)max(max((int)c.x, (int)c.y), max((int)c.z, (int)c.w)));
            tm += add; dirty |= add;
        }
        u32 ms = mring + (u32)(s & 3) * 32768u + w * 2048, ms2 = mring + (u32)(s & 3) * 32768u + 16384u + (w & 1) * 1024;
        u32 l3 = w < 2 ? l16 : OOB;
        asm volatile("" : "+s"(ms), "+s"(ms2)); PIN(l3);
        f32x4 aq0, aq1, ak0, ak1;
        v4u lf[NLF][2];
        u32 tw1 = FINE_T1(tm), tw4 = FINE_T4(tm), fv = (l16 & 16u) ? rep : (u32)s;
        asm volatile("" : "+s"(tw1), "+s"(tw4)); PIN(fv);
        SCHED();
        C2(4);
        asm volatile("s_waitcnt vmcnt(5)\n\ts_barrier" ::: "memory");
        C2(5);
        st4s(rs, ofl | early, 0, fv);
        if (__builtin_expect(dirty != 0u, 0)) {
            wait_until(tm - 44u);
            st16(rs, oso, lds_r16(ZP + 80));
        }
        SCHED();
        v4u A[10];
#define HT(k) do {} while (0)
        v4u kx[8], qf[4], kmf[4], vx[2], ones, P, tq[4];
        float fa, fb, fc, fd, sx[4], t[4], sm0, sm1, tm0, tm1, inv; u32 pa, pb, fl, pmm, tmp = 0; unsigned long long odd;
        f32x4 av0, av1, s0, s1, sm, oa, ob, rsum;
        v2u vm, pm, vh, vl, on[2];
        f32x2 zz[4];
#define LW(sl) cat8(lf[sl][0], lf[sl][1])
#define PQ(ks, tl) do { if constexpr (!(tl)) SMF_A(aq0, A[ks], wq0[ks]); else if constexpr (qslot(ks) < 0) SMF_A(aq1, A[ks], wq1[ks]); else { const v8u lw_ = LW(qslot(ks)); SMF_V(aq1, A[ks], lw_); } } while (0)
#define PK(ks, tl) do { if constexpr (!(tl)) SMF_A(ak0, A[ks], wk0[ks]); else if constexpr (kslot(ks) < 0) SMF_A(ak1, A[ks], wk1[ks]); else { const v8u lw_ = LW(kslot(ks)); SMF_V(ak1, A[ks], lw_); } } while (0)
#define PV(ks, tl) do { if constexpr (!(tl)) SMF_V(av0, A[ks], wv0[ks]); else if constexpr (vslot(ks) < 0) SMF_V(av1, A[ks], wv1[ks]); else { const v8u lw_ = LW(vslot(ks)); SMF_V(av1, A[ks], lw_); } } while (0)
#define KXR(i) kx[i] = lds_r16(LB + C_K + ((i) & 1) * 4096 + ((i) >> 1) * 1024)
#define XW(q_) do { *(LAS u32*)(q_) = pa; *(LAS u32*)((q_) + 288) = pb; } while (0)
        auto slot = [&](auto zc) __attribute__((always_inline)) {
            constexpr int z = decltype(zc)::value;
            constexpr int vs[] = {0, 8, 1, 10, 9, 2, 11, -1, -1, -1, -1, 4, 6, 12, -1, -1, -1, 13, 14, -1, 16, 18, 15, 3, 5, 17, 7, 19};
            if constexpr (z < 12) PQ(z >> 1, z & 1);
            else if constexpr (z < 20) PQ(6 + (z & 3), z >> 4);
            else if constexpr (z < 32) PK((z - 20) >> 1, z & 1);
            else if constexpr (z < 40) PK(6 + (z & 3), (z >> 2) & 1);
            else if constexpr (z < 42) { if constexpr (z == 40) MF0(s0, kx[0], qf[0]); else MF0(s1, kx[1], qf[0]); }
            else if constexpr (z < 48) { if constexpr (z & 1) MF1(s1, kx[z - 40], qf[(z - 40) >> 1]); else MF1(s0, kx[z - 40], qf[(z - 40) >> 1]); }
            else if constexpr (z == 55) MF0(sm, kmf[0], qf[0]);
            else if constexpr (z > 55 && z < 59) MF1(sm, kmf[z - 55], qf[z - 55]);
            else if constexpr (z == 62) MF0(rsum, ones, P);
            else if constexpr (z == 63) MF0(oa, vx[0], P);
            else if constexpr (z == 64) MF0(ob, vx[1], P);
            else if constexpr (z == 67) { const v2u ones2 = ones.lo; MF1H(rsum, ones2, pm); }
            else PV(vs[z - 48] >> 1, vs[z - 48] & 1);
            if constexpr (z == 20 || z == 24 || z == 26) USE(lf[4 + ((z - 20) >> 1)][1]);
            if constexpr (z == 41) USE(qf[3]);
            if constexpr (z == 45 || z == 46) USE(kx[z - 39]);
            if constexpr (z == 53) USE(lf[9][1]);
            if constexpr (z >= 54 && z < 58) USE(kmf[z - 54]);
            if constexpr (z == 61) USE(vl);
            if constexpr (z == 64) USE(lf[10][1]);
            if constexpr (z == 69) USE(lf[11][1]);
            if constexpr (z == 72) USE(lf[12][1]);
            if constexpr (z == 74) USE(lf[13][1]);
            if constexpr (z < 8) A[2 + z] = lds_r16(LB + C_MEM + (2 + z) * 1024);
            if constexpr (z >= 15 && z < 23) LF_RD(4 + ((z - 15) >> 1), (z - 15) & 1);
            if constexpr (z >= 24 && z < 27) KXR(z - 24);
            if constexpr (z == 27) XW(qw);
            if constexpr (z >= 28 && z < 31) KXR(z - 25);
            if constexpr (z == 32) asm volatile("s_waitcnt lgkmcnt(3)\n\ts_barrier" ::: "memory");
            if constexpr (z >= 33 && z < 37) qf[z - 33] = lds_r16(qr + (z - 33) * 64);
            if constexpr (z == 37 || z == 38) LF_RD(8, z - 37);
            if constexpr (z == 39 || z == 41) KXR(6 + (z == 41));
            if constexpr (z == 44) XW(kw);
            if constexpr (z == 45 || z == 46) LF_RD(9, z - 45);
            if constexpr (z == 47) vx[0] = lds_r16(VX);
            if constexpr (z == 48) { VMW(1); vh = *(LAS v2u*)(VX + 1032); }
            if constexpr (z == 54) { vl = *(LAS v2u*)(VX + 1024); vx[1] = (v4u){vh.x, vh.y, vl.x, vl.y}; }
            if constexpr (z == 49) { USE(lf[9][0]); asm volatile("s_barrier" ::: "memory"); }
            if constexpr (z >= 50 && z < 54) kmf[z - 50] = lds_r16(qr + (C_KM - C_Q) + (z - 50) * 64);
            if constexpr (z == 59 || z == 60) LF_RD(10, z - 59);
            if constexpr (z == 61 || z == 65) LF_RD(11, z == 65);
            if constexpr (z == 66 || z == 68) LF_RD(12, z == 68);
            if constexpr (z == 69 || z == 70) LF_RD(13, z - 69);
            if constexpr (z >= 72) tq[z - 72] = lds_r16(QX + (z - 72) * 1024);
            if constexpr (z == HSAMP) LD4A(fl, fkh, 0);
            SCHED();
            if constexpr (z == 19) HT(2);
            if constexpr (z == 39) HT(3);
            if constexpr (z == 51) HT(4);
            if constexpr (z == 64) HT(5);
            if constexpr (z == 20) VADD(fa, aq0[0], aq0[2]);
            if constexpr (z == 21) VADD(fb, aq0[1], aq0[3]);
            if constexpr (z == 23) VADD(fc, aq1[0], aq1[2]);
            if constexpr (z == 24) VADD(fd, aq1[1], aq1[3]);
            if constexpr (z == 25) VCVT(pa, fa, fc);
            if constexpr (z == 26) VCVT(pb, fb, fd);
            if constexpr (z >= 44 && z < 48) asm volatile("v_mov_b64 %0, 0" : "=v"(zz[z - 44]));
            if constexpr (z == 47) { av0 = __builtin_shufflevector(zz[0], zz[1], 0, 1, 2, 3); av1 = __builtin_shufflevector(zz[2], zz[3], 0, 1, 2, 3); }
            if constexpr (z == 48 || z == 49) asm volatile("v_mov_b64 %0, %1" : "=v"(on[z - 48]) : "s"(0x3F803F803F803F80ull));
            if constexpr (z == 49) ones = __builtin_shufflevector(on[0], on[1], 0, 1, 2, 3);
            if constexpr (z == 40) { VADD(fa, ak0[0], ak0[2]); VADD(fb, ak0[1], ak0[3]); }
            if constexpr (z == 42) { VADD(fd, ak1[1], ak1[3]); VADD(fc, ak1[0], ak1[2]); }
            if constexpr (z == 43) { VCVT(pb, fb, fd); VCVT(pa, fa, fc); }
            if constexpr (z >= 50 && z < 54) SEL(sx[z - 50], s0[z - 50], s1[z - 50]);
            if constexpr (z >= 54 && z < 58) VEXP(t[z - 54], sx[z - 54]);
            if constexpr (z == 58) asm volatile("v_cvt_pk_bf16_f32 %0, %2, %3\n\tv_cvt_pk_bf16_f32 %1, %4, %5" : "=&v"(P.x), "=v"(P.y) : "v"(t[0]), "v"(t[1]), "v"(t[2]), "v"(t[3]));
            if constexpr (z == 59) QPERM(P.z, P.x, "[2,3,0,1]");
            if constexpr (z == 60) QPERM(P.w, P.y, "[2,3,0,1]");
            if constexpr (z == 61) { USE(sm); VMULC(sm0, sm[0]); }
            if constexpr (z == 62) { VEXP(tm0, sm0); VMULC(sm1, sm[1]); }
            if constexpr (z == 63) VEXP(tm1, sm1);
            if constexpr (z == 64) asm volatile("v_cvt_pk_bf16_f32 %0, %2, %3\n\tv_cndmask_b32 %1, %0, 0, %4" : "=&v"(pmm), "=v"(pm.x) : "v"(tm0), "v"(tm1), "s"(himask));
            if constexpr (z == 65) asm volatile("v_cndmask_b32 %0, 0, %1, %2" : "=v"(pm.y) : "v"(pmm), "s"(himask));
            if constexpr (z == 67) { SEL(oa[0], oa[0], ob[0]); SEL(oa[1], oa[1], ob[1]); }
            if constexpr (z == 68 || z == 69) SEL(oa[z - 66], oa[z - 66], ob[z - 66]);
            if constexpr (z == 70) asm volatile("v_sub_u32 %0, %1, %2" : "=v"(pa) : "v"(rsum[0]), "s"(0x2B800000u), "v"(rsum));
            if constexpr (z == 71) asm volatile("v_cmp_le_u32 %0, %1, %2" : "=s"(odd) : "s"(SUMRANGE), "v"(pa));
            if constexpr (z == 72) VADD(fa, av0[0], av0[2]);
            if constexpr (z == 73) VADD(fb, av0[1], av0[3]);
            if constexpr (z == 74) VCVT(vm.x, fa, fb);
            SCHED();
        };
        asm volatile("s_mov_b32 m0, %2\n\t" WAIT_TXT("%0", "%1", "%9") "buffer_load_dwordx4 %4, %5, %6 offen sc1 lds\n\tbuffer_load_dwordx4 %4, %5, %6 offen offset:1024 sc1 lds\n\t"
                     "s_mov_b32 m0, %3\n\ts_nop 0\n\tbuffer_load_dwordx4 %8, %5, %7 offen sc1 lds"
                     : "=&s"(tmp) : "s"(tw1), "s"(dm), "s"(dm2), "v"(l16), "s"(srd), "s"(ms), "s"(ms2), "v"(l3), "s"(tw4) : WAIT_CLOB);
        HT(0);
        ZRD(aq0, 0); ZRD(aq1, 1); ZRD(ak0, 2); ZRD(ak1, 3);
#pragma unroll
        for (int i = 0; i < 4; ++i) { LF_RD(i, 0); LF_RD(i, 1); }
        u32 ob_ = (vsrc ^ 8192u) + (u32)((w & 1) * 256); PINS(ob_);
        USE(lf[3][1]);
        DMA32(dq, l16, ksrc ^ 8192u);
        st4s(rs, ofl | (early ^ OOB), 0, fv);
        asm volatile("s_waitcnt vmcnt(3)\n\ts_barrier" ::: "memory");
        HT(1);
#pragma unroll
        for (int ks = 0; ks < 2; ++ks) A[ks] = lds_r16(LB + C_MEM + ks * 1024);
        SCHED();
        static_for<0, 76>(slot);
        u32 fail = 0;
        if (__builtin_expect(odd != 0ull, 0)) {
            u32 mn = ~0u;
#pragma unroll 5
            for (int i = 0; i < 10; ++i) { const v4u e = lds_r16(LB + C_MEM + i * 1024); mn = min(min(min(mn, e.x ^ PSENT), e.y ^ PSENT), min(e.z ^ PSENT, e.w ^ PSENT)); }
            fail = missed < SPIN_CAP && !all_(mn != 0u) ? OOB : 0u;
            float mx = fmaxf(fmaxf(fmaxf(sx[0], sx[1]), fmaxf(sx[2], sx[3])), fmaxf(sm0, sm1));
            mx = max32(max16(fmaxf(mx, dpp<0x4E>(mx))));
#pragma unroll
            for (int i = 0; i < 4; ++i) t[i] = __builtin_amdgcn_exp2f(sx[i] - mx);
            P.x = pk(t[0], t[1]); P.y = pk(t[2], t[3]); P.z = u_(dpp<0x4E>(f_(P.x))); P.w = u_(dpp<0x4E>(f_(P.y)));
            pmm = pk(__builtin_amdgcn_exp2f(sm0 - mx), __builtin_amdgcn_exp2f(sm1 - mx));
            pm = (r & 2) ? (v2u){0u, pmm} : (v2u){pmm, 0u};
            asm volatile("s_nop 1" : "+v"(P), "+v"(pm));
            MF0(rsum, ones, P); MF0(oa, vx[0], P); MF0(ob, vx[1], P);
            NOPS(7);
            { const v2u ones2 = ones.lo; MF1H(rsum, ones2, pm); }
#pragma unroll
            for (int i = 0; i < 4; ++i) SEL(oa[i], oa[i], ob[i]);
            asm volatile("s_nop 4" :: "v"(rsum));
            oso |= fail;
        }
        v4u tk[8];
        f32x4 c0, c1;
#define TKR(i) tk[i] = lds_r16(LB + C_K + ((i) >> 2) * 4096 + ((i) & 3) * 1024)
        asm volatile("v_rcp_f32 %0, %3\n\ts_nop 2\n\tv_add_f32 %1, %4, %6\n\tv_add_f32 %2, %5, %7\n\tv_cvt_pk_bf16_f32 %1, %1, %2\n\ts_nop 0"
                     : "=&v"(inv), "=&v"(vm.y), "=&v"(fd) : "v"(rsum[0]), "v"(av1[0]), "v"(av1[1]), "v"(av1[2]), "v"(av1[3]));
        MF1H(oa, vm, pm);
        SCHED();
        c0 = __builtin_bit_cast(f32x4, lds_r16(QX + (C_MASK + 4096 - C_QX)));
        SCHED();
        {
            v4u oc; float u0, u1, u2, u3;
            asm volatile("s_nop 6\n\tv_mul_f32 %4, %8, %12\n\tv_mul_f32 %5, %9, %12\n\tv_mul_f32 %6, %10, %12\n\tv_mul_f32 %7, %11, %12\n\t"
                         "v_cvt_pk_bf16_f32 %0, %4, %5\n\tv_cvt_pk_bf16_f32 %2, %4, %5\n\tv_cvt_pk_bf16_f32 %1, %6, %7\n\tv_cvt_pk_bf16_f32 %3, %6, %7\n\t"
                         "v_permlane16_swap_b32 %0, %2\n\ts_nop 0\n\tv_permlane16_swap_b32 %1, %3"
                         : "=&v"(oc.x), "=&v"(oc.y), "=&v"(oc.z), "=&v"(oc.w), "=&v"(u0), "=&v"(u1), "=&v"(u2), "=&v"(u3) : "v"(oa[0]), "v"(oa[1]), "v"(oa[2]), "v"(oa[3]), "v"(inv));
            st16(rs, oso, oc);
        }
        SCHED();
        unsigned long long clk;
        asm volatile("s_memrealtime %0" : "=s"(clk));
        if (__builtin_expect(fail == 0u, 1)) {
        HT(6);
        C2(0);
        v4u tv[2], px, pv;
        f32x4 cm, o[4], sum;
        float ex[10], cs0, cs1;
        v2u p0, p1, tvm;
        u32 oo, kvo, oz; bool kvok;
        u32 ln = l16; PIN(ln);
        lptr VO, VMX, MK = QX + (C_MASK - C_QX);
        TKR(0);
        SCHED();
        SFOR(i, 12)
            if constexpr (i == 0) MF0(cm, kmf[0], tq[0]); else if constexpr (i < 4) MF1(cm, kmf[i], tq[i]);
            else if constexpr (i < 8) MF1(c0, tk[i - 4], tq[i - 4]); else MF1(c1, tk[i - 4], tq[i - 8]);
            if constexpr (i == 0) USE(tq[3]);
            if constexpr (i == 3) USE(tk[0]);
            if constexpr (i == 4) USE(tk[1]);
            if constexpr (i == 6) USE(tk[3]);
            if constexpr (i >= 8 && i < 11) USE(tk[i - 3]);
            if constexpr (i < 3) TKR(i + 1);
            if constexpr (i == 3) { c1 = __builtin_bit_cast(f32x4, lds_r16(MK)); TKR(4); }
            if constexpr (i >= 4 && i < 7) TKR(i + 1);
            if constexpr (i == 5) *(LAS v2u*)VMX = vm;
            if constexpr (i == 7) { vx[1] = lds_r16(VX + 1024); tv[0] = lds_r16(VO); }
            if constexpr (i == 8) tv[1] = lds_r16(VO + 1024);
            SCHED();
            if constexpr (i == 0) { VMX = lds + (ln >> 1); PIN(VMX); }
            if constexpr (i == 1) { VMX += C_VM + w * 512; PIN(VMX); }
            if constexpr (i == 2) { VO = lds + C_V + (w ^ 1) * 2048 + ln; PIN(VO); }
            if constexpr (i == 6) { USE(cm); VMULC(cs0, cm[0]); }
            if constexpr (i == 8) VMULC(cs1, cm[1]);
            if constexpr (i == 9) VEXP(ex[8], cs0);
            if constexpr (i == 10) { VEXP(ex[9], cs1); VEXP(ex[0], c0[0]); }
            if constexpr (i == 11) { VEXP(ex[1], c0[1]); VEXP(ex[2], c0[2]); VEXP(ex[3], c0[3]); }
            SCHED();
        SEND
        asm volatile("v_cvt_pk_bf16_f32 %0, %6, %7\n\tv_mov_b32 %1, 0\n\tv_mov_b32 %2, 0\n\tv_cvt_pk_bf16_f32 %3, %6, %7\n\tv_cvt_pk_bf16_f32 %4, %8, %9\n\tv_cvt_pk_bf16_f32 %5, %10, %11"
                     : "=&v"(p0.x), "=&v"(p0.y), "=&v"(p1.x), "=&v"(p1.y), "=&v"(px.x), "=&v"(px.y) : "v"(ex[8]), "v"(ex[9]), "v"(ex[0]), "v"(ex[1]), "v"(ex[2]), "v"(ex[3]));
        asm volatile("s_waitcnt lgkmcnt(0)\n\ts_barrier" : "+s"(clk) :: "memory");
        tvm = *(LAS v2u*)(VMX + (512 - (w & 1) * 1024));
        pv = lds_r16(ZP + 80);
        SCHED();
#pragma unroll
        for (int i = 0; i < 4; ++i) VEXP(ex[4 + i], c1[i]);
        asm volatile("v_cvt_pk_bf16_f32 %0, %2, %3\n\tv_cvt_pk_bf16_f32 %1, %4, %5\n\ts_nop 1" : "=&v"(px.z), "=&v"(px.w) : "v"(ex[4]), "v"(ex[5]), "v"(ex[6]), "v"(ex[7]));
        {
            const v2u ones2 = ones.lo;
            const u32 now = (u32)clk;
            auto pvm = [&](auto first) __attribute__((always_inline)) {
                SCHED();
                MF0(sum, ones, px); SCHED(); if constexpr (decltype(first)::value) { oo = ln + (ln & 0x300u) + ob_; PIN(oo); } SCHED();
                MF0(o[0], vx[0], px); NOPS(1);
                MF0(o[1], vx[1], px); NOPS(1);
                MF0(o[2], tv[0], px);
                MF1H(sum, ones2, p0); NOPS(0);
                MF1H(o[0], vm, p0); NOPS(1);
                MF1H(o[1], vm, p1); NOPS(1);
                MF0(o[3], tv[1], px);
                asm volatile("v_sub_u32 %0, %2, %3\n\tv_rcp_f32 %1, %2" : "=&v"(pa), "=&v"(inv) : "v"(sum[0]), "s"(0x2B800000u), "v"(sum));
                MF1H(o[2], tvm, p0); NOPS(2);
                MF1H(o[3], tvm, p1);
                SCHED();
            };
            pvm(IC<1>{});
            if (__builtin_expect(__builtin_amdgcn_ballot_w64(pa >= SUMRANGE) != 0ull, 0)) {
                const f32x4 m0 = __builtin_bit_cast(f32x4, lds_r16(MK + 4096)), m1 = __builtin_bit_cast(f32x4, lds_r16(MK));
                float mx = fmaxf(cs0, cs1);
#pragma unroll
                for (int i = 0; i < 4; ++i) { ex[i] = m0[i] == 0.f ? c0[i] : m0[i]; ex[4 + i] = m1[i] == 0.f ? c1[i] : m1[i]; mx = fmaxf(mx, fmaxf(ex[i], ex[4 + i])); }
                mx = max32(max16(mx));
#pragma unroll
                for (int i = 0; i < 4; ++i) px[i] = pk(__builtin_amdgcn_exp2f(ex[2 * i] - mx), __builtin_amdgcn_exp2f(ex[2 * i + 1] - mx));
                p0.x = p1.y = pk(__builtin_amdgcn_exp2f(cs0 - mx), __builtin_amdgcn_exp2f(cs1 - mx));
                asm volatile("s_nop 7" : "+v"(px), "+v"(p0), "+v"(p1));
                pvm(IC<0>{});
                NOPS(3);
            }
            asm volatile("" : "+v"(o[0]), "+v"(o[1]));
            st16(rs, oo, (v4u){pk2(o[0].lo * inv), pk2(o[0].hi * inv), pk2(o[1].lo * inv), pk2(o[1].hi * inv)});
            asm volatile("" : "+v"(o[2]), "+v"(o[3]));
            st16(rs, oo ^ 2048u, (v4u){pk2(o[2].lo * inv), pk2(o[2].hi * inv), pk2(o[3].lo * inv), pk2(o[3].hi * inv)});
            SCHED();
            C2(1);
            kb += NH * 16384;
            ksrc = kb + (((kmask >> ((s + 1) & 63)) & 1ull) ? 8192u : 0u);
            vsrc = kb + (((vmask >> ((s + 1) & 63)) & 1ull) ? W_VO - W_KQ + 8192u : W_VO - W_KQ);
            oz = (oso & ~(3u << 17)) | (u32)((s + 3) & 3) << 17; oso = (oso & ~(3u << 17)) | (u32)((s + 1) & 3) << 17;
            VMW(3); SCHED();
            C2(2);
            PIN(fl); kvok = ((u32)__builtin_amdgcn_ballot_w64(fl < (u32)s + 2) & 7u) == 0u;
            kvo = l16 | (kvok ? 0u : OOB);
            DMA32(dv, kvo, vsrc); DMA32(dk, kvo, ksrc);
            st16(rs, oz, pv);
            SCHED();
            C2(3);
            rep = (u32)s << 16 | min(missed, 1u) << 14 | min(now - tm, 0x3FFFu);
            if (h == 0 && w == 3) { LST(L * 64 + s, now); LST(128 + L * 64 + s, ((int)(rdl(fl, 3) - now) >= 4) | ((int)kvok << 1) | ((int)min(missed, 3u) << 2)); LST(640 + L * 64 + s, tm); LST(1728 + L * 64 + s, __builtin_amdgcn_s_memtime()); }
            tm = rdl(fl, 5);
            missed = missed == SPIN_CAP ? missed : 0u;
            dirty = 0;
            if (__builtin_expect(rdl(fl, 3) - now + CH_LATE > 4000u && s + 1 < NSEG, 0)) {
                dirty = 1;
#pragma unroll 1
                for (int n = 0; n < 64; ++n) {
                    CFENCE();
                    const u32 hv = ld4(rs, fkh);
                    tm = rdl(hv, 5);
                    if (rdl(hv, 3) - now + CH_LATE <= 4000u) break;
                }
            }
        }
        LS2(L == 0 && w == 0, h);
        if (__builtin_expect(!kvok, 0) && s + 1 < NSEG) {
            dirty = 1;
            wait_flags(rs, ln < 48u ? fkh : OOB, (u32)s + 2, dead);
            DMA32(dv, l16, vsrc); DMA32(dk, l16, ksrc);
            const u32 now = rt32() + CH_KV;
            if ((int)(tm - now) < 0) next_slot(now);
        }
        ++s;
        } else {
            u32 ln = l16; PIN(ln);
            oso = (ln & 256u) ? oso : oso & ~OOB;
            asm volatile("s_waitcnt vmcnt(1) lgkmcnt(0)" :: "s"(clk) : "memory"); SCHED(); PIN(fl);
            const u32 was = tm - tmp;
            tm = rdl(fl, 5); dirty = 1;
            if ((int)(tm - rdl(fl, 4) - was) > 24) tm -= rdl(fl, 4);
            if ((++missed & 7u) == 0u) {
                *(LAS u32*)(lds + C_DUMP + (w == 0 ? 0 : 64)) = ld4(rs, foff(F_ERR));
                BAR();
                missed = uu(*(LAS u32*)(lds + C_DUMP)) != 0u ? SPIN_CAP : missed;
                st4s(rs, missed == SPIN_CAP ? foff(F_ERR) : OOB, 0, 1u);
            }
        }
    }
    DRAIN(); BAR();
    st4s(rs, ofl, 0, (l16 & 16u) ? rep : NSEG);
}

#define WARM1 0
#define WO_NI 4
#define WROT 0
DEV void role_wo(const Params& p, lptr lds, int j) {
    const int tid = threadIdx.x, lane = tid & 63, w = uni(tid >> 6), g = lane >> 4, r = lane & 15;
    const int rg = j & 1, cg = j >> 1, rot = cg & WROT;
    const rsrc_t rs = mk_rsrc(p.ws, W_END);
    MK_SRD();
    v4u praw;
    v8u wo[8][5];
    {
        const v4u so_ = mk_srd(p.Wo, DM * NQ * 2);
        const int one = (0x3F2FF1F0u >> (4 * (cg & 7))) & 15;
        u32 vo[3];
#pragma unroll
        for (int i = 0; i < 3; ++i) { const int n = 64 * i + lane; vo[i] = n % 6 < 5 ? (u32)((n / 6) * (DM * 2) + cg * 80 + (n % 6) * 16) : ((n / 6) & 3) == one ? (u32)((n / 6) * (DM * 2) + (cg >> 3) * 640 + 304 + rg * 16) : OOB; }
        auto fill = [&](int b) {
            const u32 dst = uu(w * 36864 + (b % 3) * 12288), src = uu((8 * w + ((b + rot) & 7)) * (DH * DM * 2));
#pragma unroll
            for (int i = 0; i < 12; ++i) DMAIN(so_, dst + i * 1024, vo[i % 3], src + (i / 3) * (32 * DM * 2));
        };
#pragma unroll 1
        for (int b = 0; b < 3; ++b) fill(b);
        const int q4 = r >> 2;
        lptr tb = lds + w * 36864 + (32 * (g >> 1) + 16 * (q4 >> 1) + 8 * ((r >> 1) & 1) + 4 * (g & 1) + (q4 & 1)) * 96 + (r & 1) * 8;
        SFOR(b, 8)
            asm volatile("s_waitcnt vmcnt(%0) ; HZCLR" :: "i"(b < 5 ? 24 : 85 - 12 * b) : "memory");
#pragma unroll
            for (int t = 0; t < 5; ++t) {
                lptr q = tb + (b % 3) * 12288 + (t == 0 ? 4 : (w + t) & 3) * 16;
                wo[b][t] = tr_frag(q, q + 192, q + 64 * 96, q + 64 * 96 + 192);
                if (t < 4) asm volatile("" : "+a"(wo[b][t])); else asm volatile("" : "+v"(wo[b][t]));
            }
            CFENCE();
            if constexpr (b < 5) fill(b + 3);
            if constexpr (b == 4) asm volatile("buffer_load_dwordx4 %0, %1, %2, 0 offen sc1" : "=v"(praw) : "v"(pol_off()), "s"(srd) : "memory");
            SCHED();
        SEND
    }
    VMW(0); BAR(); SCHED(); PIN(praw);
    EV(6);
    const u32 idx = (r & 2) ? 0xEEEEu : 0x4444u;
    bool dead = false;
    if (!boot_ok(p, pol_off(), praw, 0u)) praw.w = boot_wait(p, rs, pol_off(), 0u, dead);
    const u32 pol0 = pol_of(praw.w) & 1u;
    const u32 l16 = (u32)lane * 16u;
    u32 oo[8];
#pragma unroll
    for (int i = 0; i < 8; ++i) oo[i] = uu(W_ORING + ring_map((u32)(rg * 32768 + (8 * w + ((i + rot) & 7)) * 1024), pol0));
    const u32 hint = W_MRING + 4 * 32768u + pol0 * 8192u;
    u32 hvo[2];
#pragma unroll
    for (int l = 0; l < 2; ++l) hvo[l] = lane < 2 ? hint + (u32)(l ^ 1) * (MB / 4) + (u32)lane * 4u : lane == 2 ? hint + (u32)l * (MB / 4) + 128u : lane == 3 ? foff(F_ERR) : lane == 4 ? hint + (u32)l * (MB / 4) + 256u : OOB;
    const int xo = g * 128 + (r & 7) * 16, O = 5 * cg + (r < 8 ? w : 4), q2 = 2 + (r >> 3);
    lptr xw0 = lds + (r < 8 ? 2048 + w * 4 : ((w + 1) & 3) * 512 + 4) + xo, xw1 = lds + ((w + q2) & 3) * 512 + q2 * 4 + xo;
    lptr xr = lds + (r < 8 ? w : 4) * 512 + xo;
    if (r < 8) { *(LAS u32*)xr = 0u; *(LAS u32*)(xr + 8192) = 0u; }
    const u32 mpo = (r & 4) == 0 && (r < 8 || w == 0) ? W_MRING + ring_map((u32)((O >> 1) * 256 + rg * 128 + (4 * (g & 1) + 2 * (O & 1) + (g >> 1)) * 16 + (r & 3) * 4), pol0) : OOB;
    constexpr u32 PER0 = CH_W0 + CH_MW + CH_H0 + CH_MH;
    const bool lead = j == 0 && w == 3;
    u32 hso[2] = {OOB, OOB};
    u32 t = 0, tn = 0;
    int sn[2] = {0, WARM1 ? 0 : NSEG};
    u32 hav = 16 * CH_H0, wav = 16 * CH_W0, mx = 16 * CH_M0, ok[2] = {0u, 0u}, dw[2] = {0u, 0u};
    u32 zero = 0, rep = 0xFFFFu, failed = 1, pmp = OOB, mp = OOB, mq = OOB, cw = 0, vo = l16, tp = 0, tw1 = 0, stop = 0;
    f32x4 acc[5] = {};
    if (tid < 20) *(LAS u32*)(lds + 5632 + tid * 4) = 0u;
    unsigned long long clk = 0;
    const u32 psent = PSENT;
    lptr sw = lds + 5120 + w * 4;
    asm volatile("" : "+v"(zero), "+v"(sw), "+v"(xw0), "+v"(xw1), "+v"(xr));
    auto turn = [&](auto lc) __attribute__((always_inline)) {
        constexpr int L = decltype(lc)::value, M = L ^ 1;
        u32 done, rd, dh, dn, nt, za, zn, pdur, hn; v3u hw;
        t = uu(t); tn = uu(tn); tp = uu(tp); dw[L] = uu(dw[L]); failed = uu(failed); rep = uu(rep);
        const int sc = sn[L];
        auto settle = [&](auto ic) __attribute__((always_inline)) {
            constexpr int i = decltype(ic)::value;
            if constexpr (i == 0) { pdur = (u32)clk - tp; dw[L] += max((int)((u32)clk + CH_WX - t), 0); PINS(pdur); PINS(dw[L]); }
            if constexpr (i == 1) {
                asm volatile("v_or_b32 %0, %2, %3\n\tv_cmp_ne_u32 vcc, %4, %5\n\tv_or_b32 %1, %2, %5\n\tv_cndmask_b32 %1, %6, %1, vcc" : "=&v"(za), "=&v"(zn) : "s"(failed << 31), "v"(pmp), "v"(mq), "v"(mp), "v"(OOB) : "vcc");
                done = failed ^ 1u; PINS(done);
            }
            SCHED();
        };
        auto ahead = [&]() __attribute__((always_inline)) {
            u32 h0 = rdl(hn, 0), h1 = rdl(hn, 1), h2 = rdl(hn, 2), h3 = rdl(hn, 3), h4 = rdl(hn, 4);
            asm volatile("" : "+s"(h0), "+s"(h1), "+s"(h2), "+s"(h3), "+s"(h4));
            tp = t; t = lead ? tn : (int)(h0 - t) < 0 ? h0 + h1 : h0;
            tn = nt; stop |= h3; rep = max(h2, h4); tw1 = t - 1u;
            asm volatile("" : "+s"(tp), "+s"(t), "+s"(tn), "+s"(stop), "+s"(rep), "+s"(tw1));
            SCHED();
        };
        auto books = [&](auto ic) __attribute__((always_inline)) {
            constexpr int i = decltype(ic)::value;
            if constexpr (i == 0) { rd = rep & 0xFFFFu; PINS(rd); }
            if constexpr (i == 1) { dn = wav >> 4; PINS(dn); pdur = min(pdur, dn + CH_CLIP); PINS(pdur); }
            if constexpr (i == 2) { pdur = (done & ok[M]) ? pdur - dn : 0u; PINS(pdur); }
            if constexpr (i == 3) { wav += (u32)max((int)pdur, (int)(pdur << CH_ATT)); PINS(wav); }
            if constexpr (i == 4) { dh = hav >> 4; PINS(dh); nt = min(rd, dh + CH_CLIP); PINS(nt); }
            if constexpr (i == 5) { nt = rd < 0x3FFFu ? nt - dh : 0u; PINS(nt); }
            if constexpr (i == 6) { hav += (u32)max((int)nt, (int)(nt << CH_ATT)); PINS(hav); }
            if constexpr (i == 7) { rd = (ok[M] & ~done) * (CH_UP * 16u); PINS(rd); mx = (u32)max((int)(mx - CH_DEC), (int)rd); ok[M] = done; PINS(mx); }
            if constexpr (i == 8) { dh = (hav + mx) / 16u + (sn[1] < 2 && sn[0] >= CH_NEAR ? CH_MH + CH_SOLO : CH_MH); PINS(dh); }
            if constexpr (i == 9) { dn = (wav + mx) / 16u + (sn[0] < CH_NEAR ? CH_MW + CH_EARLY : CH_MW); PINS(dn); }
            if constexpr (i == 10) { nt = dw[L] + dh; dh += dn; dw[L] = dn; sn[M] += (int)done; PINS(nt); PINS(dh); PINS(sn[M]); }
            if constexpr (i == 11) { rd = tn - t + (dh >> 1); PINS(rd); }
            if constexpr (i == 12) { rd = sn[M] + 1 < NSEG ? rd : 0u; PINS(rd); }
            if constexpr (i == 13) { nt = t + (u32)max((int)nt, (int)rd); PINS(nt); }
            if constexpr (i == 14) asm volatile("v_mov_b32 %0, %1" : "=v"(hw.x) : "s"(nt));
            if constexpr (i == 15) asm volatile("v_mov_b32 %0, %1" : "=v"(hw.y) : "s"(dh));
            if constexpr (i == 16) asm volatile("v_mov_b32 %0, %1" : "=v"(hw.z) : "s"(nt + dn));
            SCHED();
        };
        if (__builtin_expect(sc + 1 < NSEG, 1)) {
            u32 zb;
#define WT(k) do {} while (0)
            v4u d[8];
            {
                u32 tmp;
                asm volatile(LOOP_TXT("%1", "%2") "buffer_load_dwordx4 %0, %3, %4, %5 offen sc1\n\ts_waitcnt lgkmcnt(0)"
                             : "=&v"(d[0]), "=&s"(tmp) : "s"(uu(tw1)), "v"(vo), "s"(srd), "s"(oo[0]) : "vcc", "scc", "memory");
            }
            WT(0);
            SFOR(i, 7)
                asm volatile("buffer_load_dwordx4 %0, %1, %2, %3 offen sc1" : "=v"(d[i + 1]) : "v"(vo), "s"(srd), "s"(oo[i + 1]) : "memory");
                SCHED();
                if constexpr (i == 0) { settle(IC<0>{}); static_for<0, 2>(books); }
                if constexpr (i == 1) { settle(IC<1>{}); books(IC<2>{}); }
                if constexpr (i == 2) {
                    mp = mpo + (L * (MB / 4) + (u32)((sc + 1) & 3) * 32768u); pmp = mpo + (L * (MB / 4) + (u32)(sc & 3) * 32768u);
                    zb = mp | ok[L] << 31;
                    PIN(mp); PIN(pmp); PIN(zb);
                }
                if constexpr (i == 3) static_for<3, 7>(books);
                if constexpr (i == 4) { books(IC<7>{}); books(IC<9>{}); }
                if constexpr (i == 5) { books(IC<8>{}); books(IC<10>{}); }
                SCHED();
            SEND
            LD4A(hn, hvo[L], 0);
            *(LAS u32*)(sw + L * 8192) = zero;
            SCHED();
            WT(1);
            float f0[5], f1[5], xs; unsigned long long bad; v4u sv;
#define WSW(o_) asm volatile("v_permlane32_swap_b32 %0, %1" : "+v"(f0[o_]), "+v"(f1[o_]))
#define WAD(o_) asm volatile("v_add_f32_dpp %0, %1, %0 row_ror:8 row_mask:0xf bank_mask:0xf\n\tv_add_f32_dpp %0, %2, %3 row_ror:8 row_mask:0xf bank_mask:0xc" : "+v"(f0[o_]) : "v"(f1[o_]), "v"(f0[o_ + 1]), "v"(f1[o_ + 1]))
#define WFO(o_) do { VADD(f0[o_], acc[o_][0], acc[o_][2]); VADD(f1[o_], acc[o_][1], acc[o_][3]); } while (0)
#define WOWN(k) do { if constexpr (WO_NI + (k) < 8) SMF_V(acc[4], d[(WO_NI + (k)) & 7], wo[(WO_NI + (k)) & 7][4]); SCHED(); } while (0)
            SFOR(i, 40)
                constexpr int pc = i / 5, oc = i % 5;
                if constexpr (oc == 0) VMW((pc == 0 ? 8 : 11) - pc);
                if constexpr (oc < 4) SMF_A(acc[oc], d[pc], wo[pc][oc]); else if constexpr (pc < WO_NI) SMF_V(acc[4], d[pc], wo[pc][4]);
                SCHED();
                if constexpr (i == 0) WT(2);
                if constexpr (i == 20) WT(3);
                if constexpr (i == 35) WT(4);
                if constexpr (i == 0) st4s(rs, zb, 0, psent);
                if constexpr (i == 1) st4s(rs, za, 0, psent);
                if constexpr (i == 2) st4s(rs, zn, 0, cw);
                if constexpr (i >= 5 && i < 8) books(IC<6 + i>{});
                if constexpr (i >= 10 && i < 13) books(IC<4 + i>{});
                if constexpr (i == 13) { xs = 0.f; PIN(xs); }
                if constexpr (i == 15) { vo = l16 + (M * (MB / 2) + (u32)(sn[M] & 3) * 131072u); PIN(vo); }
                if constexpr (i == 36) NOPS(1);
                if constexpr (i == 37) NOPS(2);
                SCHED();
            SEND
            WFO(0); WFO(1);
            asm volatile("v_cmp_u_f32 %0, %1, %2" : "=s"(bad) : "v"(f0[0]), "v"(f1[0]));
            WSW(0); WSW(1); WFO(2); WAD(0); WFO(3);
            *(LAS float*)(xw0 + L * 8192) = f0[0]; SCHED();
            WSW(2); WSW(3);
            if (__builtin_expect(bad != 0ull, 0)) {
                u32 mn = ~0u;
#pragma unroll
                for (int i = 0; i < 8; ++i) { u32 e = d[i].x; PIN(e); mn = min(mn, e ^ PSENT); }
                if (all_(mn != 0u)) {
#pragma unroll
                    for (int i = 0; i < 8; ++i) { v4u e = d[i]; PIN(e); mn = min(min(mn, e.y ^ PSENT), min(e.z ^ PSENT, e.w ^ PSENT)); }
                }
                *(LAS u32*)(sw + L * 8192) = all_(mn != 0u) ? 0u : 1u;
            }
            WAD(2);
            *(LAS float*)(xw1 + L * 8192) = f0[2]; SCHED();
            WOWN(0); WOWN(1); WOWN(2); WOWN(3);
            WT(5);
            f32x4 xa; float nb;
            asm volatile("s_waitcnt lgkmcnt(0)\n\ts_barrier\n\tds_read_b128 %0, %2 offset:%4\n\tds_read_b128 %1, %3 offset:%4+5120\n\ts_nop 3"
                         : "=&v"(xa), "=&v"(sv) : "v"(xr), "v"(zero), "i"(L * 8192) : "memory");
            WT(6);
            WFO(4);
            asm volatile("s_nop 0\n\tv_permlane32_swap_b32 %1, %2\n\ts_nop 1\n\tv_add_f32_dpp %0, %2, %1 row_ror:8 row_mask:0xf bank_mask:0x3\n\ts_waitcnt lgkmcnt(1)" : "+v"(xs), "+v"(f0[4]), "+v"(f1[4]));
            asm volatile("v_add_f32 %0, %0, %4\n\tv_add_f32 %1, %5, %6\n\tv_add_f32 %0, %0, %7\n\tv_add_f32 %0, %0, %1\n\tv_cmp_u_f32 vcc, %0, %0\n\ts_nop 0\n\t"
                         "v_mov_b32_dpp %1, %0 row_shl:4 row_mask:0xf bank_mask:0xf\n\tv_cndmask_b32 %3, %8, %9, vcc\n\tv_cvt_pk_bf16_f32 %2, %0, %1"
                         : "+v"(xs), "=&v"(nb), "=&v"(cw), "=&v"(mq) : "v"(xa[0]), "v"(xa[1]), "v"(xa[2]), "v"(xa[3]), "v"(mp), "v"(OOB) : "vcc");
            st4s(rs, mq, 0, cw);
            SCHED();
            WT(7);
            asm volatile("s_memrealtime %0" : "=s"(clk));
            __builtin_amdgcn_raw_buffer_store_b96(hw, rs, hso[L], 0, 16);
#pragma unroll
            for (int q = 0; q < 5; ++q) acc[q] = __builtin_bit_cast(f32x4, lds_r16(lds + zero + (5632 + 16 * q)));
            VMW(5); SCHED(); PIN(hn);
            asm volatile("s_waitcnt lgkmcnt(5)" ::: "memory");
            SCHED(); PIN(sv);
            failed = uu(sv.x | sv.y | sv.z | sv.w);
            ahead();
            asm volatile("s_waitcnt lgkmcnt(0)" : "+s"(clk) :: "memory");
        } else {
            BAR();
            static_for<0, 2>(settle);
            st4s(rs, za, 0, psent);
            st4s(rs, zn, 0, cw);
            static_for<0, 14>(books);
            failed = 1u;
            hn = ld4(rs, hvo[L]);
            vo = l16 + (M * (MB / 2) + (u32)(sn[M] & 3) * 131072u);
            ahead();
            clk = __builtin_amdgcn_s_memrealtime();
        }
    };
#pragma unroll 1
    for (int k = 0; k < 2 * SPIN_CAP && !stop && (sn[0] + 1 < NSEG || sn[1] + 1 < NSEG); ++k) {
        turn(IC<0>{}); turn(IC<1>{});
        if (__builtin_expect(k != 0, 1)) continue;
        if (lead) {
            u32 go = 0;
#pragma unroll 1
            for (int n = 0; n < SPIN_CAP && !all_(go != 0u); ++n) { CFENCE(); go = ld4(rs, foff(F_GO + (lane & 31))); }
            t = rt32();
            float last = (float)(int)(go - t);
            last = fmaxf(last, dpp<0xB1>(last)); last = fmaxf(last, dpp<0x4E>(last)); last = fmaxf(last, dpp<0x124>(last)); last = fmaxf(last, dpp<0x128>(last));
            t += uu((u32)min(max(CH_FIRST + (int)max16(last), CH_LEAD), CH_FIRST)); tn = t + PER0 / 2 + CH_REC;
            st16(rs, lane < 2 ? hint + (u32)lane * (MB / 4) : OOB, (v4u){lane == 0 ? t : tn, PER0, (lane == 0 ? t : tn) + (CH_W0 + CH_MW + CH_REC), 0u});
        } else {
            for (int n = 0; n < SPIN_CAP && !dead; ++n) {
                CFENCE();
                const u32 hv = ld4(rs, hvo[1]);
                t = rdl(hv, 0);
                if (rdl(hv, 1) < 400u) break;
                __builtin_amdgcn_s_sleep(1);
            }
        }
        hso[0] = (lead && lane == 0) ? hint : OOB; hso[1] = hso[0] + MB / 4;
        if (!WARM1) sn[1] = 0;
        rep = 0xFFFFu; dw[0] = dw[1] = CH_W0 + CH_MW + CH_REC;
        tw1 = t - 1u;
    }
    if (sn[0] + 1 < NSEG || sn[1] + 1 < NSEG) st4s(rs, tid == 0 ? foff(F_ERR) : OOB, 0, 1u);
}

__global__ void __launch_bounds__(256, 1) k_main(Args args) {
    Params p;
    (Args&)p = args;
    lptr lds = (lptr)(u32)16 - 16;
    if (blockIdx.x == 0) LST(256, rt32());
    unsigned long long pc; int len;
    asm volatile("s_getpc_b64 %0\n.Lk_main_here:\n\ts_mov_b32 %1, .Lk_end_here-.Lk_main_here" : "=s"(pc), "=s"(len));
    const v4u code = ldin16(mk_rsrc((const void*)(pc & ~15ull), (u32)max(len, 0)), (((blockIdx.x >> 3) & 31u) * 256u + threadIdx.x) * 16u, 0);
    asm volatile("" :: "s"(p.x), "s"(p.Wo), "s"(p.ws) : "memory");
    if (threadIdx.x == 0) {
        const v4u e = ld16s(mk_rsrc(p.ws, W_END), foff(F_EP), 0);
        const unsigned long long tag = (unsigned long long)e.y << 32 | (e.x + 1u);
        u32 role = ((u32)__builtin_amdgcn_s_getreg(20 | (3 << 11)) & 7u) * 32u + ((blockIdx.x >> 3) & 31u);
#pragma unroll 1
        for (int n = 0; n < 256; ++n, role = (role + 1u) & 255u)
            if (__hip_atomic_exchange((GAS unsigned long long*)(p.ws + W_SLOT) + role * 8u, tag, __ATOMIC_RELAXED, __HIP_MEMORY_SCOPE_AGENT) != tag) break;
        lds_w16(lds, (v4u){e.x + 1u, e.y, e.z, role});
    }
    __syncthreads();
    const v4u boot = lds_r16(lds);
    p.e0 = uu(boot.x); p.e1 = uu(boot.y); p.e2 = uu(boot.z);
    const int role = uni((int)boot.w), x = role >> 5, i = role & 31;
    asm volatile("" :: "v"(code));
    __syncthreads();
    LST(8192 + blockIdx.x * 16, role);
    if (x < 2) { role_head(p, lds, i >> 4, x * 16 + (i & 15)); __builtin_amdgcn_endpgm(); }
    if (x == 2) { role_wo(p, lds, i); __builtin_amdgcn_endpgm(); }
    const int no = x == 3 ? 30 : x < 6 ? 5 : 0;
    if (x > 3 && i >= 8) { role_qkv(p, lds, (x - 4) * 24 + i - 8); __builtin_amdgcn_endpgm(); }
    const bool isout = i < no;
    const int og = x == 3 ? i / 5 : x + 2, ng = i % 5, L = og >> 2, kq = og & 3;
    const int k = i - no, w = uni(threadIdx.x >> 6);
    const int j = isout ? (L == 1 ? 0 : 44) + kq * 5 + ng : x == 3 ? 36 + k : k < 2 ? 12 + 2 * x + k : x < 6 ? 24 + x : k < 5 ? 10 + 3 * x + k : 15 + 3 * x + k;
    const rsrc_t rs = mk_rsrc(p.ws, W_END);
    wait_until(rt32() + (j < NKV ? ST_KV : ST_OUT));
    if (j < NKV) {
        const int nt = j < 320 - 7 * NKV ? 8 : 7;
#pragma unroll 1
        for (int q = 0; q < 4; ++q) kv_fetch(p, j + NKV * q, uu(q * KT_BYTES));
        Filt f;
        u32 warm[3];
#pragma unroll 1
        for (int q = 0; q < nt; ++q) {
            VMW(15); BAR();
            if (q == 0) EV(2);
            kv_fetch(p, j + NKV * (q + 4), uu(((q + 4) & 7) * KT_BYTES));
            kv_tile(p, rs, lds + q * KT_BYTES, lds + (q == 0 ? 8 : q - 1) * KT_BYTES, j + NKV * q);
        }
        const int g = j < 20 ? j : j < 28 ? j + 16 : j - 8;
        filt_load<5, 0>(p, g, f);
        VMW(10); BAR();
        boot_set(p, rs, threadIdx.x == 0 ? foff(F_KV + j) : OOB, 2u);
        EV(4);
        { bool dead = false; boot_wait(p, rs, pol_off(), 0u, dead); }
#pragma unroll
        for (int q = 0; q < 3; ++q)
            warm[q] = __builtin_amdgcn_raw_buffer_load_b32(mk_rsrc(p.ssmk, ACT), 176u * 4096u + ((u32)((q * NKV + j) * 256) + threadIdx.x) * 64u, 0, 0);
        if (g >= 36) conv_job<3>(p, rs, lds, g, f, 0); else conv_job<0>(p, rs, lds, g, f, 0);
        asm volatile("" :: "v"(warm[0]), "v"(warm[1]), "v"(warm[2]));
        const int g0 = j < 20 ? 36 + j : j < 36 ? 28 + j : 44 + j, g1 = j < 20 ? g0 + 1 : j < 36 ? 80 : 160;
        if (g < 36) filt_load<5, 0>(p, g0, f);
        DRAIN(); BAR();
        st4s(rs, threadIdx.x == 0 ? foff(F_CV + g) : OOB, 0, 2u);
        if (g < 36) {
#pragma unroll 1
            for (int n = g0; n < g1; n += 8) conv_job<2>(p, rs, lds, n, f, min(n + 8, 159));
            DRAIN(); BAR();
            st4s(rs, threadIdx.x == 0 ? foff(F_CV + g) : OOB, 0, 3u);
            if (j >= 36) { EV(15); __builtin_amdgcn_endpgm(); }
        }
    }
    if (!isout) { if (j < 28) role_row<0>(p, lds, (j - 20) * 4 + w); else role_row<1>(p, lds, (j - 28) * 4 + w); __builtin_amdgcn_endpgm(); }
    role_out(p, lds, L, kq, ng);
}

__global__ void k_end() { asm volatile(".Lk_end_here:"); }

extern "C" void kernel_launch(void* const* d_in, const int* in_sizes, int n_in, void* d_out, int out_size, void* d_ws, size_t ws_size,
                              hipStream_t stream) {
    Args p;
    p.x = (const u16*)d_in[0]; p.mem0 = (const u16*)d_in[1]; p.Wq = (const u16*)d_in[2]; p.Wk = (const u16*)d_in[3];
    p.Wv = (const u16*)d_in[4]; p.Wo = (const u16*)d_in[5];
    p.bk = (const u16*)d_in[8]; p.bv = (const u16*)d_in[9]; p.shift = (const u16*)d_in[10]; p.ssmk = (const u16*)d_in[11]; p.ssmD = (const u16*)d_in[12];
    p.out = (u16*)d_out;
    p.ws = (unsigned char*)(((size_t)d_ws + 2 * MB - 1) & ~(size_t)(2 * MB - 1));
    (void)hipFuncSetAttribute((const void*)k_main, hipFuncAttributeMaxDynamicSharedMemorySize, LDS_BYTES);
    hipLaunchKernelGGL(k_main, dim3(256), dim3(256), LDS_BYTES, stream, p);
}
```

```cpp
#include <hip/hip_runtime.h>
#include <hip/hip_bf16.h>

typedef unsigned short u16;
typedef unsigned int u32;
typedef u32 v2u __attribute__((ext_vector_type(2)));
typedef u32 v3u __attribute__((ext_vector_type(3)));
typedef u32 v4u __attribute__((ext_vector_type(4)));
typedef u32 v8u __attribute__((ext_vector_type(8)));
typedef float f32x2 __attribute__((ext_vector_type(2)));
typedef float f32x4 __attribute__((ext_vector_type(4)));
typedef float f32x16 __attribute__((ext_vector_type(16)));
typedef __bf16 bf16x2 __attribute__((ext_vector_type(2)));
typedef __bf16 bf16x8 __attribute__((ext_vector_type(8)));
typedef short s16x4 __attribute__((ext_vector_type(4)));
#define LAS __attribute__((address_space(3)))
#define GAS __attribute__((address_space(1)))
typedef LAS unsigned char* lptr;
#define DEV __device__ __forceinline__

constexpr int SEQ = 2048, DM = 640, NH = 32, DH = 128, NSEG = 64, NQ = NH * DH;

constexpr u32 MB = 1u << 20;
constexpr u32 W_ORING = 0;
constexpr u32 W_MRING = MB;
constexpr u32 W_CTRL = 4 * MB;
constexpr u32 ACT = SEQ * DM * 2;
constexpr u32 H_TILE = 128 * 1024;
constexpr u32 W_H2 = 8 * MB;
constexpr u32 W_KQ = 16 * MB;
constexpr u32 W_VO = W_KQ + 64 * MB;
constexpr u32 W_P = W_VO + 64 * MB;
constexpr u32 P_ITEM = 512 * 1024;
constexpr u32 W_H1 = W_P + 44 * MB, W_Y = W_H1 + 8 * MB, W_X2 = W_Y + ACT;
constexpr u32 W_KVT = W_X2 + ACT;
constexpr u32 W_END = W_KVT + 3 * MB;
constexpr u32 NREG = 12;
static_assert((size_t)W_END + 2 * MB <= 256ull * MB && W_H1 >= (NREG - 1) * 16 * MB + 2 * MB && W_H1 <= NREG * 16 * MB, "workspace");
constexpr u32 F_EP = 0, F_RG = 32, F_KV = 64, F_LN = 128,
              F_QKV = 320, F_O = F_QKV + 192, F_P = F_O + 64, F_H2 = F_P + 64, F_GO = F_H2 + 32, F_CV = F_H2 + 64, F_ERR = F_H2 + 16, F_N = F_CV + 192;
constexpr u32 W_SLOT = 3 * MB;
DEV u32 foff(u32 idx) { return W_CTRL + (idx >> 6) * 16384u + (idx & 63u) * 128u; }
DEV u32 near_(u32 o, u32 side, u32 pol) { return o + ((side ^ (pol >> (o >> 24)) ^ (o >> 21)) & 1u) * 8192u; }
DEV u32 p_item(int s) { return W_P + (u32)s * P_ITEM; }

constexpr u32 OOB = 0x80000000u;
constexpr u32 PSENT = 0x7F817F81u;
constexpr int SPIN_CAP = 40000;
constexpr int LDS_BYTES = 160 * 1024;
#define CALM 8
#define CALM_L 1
#define ST_KV 0
#define ST_OUT 0

#define EV(k) do {} while (0)
#define LST(idx, v) do {} while (0)
#define LS2(cond, idx) do {} while (0)
struct Args {
    const u16 *x, *mem0, *Wq, *Wk, *Wv, *Wo, *bk, *bv, *shift, *ssmk, *ssmD;
    u16* out;
    unsigned char* ws;
};
struct Params : Args { u32 e0, e1, e2; };

typedef __amdgpu_buffer_rsrc_t rsrc_t;
DEV rsrc_t mk_rsrc(const void* p, u32 bytes) { return __builtin_amdgcn_make_buffer_rsrc((void*)p, 0, bytes, 0x00020000); }
DEV v4u ld16s(rsrc_t r, u32 off, u32 so) { return __builtin_amdgcn_raw_buffer_load_b128(r, off, so, 16); }
DEV v2u ld8s(rsrc_t r, u32 off, u32 so) { return __builtin_amdgcn_raw_buffer_load_b64(r, off, so, 16); }
DEV v4u ldin16(rsrc_t r, u32 off, u32 so) { return __builtin_amdgcn_raw_buffer_load_b128(r, off, so, 0); }
DEV v2u ldin8(rsrc_t r, u32 off, u32 so) { return __builtin_amdgcn_raw_buffer_load_b64(r, off, so, 0); }
DEV u32 ld4(rsrc_t r, u32 off) { return __builtin_amdgcn_raw_buffer_load_b32(r, off, 0, 16); }
DEV void st16(rsrc_t r, u32 off, v4u v) { __builtin_amdgcn_raw_buffer_store_b128(v, r, off, 0, 16); }
DEV void st8s(rsrc_t r, u32 off, u32 so, v2u v) { __builtin_amdgcn_raw_buffer_store_b64(v, r, off, so, 16); }
DEV void st4s(rsrc_t r, u32 off, u32 so, u32 v) { __builtin_amdgcn_raw_buffer_store_b32(v, r, off, so, 16); }
#define DRAIN() asm volatile("s_waitcnt vmcnt(0)" ::: "memory")
#define VMW(n) asm volatile("s_waitcnt vmcnt(%0)" :: "i"(n) : "memory")
#define BAR() asm volatile("s_waitcnt lgkmcnt(0)\n\ts_barrier" ::: "memory")
#define SCHED() __builtin_amdgcn_sched_barrier(0)
#define CFENCE() asm volatile("" ::: "memory")
#define NOPS(n) asm volatile("s_nop %0" :: "i"(n))
#define PIN(x) asm volatile("" : "+v"(x))
#define PINS(x) asm volatile("" : "+s"(x))
#define USE(x) asm volatile("" :: "v"(x))

template <int V> struct IC { static constexpr int value = V; };
template <int I, int N, class F> DEV void static_for(F&& f) { if constexpr (I < N) { f(IC<I>{}); static_for<I + 1, N>(f); } }
#define SFOR(var, n) static_for<0, n>([&](auto ic_) __attribute__((always_inline)) { constexpr int var = decltype(ic_)::value;
#define SEND });

DEV u32 pk(float a, float b) { bf16x2 v = {(__bf16)a, (__bf16)b}; return __builtin_bit_cast(u32, v); }
DEV float lo_f(u32 u) { return __builtin_bit_cast(float, u << 16); }
DEV float hi_f(u32 u) { return __builtin_bit_cast(float, u & 0xffff0000u); }
DEV f32x2 up2(u32 u) { return (f32x2){lo_f(u), hi_f(u)}; }
DEV u32 pk2(f32x2 v) { return pk(v.x, v.y); }
DEV f32x2 rb2(f32x2 a) { f32x2 r; asm("v_cvt_pk_bf16_f32 %0, 0, %1" : "=v"(r.x) : "v"(a.x)); asm("v_cvt_pk_bf16_f32 %0, 0, %1" : "=v"(r.y) : "v"(a.y)); return r; }
DEV float rbf(float a) { return (float)(__bf16)a; }
DEV v4u lds_r16(lptr p) { return *(LAS v4u*)p; }
DEV void lds_w16(lptr p, v4u v) { *(LAS v4u*)p = v; }
DEV v2u lds_tr(lptr p) { return __builtin_bit_cast(v2u, __builtin_amdgcn_ds_read_tr16_b64_v4i16((LAS s16x4*)p)); }
DEV v4u tr2(lptr p0, lptr p1) { const v2u a = lds_tr(p0), b = lds_tr(p1); return (v4u){a.x, a.y, b.x, b.y}; }
DEV v8u cat8(v4u a, v4u b) { return __builtin_shufflevector(a, b, 0, 1, 2, 3, 4, 5, 6, 7); }
DEV f32x4 mfma16(v4u a, v4u b, f32x4 c) { return __builtin_amdgcn_mfma_f32_16x16x32_bf16(__builtin_bit_cast(bf16x8, a), __builtin_bit_cast(bf16x8, b), c, 0, 0, 0); }
DEV f32x16 mfma32(v4u a, v4u b, f32x16 c) { return __builtin_amdgcn_mfma_f32_32x32x16_bf16(__builtin_bit_cast(bf16x8, a), __builtin_bit_cast(bf16x8, b), c, 0, 0, 0); }

DEV float f_(u32 u) { return __builtin_bit_cast(float, u); }
DEV u32 u_(float f) { return __builtin_bit_cast(u32, f); }
template <int CTRL> DEV float dpp(float v) { return f_((u32)__builtin_amdgcn_update_dpp(0, (int)u_(v), CTRL, 0xf, 0xf, false)); }
DEV float max16(float v) { auto r = __builtin_amdgcn_permlane16_swap(u_(v), u_(v), false, false); return fmaxf(f_(r[0]), f_(r[1])); }
DEV float max32(float v) { auto r = __builtin_amdgcn_permlane32_swap(u_(v), u_(v), false, false); return fmaxf(f_(r[0]), f_(r[1])); }
DEV float add16(float v) { auto r = __builtin_amdgcn_permlane16_swap(u_(v), u_(v), false, false); return f_(r[0]) + f_(r[1]); }
DEV float add32(float v) { auto r = __builtin_amdgcn_permlane32_swap(u_(v), u_(v), false, false); return f_(r[0]) + f_(r[1]); }
DEV float wave_sum(float v) {
    v += dpp<0xB1>(v);
    v += dpp<0x4E>(v);
    v += dpp<0x124>(v);
    v += dpp<0x128>(v);
    return add32(add16(v));
}
DEV bool all_(bool c) { return __builtin_amdgcn_ballot_w64(!c) == 0ull; }
DEV int uni(int v) { return __builtin_amdgcn_readfirstlane(v); }
DEV u32 uu(u32 v) { return (u32)__builtin_amdgcn_readfirstlane((int)v); }
DEV u32 rdl(u32 v, int l) { return (u32)__builtin_amdgcn_readlane((int)v, l); }
DEV u32 rt32() { return (u32)__builtin_amdgcn_s_memrealtime(); }

DEV void wait_flags(rsrc_t rs, u32 off, u32 want, bool& dead, bool calm = false) {
    if (dead) return;
#pragma unroll 1
    for (int n = 0;; ++n) {
        CFENCE();
        const u32 v = ld4(rs, off);
        if (all_(off >= OOB || v >= want)) break;
        __builtin_amdgcn_s_sleep(1);
        if (calm) __builtin_amdgcn_s_sleep(CALM);
        if ((n & 63) == 63) { if (uni((int)ld4(rs, foff(F_ERR))) != 0) { dead = true; break; } }
        if (n > SPIN_CAP) { st4s(rs, foff(F_ERR), 0, 1u); dead = true; break; }
    }
    CFENCE();
}

DEV bool boot_ok(const Params& p, u32 off, v4u v, u32 want) { return all_(off >= OOB || (v.x == p.e0 && v.y == p.e1 && v.z == p.e2 && v.w >= want)); }
DEV void boot_set(const Params& p, rsrc_t rs, u32 off, u32 v) { st16(rs, off, (v4u){p.e0, p.e1, p.e2, v}); }
DEV u32 boot_wait(const Params& p, rsrc_t rs, u32 off, u32 want, bool& dead) {
    v4u v;
#pragma unroll 1
    for (int n = 0;; ++n) {
        CFENCE();
        v = ld16s(rs, off, 0);
        if (boot_ok(p, off, v, want) || dead) break;
        __builtin_amdgcn_s_sleep(1);
        if (n > SPIN_CAP) { st4s(rs, foff(F_ERR), 0, 1u); dead = true; }
    }
    CFENCE();
    return v.w;
}

DEV u32 pol_off() { const int lane = threadIdx.x & 63; return lane < 32 ? foff(F_RG + lane) : OOB; }
DEV u32 pol_of(u32 raw) {
    const int lane = threadIdx.x & 63;
    const float v = add16(__builtin_amdgcn_fmed3f((float)((int)raw - (1 << 20)), -250.f, 250.f));
    const float lo = dpp<0x121>(v), hi = dpp<0x12F>(v), ex = dpp<0x122>(v) + dpp<0x123>(v) + dpp<0x124>(v);
    const float sum = v + lo + ((lane & 15) == 11 ? 0.f : hi) + ((lane & 15) == 0 ? ex : 0.f);
    return (u32)__builtin_amdgcn_ballot_w64(sum >= 0.f) & 0xFFFFu;
}
DEV u32 get_pol(const Params& p, rsrc_t rs, bool& dead) { return pol_of(boot_wait(p, rs, pol_off(), 0u, dead)); }
DEV u32 hblk(u32 base, int ls, int h, u32 side, u32 pol) { return near_(base + (u32)(ls * NH + h) * 16384u, side, pol); }
DEV f32x2 ln_stats(const f32x2 (&x)[5]) {
    const float c = f_(uu(u_(x[0].x)));
    f32x2 d[5];
#pragma unroll
    for (int i = 0; i < 5; ++i) d[i] = x[i] - c;
    const f32x2 s2 = (d[0] + d[1]) + (d[2] + d[3]) + d[4], q = (d[0] * d[0] + d[1] * d[1]) + (d[2] * d[2] + (d[3] * d[3] + d[4] * d[4]));
    const float dm = wave_sum(s2.x + s2.y) * (1.0f / DM), mean = c + dm;
    const float var = fmaxf(wave_sum(q.x + q.y) * (1.0f / DM) - dm * dm, 0.f);
    const float sd = rbf(__builtin_amdgcn_sqrtf(rbf(rbf(var) + 1e-5f)));
    return (f32x2){rbf(mean), __builtin_amdgcn_rcpf(sd)};
}
DEV void store_h(rsrc_t rs, u32 tile, int row, const u32 (&hp)[5], u32 off = 0) {
    const u32 lane = threadIdx.x & 63;
    const u32 sw = (u32)(row & 15) * 16u | off, o8 = (lane >> 5) * 16384u + (((lane & 31u) * 8u) ^ sw), hb = tile + (u32)(row & 31) * 256u;
    st8s(rs, o8, hb, (v2u){hp[0], hp[1]});
    st8s(rs, o8, hb + 32768, (v2u){hp[2], hp[3]});
    st4s(rs, (lane * 4) ^ sw, hb + 65536, hp[4]);
}

DEV void ln_row(rsrc_t rs, int row, v2u a, v2u b, u32 c) {
    const f32x2 x[5] = {up2(a.x), up2(a.y), up2(b.x), up2(b.y), up2(c)};
    const f32x2 st = ln_stats(x);
    u32 hp[5];
#pragma unroll
    for (int q = 0; q < 5; ++q) hp[q] = pk2(up2(pk2(x[q] - st.x)) * st.y);
    store_h(rs, W_H1 + (u32)(row >> 5) * H_TILE, row, hp);
}

#define DMA16(ldsb, vo, so) asm volatile("s_mov_b32 m0, %0\n\ts_nop 0\n\tbuffer_load_dwordx4 %1, %2, %3 offen sc1 lds" :: "s"(ldsb), "v"(vo), "s"(srd), "s"(so) : "memory")
#define DMA16I(ldsb, lo, vo, so, go) do { u32 so_; asm volatile("s_add_u32 m0, %1, %5\n\ts_add_u32 %0, %4, %6\n\tbuffer_load_dwordx4 %2, %3, %0 offen sc1 lds" \
                         : "=&s"(so_) : "s"(ldsb), "v"(vo), "s"(srd), "s"(so), "i"(lo), "i"(go) : "memory", "scc"); } while (0)
#define DMA32(ldsb, vo, so) asm volatile("s_mov_b32 m0, %0\n\ts_nop 0\n\tbuffer_load_dwordx4 %1, %2, %3 offen sc1 lds\n\tbuffer_load_dwordx4 %1, %2, %3 offen offset:1024 sc1 lds" \
                         :: "s"(ldsb), "v"(vo), "s"(srd), "s"(so) : "memory")
#define INPOL "nt"
#define OUTAUX 16
#define INAUX 2
#define DMANT(sr, ldsb, vo, so) asm volatile("s_mov_b32 m0, %0\n\ts_nop 0\n\tbuffer_load_dwordx4 %1, %2, %3 offen " INPOL " lds" :: "s"(ldsb), "v"(vo), "s"(sr), "s"(so) : "memory")
#define DMAIN(sr, ldsb, vo, so) asm volatile("s_mov_b32 m0, %0\n\ts_nop 0\n\tbuffer_load_dwordx4 %1, %2, %3 offen lds" :: "s"(ldsb), "v"(vo), "s"(sr), "s"(so) : "memory")
DEV const u16* sel(bool c, const u16* a, const u16* b) { PINS(a); PINS(b); return c ? a : b; }
DEV v4u mk_srd(const void* q, u32 bytes) { return (v4u){uu((u32)(size_t)q), uu((u32)((size_t)q >> 32) & 0xffffu), bytes, 0x00020000u}; }
#define MK_SRD() const v4u srd = mk_srd(p.ws, W_END)
DEV u32 code_fetch() {
    unsigned long long pc; int back, len; u32 d;
    asm volatile("s_getpc_b64 %0\n9:\n\ts_mov_b32 %1, 9b-.Lk_main_here\n\ts_mov_b32 %2, .Lk_end_here-.Lk_main_here" : "=s"(pc), "=s"(back), "=s"(len));
    asm volatile("buffer_load_dword %0, %1, %2, 0 offen" : "=v"(d) : "v"((((blockIdx.x >> 3) & 31u) * 256u + threadIdx.x) * 16u), "s"(mk_srd((const void*)(pc - back), (u32)max(len, 0))) : "memory");
    return d;
}

constexpr int CV_PAD = 40;
constexpr int CV_PL = 4416;
constexpr int CV_F = 8 * CV_PL;
constexpr int CV_KB = 80;
constexpr int CV_KV = 65 * CV_KB;
constexpr int CV_CH = CV_F + CV_KV;
static_assert(CV_CH % 16 == 0 && 4 * CV_CH <= LDS_BYTES, "conv lds");

struct Filt { v4u a[5], b[5]; };
DEV int chan(int grp, int w) { return 16 * (grp >> 2) + (grp & 3) + 4 * w; }
template <int N = 5, int AUX = INAUX> DEV void filt_load(const Params& p, int grp, Filt& f) {
    const int tid = threadIdx.x, lane = tid & 63, d = chan(grp, tid >> 6);
    const rsrc_t rf = mk_rsrc(p.ssmk, ACT);
#pragma unroll
    for (int i = 0; i < N; ++i) {
        const u32 s0 = (u32)(8 * (lane + 64 * i) - CV_PAD);
        f.a[i] = __builtin_amdgcn_raw_buffer_load_b128(rf, s0 < SEQ ? (u32)(d * SEQ + s0) * 2u : OOB, 0, AUX);
        f.b[i] = __builtin_amdgcn_raw_buffer_load_b128(rf, s0 + 8 < SEQ ? (u32)(d * SEQ + s0 + 8) * 2u : OOB, 0, AUX);
    }
}
constexpr int KT_BYTES = 18 * 1024, KT_X = 64 * 144, NKV = 44, KT_DUMP = 8 * KT_BYTES + KT_X;
DEV void kv_fetch(const Params& p, int tile, u32 dst) {
    const int lane = threadIdx.x & 63, w = uni(threadIdx.x >> 6);
    const v4u sk = mk_srd(p.bk, ACT), sv = mk_srd(p.bv, ACT), ss = mk_srd(sel(w == 0, p.bk, p.shift), w == 0 ? ACT : DM * 8);
    const u32 vo = tile < 320 ? (u32)((lane >> 3) * DM + (tile % 10) * 64 + (lane & 7) * 8) * 2u : OOB, r0 = uu((u32)((tile / 10) * 64 + 8 * w) * (DM * 2)), d = uu(dst + w * 1024);
    DMANT(sk, d + 1024, vo, r0); DMANT(sk, d + 5120, vo, r0 + 32 * DM * 2); DMANT(sv, d + 9216, vo, r0); DMANT(sv, d + 13312, vo, r0 + 32 * DM * 2);
    DMAIN(ss, uu(w == 0 ? dst : w == 1 ? dst + 17408 : KT_DUMP), w == 0 ? (tile >= 10 ? vo : OOB) : w == 1 && lane < 32 ? (u32)lane * 16u : OOB,
          uu(w == 0 ? (tile >= 10 ? r0 - 8 * DM * 2 : 0u) : (u32)(tile % 10) * 512u));
}
DEV void kv_tile(const Params& p, rsrc_t rs, lptr src, lptr xt, int tile) {
    const int tid = threadIdx.x;
    const v4u sv = lds_r16(src + 17408 + (tid & 31) * 16);
    lptr kp = src + ((tid >> 5) * 8 + 5) * 128 + (tid & 31) * 4;
    f32x2 kf[11];
#pragma unroll
    for (int i = 0; i < 11; ++i) kf[i] = up2(*(LAS u32*)(kp + i * 128));
    const f32x2 sh[4] = {{lo_f(sv.x), lo_f(sv.z)}, {hi_f(sv.x), hi_f(sv.z)}, {lo_f(sv.y), lo_f(sv.w)}, {hi_f(sv.y), hi_f(sv.w)}};
    u32 kv[8];
#pragma unroll
    for (int i = 0; i < 8; ++i) {
        const f32x2 acc = rb2(rb2(rb2(rb2(kf[i] * sh[3]) + rb2(kf[i + 1] * sh[2])) + rb2(kf[i + 2] * sh[1])) + rb2(kf[i + 3] * sh[0]));
        kv[i] = pk2(acc * up2(*(LAS u32*)(kp + 9216 + (i - 5) * 128)));
    }
    lds_w16(xt + (2 * (tid & 31)) * 144 + (tid >> 5) * 16, (v4u){__builtin_amdgcn_perm(kv[6], kv[7], 0x05040100u), __builtin_amdgcn_perm(kv[4], kv[5], 0x05040100u),
                                                             __builtin_amdgcn_perm(kv[2], kv[3], 0x05040100u), __builtin_amdgcn_perm(kv[0], kv[1], 0x05040100u)});
    lds_w16(xt + (2 * (tid & 31) + 1) * 144 + (tid >> 5) * 16, (v4u){__builtin_amdgcn_perm(kv[6], kv[7], 0x07060302u), __builtin_amdgcn_perm(kv[4], kv[5], 0x07060302u),
                                                                 __builtin_amdgcn_perm(kv[2], kv[3], 0x07060302u), __builtin_amdgcn_perm(kv[0], kv[1], 0x07060302u)});
    BAR();
#pragma unroll
    for (int i = 0; i < 2; ++i) {
        const int ch = i * 32 + (tid >> 3);
        st16(rs, W_KVT + (u32)(((tile % 10) * 64 + ch) * SEQ + (tile / 10) * 64 + (tid & 7) * 8) * 2u, lds_r16(xt + ch * 144 + (tid & 7) * 16));
    }
}
template <int MODE> DEV void conv_job(const Params& p, rsrc_t rs, lptr lds, int grp, Filt& f, int next, u32 fo2 = OOB, u32 fv2 = 0) {
    const int tid = threadIdx.x, lane = tid & 63, w = uni(tid >> 6), bq = lane & 31, hh = lane >> 5;
    constexpr bool LO = MODE & 1, EARLY = MODE == 0 || MODE == 3, MINI = MODE == 4;
    constexpr int CV_PL = MINI ? 1088 : ::CV_PL, CV_F = 8 * CV_PL, CV_X = MINI ? CV_F + 17 * CV_KB : 3072, CV_CH = MINI ? CV_X + 1024 : ::CV_CH;
    lptr F = lds + w * CV_CH;
    lptr fa = F + (bq & 7) * CV_PL + ((bq >> 3) - hh + 4) * 16;
    lptr kb = F + CV_F + CV_KB + hh * 16, kh = kb + (bq + 32) * CV_KB;
    bool dead = false;
    v4u kvr[4];
    const u32 D = p.ssmD[chan(grp, w)];
    auto kv_load = [&]() {
        if (EARLY) boot_wait(p, rs, lane < NKV ? foff(F_KV + lane) : OOB, 2u, dead);
#pragma unroll
        for (int i = 0; i < (LO ? 2 : 4); ++i) kvr[i] = ld16s(rs, (u32)lane * 16u, W_KVT + (u32)(chan(grp, w) * SEQ + i * 512) * 2u);
    };
    auto windows = [&](int i) {
        if (i < 4 || lane < 16) {
            const u32 e[8] = {f.a[i].x, f.a[i].y, f.a[i].z, f.a[i].w, f.b[i].x, f.b[i].y, f.b[i].z, f.b[i].w};
            u32 o[7];
#pragma unroll
            for (int q = 0; q < 7; ++q) o[q] = __builtin_amdgcn_alignbit(e[q + 1], e[q], 16);
#pragma unroll
            for (int q = 0; q < 4; ++q) {
                lds_w16(F + (2 * q) * CV_PL + (64 * i + lane) * 16, (v4u){o[q], o[q + 1], o[q + 2], o[q + 3]});
                lds_w16(F + (2 * q + 1) * CV_PL + (64 * i + lane) * 16, (v4u){e[q + 1], e[q + 2], e[q + 3], e[q + 4]});
            }
        }
    };
    lptr yw = lds + (lane >> 4) * CV_CH + CV_X + (lane & 14) * 64 + w * 32 + (lane & 1) * 16, yr = F + CV_X + lane * 16;
    const u32 yo = W_Y + (u32)((((w & 1) * 8 + (lane >> 3)) * 8 + (w >> 1)) * DM + (grp & 3) * 160 + 4 * (grp >> 2)) * 32u + (u32)(lane & 7) * 16u;
    v4u dg[2];
    auto mf = [](v4u a, v4u b, f32x16 c) {
        if (!MINI) return mfma32(a, b, c);
        asm volatile("s_nop 1\n\tv_mfma_f32_32x32x16_bf16 %0, %1, %2, %0" : "+v"(c) : "v"(a), "v"(b));
        return c;
    };
    auto skip = [&](lptr k) { const f32x16 z = {}; return mf(lds_r16(k + 32), dg[1], mf(lds_r16(k), dg[0], z)); };
    auto pack = [&](const f32x16& ac, const f32x16& sk, int q) {
        const int r = 4 * (q & 3);
        u32 v[4];
#pragma unroll
        for (int i = 0; i < 4; ++i) v[i] = pk2(rb2((f32x2){ac[r + 2 * i], ac[r + 2 * i + 1]}) + rb2((f32x2){sk[r + 2 * i], sk[r + 2 * i + 1]}));
        const auto x = __builtin_amdgcn_permlane32_swap(v[0], v[2], false, false), y = __builtin_amdgcn_permlane32_swap(v[1], v[3], false, false);
        return (v4u){x[0], y[0], x[1], y[1]};
    };
    auto first = [&]() { asm volatile("s_waitcnt vmcnt(0)\n\ts_barrier" ::: "memory"); st4s(rs, EARLY ? (tid == 0 ? foff(F_CV + grp) : OOB) : fo2, 0, EARLY ? 1u : fv2); EV(12); };
    if (MODE != 2 && !MINI) EV(8);
    if (MINI) kvr[0] = f.a[4]; else if (!EARLY) kv_load();
    if (lane < CV_KB / 16) lds_w16(F + CV_F + lane * 16, (v4u){0u, 0u, 0u, 0u});
    windows(0);
    if (!MINI) { windows(1); windows(2); }
    if (!LO && !MINI) windows(4);
    if (EARLY) kv_load();
    if (MODE == 2) windows(3);
    if (MODE != 2 && !MINI) EV(9);
#pragma unroll
    for (int i = 0; i < (MINI ? 1 : LO ? 2 : 4); ++i) lds_w16(F + CV_F + (16 * i + (lane >> 2) + 1) * CV_KB + (lane & 3) * 16, kvr[i]);
#pragma unroll
    for (int ks = 0; ks < 2; ++ks)
#pragma unroll
        for (int q = 0; q < 4; ++q) { const int j = 16 * ks + 8 * hh + 7 - bq; dg[ks][q] = j == 2 * q ? D : j == 2 * q + 1 ? D << 16 : 0u; }
    f32x16 acc0 = {}, acc1 = {};
    if (MODE == 1) first();
    if (EARLY || MINI) {
        const int bk = MINI ? bq & 15 : bq;
        v4u fr[2][4];
        auto rd = [&](int m) { lptr kl = kb + max(bk - m, -1) * CV_KB; fr[m & 1][0] = lds_r16(kl); fr[m & 1][1] = lds_r16(fa + m * 64); fr[m & 1][2] = lds_r16(kl + 32); fr[m & 1][3] = lds_r16(fa + m * 64 - 32); };
        rd(0); rd(1);
        f32x16 sk = skip(kb + bk * CV_KB);
#pragma unroll
        for (int m = 0; m < 8; ++m) {
            acc0 = mf(fr[m & 1][0], fr[m & 1][1], acc0); acc0 = mf(fr[m & 1][2], fr[m & 1][3], acc0);
            if (m < 6) rd(m + 2);
            SCHED();
        }
        if (MINI) asm volatile("s_nop 11" : "+v"(acc0), "+v"(sk));
        lds_w16(yw, pack(acc0, sk, 0));
        BAR();
        st16(rs, w < 2 ? yo : OOB, lds_r16(yr));
        EV(11);
        if (MINI) { BAR(); return; }
    }
    if (LO) {
        constexpr int M0 = EARLY ? 8 : 0;
        v4u a[2][2], l[2][2];
#pragma unroll
        for (int u = 0; u < 2; ++u) { lptr kl = kb + max(bq - M0 - u, -1) * CV_KB; a[u][0] = lds_r16(fa + (M0 + u) * 64); l[u][0] = lds_r16(kl); a[u][1] = lds_r16(fa + (M0 + u) * 64 - 32); l[u][1] = lds_r16(kl + 32); }
#pragma unroll 1
        for (int m = M0; m < 32; m += 2) {
            if (MODE == 3 && m == 20) first();
#pragma unroll
            for (int u = 0; u < 2; ++u) {
                lptr kl = kb + max(bq - m - u - 2, -1) * CV_KB;
                acc0 = mfma32(l[u][0], a[u][0], acc0); a[u][0] = lds_r16(fa + (m + u + 2) * 64); l[u][0] = lds_r16(kl); SCHED();
                acc0 = mfma32(l[u][1], a[u][1], acc0); a[u][1] = lds_r16(fa + (m + u + 2) * 64 - 32); l[u][1] = lds_r16(kl + 32); SCHED();
            }
        }
    } else {
        if (MODE == 0) windows(3); else filt_load<5, 0>(p, next, f);
        v4u a0 = lds_r16(fa), a1 = lds_r16(fa - 32), c0 = lds_r16(fa + 2048), c1 = lds_r16(fa + 2048 - 32);
        v4u h0 = lds_r16(kh), h1 = lds_r16(kh + 32), l0 = lds_r16(kb + bq * CV_KB), l1 = lds_r16(kb + bq * CV_KB + 32);
#pragma unroll 2
        for (int m = 0; m < (MODE == 0 ? 8 : 32); ++m) {
            lptr kl = kb + max(bq - m - 1, -1) * CV_KB;
            if (MODE == 0 && m == 4) first();
            acc1 = mfma32(h0, a0, acc1); h0 = lds_r16(kh - (m + 1) * CV_KB); a0 = lds_r16(fa + (m + 1) * 64); SCHED();
            acc1 = mfma32(l0, c0, acc1); c0 = lds_r16(fa + (m + 1) * 64 + 2048); l0 = lds_r16(kl); SCHED();
            acc1 = mfma32(h1, a1, acc1); h1 = lds_r16(kh - (m + 1) * CV_KB + 32); a1 = lds_r16(fa + (m + 1) * 64 - 32); SCHED();
            acc1 = mfma32(l1, c1, acc1); c1 = lds_r16(fa + (m + 1) * 64 + 2048 - 32); l1 = lds_r16(kl + 32); SCHED();
        }
        if (MODE == 0) {
#pragma unroll 2
            for (int m = 8; m < 32; ++m) {
                lptr kl = kb + max(bq - m - 1, -1) * CV_KB;
                acc1 = mfma32(h0, a0, acc1); h0 = lds_r16(kh - (m + 1) * CV_KB); SCHED();
                acc0 = mfma32(l0, a0, acc0); a0 = lds_r16(fa + (m + 1) * 64); SCHED();
                acc1 = mfma32(l0, c0, acc1); c0 = lds_r16(fa + (m + 1) * 64 + 2048); l0 = lds_r16(kl); SCHED();
                acc1 = mfma32(h1, a1, acc1); h1 = lds_r16(kh - (m + 1) * CV_KB + 32); SCHED();
                acc0 = mfma32(l1, a1, acc0); a1 = lds_r16(fa + (m + 1) * 64 - 32); SCHED();
                acc1 = mfma32(l1, c1, acc1); c1 = lds_r16(fa + (m + 1) * 64 + 2048 - 32); l1 = lds_r16(kl + 32); SCHED();
            }
        }
    }
    if (MODE != 2) EV(13);
    v4u yy[4];
    if (MODE != 2) { const f32x16 sk = skip(kb + bq * CV_KB); yy[0] = pack(acc0, sk, 0); yy[1] = pack(acc0, sk, 2); }
    if (!LO) { const f32x16 sk = skip(kh); yy[2] = pack(acc1, sk, 0); yy[3] = pack(acc1, sk, 2); BAR(); }
#pragma unroll
    for (int q = (MODE == 2 ? 2 : 0); q < (LO ? 2 : 4); ++q) lds_w16(yw + q * CV_PL, yy[q]);
    BAR();
#pragma unroll
    for (int q = (MODE == 2 ? 2 : 0); q < (LO ? 2 : 4); ++q) st16(rs, q == 0 && w < 2 ? OOB : yo + (u32)q * (2 * DM * 32), lds_r16(yr + q * CV_PL));
    BAR();
    if (MODE != 2) EV(14);
}

#define NPROBE 6
DEV int probe(rsrc_t rs, int c) {
    const int lane = threadIdx.x & 63;
    const u32 k = (u32)(c & 15) < NREG ? (u32)(c & 15) : 0u;
    const u32 off0 = k * (16 * MB) + 2 * MB - 65536 + ((u32)(c & 15) >= NREG ? (u32)(c & 3) + 1u : 0u) * 256 + (u32)(c >> 4) * 128;
    st4s(rs, lane == 0 ? off0 : OOB, 0, 0u); DRAIN();
    int d0 = 1 << 16, d1 = 1 << 16;
#pragma unroll 1
    for (int q = 0; q < NPROBE; ++q) {
        const u32 o = lane == 0 ? off0 + (u32)(q & 1) * 8192 + (u32)(q >> 1) * 18432 : OOB;
        const unsigned long long t0 = __builtin_amdgcn_s_memtime();
        st4s(rs, o, 0, 0u); DRAIN();
        const int dt = (int)(__builtin_amdgcn_s_memtime() - t0);
        if (q & 1) d1 = min(d1, dt); else d0 = min(d0, dt);
    }
    const u32 xcc = (u32)__builtin_amdgcn_s_getreg(20 | (3 << 11)) & 0xfu;
    return (d0 - d1) * ((xcc & 4u) ? -1 : 1);
}
DEV int pre_rings(const Params& p, rsrc_t rs, int j) {
    const u32 gt = (u32)(j * 256 + threadIdx.x) * 16u, n = ((gt >> 14) << 9) + ((gt & 8191u) >> 4);
    const v4u ps = {PSENT, PSENT, PSENT, PSENT};
    v4u m = ps;
    if (n < 640u) m = *(const v4u*)(p.mem0 + (2 * ((n >> 2) & 3) + (n & 1)) * DM + (n >> 4) * 16 + (n & 2) * 4);
    int vote = 0;
    if (uni(threadIdx.x >> 6) == 0) vote = probe(rs, j);
    BAR();
    EV(2);
#pragma unroll
    for (u32 i = 0; i < 8; ++i) st16(rs, W_ORING + gt + i * 131072u, ps);
#pragma unroll
    for (u32 i = 0; i < 2; ++i) st16(rs, gt < 16384u ? W_MRING + i * (MB / 4) + 131072u + gt : OOB, ps);
    if (n < 640u) {
        m = (v4u){__builtin_amdgcn_perm(m.z, m.x, 0x05040100u), __builtin_amdgcn_perm(m.z, m.x, 0x07060302u), __builtin_amdgcn_perm(m.w, m.y, 0x05040100u), __builtin_amdgcn_perm(m.w, m.y, 0x07060302u)};
#pragma unroll
        for (int i = 0; i < 4; ++i) m[i] = m[i] == PSENT ? 0x7FC17FC1u : m[i];
    }
    st16(rs, W_MRING + gt, m);
    st16(rs, W_MRING + MB / 4 + gt, m);
    static_assert(2 * (F_N - F_QKV) == 32 * 36, "clear");
    st4s(rs, threadIdx.x < 36 ? foff(F_QKV) + (u32)(j * 36 + threadIdx.x) * 128u : OOB, 0, 0u);
    return vote;
}

#define M32A(first, d, a, b) do { if (first) asm volatile("v_mfma_f32_32x32x16_bf16 %0, %1, %2, 0" : "=&v"(d) : "a"(a), "v"(b)); \
                                  else asm volatile("v_mfma_f32_32x32x16_bf16 %0, %1, %2, %0" : "+v"(d) : "a"(a), "v"(b)); } while (0)
#define M32B(first, d, a, b) do { if (first) asm volatile("v_mfma_f32_32x32x16_bf16 %0, %1, %2, 0" : "=&v"(d) : "v"(a), "a"(b)); \
                                  else asm volatile("v_mfma_f32_32x32x16_bf16 %0, %1, %2, %0" : "+v"(d) : "v"(a), "a"(b)); } while (0)
#define VCVT(d, a, b) asm volatile("v_cvt_pk_bf16_f32 %0, %1, %2" : "=v"(d) : "v"(a), "v"(b))
#define LD4A(d, vo, so) asm volatile("buffer_load_dword %0, %1, %2, %3 offen sc1" : "=v"(d) : "v"(vo), "s"(srd), "s"(so) : "memory")
DEV u32 wst_src(int lane, int ld) { return (u32)((lane >> 4) * ld + 8 * ((lane & 15) ^ ((lane >> 4) << 2))) * 2u; }
DEV u32 wst_rd(int lane, int w) { const int q4 = (lane >> 2) & 3; return (u32)(q4 * 256 + ((w ^ q4) * 64) + ((lane >> 4) & 1) * 32 + (lane & 3) * 8); }

constexpr int QK_A = 65536;
#define QLEAD 3
#define QFIRST 2
static_assert(2 * QFIRST <= 12, "the first rounds stay within the first LN rows");
static_assert(QFIRST < QLEAD, "a round of the lead carries the flag of the convolution");
#define QW0 10
#define QW1 3
#define QSAMP 72
#define QPK1(d, a, a_, q_) do { if constexpr (V) VCVT(d[q_], a[4 * (a_) + 8 * ((q_) >> 1) + 2 * ((q_) & 1)], a[4 * (a_) + 8 * ((q_) >> 1) + 2 * ((q_) & 1) + 1]); else VCVT(d[q_], a[4 * (a_) + (q_)], a[4 * (a_) + (q_) + 8]); } while (0)
#define QSC(a, i) asm volatile("v_mul_f32 %0, %2, %0\n\tv_mul_f32 %1, %2, %1" : "+v"(a[i]), "+v"(a[(i) + 8]) : "s"(ksc))
#define QPK(d, a, a_, q_) do { if constexpr (!V) { QSC(a, 4 * (a_) + (q_)); QSC(a, 4 * (a_) + (q_) + 1); } QPK1(d, a, a_, q_); QPK1(d, a, a_, (q_) + 1); } while (0)
DEV void role_qkv(const Params& p, lptr lds, int c) {
    const int grp = 64 + c;
    const int tid = threadIdx.x, lane = tid & 63, w = uni(tid >> 6), hh = lane >> 5, r32 = lane & 31;
    const int m = c >> 5, h = c & 31;
    const rsrc_t rs = mk_rsrc(p.ws, W_END);
    v4u wt[40];
    Filt flt;
    bool dead = false;
    const u32 ksc = m == 1 ? 0x3e0293eeu : 0x3f800000u;
    u32 pol, h2flag, fo, ol, ar[8];
    const u32 ds = uu(w * 1024), dx = ds + QK_A;
#define XRD(ks) lds_r16(lds + ar[(ks) & 7] + ((ks) >> 3) * 8192)
    const u32 t16 = (u32)tid * 16u, lnf = tid < 96 ? foff(F_LN + tid) : OOB;
    MK_SRD();
#define QDMA(i, dst, vo, so) DMA16I(dst, (i) * 4096, vo, so, ((i) >> 1) * 16384 + ((i) & 1) * 4096)
    {
        const v4u sw = mk_srd(sel(m == 0, p.Wq, sel(m == 1, p.Wk, p.Wv)), DM * NQ * 2), sx = mk_srd(p.x, ACT);
        const u32 vo = wst_src(lane, NQ) + (u32)h * (DH * 2), x8 = (u32)lane * 8u, x4 = (u32)lane * 4u;
        v2u xa[6], xb[6]; u32 xc[6];
#pragma unroll
        for (int i = 0; i < 6; ++i)
            asm volatile("buffer_load_dwordx2 %0, %3, %5, %6 offen\n\tbuffer_load_dwordx2 %1, %3, %5, %6 offen offset:512\n\tbuffer_load_dword %2, %4, %5, %6 offen offset:1024"
                         : "=&v"(xa[i]), "=&v"(xb[i]), "=&v"(xc[i]) : "v"(x8), "v"(x4), "s"(sx), "s"(uu(min(4 * c + w + 384 * i, SEQ - 1) * (DM * 2))) : "memory");
        EV(1);
        auto wfill = [&](int i0, int n) {
#pragma unroll 1
            for (int i = i0; i < i0 + n; ++i) DMAIN(sw, uu((4 * i + w) * 1024), vo, uu((16 * i + 4 * w) * (NQ * 2)));
        };
        const int bl = (int)((sw.x >> 8) + h) & 31, hn = (bl + 2) & 3;
        const u32 va = hn == 3 || bl == 31 ? OOB : vo + (u32)((((h + 3 - hn) & 3) - (h & 3)) * 256), vb = hn == 3 || (bl >> 2) != 6 ? OOB : vo + (u32)((31 - bl) * 256);
        wfill(0, QW0);
        asm volatile("s_waitcnt vmcnt(%0) ; HZCLR" :: "i"(QW0) : "memory");
        EV(2);
#pragma unroll
        for (int i = 0; i < 6; ++i) {
            wfill(QW0 + QW1 * i, QW1);
            DMAIN(sw, uu((4 * 39 + w) * 1024), i < 4 ? va : vb, uu((16 * (39 - 3 * (i & 3) - hn) + 4 * w) * (NQ * 2)));
            if (i == 2) { VMW(2 * QW1 + 5); BAR(); boot_set(p, rs, tid == 0 ? foff(F_LN + c) : OOB, 1u); }
            asm volatile("" : "+v"(xa[i]), "+v"(xb[i]), "+v"(xc[i]));
            const int row = 4 * c + w + 384 * i;
            if (i < 5 || row < SEQ) {
                ln_row(rs, row, xa[i], xb[i], xc[i]);
            }
        }
        EV(3);
        constexpr int K0 = QW0 + 6 * QW1;
        static_assert(K0 * 4096 >= QK_A + 40960, "the first tile's place is free");
        v4u fv, praw;
        asm volatile("buffer_load_dwordx4 %0, %2, %4, 0 offen sc1\n\tbuffer_load_dwordx4 %1, %3, %4, 0 offen sc1" : "=&v"(praw), "=&v"(fv) : "v"(pol_off()), "v"(lnf), "s"(srd) : "memory");
        const u32 code = code_fetch();
        wfill(K0, 40 - K0);
        asm volatile("s_waitcnt vmcnt(%2) ; HZCLR\n\ts_barrier" : "+v"(praw), "+v"(fv) : "i"(40 - K0), "v"(code) : "memory");
        boot_set(p, rs, tid == 0 ? foff(F_LN + c) : OOB, 2u);
        EV(4);
        lptr tb = lds + hh * 2048 + wst_rd(lane, w);
#define WCV(k) do { lptr q_ = tb + ((k) >> 3) * 32768 + ((k) & 7) * 4096; wt[k] = tr2(q_, q_ + 1024); } while (0)
        SFOR(k, K0) WCV(k); asm volatile("" : "+a"(wt[k])); SCHED(); SEND
        if (!boot_ok(p, lnf, fv, 1u)) boot_wait(p, rs, lnf, 1u, dead);
        if (!boot_ok(p, pol_off(), praw, 0u)) praw.w = boot_wait(p, rs, pol_off(), 0u, dead);
        BAR();
        EV(5);
        { const u32 so = W_H1; SFOR(i, 10) QDMA(i, ds, t16, so); SEND }
        { const u32 so = W_H1 + H_TILE; SFOR(i, 10) QDMA(i, dx, t16, so); SEND }
        pol = pol_of(praw.w);
        h2flag = tid < 8 ? near_(foff(F_H2 + lane), 1u, pol) : OOB;
        ol = (m == 2 ? (u32)(2 * w + (r32 >> 4)) * 1024u : (u32)((r32 >> 4) * 4 + w) * 1024u) + (u32)(hh * 16 + (r32 & 15)) * 16u;
        fo = near_(foff(F_QKV + m * 64 + h), 0u, pol);
        u32 oa = ol + near_((m == 2 ? W_VO : W_KQ) + (u32)h * 16384u, m == 1 ? 0u : 1u, pol);
#pragma unroll
        for (int j = 0; j < 8; ++j) { ar[j] = (u32)(r32 * 256 + ((2 * j + hh) ^ (r32 & 15)) * 16); PIN(ar[j]); }
        PIN(h2flag); PIN(ol); PIN(oa); PINS(fo); PINS(pol);
        VMW(10); BAR();
        EV(6);
        auto first = [&](auto isv) __attribute__((always_inline)) {
            constexpr bool V = decltype(isv)::value;
            v4u x[8], pp, pq; f32x16 a;
#pragma unroll
            for (int ks = 0; ks < 5; ++ks) x[ks] = XRD(ks);
            SFOR(ks, 40)
                if constexpr (V) M32B(ks == 0, a, x[ks & 7], wt[ks]); else M32A(ks == 0, a, wt[ks], x[ks & 7]);
                if constexpr (ks < 35) x[(ks + 5) & 7] = XRD(ks + 5);
                if constexpr (ks % 2 == 0 && K0 + ks / 2 < 40) WCV(K0 + ks / 2);
                SCHED();
                if constexpr (ks >= 3 && ks % 2 == 1 && K0 + (ks - 3) / 2 < 40) asm volatile("" : "+a"(wt[K0 + (ks - 3) / 2]));
                if constexpr (ks >= 27 && ks < 35) asm volatile("v_xor_b32 %0, 0x10000, %0" : "+v"(ar[(ks + 5) & 7]));
                SCHED();
            SEND
            NOPS(11);
            QPK(pp, a, 0, 0); QPK(pp, a, 0, 2); st16(rs, oa, pp);
            QPK(pq, a, 1, 0); QPK(pq, a, 1, 2); st16(rs, oa + 512, pq);
        };
        if (m == 2) first(IC<1>{}); else first(IC<0>{});
    }
    int n0 = 0, n1 = -QLEAD;
    const u32 bfo = tid < 192 ? lnf : tid - 192u < NKV ? foff(F_KV - 192 + tid) : OOB;
    v4u brk = {};
    auto run = [&](auto isv) __attribute__((always_inline)) {
      constexpr bool V = decltype(isv)::value;
      v4u x[8] = {};
      f32x16 a0 = {};
      bool fresh = true;
      u32 fv = 0, fo0 = tid == 192 ? fo : n1 > -QLEAD && tid == 194 ? foff(F_CV + grp) : OOB, fo1 = tid == 192 ? fo : tid == 193 ? fo + 32 * 128 : OOB;
      PIN(fo0); PIN(fo1);
      if (n1 == -QLEAD) { DRAIN(); BAR(); st4s(rs, fo0, 0, 1u); EV(7); }
#define QS(k) do {} while (0)
#pragma unroll 1
      for (const int end = n1 == -QLEAD ? QFIRST - QLEAD : NSEG; n1 < end; ++n1) {
        const int cur = n1 < 0 ? n0 + 1 : 64 | n1, nxt = n1 < 0 ? n0 + 2 : n0 + 1;
        u32 xs = n1 < 0 ? W_H1 + (u32)cur * H_TILE : near_(W_H2 + (u32)n1 * H_TILE, 1u, pol);
        PINS(xs);
        QS(0);
        if (w == 0 && n1 >= 0 && ((u32)__builtin_amdgcn_ballot_w64(fv <= (u32)n1) & 255u) != 0u) wait_flags(rs, h2flag, (u32)n1 + 1, dead);
        BAR();
        QS(1);
        f32x16 a1;
        u32 ss, sv, hb, oa, qfo, qfv;
        v4u pp, pq;
        SCHED();
        auto slot = [&](auto zc) __attribute__((always_inline)) {
            constexpr int z = decltype(zc)::value, ks = (z + 20) % 40;
            if constexpr (z < 20) { if constexpr (V) M32B(false, a0, x[ks & 7], wt[ks]); else M32A(false, a0, wt[ks], x[ks & 7]); }
            else if constexpr (z < 60) { if constexpr (V) M32B(z == 20, a1, x[ks & 7], wt[ks]); else M32A(z == 20, a1, wt[ks], x[ks & 7]); }
            else if constexpr (z == 60) {
                if constexpr (V) asm volatile("v_mfma_f32_32x32x16_bf16 %0, %1, %2, 0" : "+v"(a0) : "v"(x[ks & 7]), "a"(wt[ks]));
                else asm volatile("v_mfma_f32_32x32x16_bf16 %0, %1, %2, 0" : "+v"(a0) : "a"(wt[ks]), "v"(x[ks & 7]));
            } else { if constexpr (V) M32B(false, a0, x[ks & 7], wt[ks]); else M32A(false, a0, wt[ks], x[ks & 7]); }
            if constexpr (z == 18) { QS(2); asm volatile("s_waitcnt vmcnt(6) lgkmcnt(0)\n\ts_barrier" ::: "memory"); QS(3); }
            if constexpr (z < 15 || z > 20) x[(ks + 5) & 7] = XRD((ks + 5) % 40);
            if constexpr (z >= 18 && z <= 20) { x[2 * (z - 18)] = XRD(2 * (z - 18)); x[2 * (z - 18) + 1] = XRD(2 * (z - 18) + 1); }
            SCHED();
            if constexpr (z < 4) QDMA(z, dx, t16, xs);
            if constexpr (z >= 5 && z < 17 && (z & 1)) QDMA((z + 3) >> 1, dx, t16, xs);
            if constexpr ((z >= 7 && z < 15) || (z >= 47 && z < 55)) asm volatile("v_xor_b32 %0, 0x10000, %0" : "+v"(ar[(ks + 5) & 7]));
            if constexpr (z == 16) { ss = W_H1 + (u32)nxt * H_TILE; PINS(ss); }
            if constexpr (z == 17) { sv = t16 | (nxt >= NSEG ? OOB : 0u); PIN(sv); }
            if constexpr (z >= 21 && z < 41 && (z & 1)) QDMA((z - 21) >> 1, ds, sv, ss);
            if constexpr (z == 22 || z == 24) QPK(pp, a0, 0, z - 22);
            if constexpr (z == 26 || z == 28) QPK(pq, a0, 1, z - 26);
            if constexpr (z == 30) { QS(4); asm volatile("s_waitcnt vmcnt(5)\n\ts_barrier" ::: "memory"); QS(5); }
            if constexpr (z == 32 || z == 44) { hb = (m == 2 ? W_VO : W_KQ) + (u32)((z == 32 ? n0 : cur) * NH + h) * 16384u; PINS(hb); }
            if constexpr (z == 34 || z == 45) { hb = near_(hb, m == 1 ? 0u : 1u, pol); PINS(hb); }
            if constexpr (z == 36) { hb = n0 < NSEG && !fresh ? hb : OOB; PINS(hb); }
            if constexpr (z == 38 || z == 46) { oa = ol + hb; PIN(oa); }
            if constexpr (z == 41) st16(rs, oa, pp);
            if constexpr (z == 43) st16(rs, oa + 512, pq);
            if constexpr (z == 54) { QS(6); asm volatile("s_waitcnt vmcnt(4)\n\ts_barrier" ::: "memory"); QS(7); }
            if constexpr (z == 56) { if (__builtin_expect(n1 == QFIRST - QLEAD - 1, 0)) { filt_load<3, 0>(p, grp, flt); brk = ld16s(rs, bfo, 0); } }
            if constexpr (z == 60) NOPS(5);
            if constexpr (z == 63) st16(rs, oa, pp);
            if constexpr (z == 65) st16(rs, oa + 512, pq);
            if constexpr (z == 61 || z == 62) QPK(pp, a1, 0, 2 * (z - 61));
            if constexpr (z == 63 || z == 64) QPK(pq, a1, 1, 2 * (z - 63));
            if constexpr (z == 67) { qfo = n1 >= 0 ? fo1 : fo0; PIN(qfo); }
            if constexpr (z == 68) { qfv = tid == 193 ? (u32)n1 + 1u : (u32)min(nxt, NSEG); PIN(qfv); }
            if constexpr (z == QSAMP) LD4A(fv, h2flag, 0);
            SCHED();
        };
        if (__builtin_expect(!fresh, 1)) static_for<0, 16>(slot);
        static_for<16, 80>(slot);
        QS(8);
        asm volatile("s_waitcnt vmcnt(1)\n\ts_barrier" ::: "memory");
        st4s(rs, qfo, 0, qfv);
        QS(9);
        asm volatile("s_waitcnt vmcnt(1)" : "+v"(fv) :: "memory");
        n0 = nxt; fresh = false;
      }
      if (n1 < 0) {
        SFOR(z, 20)
            if constexpr (V) M32B(false, a0, x[(z + 20) & 7], wt[z + 20]); else M32A(false, a0, wt[z + 20], x[(z + 20) & 7]);
            if constexpr (z < 15) x[(z + 25) & 7] = XRD(z + 25);
            if constexpr (z >= 7 && z < 15) asm volatile("v_xor_b32 %0, 0x10000, %0" : "+v"(ar[(z + 25) & 7]));
            SCHED();
        SEND
        u32 oa = ol + near_((m == 2 ? W_VO : W_KQ) + (u32)(n0 * NH + h) * 16384u, m == 1 ? 0u : 1u, pol);
        v4u pp, pq;
        asm volatile("s_nop 11" : "+v"(oa));
        QPK(pp, a0, 0, 0); QPK(pp, a0, 0, 2); st16(rs, oa, pp);
        QPK(pq, a0, 1, 0); QPK(pq, a0, 1, 2); st16(rs, oa + 512, pq);
      }
    };
#pragma unroll 1
    for (;;) {
        if (m == 2) run(IC<1>{}); else run(IC<0>{});
        if (n1 > 0) break;
        if (!boot_ok(p, bfo, brk, 2u)) boot_wait(p, rs, bfo, 2u, dead);
        BAR();
        conv_job<1>(p, rs, lds, grp, flt, 0, tid == 1 ? fo : OOB, (u32)n0 + 1u);
        const u32 so = uu(W_H1 + (u32)(n0 + 1) * H_TILE);
        SFOR(i, 10) QDMA(i, dx, t16, so); SEND
    }
}

constexpr int OU_SEL = 131072;
#define OSAMP 46

DEV void role_out(const Params& p, lptr lds, int L, int kq, int ng) {
    const int tid = threadIdx.x, lane = tid & 63, w = uni(tid >> 6), hh = lane >> 5, r32 = lane & 31;
    const rsrc_t rs = mk_rsrc(p.ws, W_END);
    v4u wo[64];
    u32 code;
    Filt flt;
    bool dead = false;
    {
        const v4u sw = mk_srd(p.Wo, DM * NQ * 2);
        const u32 vo = wst_src(lane, DM) + (ng == 4 ? 1024u : (u32)((ng >> 1) * 512 + (ng & 1) * 128 + ((lane ^ (lane >> 2)) & 8) * 16));
        auto fill = [&](int b) {
#pragma unroll 1
            for (int i = 0; i < 8; ++i) DMAIN(sw, uu((b % 5) * 32768 + (4 * i + w) * 1024), vo, uu((kq * 1024 + b * 128 + 16 * i + 4 * w) * (DM * 2)));
        };
#pragma unroll 1
        for (int b = 0; b < 5; ++b) fill(b);
        lptr tb = lds + hh * 1024 + wst_rd(lane, w);
        SFOR(b, 8)
            VMW(b < 3 ? 32 : 63 - 8 * b); BAR();
            lptr q = tb + (b % 5) * 32768;
#pragma unroll
            for (int k = 0; k < 8; ++k) { lptr a = q + (k >> 1) * 8192 + (k & 1) * 2048; wo[8 * b + k] = tr2(a, a + 4096); if (b < 7) asm volatile("" : "+a"(wo[8 * b + k])); else PIN(wo[8 * b + k]); }
            if constexpr (b < 3) { BAR(); fill(b + 5); }
            if constexpr (b == 2) filt_load<3, 0>(p, L == 0 ? 44 + kq * 5 + ng : 1 << 12, flt);
            if constexpr (b == 2) code = code_fetch();
        SEND
    }
    EV(6);
    const u32 pol = get_pol(p, rs, dead);
    USE(code);
    if (L == 0) {
        BAR();
        conv_job<3>(p, rs, lds, 44 + kq * 5 + ng, flt, 0);
        DRAIN(); BAR();
        st4s(rs, tid == 0 ? foff(F_CV + 44 + kq * 5 + ng) : OOB, 0, 2u);
    }
    EV(15);
    MK_SRD();
    const u32 ofl = (lane < 8 && w == 0) ? near_(foff(F_O + L * 32 + kq * 8 + lane), 0u, pol) : OOB;
    const u32 l16 = (u32)lane * 16u;
    const u32 fo = (tid == 192) ? near_(foff(F_P + L * 32 + kq * 5 + ng), 1u, pol) : OOB;
    auto osrc = [&](int s) { return uu(hblk(W_VO, L * NSEG + s, kq * 8, 0u, pol) + w * 1024); };
#define ODMA(i, dst, vo, so) DMA16I(dst, (i) * 4096, vo, so, ((i) >> 1) * 16384 + ((i) & 1) * 4096)
    const u32 pvo = (u32)(kq * 5 + ng) * 16384u + (u32)w * 2048u + l16;
    lptr tw = lds + OU_SEL + 256 + r32 * 272 + (ng == 4 ? hh * 32 + w * 4 : (w & 1) * 128 + hh * 16 + (w >> 1) * 8), tr = lds + OU_SEL + 256 + (8 * w + (lane >> 4)) * 272 + (lane & 15) * 16;
    lptr selw = lds + OU_SEL + (w == 0 ? 0 : 64);
    u32 pfo = OOB;
    u32 buf = 0;
    if (w == 0) wait_flags(rs, ofl, 1u, dead);
    BAR();
    for (int s = 0; s < NSEG; ++s) {
        u32 src = osrc(s), dst = uu(buf + w * 1024);
        lptr A = lds + buf + l16;
        asm volatile("" : "+s"(src), "+s"(dst));
#define OS(k) do {} while (0)
        OS(0);
        OS(1);
        SFOR(i, 10) ODMA(i, dst, l16, src); SEND
        OS(2);
        asm volatile("s_waitcnt vmcnt(7)\n\ts_barrier" ::: "memory");
        OS(3);
        LS2(L == 0 && w == 1, 96 + kq * 5 + ng);
        v4u o[64];
#pragma unroll
        for (int k = 0; k < 3; ++k) o[k] = lds_r16(A + k * 1024);
        st4s(rs, pfo, 0, (u32)s);
        u32 fv, pa; int rdy;
        f32x16 acc;
        SCHED();
        SFOR(k, 64)
            if constexpr (k < 56) M32A(k == 0, acc, wo[k], o[k]); else asm volatile("v_mfma_f32_32x32x16_bf16 %0, %1, %2, %0" : "+v"(acc) : "v"(wo[k]), "v"(o[k]));
            if constexpr (k < 61) o[k + 3] = lds_r16(A + (k + 3) * 1024);
            SCHED();
            if constexpr (k < 12 && (k & 1) == 0) ODMA(10 + (k >> 1), dst, l16, src);
            if constexpr (k == 7) OS(4);
            if constexpr (k == 8) OS(5);
            if constexpr (k == 23) OS(6);
            if constexpr (k == 24) OS(7);
            if constexpr (k == 43) OS(8);
            if constexpr (k == 44) OS(9);
            if constexpr (k == 59) OS(10);
            if constexpr (k == 63) OS(11);
            if constexpr (k == 7) asm volatile("s_waitcnt vmcnt(8)\n\ts_barrier" ::: "memory");
            if constexpr (k == 23) asm volatile("s_waitcnt vmcnt(4)\n\ts_barrier" ::: "memory");
            if constexpr (k == 30) { pa = pvo + near_(p_item(s), (u32)L, pol); PIN(pa); }
            if constexpr (k == 43) asm volatile("s_waitcnt vmcnt(0)\n\ts_barrier" ::: "memory");
            if constexpr (k == OSAMP) LD4A(fv, ofl, 0);
            if constexpr (k == 62) {
                asm volatile("s_waitcnt vmcnt(0)" : "+v"(fv) :: "memory");
                *(LAS int*)selw = (int)((u32)__builtin_amdgcn_ballot_w64(fv < (u32)s + 2) & 255u);
            }
            SCHED();
        SEND
        asm volatile("s_nop 11" : "+v"(acc));
        u32 pc[8];
#pragma unroll
        for (int i = 0; i < 8; ++i) pc[i] = pk(acc[2 * i], acc[2 * i + 1]);
        if (ng != 4) {
#pragma unroll
            for (int i = 0; i < 4; ++i) *(LAS v2u*)(tw + i * 32) = (v2u){pc[2 * i], pc[2 * i + 1]};
        } else {
#pragma unroll
            for (int i = 0; i < 8; ++i) *(LAS u32*)(tw + (i >> 1) * 64 + (i & 1) * 16) = pc[i];
        }
        BAR();
        const v4u p0 = lds_r16(tr), p1 = lds_r16(tr + 4 * 272);
        rdy = *(LAS int*)(lds + OU_SEL);
        st16(rs, pa, p0); st16(rs, pa + 1024, p1);
        const bool ready = uni(rdy) == 0;
        SCHED();
        OS(12);
        if (ready) pfo = fo;
        else {
            LD4A(fv, ofl, 0);
            asm volatile("s_waitcnt vmcnt(1)\n\ts_barrier" ::: "memory");
            st4s(rs, fo, 0, (u32)s + 1); pfo = OOB;
            OS(14);
            LS2(L == 0 && w == 1, 160 + kq * 5 + ng);
            asm volatile("s_waitcnt vmcnt(1)" : "+v"(fv) :: "memory");
            if (w == 0 && s + 1 < NSEG && ((u32)__builtin_amdgcn_ballot_w64(fv < (u32)s + 2) & 255u) != 0u) wait_flags(rs, ofl, (u32)s + 2, dead, L >= CALM_L && s + 2 < NSEG);
            BAR();
        }
        OS(13);
        buf ^= 65536u;
    }
}

template <int L> DEV void role_row(const Params& p, lptr lds, int ri) {
    const int lane = threadIdx.x & 63, w = uni(threadIdx.x >> 6);
    const rsrc_t rs = mk_rsrc(p.ws, W_END);
    bool dead = false;
    EV(3);
    const u32 pol = get_pol(p, rs, dead);
    const u32 fp = threadIdx.x < 20 ? near_(foff(F_P + L * 32 + lane), 1u, pol) : OOB, fo = threadIdx.x != 192 ? OOB : L == 0 ? near_(foff(F_H2 + (ri >> 2)), 1u, pol) : ri == 3 ? foff(F_EP) : OOB;
    u32 fv = 0;
    MK_SRD();
    auto yfill = [&](int b, int i) { DMA32(uu((b & 1) * 40960 + w * 10240 + i * 2048), (u32)lane * 16u, uu(W_Y + (u32)((ri >> 1) * 8 + b) * 20480u + (w & 1) * 10240 + i * 2048)); };
    auto ywait = [&](u32 v) {
        wait_flags(rs, threadIdx.x < (v == 1u ? 96u : v == 2u ? 160u : 36u) ? foff(v == 1u && threadIdx.x - 36u < 8u ? F_GO - 60 + threadIdx.x : F_CV + threadIdx.x + (v == 1u && w == 1 ? 96u : 0u)) : OOB, v, dead);
    };
    v4u y8[10] = {};
    v2u xa, xb; u32 xc;
    const v4u sx = L == 0 ? mk_srd(p.x, ACT) : mk_srd(p.ws + W_X2, ACT);
    auto xload = [&](int s) {
        asm volatile("buffer_load_dwordx2 %0, %3, %5, %6 offen sc1\n\tbuffer_load_dwordx2 %1, %3, %5, %6 offen offset:512 sc1\n\tbuffer_load_dword %2, %4, %5, %6 offen offset:1024 sc1"
                     : "=&v"(xa), "=&v"(xb), "=&v"(xc) : "v"((u32)lane * 8u), "v"((u32)lane * 4u), "s"(sx), "s"(uu((u32)(min(s, NSEG - 1) * 32 + ri) * (DM * 2))) : "memory");
        SCHED();
    };
    if (L == 1) wait_flags(rs, lane == 0 ? near_(foff(F_H2 + (ri >> 2)), 1u, pol) : OOB, 1u, dead);
    EV(5);
    xa = xb = (v2u){0u, 0u}; xc = 0u;
    for (int s = -1; s < NSEG; ++s) {
        const u32 off = s < 0 ? OOB : 0u;
        u32 xi[5] = {xa.x, xa.y, xb.x, xb.y, xc};
        u32 pa = (near_(p_item(s), (u32)L, pol) + (u32)ri * 256u) | off, l8 = (u32)lane * 8u | off, l4 = (u32)lane * 4u | off, pc = pa + 65536u + (u32)(lane >> 4) * 81920u + (u32)(lane & 15) * 16u;
        pa += (u32)(lane >> 5) * 81920u + (u32)((lane >> 4) & 1) * 16384u + (u32)(lane & 15) * 16u;
        u32 yf = s == 2 ? foff(F_CV + min(threadIdx.x, 159u)) : s == 26 ? foff(F_CV + min(lane, 35)) : OOB;
        asm volatile("" : "+v"(xi[0]), "+v"(xi[1]), "+v"(xi[2]), "+v"(xi[3]), "+v"(xi[4]), "+v"(pa), "+v"(pc), "+v"(yf), "+v"(l8), "+v"(l4));
#define RS(k) do {} while (0)
        RS(0);
        if (w == 0 && ((u32)__builtin_amdgcn_ballot_w64(fv < (u32)(s + 1)) & 0xFFFFFu) != 0u) wait_flags(rs, fp, (u32)s + 1, dead);
        BAR();
        RS(1);
        v4u d[5];
        asm volatile("buffer_load_dwordx4 %0, %5, %7, 0 offen sc1\n\tbuffer_load_dwordx4 %1, %5, %7, %8 offen sc1\n\tbuffer_load_dwordx4 %2, %5, %7, %9 offen sc1\n\t"
                     "buffer_load_dwordx4 %3, %5, %7, %10 offen sc1\n\tbuffer_load_dwordx4 %4, %6, %7, 0 offen sc1"
                     : "=&v"(d[0]), "=&v"(d[1]), "=&v"(d[2]), "=&v"(d[3]), "=&v"(d[4]) : "v"(pa), "v"(pc), "s"(srd), "s"(163840), "s"(32768), "s"(196608) : "memory");
        RS(2);
        LS2(L == 0, 192 + ri);
        SCHED();
        f32x2 y[5];
        {
            if ((s & 7) == 0 || s < 0) {
                lptr ya = lds + ((s >> 3) & 1) * 40960 + (w >> 1) * 20480 + (w & 1) * 16, yc = ya + 4096 + (lane >> 1) * 32 + (lane & 1) * 10240;
#pragma unroll
                for (int i = 0; i < 8; ++i) y8[i] = lds_r16(ya + lane * 32 + (i >> 2) * 2048 + (i & 3) * 5120);
                y8[8] = lds_r16(yc); y8[9] = lds_r16(yc + 5120);
            }
#pragma unroll
            for (int i = 0; i < 10; ++i) {
                y[i >> 1][i & 1] = lo_f(y8[i].x);
                y8[i] = (v4u){__builtin_amdgcn_alignbit(y8[i].y, y8[i].x, 16), __builtin_amdgcn_alignbit(y8[i].z, y8[i].y, 16), __builtin_amdgcn_alignbit(y8[i].w, y8[i].z, 16), y8[i].w >> 16};
                PIN(y8[i]);
            }
            asm volatile("" : "+v"(y[0]), "+v"(y[1]), "+v"(y[2]), "+v"(y[3]), "+v"(y[4]));
        }
        u32 yv;
        {
            xload(s + 1);
            const bool yy = (s & 7) >= 3 && (u32)s < NSEG - 8;
            u32 yo = yy ? (u32)lane * 16u : OOB;
            DMA32(uu(yy ? (((s >> 3) + 1) & 1) * 40960 + w * 10240 + ((s & 7) - 3) * 2048 : 81920), yo, uu(W_Y + (u32)((ri >> 1) * 8 + (s >> 3) + 1) * 20480u + (w & 1) * 10240 + ((s & 7) - 3) * 2048));
            LD4A(yv, yf, 0);
            asm volatile("s_waitcnt vmcnt(6)" : "+v"(d[0]), "+v"(d[1]), "+v"(d[2]), "+v"(d[3]), "+v"(d[4]));
        }
        f32x2 o[5];
        {
            f32x2 t[5][2];
#pragma unroll
            for (int i = 0; i < 5; ++i) {
                const auto e = __builtin_amdgcn_permlane32_swap(d[i].x, d[i].z, false, false), f = __builtin_amdgcn_permlane32_swap(d[i].y, d[i].w, false, false);
                t[i][0] = up2(e[0]) + up2(e[1]); t[i][1] = up2(f[0]) + up2(f[1]);
            }
            o[0] = t[0][0] + t[1][0]; o[1] = t[0][1] + t[1][1]; o[2] = t[2][0] + t[3][0]; o[3] = t[2][1] + t[3][1];
            const auto e = __builtin_amdgcn_permlane16_swap(u_(t[4][0].x), u_(t[4][1].x), false, false), f = __builtin_amdgcn_permlane16_swap(u_(t[4][0].y), u_(t[4][1].y), false, false);
            o[4] = (f32x2){f_(e[0]) + f_(e[1]), f_(f[0]) + f_(f[1])};
        }
        f32x2 x1[5];
#pragma unroll
        for (int i = 0; i < 5; ++i) x1[i] = up2(pk2(up2(xi[i]) + up2(pk2(o[i]))));
        asm volatile("" : "+v"(x1[0]), "+v"(x1[1]), "+v"(x1[2]), "+v"(x1[3]), "+v"(x1[4]));
        RS(3);
        if (s < 0) EV(6);
        const f32x2 st1 = ln_stats(x1);
        u32 xp[5];
#pragma unroll
        for (int i = 0; i < 5; ++i) xp[i] = pk2(x1[i] + ((x1[i] - st1.x) * st1.y) * y[i]);
        const int row = max(s, 0) * 32 + ri;
        RS(4);
        if (L == 0) {
            const u32 ro = (u32)row * (DM * 2);
            f32x2 x2[5];
#pragma unroll
            for (int i = 0; i < 5; ++i) x2[i] = up2(xp[i]);
            st8s(rs, l8, W_X2 + ro, (v2u){xp[0], xp[1]});
            st8s(rs, l8 + 512, W_X2 + ro, (v2u){xp[2], xp[3]});
            st4s(rs, l4 + 1024, W_X2 + ro, xp[4]);
            SCHED();
            const f32x2 st2 = ln_stats(x2);
            u32 hp[5];
#pragma unroll
            for (int i = 0; i < 5; ++i) hp[i] = pk2((x2[i] - st2.x) * st2.y);
            store_h(rs, near_(W_H2 + (u32)s * H_TILE, 1u, pol), row, hp, off);
        } else {
            const rsrc_t ro = mk_rsrc(p.out, ACT);
            __builtin_amdgcn_raw_buffer_store_b64((v2u){xp[0], xp[1]}, ro, l8, (u32)row * (DM * 2), OUTAUX);
            __builtin_amdgcn_raw_buffer_store_b64((v2u){xp[2], xp[3]}, ro, l8 + 512u, (u32)row * (DM * 2), OUTAUX);
            __builtin_amdgcn_raw_buffer_store_b32(xp[4], ro, l4 + 1024u, (u32)row * (DM * 2), OUTAUX);
        }
        RS(5);
        if (L == 1 && s == NSEG - 1) __builtin_amdgcn_endpgm();
        LD4A(fv, fp, 0);
        if (L == 0) asm volatile("s_waitcnt vmcnt(1)\n\ts_barrier" ::: "memory");
        st4s(rs, L == 0 || s == 40 ? fo : OOB, 0, L == 0 ? (u32)s + 1 : p.e0);
        RS(6);
        VMW(1); SCHED();
        asm volatile("" : "+v"(fv), "+v"(yv), "+v"(xa), "+v"(xb), "+v"(xc));
        if (__builtin_expect(s == 2 || s == 26, 0) && !all_(yv >= (s == 2 ? 2u : 3u))) ywait(s == 2 ? 2u : 3u);
        if (__builtin_expect(s < 0, 0)) {
            EV(7);
            ywait(1u);
            BAR();
            EV(15);
#pragma unroll 1
            for (int i = 0; i < 5; ++i) yfill(0, i);
            fv = ld4(rs, fp);
            DRAIN();
        }
        RS(7);
        if (ri == 0 && s >= 0) LST(320 + L * 64 + s, rt32());
        LS2(L == 0, 224 + ri);
    }
}

constexpr float SCALE = 0.08838834764831845f, LOG2E = 1.4426950408889634f;
constexpr int C_K = 0, C_V = 8192, C_QX = 16384, C_MEM = 24576, C_MASK = C_MEM + 10240,
              C_Q = C_MASK + 1024, C_ZERO = C_Q + 2304, C_KM = C_Q + 4096, C_VM = C_KM + 4096, C_DUMP = C_VM + 2048, C_WL = C_DUMP + 1024;
constexpr int NLF = 14;
static_assert(C_WL % 16 == 0 && C_WL + 4 * NLF * 2048 <= LDS_BYTES, "head lds");
#define CH_H0 98
#define CH_W0 80
#define CH_MH 11
#define CH_MW 17
#define CH_REC 16
#define CH_LATE 30
#define CH_SOLO 6
#define CH_KV 12
#define CH_KV0 44
#define CH_M0 3
#define CH_DEC 2u
#define CH_EARLY 16
#define CH_NEAR 4
#define CH_FIRST 160
#define CH_ATT 2
#define CH_CLIP 12u
#define CH_LEAD 70
#define CH_UP 3
#define CH_WX 6
DEV void wait_until(u32 t) {
    unsigned long long c;
    asm volatile("1:\n\ts_memrealtime %0\n\ts_waitcnt lgkmcnt(0)\n\ts_mov_b64 vcc, %0\n\ts_sub_u32 vcc_lo, %1, vcc_lo\n\ts_cmpk_lt_u32 vcc_lo, 400\n\ts_cbranch_scc1 1b" : "=&s"(c) : "s"(uu(t - 1u)) : "vcc", "scc", "memory");
}
#define CH_FD 14
#define CH_FC 34
#define FINE_T1(t) uu((t) - (CH_FD + 1u))
#define FINE_T4(t) uu(4u * (t) - CH_FC)
#define NOP8 "s_nop 0\n\ts_nop 0\n\ts_nop 0\n\ts_nop 0\n\ts_nop 0\n\ts_nop 0\n\ts_nop 0\n\ts_nop 0\n\t"
#define LOOP_TXT(tmp, t1) "s_memrealtime vcc\n1:\n\ts_waitcnt lgkmcnt(0)\n\ts_sub_u32 " tmp ", " t1 ", vcc_lo\n\ts_memrealtime vcc\n\ts_cmpk_lt_u32 " tmp ", 400\n\ts_cbranch_scc1 1b\n\t"
#define WAIT_TXT(tmp, t1, t4) LOOP_TXT(tmp, t1) \
    "s_nop 1\n\ts_memrealtime s[90:91]\n\ts_nop 3\n\ts_memrealtime s[92:93]\n\ts_nop 3\n\ts_memrealtime s[94:95]\n\ts_getpc_b64 s[96:97]\n2:\n\ts_waitcnt lgkmcnt(0)\n\t" \
    "s_add_u32 s90, s90, vcc_lo\n\ts_add_u32 s92, s92, s94\n\ts_sub_u32 s90, " t4 ", s90\n\ts_sub_u32 s90, s90, s92\n\ts_max_i32 s90, s90, 0\n\ts_min_i32 s90, s90, 29\n\t" \
    "s_mul_i32 s90, s90, 87\n\ts_lshr_b32 s90, s90, 4\n\ts_andn2_b32 s90, s90, 3\n\ts_sub_u32 s90, 3f-2b, s90\n\ts_add_u32 s96, s96, s90\n\ts_addc_u32 s97, s97, 0\n\t" \
    "s_setpc_b64 s[96:97]\n\t" NOP8 NOP8 NOP8 NOP8 NOP8 "3:\n\t"
#define WAIT_CLOB "vcc", "scc", "s90", "s91", "s92", "s93", "s94", "s95", "s96", "s97", "memory"
#define SMF_A(acc, a, b) asm volatile("v_smfmac_f32_16x16x64_bf16 %0, %1, %2, %3" : "+v"(acc) : "v"(a), "a"(b), "v"(idx))
#define SMF_V(acc, a, b) asm volatile("v_smfmac_f32_16x16x64_bf16 %0, %1, %2, %3" : "+v"(acc) : "v"(a), "v"(b), "v"(idx))
#define MF0(d, a, b) asm volatile("v_mfma_f32_16x16x32_bf16 %0, %1, %2, 0" : "=&v"(d) : "v"(a), "v"(b))
#define MF1(d, a, b) asm volatile("v_mfma_f32_16x16x32_bf16 %0, %1, %2, %0" : "+v"(d) : "v"(a), "v"(b))
#define MF0H(d, a, b) asm volatile("v_mfma_f32_16x16x16_bf16 %0, %1, %2, 0" : "=&v"(d) : "v"(a), "v"(b))
#define MF1H(d, a, b) asm volatile("v_mfma_f32_16x16x16_bf16 %0, %1, %2, %0" : "+v"(d) : "v"(a), "v"(b))
DEV v8u tr_frag(lptr p0, lptr p1, lptr p2, lptr p3) { return cat8(tr2(p0, p1), tr2(p2, p3)); }
constexpr int qslot(int ks) { return (ks & 1) && ks < 8 ? ks >> 1 : -1; }
constexpr int kslot(int ks) { return ks < 4 ? 4 + ks : -1; }
constexpr int vslot(int ks) { return ks >= 4 ? 4 + ks : -1; }
DEV u32 ring_map(u32 off, u32 half) { return ((off >> 13) << 14) + (half << 13) + (off & 8191u); }
#define ZRD(x, i) x = __builtin_bit_cast(f32x4, lds_r16(ZP + 16 * (i)))
#define VMAX(d, a, b) asm volatile("v_max_f32 %0, %1, %2" : "=v"(d) : "v"(a), "v"(b))
#define VMAX3(d, a, b, c_) asm volatile("v_max3_f32 %0, %1, %2, %3" : "=v"(d) : "v"(a), "v"(b), "v"(c_))
#define VADD(d, a, b) asm volatile("v_add_f32 %0, %1, %2" : "=v"(d) : "v"(a), "v"(b))
#define VEXP(d, a) asm volatile("v_exp_f32 %0, %1" : "=v"(d) : "v"(a))
#define VFMC(d, a, b) asm volatile("v_fmamk_f32 %0, %1, 0x3e0293ee, %2" : "=v"(d) : "v"(a), "v"(b))
#define VMULC(d, a) asm volatile("v_mul_f32 %0, 0x3e0293ee, %1" : "=v"(d) : "v"(a))
#define SEL(d, a, b) asm volatile("v_cndmask_b32 %0, %1, %2, %3" : "=v"(d) : "v"(a), "v"(b), "s"(himask))
#define QPERM(d, a, q) asm volatile("v_mov_b32_dpp %0, %1 quad_perm:" q " row_mask:0xf bank_mask:0xf" : "=v"(d) : "v"(a))

#define SUMRANGE 0x28000000u
#define FL_EARLY 1
#define HSAMP 71
DEV void role_head(const Params& p, lptr lds, int L, int h) {
    const int tid = threadIdx.x, lane = tid & 63, w = uni(tid >> 6), g = lane >> 4, r = lane & 15;
    const rsrc_t rs = mk_rsrc(p.ws, W_END);
    v8u wq0[10], wq1[10], wk0[10], wk1[10], wv0[10], wv1[10];
    lptr WL = lds + C_WL + w * (NLF * 2048) + lane * 16;
    MK_SRD();
    bool dead = false, have;
    const u32 l16 = (u32)lane * 16u;
    u32 pol, pol0, mring, hint, fkh, ksrc, vsrc, kb = uu(W_KQ + (u32)(L * NSEG * NH + h) * 16384u + w * 2048);
    unsigned long long kmask, vmask;
    u32 dk = uu(C_K + w * 2048), dv = dk + (C_V - C_K), dq = dk + (C_QX - C_K), dm = uu(C_MEM + w * 2048), dm2 = uu(w < 2 ? C_MEM + 8192 + w * 1024 : C_DUMP);
    {
        constexpr int GS = 1056, CS = 48 * GS;
        const v4u sq = mk_srd(p.Wq, DM * NQ * 2), sk = mk_srd(p.Wk, DM * NQ * 2), sv = mk_srd(p.Wv, DM * NQ * 2);
        const u32 vo = (u32)(g * NQ + h * DH + 8 * (r ^ ((g & 2) << 2))) * 2u;
        auto fill = [&](int ch) {
#pragma unroll 1
            for (int i = 0; i < 4; ++i) {
                const u32 dst = uu((ch % 3) * CS + (4 * i + w) * GS), so = uu((ch * 64 + 16 * i + 4 * w) * (NQ * 2));
                DMAIN(sq, dst, vo, so); DMAIN(sk, dst + 16 * GS, vo, so); DMAIN(sv, dst + 32 * GS, vo, so);
            }
        };
        const int vote = L == 1 ? pre_rings(p, rs, h) : 0;
        fill(0);
        if (L == 1) { VMW(12); BAR(); EV(3); }
        boot_set(p, rs, tid == 0 && L == 1 ? foff(F_RG + h) : OOB, (u32)((1 << 20) + vote));
        fill(1); fill(2);
        const int q4 = r >> 2;
        lptr tb = lds + (4 * (g >> 1) + q4) * GS + (g & 1) * 512 + ((w * 64 + (r & 3) * 8) ^ ((g & 1) << 7));
        v4u praw; u32 f0, code;
        SFOR(ch, 10)
            asm volatile("s_waitcnt vmcnt(%0) ; HZCLR" :: "i"(ch == 0 || (ch >= 4 && ch < 7) ? 25 : ch < 7 ? 24 : ch == 7 ? 25 : ch == 8 ? 13 : 2) : "memory");
            BAR();
            auto frag = [&](int m, int t) { lptr b = tb + (ch % 3) * CS + m * 16 * GS + t * 32; return tr_frag(b, b + 256, b + 8 * GS, b + 8 * GS + 256); };
            wq0[ch] = frag(0, 0); wq1[ch] = frag(0, 1); wk0[ch] = frag(1, 0); wk1[ch] = frag(1, 1); wv0[ch] = frag(2, 0); wv1[ch] = frag(2, 1);
            asm volatile("" : "+a"(wq0[ch]), "+a"(wk0[ch]), "+v"(wv0[ch]), "+v"(wv1[ch]));
            if constexpr (qslot(ch) < 0) asm volatile("" : "+a"(wq1[ch])); else asm volatile("" : "+v"(wq1[ch]));
            if constexpr (kslot(ch) < 0) asm volatile("" : "+a"(wk1[ch])); else asm volatile("" : "+v"(wk1[ch]));
            BAR();
            if constexpr (ch < 7) fill(ch + 3);
            if constexpr (ch == 3) asm volatile("buffer_load_dwordx4 %0, %1, %2, 0 offen sc1" : "=v"(praw) : "v"(pol_off()), "s"(srd) : "memory");
            if constexpr (ch == 6) code = code_fetch();
            if constexpr (ch == 7) {
                PIN(praw);
                if (!boot_ok(p, pol_off(), praw, 0u)) praw.w = boot_wait(p, rs, pol_off(), 0u, dead);
                pol = pol_of(praw.w); pol0 = pol & 1u;
                mring = W_MRING + (u32)L * (MB / 4) + pol0 * 8192u; hint = mring + 4 * 32768u;
                fkh = lane < 3 ? near_(foff(F_QKV + lane * 64 + L * 32 + h), 0u, pol) : lane < 6 ? hint + (u32)(lane - 3) * 4u : OOB;
                kmask = __builtin_amdgcn_ballot_w64((hblk(W_KQ, L * NSEG + lane, h, 0u, pol) & 8192u) != 0u);
                vmask = __builtin_amdgcn_ballot_w64((hblk(W_VO, L * NSEG + lane, h, 1u, pol) & 8192u) != 0u);
                ksrc = kb + ((u32)kmask & 1u) * 8192u; vsrc = kb + (W_VO - W_KQ) + ((u32)vmask & 1u) * 8192u;
                PIN(fkh);
                asm volatile("" : "+s"(pol), "+s"(kmask), "+s"(vmask), "+s"(ksrc), "+s"(vsrc), "+s"(dk), "+s"(dv), "+s"(dq), "+s"(dm), "+s"(dm2));
            }
            if constexpr (ch == 8) LD4A(f0, fkh, 0);
            SCHED();
        SEND
        VMW(0); SCHED(); PIN(f0); USE(code);
        have = ((u32)__builtin_amdgcn_ballot_w64(f0 == 0u) & 7u) == 0u;
        if (have) { DMA32(dv, l16, vsrc); DMA32(dk, l16, ksrc); }
#pragma unroll
        for (int ch = 0; ch < 10; ++ch) {
            if (qslot(ch) >= 0) { lds_w16(WL + qslot(ch) * 2048, wq1[ch].lo); lds_w16(WL + qslot(ch) * 2048 + 1024, wq1[ch].hi); }
            if (kslot(ch) >= 0) { lds_w16(WL + kslot(ch) * 2048, wk1[ch].lo); lds_w16(WL + kslot(ch) * 2048 + 1024, wk1[ch].hi); }
            if (vslot(ch) >= 0) { lds_w16(WL + vslot(ch) * 2048, wv1[ch].lo); lds_w16(WL + vslot(ch) * 2048 + 1024, wv1[ch].hi); }
        }
        CFENCE();
    }
    const u32 idx = (r & 2) ? 0xEEEEu : 0x4444u;
    const unsigned long long himask = 0xCCCCCCCCCCCCCCCCull;
    EV(6);
    if (L == 1) {
        static_assert(4 * (8 * 1088 + 17 * CV_KB + 1024) <= C_WL, "conv in the head's buffers");
        Filt f[3];
#pragma unroll
        for (int i = 0; i < 3; ++i) filt_load<1, 0>(p, 64 + 3 * h + i, f[i]);
        boot_wait(p, rs, lane < 40 ? foff(F_KV + lane) : OOB, 1u, dead);
#pragma unroll
        for (int i = 0; i < 3; ++i) f[i].a[4] = ld16s(rs, lane < 32 ? l16 : OOB, W_KVT + (u32)chan(64 + 3 * h + i, w) * (SEQ * 2));
#pragma unroll 1
        for (int i = 0; i < 3; ++i) {
            conv_job<4>(p, rs, lds, 64 + 3 * h + i, f[0], 0);
            f[0].a[0] = f[1].a[0]; f[0].b[0] = f[1].b[0]; f[0].a[4] = f[1].a[4]; f[1].a[0] = f[2].a[0]; f[1].b[0] = f[2].b[0]; f[1].a[4] = f[2].a[4];
        }
        DRAIN(); BAR();
        st4s(rs, tid == 0 ? foff(F_CV + 160 + h) : OOB, 0, 1u);
        EV(12);
    }
    u32 tm = 0;
    lptr qw = lds + C_Q + (2 * g) * 288 + (32 * w + 2 * r) * 2;
    lptr kw = qw + (C_KM - C_Q);
    lptr qr = lds + C_Q + (2 * (r >> 2) + (r & 1)) * 288 + g * 16;
    lptr LB = lds + l16, VX = lds + C_V + w * 2048 + l16, QX = lds + C_QX + (w & 1) * 4096 + l16;
    if (tid >= 64 && tid < 70) { const u32 z = tid == 68 ? 0x3F803F80u : tid == 69 ? PSENT : 0u; lds_w16(lds + C_ZERO + (tid - 64) * 16, (v4u){z, z, z, z}); }
    if (w < 3) {
        const u32 ng = u_(-1000000000.0f * LOG2E);
        v4u mk;
#pragma unroll
        for (int i = 0; i < 4; ++i) mk[i] = w == 0 || (w == 1 && 4 * g + i > r) ? ng : 0u;
        lds_w16(lds + C_MASK + w * 4096 + l16, mk);
    }
    u32 zaddr = 0; PIN(zaddr);
    lptr ZP = lds + zaddr + C_ZERO;
#define LF_RD(slot, hf) lf[slot][hf] = lds_r16(WL + (slot) * 2048 + (hf) * 1024)
    const u32 ofl = tid == 192 ? near_(foff(F_O + L * 32 + h), 0u, pol) : tid == 193 && (h & 15) == 0 ? hint + 128u + (u32)h * 8u : OOB;
    u32 oso = (g & 1) ? OOB : W_ORING + (u32)L * (MB / 2) + ring_map((u32)((r >> 3) * 32768 + h * 1024 + w * 256 + (g >> 1) * 128 + (r & 7) * 16), pol0);
    u32 missed = 0, dirty = 1, rep = PSENT;
    const u32 early = L < FL_EARLY ? 0u : OOB;
    asm volatile("" : "+v"(oso), "+v"(qw), "+v"(kw), "+v"(qr), "+v"(VX), "+v"(QX));
    if (!have) { wait_flags(rs, lane < 3 ? fkh : OOB, 1u, dead); DMA32(dv, l16, vsrc); DMA32(dk, l16, ksrc); }
    EV(7);
    st4s(rs, L == 0 && tid == 192 ? foff(F_GO + h) : OOB, 0, rt32() | 1u);
    EV(8);
    auto next_slot = [&](u32 now) {
        const u32 hv = ld4(rs, fkh), per = rdl(hv, 4);
        if (per < 400u) { tm = rdl(hv, 5) - per; for (int n = 0; n < 3 && (int)(tm - now) < 0; ++n) tm += per; }
    };
    if (L == 1) { next_slot(rt32() + CH_KV0); tm -= 24u; }
#define C2(k) do {} while (0)
    for (int s = 0; s < NSEG;) {
        {
            *(LAS u32*)(lds + C_DUMP + 128 + w * 4) = tm;
            BAR();
            const v4u c = lds_r16(lds + C_DUMP + 128) - tm;
            const u32 add = uu((u32)max(max((int)c.x, (int)c.y), max((int)c.z, (int)c.w)));
            tm += add; dirty |= add;
        }
        u32 ms = mring + (u32)(s & 3) * 32768u + w * 2048, ms2 = mring + (u32)(s & 3) * 32768u + 16384u + (w & 1) * 1024;
        u32 l3 = w < 2 ? l16 : OOB;
        asm volatile("" : "+s"(ms), "+s"(ms2)); PIN(l3);
        f32x4 aq0, aq1, ak0, ak1;
        v4u lf[NLF][2];
        u32 tw1 = FINE_T1(tm), tw4 = FINE_T4(tm), fv = (l16 & 16u) ? rep : (u32)s;
        asm volatile("" : "+s"(tw1), "+s"(tw4)); PIN(fv);
        SCHED();
        C2(4);
        asm volatile("s_waitcnt vmcnt(5)\n\ts_barrier" ::: "memory");
        C2(5);
        st4s(rs, ofl | early, 0, fv);
        if (__builtin_expect(dirty != 0u, 0)) {
            wait_until(tm - 44u);
            st16(rs, oso, lds_r16(ZP + 80));
        }
        SCHED();
        v4u A[10];
#define HT(k) do {} while (0)
        v4u kx[8], qf[4], kmf[4], vx[2], ones, P, tq[4];
        float fa, fb, fc, fd, sx[4], t[4], sm0, sm1, tm0, tm1, inv; u32 pa, pb, fl, pmm, tmp = 0; unsigned long long odd;
        f32x4 av0, av1, s0, s1, sm, oa, ob, rsum;
        v2u vm, pm, vh, vl, on[2];
        f32x2 zz[4];
#define LW(sl) cat8(lf[sl][0], lf[sl][1])
#define PQ(ks, tl) do { if constexpr (!(tl)) SMF_A(aq0, A[ks], wq0[ks]); else if constexpr (qslot(ks) < 0) SMF_A(aq1, A[ks], wq1[ks]); else { const v8u lw_ = LW(qslot(ks)); SMF_V(aq1, A[ks], lw_); } } while (0)
#define PK(ks, tl) do { if constexpr (!(tl)) SMF_A(ak0, A[ks], wk0[ks]); else if constexpr (kslot(ks) < 0) SMF_A(ak1, A[ks], wk1[ks]); else { const v8u lw_ = LW(kslot(ks)); SMF_V(ak1, A[ks], lw_); } } while (0)
#define PV(ks, tl) do { if constexpr (!(tl)) SMF_V(av0, A[ks], wv0[ks]); else if constexpr (vslot(ks) < 0) SMF_V(av1, A[ks], wv1[ks]); else { const v8u lw_ = LW(vslot(ks)); SMF_V(av1, A[ks], lw_); } } while (0)
#define KXR(i) kx[i] = lds_r16(LB + C_K + ((i) & 1) * 4096 + ((i) >> 1) * 1024)
#define XW(q_) do { *(LAS u32*)(q_) = pa; *(LAS u32*)((q_) + 288) = pb; } while (0)
        auto slot = [&](auto zc) __attribute__((always_inline)) {
            constexpr int z = decltype(zc)::value;
            constexpr int vs[] = {0, 8, 1, 10, 9, 2, 11, -1, -1, -1, -1, 4, 6, 12, -1, -1, -1, 13, 14, -1, 16, 18, 15, 3, 5, 17, 7, 19};
            if constexpr (z < 12) PQ(z >> 1, z & 1);
            else if constexpr (z < 20) PQ(6 + (z & 3), z >> 4);
            else if constexpr (z < 32) PK((z - 20) >> 1, z & 1);
            else if constexpr (z < 40) PK(6 + (z & 3), (z >> 2) & 1);
            else if constexpr (z < 42) { if constexpr (z == 40) MF0(s0, kx[0], qf[0]); else MF0(s1, kx[1], qf[0]); }
            else if constexpr (z < 48) { if constexpr (z & 1) MF1(s1, kx[z - 40], qf[(z - 40) >> 1]); else MF1(s0, kx[z - 40], qf[(z - 40) >> 1]); }
            else if constexpr (z == 55) MF0(sm, kmf[0], qf[0]);
            else if constexpr (z > 55 && z < 59) MF1(sm, kmf[z - 55], qf[z - 55]);
            else if constexpr (z == 62) MF0(rsum, ones, P);
            else if constexpr (z == 63) MF0(oa, vx[0], P);
            else if constexpr (z == 64) MF0(ob, vx[1], P);
            else if constexpr (z == 67) { const v2u ones2 = ones.lo; MF1H(rsum, ones2, pm); }
            else PV(vs[z - 48] >> 1, vs[z - 48] & 1);
            if constexpr (z == 20 || z == 24 || z == 26) USE(lf[4 + ((z - 20) >> 1)][1]);
            if constexpr (z == 41) USE(qf[3]);
            if constexpr (z == 45 || z == 46) USE(kx[z - 39]);
            if constexpr (z == 53) USE(lf[9][1]);
            if constexpr (z >= 54 && z < 58) USE(kmf[z - 54]);
            if constexpr (z == 61) USE(vl);
            if constexpr (z == 64) USE(lf[10][1]);
            if constexpr (z == 69) USE(lf[11][1]);
            if constexpr (z == 72) USE(lf[12][1]);
            if constexpr (z == 74) USE(lf[13][1]);
            if constexpr (z < 8) A[2 + z] = lds_r16(LB + C_MEM + (2 + z) * 1024);
            if constexpr (z >= 15 && z < 23) LF_RD(4 + ((z - 15) >> 1), (z - 15) & 1);
            if constexpr (z >= 24 && z < 27) KXR(z - 24);
            if constexpr (z == 27) XW(qw);
            if constexpr (z >= 28 && z < 31) KXR(z - 25);
            if constexpr (z == 32) asm volatile("s_waitcnt lgkmcnt(3)\n\ts_barrier" ::: "memory");
            if constexpr (z >= 33 && z < 37) qf[z - 33] = lds_r16(qr + (z - 33) * 64);
            if constexpr (z == 37 || z == 38) LF_RD(8, z - 37);
            if constexpr (z == 39 || z == 41) KXR(6 + (z == 41));
            if constexpr (z == 44) XW(kw);
            if constexpr (z == 45 || z == 46) LF_RD(9, z - 45);
            if constexpr (z == 47) vx[0] = lds_r16(VX);
            if constexpr (z == 48) { VMW(1); vh = *(LAS v2u*)(VX + 1032); }
            if constexpr (z == 54) { vl = *(LAS v2u*)(VX + 1024); vx[1] = (v4u){vh.x, vh.y, vl.x, vl.y}; }
            if constexpr (z == 49) { USE(lf[9][0]); asm volatile("s_barrier" ::: "memory"); }
            if constexpr (z >= 50 && z < 54) kmf[z - 50] = lds_r16(qr + (C_KM - C_Q) + (z - 50) * 64);
            if constexpr (z == 59 || z == 60) LF_RD(10, z - 59);
            if constexpr (z == 61 || z == 65) LF_RD(11, z == 65);
            if constexpr (z == 66 || z == 68) LF_RD(12, z == 68);
            if constexpr (z == 69 || z == 70) LF_RD(13, z - 69);
            if constexpr (z >= 72) tq[z - 72] = lds_r16(QX + (z - 72) * 1024);
            if constexpr (z == HSAMP) LD4A(fl, fkh, 0);
            SCHED();
            if constexpr (z == 19) HT(2);
            if constexpr (z == 39) HT(3);
            if constexpr (z == 51) HT(4);
            if constexpr (z == 64) HT(5);
            if constexpr (z == 20) VADD(fa, aq0[0], aq0[2]);
            if constexpr (z == 21) VADD(fb, aq0[1], aq0[3]);
            if constexpr (z == 23) VADD(fc, aq1[0], aq1[2]);
            if constexpr (z == 24) VADD(fd, aq1[1], aq1[3]);
            if constexpr (z == 25) VCVT(pa, fa, fc);
            if constexpr (z == 26) VCVT(pb, fb, fd);
            if constexpr (z >= 44 && z < 48) asm volatile("v_mov_b64 %0, 0" : "=v"(zz[z - 44]));
            if constexpr (z == 47) { av0 = __builtin_shufflevector(zz[0], zz[1], 0, 1, 2, 3); av1 = __builtin_shufflevector(zz[2], zz[3], 0, 1, 2, 3); }
            if constexpr (z == 48 || z == 49) asm volatile("v_mov_b64 %0, %1" : "=v"(on[z - 48]) : "s"(0x3F803F803F803F80ull));
            if constexpr (z == 49) ones = __builtin_shufflevector(on[0], on[1], 0, 1, 2, 3);
            if constexpr (z == 40) { VADD(fa, ak0[0], ak0[2]); VADD(fb, ak0[1], ak0[3]); }
            if constexpr (z == 42) { VADD(fd, ak1[1], ak1[3]); VADD(fc, ak1[0], ak1[2]); }
            if constexpr (z == 43) { VCVT(pb, fb, fd); VCVT(pa, fa, fc); }
            if constexpr (z >= 50 && z < 54) SEL(sx[z - 50], s0[z - 50], s1[z - 50]);
            if constexpr (z >= 54 && z < 58) VEXP(t[z - 54], sx[z - 54]);
            if constexpr (z == 58) asm volatile("v_cvt_pk_bf16_f32 %0, %2, %3\n\tv_cvt_pk_bf16_f32 %1, %4, %5" : "=&v"(P.x), "=v"(P.y) : "v"(t[0]), "v"(t[1]), "v"(t[2]), "v"(t[3]));
            if constexpr (z == 59) QPERM(P.z, P.x, "[2,3,0,1]");
            if constexpr (z == 60) QPERM(P.w, P.y, "[2,3,0,1]");
            if constexpr (z == 61) { USE(sm); VMULC(sm0, sm[0]); }
            if constexpr (z == 62) { VEXP(tm0, sm0); VMULC(sm1, sm[1]); }
            if constexpr (z == 63) VEXP(tm1, sm1);
            if constexpr (z == 64) asm volatile("v_cvt_pk_bf16_f32 %0, %2, %3\n\tv_cndmask_b32 %1, %0, 0, %4" : "=&v"(pmm), "=v"(pm.x) : "v"(tm0), "v"(tm1), "s"(himask));
            if constexpr (z == 65) asm volatile("v_cndmask_b32 %0, 0, %1, %2" : "=v"(pm.y) : "v"(pmm), "s"(himask));
            if constexpr (z == 67) { SEL(oa[0], oa[0], ob[0]); SEL(oa[1], oa[1], ob[1]); }
            if constexpr (z == 68 || z == 69) SEL(oa[z - 66], oa[z - 66], ob[z - 66]);
            if constexpr (z == 70) asm volatile("v_sub_u32 %0, %1, %2" : "=v"(pa) : "v"(rsum[0]), "s"(0x2B800000u), "v"(rsum));
            if constexpr (z == 71) asm volatile("v_cmp_le_u32 %0, %1, %2" : "=s"(odd) : "s"(SUMRANGE), "v"(pa));
            if constexpr (z == 72) VADD(fa, av0[0], av0[2]);
            if constexpr (z == 73) VADD(fb, av0[1], av0[3]);
            if constexpr (z == 74) VCVT(vm.x, fa, fb);
            SCHED();
        };
        asm volatile("s_mov_b32 m0, %2\n\t" WAIT_TXT("%0", "%1", "%9") "buffer_load_dwordx4 %4, %5, %6 offen sc1 lds\n\tbuffer_load_dwordx4 %4, %5, %6 offen offset:1024 sc1 lds\n\t"
                     "s_mov_b32 m0, %3\n\ts_nop 0\n\tbuffer_load_dwordx4 %8, %5, %7 offen sc1 lds"
                     : "=&s"(tmp) : "s"(tw1), "s"(dm), "s"(dm2), "v"(l16), "s"(srd), "s"(ms), "s"(ms2), "v"(l3), "s"(tw4) : WAIT_CLOB);
        HT(0);
        ZRD(aq0, 0); ZRD(aq1, 1); ZRD(ak0, 2); ZRD(ak1, 3);
#pragma unroll
        for (int i = 0; i < 4; ++i) { LF_RD(i, 0); LF_RD(i, 1); }
        u32 ob_ = (vsrc ^ 8192u) + (u32)((w & 1) * 256); PINS(ob_);
        USE(lf[3][1]);
        DMA32(dq, l16, ksrc ^ 8192u);
        st4s(rs, ofl | (early ^ OOB), 0, fv);
        asm volatile("s_waitcnt vmcnt(3)\n\ts_barrier" ::: "memory");
        HT(1);
#pragma unroll
        for (int ks = 0; ks < 2; ++ks) A[ks] = lds_r16(LB + C_MEM + ks * 1024);
        SCHED();
        static_for<0, 76>(slot);
        u32 fail = 0;
        if (__builtin_expect(odd != 0ull, 0)) {
            u32 mn = ~0u;
#pragma unroll 5
            for (int i = 0; i < 10; ++i) { const v4u e = lds_r16(LB + C_MEM + i * 1024); mn = min(min(min(mn, e.x ^ PSENT), e.y ^ PSENT), min(e.z ^ PSENT, e.w ^ PSENT)); }
            fail = missed < SPIN_CAP && !all_(mn != 0u) ? OOB : 0u;
            float mx = fmaxf(fmaxf(fmaxf(sx[0], sx[1]), fmaxf(sx[2], sx[3])), fmaxf(sm0, sm1));
            mx = max32(max16(fmaxf(mx, dpp<0x4E>(mx))));
#pragma unroll
            for (int i = 0; i < 4; ++i) t[i] = __builtin_amdgcn_exp2f(sx[i] - mx);
            P.x = pk(t[0], t[1]); P.y = pk(t[2], t[3]); P.z = u_(dpp<0x4E>(f_(P.x))); P.w = u_(dpp<0x4E>(f_(P.y)));
            pmm = pk(__builtin_amdgcn_exp2f(sm0 - mx), __builtin_amdgcn_exp2f(sm1 - mx));
            pm = (r & 2) ? (v2u){0u, pmm} : (v2u){pmm, 0u};
            asm volatile("s_nop 1" : "+v"(P), "+v"(pm));
            MF0(rsum, ones, P); MF0(oa, vx[0], P); MF0(ob, vx[1], P);
            NOPS(7);
            { const v2u ones2 = ones.lo; MF1H(rsum, ones2, pm); }
#pragma unroll
            for (int i = 0; i < 4; ++i) SEL(oa[i], oa[i], ob[i]);
            asm volatile("s_nop 4" :: "v"(rsum));
            oso |= fail;
        }
        v4u tk[8];
        f32x4 c0, c1;
#define TKR(i) tk[i] = lds_r16(LB + C_K + ((i) >> 2) * 4096 + ((i) & 3) * 1024)
        asm volatile("v_rcp_f32 %0, %3\n\ts_nop 2\n\tv_add_f32 %1, %4, %6\n\tv_add_f32 %2, %5, %7\n\tv_cvt_pk_bf16_f32 %1, %1, %2\n\ts_nop 0"
                     : "=&v"(inv), "=&v"(vm.y), "=&v"(fd) : "v"(rsum[0]), "v"(av1[0]), "v"(av1[1]), "v"(av1[2]), "v"(av1[3]));
        MF1H(oa, vm, pm);
        SCHED();
        c0 = __builtin_bit_cast(f32x4, lds_r16(QX + (C_MASK + 4096 - C_QX)));
        SCHED();
        {
            v4u oc; float u0, u1, u2, u3;
            asm volatile("s_nop 6\n\tv_mul_f32 %4, %8, %12\n\tv_mul_f32 %5, %9, %12\n\tv_mul_f32 %6, %10, %12\n\tv_mul_f32 %7, %11, %12\n\t"
                         "v_cvt_pk_bf16_f32 %0, %4, %5\n\tv_cvt_pk_bf16_f32 %2, %4, %5\n\tv_cvt_pk_bf16_f32 %1, %6, %7\n\tv_cvt_pk_bf16_f32 %3, %6, %7\n\t"
                         "v_permlane16_swap_b32 %0, %2\n\ts_nop 0\n\tv_permlane16_swap_b32 %1, %3"
                         : "=&v"(oc.x), "=&v"(oc.y), "=&v"(oc.z), "=&v"(oc.w), "=&v"(u0), "=&v"(u1), "=&v"(u2), "=&v"(u3) : "v"(oa[0]), "v"(oa[1]), "v"(oa[2]), "v"(oa[3]), "v"(inv));
            st16(rs, oso, oc);
        }
        SCHED();
        unsigned long long clk;
        asm volatile("s_memrealtime %0" : "=s"(clk));
        if (__builtin_expect(fail == 0u, 1)) {
        HT(6);
        C2(0);
        v4u tv[2], px, pv;
        f32x4 cm, o[4], sum;
        float ex[10], cs0, cs1;
        v2u p0, p1, tvm;
        u32 oo, kvo, oz; bool kvok;
        u32 ln = l16; PIN(ln);
        lptr VO, VMX, MK = QX + (C_MASK - C_QX);
        TKR(0);
        SCHED();
        SFOR(i, 12)
            if constexpr (i == 0) MF0(cm, kmf[0], tq[0]); else if constexpr (i < 4) MF1(cm, kmf[i], tq[i]);
            else if constexpr (i < 8) MF1(c0, tk[i - 4], tq[i - 4]); else MF1(c1, tk[i - 4], tq[i - 8]);
            if constexpr (i == 0) USE(tq[3]);
            if constexpr (i == 3) USE(tk[0]);
            if constexpr (i == 4) USE(tk[1]);
            if constexpr (i == 6) USE(tk[3]);
            if constexpr (i >= 8 && i < 11) USE(tk[i - 3]);
            if constexpr (i < 3) TKR(i + 1);
            if constexpr (i == 3) { c1 = __builtin_bit_cast(f32x4, lds_r16(MK)); TKR(4); }
            if constexpr (i >= 4 && i < 7) TKR(i + 1);
            if constexpr (i == 5) *(LAS v2u*)VMX = vm;
            if constexpr (i == 7) { vx[1] = lds_r16(VX + 1024); tv[0] = lds_r16(VO); }
            if constexpr (i == 8) tv[1] = lds_r16(VO + 1024);
            SCHED();
            if constexpr (i == 0) { VMX = lds + (ln >> 1); PIN(VMX); }
            if constexpr (i == 1) { VMX += C_VM + w * 512; PIN(VMX); }
            if constexpr (i == 2) { VO = lds + C_V + (w ^ 1) * 2048 + ln; PIN(VO); }
            if constexpr (i == 6) { USE(cm); VMULC(cs0, cm[0]); }
            if constexpr (i == 8) VMULC(cs1, cm[1]);
            if constexpr (i == 9) VEXP(ex[8], cs0);
            if constexpr (i == 10) { VEXP(ex[9], cs1); VEXP(ex[0], c0[0]); }
            if constexpr (i == 11) { VEXP(ex[1], c0[1]); VEXP(ex[2], c0[2]); VEXP(ex[3], c0[3]); }
            SCHED();
        SEND
        asm volatile("v_cvt_pk_bf16_f32 %0, %6, %7\n\tv_mov_b32 %1, 0\n\tv_mov_b32 %2, 0\n\tv_cvt_pk_bf16_f32 %3, %6, %7\n\tv_cvt_pk_bf16_f32 %4, %8, %9\n\tv_cvt_pk_bf16_f32 %5, %10, %11"
                     : "=&v"(p0.x), "=&v"(p0.y), "=&v"(p1.x), "=&v"(p1.y), "=&v"(px.x), "=&v"(px.y) : "v"(ex[8]), "v"(ex[9]), "v"(ex[0]), "v"(ex[1]), "v"(ex[2]), "v"(ex[3]));
        asm volatile("s_waitcnt lgkmcnt(0)\n\ts_barrier" : "+s"(clk) :: "memory");
        tvm = *(LAS v2u*)(VMX + (512 - (w & 1) * 1024));
        pv = lds_r16(ZP + 80);
        SCHED();
#pragma unroll
        for (int i = 0; i < 4; ++i) VEXP(ex[4 + i], c1[i]);
        asm volatile("v_cvt_pk_bf16_f32 %0, %2, %3\n\tv_cvt_pk_bf16_f32 %1, %4, %5\n\ts_nop 1" : "=&v"(px.z), "=&v"(px.w) : "v"(ex[4]), "v"(ex[5]), "v"(ex[6]), "v"(ex[7]));
        {
            const v2u ones2 = ones.lo;
            const u32 now = (u32)clk;
            auto pvm = [&](auto first) __attribute__((always_inline)) {
                SCHED();
                MF0(sum, ones, px); SCHED(); if constexpr (decltype(first)::value) { oo = ln + (ln & 0x300u) + ob_; PIN(oo); } SCHED();
                MF0(o[0], vx[0], px); NOPS(1);
                MF0(o[1], vx[1], px); NOPS(1);
                MF0(o[2], tv[0], px);
                MF1H(sum, ones2, p0); NOPS(0);
                MF1H(o[0], vm, p0); NOPS(1);
                MF1H(o[1], vm, p1); NOPS(1);
                MF0(o[3], tv[1], px);
                asm volatile("v_sub_u32 %0, %2, %3\n\tv_rcp_f32 %1, %2" : "=&v"(pa), "=&v"(inv) : "v"(sum[0]), "s"(0x2B800000u), "v"(sum));
                MF1H(o[2], tvm, p0); NOPS(2);
                MF1H(o[3], tvm, p1);
                SCHED();
            };
            pvm(IC<1>{});
            if (__builtin_expect(__builtin_amdgcn_ballot_w64(pa >= SUMRANGE) != 0ull, 0)) {
                const f32x4 m0 = __builtin_bit_cast(f32x4, lds_r16(MK + 4096)), m1 = __builtin_bit_cast(f32x4, lds_r16(MK));
                float mx = fmaxf(cs0, cs1);
#pragma unroll
                for (int i = 0; i < 4; ++i) { ex[i] = m0[i] == 0.f ? c0[i] : m0[i]; ex[4 + i] = m1[i] == 0.f ? c1[i] : m1[i]; mx = fmaxf(mx, fmaxf(ex[i], ex[4 + i])); }
                mx = max32(max16(mx));
#pragma unroll
                for (int i = 0; i < 4; ++i) px[i] = pk(__builtin_amdgcn_exp2f(ex[2 * i] - mx), __builtin_amdgcn_exp2f(ex[2 * i + 1] - mx));
                p0.x = p1.y = pk(__builtin_amdgcn_exp2f(cs0 - mx), __builtin_amdgcn_exp2f(cs1 - mx));
                asm volatile("s_nop 7" : "+v"(px), "+v"(p0), "+v"(p1));
                pvm(IC<0>{});
                NOPS(3);
            }
            asm volatile("" : "+v"(o[0]), "+v"(o[1]));
            st16(rs, oo, (v4u){pk2(o[0].lo * inv), pk2(o[0].hi * inv), pk2(o[1].lo * inv), pk2(o[1].hi * inv)});
            asm volatile("" : "+v"(o[2]), "+v"(o[3]));
            st16(rs, oo ^ 2048u, (v4u){pk2(o[2].lo * inv), pk2(o[2].hi * inv), pk2(o[3].lo * inv), pk2(o[3].hi * inv)});
            SCHED();
            C2(1);
            kb += NH * 16384;
            ksrc = kb + (((kmask >> ((s + 1) & 63)) & 1ull) ? 8192u : 0u);
            vsrc = kb + (((vmask >> ((s + 1) & 63)) & 1ull) ? W_VO - W_KQ + 8192u : W_VO - W_KQ);
            oz = (oso & ~(3u << 17)) | (u32)((s + 3) & 3) << 17; oso = (oso & ~(3u << 17)) | (u32)((s + 1) & 3) << 17;
            VMW(3); SCHED();
            C2(2);
            PIN(fl); kvok = ((u32)__builtin_amdgcn_ballot_w64(fl < (u32)s + 2) & 7u) == 0u;
            kvo = l16 | (kvok ? 0u : OOB);
            DMA32(dv, kvo, vsrc); DMA32(dk, kvo, ksrc);
            st16(rs, oz, pv);
            SCHED();
            C2(3);
            rep = (u32)s << 16 | min(missed, 1u) << 14 | min(now - tm, 0x3FFFu);
            if (h == 0 && w == 3) { LST(L * 64 + s, now); LST(128 + L * 64 + s, ((int)(rdl(fl, 3) - now) >= 4) | ((int)kvok << 1) | ((int)min(missed, 3u) << 2)); LST(640 + L * 64 + s, tm); LST(1728 + L * 64 + s, __builtin_amdgcn_s_memtime()); }
            tm = rdl(fl, 5);
            missed = missed == SPIN_CAP ? missed : 0u;
            dirty = 0;
            if (__builtin_expect(rdl(fl, 3) - now + CH_LATE > 4000u && s + 1 < NSEG, 0)) {
                dirty = 1;
#pragma unroll 1
                for (int n = 0; n < 64; ++n) {
                    CFENCE();
                    const u32 hv = ld4(rs, fkh);
                    tm = rdl(hv, 5);
                    if (rdl(hv, 3) - now + CH_LATE <= 4000u) break;
                }
            }
        }
        LS2(L == 0 && w == 0, h);
        if (__builtin_expect(!kvok, 0) && s + 1 < NSEG) {
            dirty = 1;
            wait_flags(rs, ln < 48u ? fkh : OOB, (u32)s + 2, dead);
            DMA32(dv, l16, vsrc); DMA32(dk, l16, ksrc);
            const u32 now = rt32() + CH_KV;
            if ((int)(tm - now) < 0) next_slot(now);
        }
        ++s;
        } else {
            u32 ln = l16; PIN(ln);
            oso = (ln & 256u) ? oso : oso & ~OOB;
            asm volatile("s_waitcnt vmcnt(1) lgkmcnt(0)" :: "s"(clk) : "memory"); SCHED(); PIN(fl);
            const u32 was = tm - tmp;
            tm = rdl(fl, 5); dirty = 1;
            if ((int)(tm - rdl(fl, 4) - was) > 24) tm -= rdl(fl, 4);
            if ((++missed & 7u) == 0u) {
                *(LAS u32*)(lds + C_DUMP + (w == 0 ? 0 : 64)) = ld4(rs, foff(F_ERR));
                BAR();
                missed = uu(*(LAS u32*)(lds + C_DUMP)) != 0u ? SPIN_CAP : missed;
                st4s(rs, missed == SPIN_CAP ? foff(F_ERR) : OOB, 0, 1u);
            }
        }
    }
    DRAIN(); BAR();
    st4s(rs, ofl, 0, (l16 & 16u) ? rep : NSEG);
}

#define WARM1 0
#define WO_NI 4
#define WROT 0
DEV void role_wo(const Params& p, lptr lds, int j) {
    const int tid = threadIdx.x, lane = tid & 63, w = uni(tid >> 6), g = lane >> 4, r = lane & 15;
    const int rg = j & 1, cg = j >> 1, rot = cg & WROT;
    const rsrc_t rs = mk_rsrc(p.ws, W_END);
    MK_SRD();
    v4u praw; u32 code;
    v8u wo[8][5];
    {
        const v4u so_ = mk_srd(p.Wo, DM * NQ * 2);
        const int one = (0x3F2FF1F0u >> (4 * (cg & 7))) & 15;
        u32 vo[3];
#pragma unroll
        for (int i = 0; i < 3; ++i) { const int n = 64 * i + lane; vo[i] = n % 6 < 5 ? (u32)((n / 6) * (DM * 2) + cg * 80 + (n % 6) * 16) : ((n / 6) & 3) == one ? (u32)((n / 6) * (DM * 2) + (cg >> 3) * 640 + 304 + rg * 16) : OOB; }
        auto fill = [&](int b) {
            const u32 dst = uu(w * 36864 + (b % 3) * 12288), src = uu((8 * w + ((b + rot) & 7)) * (DH * DM * 2));
#pragma unroll
            for (int i = 0; i < 12; ++i) DMAIN(so_, dst + i * 1024, vo[i % 3], src + (i / 3) * (32 * DM * 2));
        };
#pragma unroll 1
        for (int b = 0; b < 3; ++b) fill(b);
        const int q4 = r >> 2;
        lptr tb = lds + w * 36864 + (32 * (g >> 1) + 16 * (q4 >> 1) + 8 * ((r >> 1) & 1) + 4 * (g & 1) + (q4 & 1)) * 96 + (r & 1) * 8;
        SFOR(b, 8)
            asm volatile("s_waitcnt vmcnt(%0) ; HZCLR" :: "i"(b < 5 ? 24 : 86 - 12 * b) : "memory");
#pragma unroll
            for (int t = 0; t < 5; ++t) {
                lptr q = tb + (b % 3) * 12288 + (t == 0 ? 4 : (w + t) & 3) * 16;
                wo[b][t] = tr_frag(q, q + 192, q + 64 * 96, q + 64 * 96 + 192);
                if (t < 4) asm volatile("" : "+a"(wo[b][t])); else asm volatile("" : "+v"(wo[b][t]));
            }
            CFENCE();
            if constexpr (b < 5) fill(b + 3);
            if constexpr (b == 4) { asm volatile("buffer_load_dwordx4 %0, %1, %2, 0 offen sc1" : "=v"(praw) : "v"(pol_off()), "s"(srd) : "memory"); code = code_fetch(); }
            SCHED();
        SEND
    }
    VMW(0); BAR(); SCHED(); PIN(praw); USE(code);
    EV(6);
    const u32 idx = (r & 2) ? 0xEEEEu : 0x4444u;
    bool dead = false;
    if (!boot_ok(p, pol_off(), praw, 0u)) praw.w = boot_wait(p, rs, pol_off(), 0u, dead);
    const u32 pol0 = pol_of(praw.w) & 1u;
    const u32 l16 = (u32)lane * 16u;
    u32 oo[8];
#pragma unroll
    for (int i = 0; i < 8; ++i) oo[i] = uu(W_ORING + ring_map((u32)(rg * 32768 + (8 * w + ((i + rot) & 7)) * 1024), pol0));
    const u32 hint = W_MRING + 4 * 32768u + pol0 * 8192u;
    u32 hvo[2];
#pragma unroll
    for (int l = 0; l < 2; ++l) hvo[l] = lane < 2 ? hint + (u32)(l ^ 1) * (MB / 4) + (u32)lane * 4u : lane == 2 ? hint + (u32)l * (MB / 4) + 128u : lane == 3 ? foff(F_ERR) : lane == 4 ? hint + (u32)l * (MB / 4) + 256u : OOB;
    const int xo = g * 128 + (r & 7) * 16, O = 5 * cg + (r < 8 ? w : 4), q2 = 2 + (r >> 3);
    lptr xw0 = lds + (r < 8 ? 2048 + w * 4 : ((w + 1) & 3) * 512 + 4) + xo, xw1 = lds + ((w + q2) & 3) * 512 + q2 * 4 + xo;
    lptr xr = lds + (r < 8 ? w : 4) * 512 + xo;
    if (r < 8) { *(LAS u32*)xr = 0u; *(LAS u32*)(xr + 8192) = 0u; }
    const u32 mpo = (r & 4) == 0 && (r < 8 || w == 0) ? W_MRING + ring_map((u32)((O >> 1) * 256 + rg * 128 + (4 * (g & 1) + 2 * (O & 1) + (g >> 1)) * 16 + (r & 3) * 4), pol0) : OOB;
    constexpr u32 PER0 = CH_W0 + CH_MW + CH_H0 + CH_MH;
    const bool lead = j == 0 && w == 3;
    u32 hso[2] = {OOB, OOB};
    u32 t = 0, tn = 0;
    int sn[2] = {0, WARM1 ? 0 : NSEG};
    u32 hav = 16 * CH_H0, wav = 16 * CH_W0, mx = 16 * CH_M0, ok[2] = {0u, 0u}, dw[2] = {0u, 0u};
    u32 zero = 0, rep = 0xFFFFu, failed = 1, pmp = OOB, mp = OOB, mq = OOB, cw = 0, vo = l16, tp = 0, tw1 = 0, stop = 0;
    f32x4 acc[5] = {};
    if (tid < 20) *(LAS u32*)(lds + 5632 + tid * 4) = 0u;
    unsigned long long clk = 0;
    const u32 psent = PSENT;
    lptr sw = lds + 5120 + w * 4;
    asm volatile("" : "+v"(zero), "+v"(sw), "+v"(xw0), "+v"(xw1), "+v"(xr));
    auto turn = [&](auto lc) __attribute__((always_inline)) {
        constexpr int L = decltype(lc)::value, M = L ^ 1;
        u32 done, rd, dh, dn, nt, za, zn, pdur, hn; v3u hw;
        t = uu(t); tn = uu(tn); tp = uu(tp); dw[L] = uu(dw[L]); failed = uu(failed); rep = uu(rep);
        const int sc = sn[L];
        auto settle = [&](auto ic) __attribute__((always_inline)) {
            constexpr int i = decltype(ic)::value;
            if constexpr (i == 0) { pdur = (u32)clk - tp; dw[L] += max((int)((u32)clk + CH_WX - t), 0); PINS(pdur); PINS(dw[L]); }
            if constexpr (i == 1) {
                asm volatile("v_or_b32 %0, %2, %3\n\tv_cmp_ne_u32 vcc, %4, %5\n\tv_or_b32 %1, %2, %5\n\tv_cndmask_b32 %1, %6, %1, vcc" : "=&v"(za), "=&v"(zn) : "s"(failed << 31), "v"(pmp), "v"(mq), "v"(mp), "v"(OOB) : "vcc");
                done = failed ^ 1u; PINS(done);
            }
            SCHED();
        };
        auto ahead = [&]() __attribute__((always_inline)) {
            u32 h0 = rdl(hn, 0), h1 = rdl(hn, 1), h2 = rdl(hn, 2), h3 = rdl(hn, 3), h4 = rdl(hn, 4);
            asm volatile("" : "+s"(h0), "+s"(h1), "+s"(h2), "+s"(h3), "+s"(h4));
            tp = t; t = lead ? tn : (int)(h0 - t) < 0 ? h0 + h1 : h0;
            tn = nt; stop |= h3; rep = max(h2, h4); tw1 = t - 1u;
            asm volatile("" : "+s"(tp), "+s"(t), "+s"(tn), "+s"(stop), "+s"(rep), "+s"(tw1));
            SCHED();
        };
        auto books = [&](auto ic) __attribute__((always_inline)) {
            constexpr int i = decltype(ic)::value;
            if constexpr (i == 0) { rd = rep & 0xFFFFu; PINS(rd); }
            if constexpr (i == 1) { dn = wav >> 4; PINS(dn); pdur = min(pdur, dn + CH_CLIP); PINS(pdur); }
            if constexpr (i == 2) { pdur = (done & ok[M]) ? pdur - dn : 0u; PINS(pdur); }
            if constexpr (i == 3) { wav += (u32)max((int)pdur, (int)(pdur << CH_ATT)); PINS(wav); }
            if constexpr (i == 4) { dh = hav >> 4; PINS(dh); nt = min(rd, dh + CH_CLIP); PINS(nt); }
            if constexpr (i == 5) { nt = rd < 0x3FFFu ? nt - dh : 0u; PINS(nt); }
            if constexpr (i == 6) { hav += (u32)max((int)nt, (int)(nt << CH_ATT)); PINS(hav); }
            if constexpr (i == 7) { rd = (ok[M] & ~done) * (CH_UP * 16u); PINS(rd); mx = (u32)max((int)(mx - CH_DEC), (int)rd); ok[M] = done; PINS(mx); }
            if constexpr (i == 8) { dh = (hav + mx) / 16u + (sn[1] < 2 && sn[0] >= CH_NEAR ? CH_MH + CH_SOLO : CH_MH); PINS(dh); }
            if constexpr (i == 9) { dn = (wav + mx) / 16u + (sn[0] < CH_NEAR ? CH_MW + CH_EARLY : CH_MW); PINS(dn); }
            if constexpr (i == 10) { nt = dw[L] + dh; dh += dn; dw[L] = dn; sn[M] += (int)done; PINS(nt); PINS(dh); PINS(sn[M]); }
            if constexpr (i == 11) { rd = tn - t + (dh >> 1); PINS(rd); }
            if constexpr (i == 12) { rd = sn[M] + 1 < NSEG ? rd : 0u; PINS(rd); }
            if constexpr (i == 13) { nt = t + (u32)max((int)nt, (int)rd); PINS(nt); }
            if constexpr (i == 14) asm volatile("v_mov_b32 %0, %1" : "=v"(hw.x) : "s"(nt));
            if constexpr (i == 15) asm volatile("v_mov_b32 %0, %1" : "=v"(hw.y) : "s"(dh));
            if constexpr (i == 16) asm volatile("v_mov_b32 %0, %1" : "=v"(hw.z) : "s"(nt + dn));
            SCHED();
        };
        if (__builtin_expect(sc + 1 < NSEG, 1)) {
            u32 zb;
#define WT(k) do {} while (0)
            v4u d[8];
            {
                u32 tmp;
                asm volatile(LOOP_TXT("%1", "%2") "buffer_load_dwordx4 %0, %3, %4, %5 offen sc1\n\ts_waitcnt lgkmcnt(0)"
                             : "=&v"(d[0]), "=&s"(tmp) : "s"(uu(tw1)), "v"(vo), "s"(srd), "s"(oo[0]) : "vcc", "scc", "memory");
            }
            WT(0);
            SFOR(i, 7)
                asm volatile("buffer_load_dwordx4 %0, %1, %2, %3 offen sc1" : "=v"(d[i + 1]) : "v"(vo), "s"(srd), "s"(oo[i + 1]) : "memory");
                SCHED();
                if constexpr (i == 0) { settle(IC<0>{}); static_for<0, 2>(books); }
                if constexpr (i == 1) { settle(IC<1>{}); books(IC<2>{}); }
                if constexpr (i == 2) {
                    mp = mpo + (L * (MB / 4) + (u32)((sc + 1) & 3) * 32768u); pmp = mpo + (L * (MB / 4) + (u32)(sc & 3) * 32768u);
                    zb = mp | ok[L] << 31;
                    PIN(mp); PIN(pmp); PIN(zb);
                }
                if constexpr (i == 3) static_for<3, 7>(books);
                if constexpr (i == 4) { books(IC<7>{}); books(IC<9>{}); }
                if constexpr (i == 5) { books(IC<8>{}); books(IC<10>{}); }
                SCHED();
            SEND
            LD4A(hn, hvo[L], 0);
            *(LAS u32*)(sw + L * 8192) = zero;
            SCHED();
            WT(1);
            float f0[5], f1[5], xs; unsigned long long bad; v4u sv;
#define WSW(o_) asm volatile("v_permlane32_swap_b32 %0, %1" : "+v"(f0[o_]), "+v"(f1[o_]))
#define WAD(o_) asm volatile("v_add_f32_dpp %0, %1, %0 row_ror:8 row_mask:0xf bank_mask:0xf\n\tv_add_f32_dpp %0, %2, %3 row_ror:8 row_mask:0xf bank_mask:0xc" : "+v"(f0[o_]) : "v"(f1[o_]), "v"(f0[o_ + 1]), "v"(f1[o_ + 1]))
#define WFO(o_) do { VADD(f0[o_], acc[o_][0], acc[o_][2]); VADD(f1[o_], acc[o_][1], acc[o_][3]); } while (0)
#define WOWN(k) do { if constexpr (WO_NI + (k) < 8) SMF_V(acc[4], d[(WO_NI + (k)) & 7], wo[(WO_NI + (k)) & 7][4]); SCHED(); } while (0)
            SFOR(i, 40)
                constexpr int pc = i / 5, oc = i % 5;
                if constexpr (oc == 0) VMW((pc == 0 ? 8 : 11) - pc);
                if constexpr (oc < 4) SMF_A(acc[oc], d[pc], wo[pc][oc]); else if constexpr (pc < WO_NI) SMF_V(acc[4], d[pc], wo[pc][4]);
                SCHED();
                if constexpr (i == 0) WT(2);
                if constexpr (i == 20) WT(3);
                if constexpr (i == 35) WT(4);
                if constexpr (i == 0) st4s(rs, zb, 0, psent);
                if constexpr (i == 1) st4s(rs, za, 0, psent);
                if constexpr (i == 2) st4s(rs, zn, 0, cw);
                if constexpr (i >= 5 && i < 8) books(IC<6 + i>{});
                if constexpr (i >= 10 && i < 13) books(IC<4 + i>{});
                if constexpr (i == 13) { xs = 0.f; PIN(xs); }
                if constexpr (i == 15) { vo = l16 + (M * (MB / 2) + (u32)(sn[M] & 3) * 131072u); PIN(vo); }
                if constexpr (i == 36) NOPS(1);
                if constexpr (i == 37) NOPS(2);
                SCHED();
            SEND
            WFO(0); WFO(1);
            asm volatile("v_cmp_u_f32 %0, %1, %2" : "=s"(bad) : "v"(f0[0]), "v"(f1[0]));
            WSW(0); WSW(1); WFO(2); WAD(0); WFO(3);
            *(LAS float*)(xw0 + L * 8192) = f0[0]; SCHED();
            WSW(2); WSW(3);
            if (__builtin_expect(bad != 0ull, 0)) {
                u32 mn = ~0u;
#pragma unroll
                for (int i = 0; i < 8; ++i) { u32 e = d[i].x; PIN(e); mn = min(mn, e ^ PSENT); }
                if (all_(mn != 0u)) {
#pragma unroll
                    for (int i = 0; i < 8; ++i) { v4u e = d[i]; PIN(e); mn = min(min(mn, e.y ^ PSENT), min(e.z ^ PSENT, e.w ^ PSENT)); }
                }
                *(LAS u32*)(sw + L * 8192) = all_(mn != 0u) ? 0u : 1u;
            }
            WAD(2);
            *(LAS float*)(xw1 + L * 8192) = f0[2]; SCHED();
            WOWN(0); WOWN(1); WOWN(2); WOWN(3);
            WT(5);
            f32x4 xa; float nb;
            asm volatile("s_waitcnt lgkmcnt(0)\n\ts_barrier\n\tds_read_b128 %0, %2 offset:%4\n\tds_read_b128 %1, %3 offset:%4+5120\n\ts_nop 3"
                         : "=&v"(xa), "=&v"(sv) : "v"(xr), "v"(zero), "i"(L * 8192) : "memory");
            WT(6);
            WFO(4);
            asm volatile("s_nop 0\n\tv_permlane32_swap_b32 %1, %2\n\ts_nop 1\n\tv_add_f32_dpp %0, %2, %1 row_ror:8 row_mask:0xf bank_mask:0x3\n\ts_waitcnt lgkmcnt(1)" : "+v"(xs), "+v"(f0[4]), "+v"(f1[4]));
            asm volatile("v_add_f32 %0, %0, %4\n\tv_add_f32 %1, %5, %6\n\tv_add_f32 %0, %0, %7\n\tv_add_f32 %0, %0, %1\n\tv_cmp_u_f32 vcc, %0, %0\n\ts_nop 0\n\t"
                         "v_mov_b32_dpp %1, %0 row_shl:4 row_mask:0xf bank_mask:0xf\n\tv_cndmask_b32 %3, %8, %9, vcc\n\tv_cvt_pk_bf16_f32 %2, %0, %1"
                         : "+v"(xs), "=&v"(nb), "=&v"(cw), "=&v"(mq) : "v"(xa[0]), "v"(xa[1]), "v"(xa[2]), "v"(xa[3]), "v"(mp), "v"(OOB) : "vcc");
            st4s(rs, mq, 0, cw);
            SCHED();
            WT(7);
            asm volatile("s_memrealtime %0" : "=s"(clk));
            __builtin_amdgcn_raw_buffer_store_b96(hw, rs, hso[L], 0, 16);
#pragma unroll
            for (int q = 0; q < 5; ++q) acc[q] = __builtin_bit_cast(f32x4, lds_r16(lds + zero + (5632 + 16 * q)));
            VMW(5); SCHED(); PIN(hn);
            asm volatile("s_waitcnt lgkmcnt(5)" ::: "memory");
            SCHED(); PIN(sv);
            failed = uu(sv.x | sv.y | sv.z | sv.w);
            ahead();
            asm volatile("s_waitcnt lgkmcnt(0)" : "+s"(clk) :: "memory");
        } else {
            BAR();
            static_for<0, 2>(settle);
            st4s(rs, za, 0, psent);
            st4s(rs, zn, 0, cw);
            static_for<0, 14>(books);
            failed = 1u;
            hn = ld4(rs, hvo[L]);
            vo = l16 + (M * (MB / 2) + (u32)(sn[M] & 3) * 131072u);
            ahead();
            clk = __builtin_amdgcn_s_memrealtime();
        }
    };
#pragma unroll 1
    for (int k = 0; k < 2 * SPIN_CAP && !stop && (sn[0] + 1 < NSEG || sn[1] + 1 < NSEG); ++k) {
        turn(IC<0>{}); turn(IC<1>{});
        if (__builtin_expect(k != 0, 1)) continue;
        if (lead) {
            u32 go = 0;
#pragma unroll 1
            for (int n = 0; n < SPIN_CAP && !all_(go != 0u); ++n) { CFENCE(); go = ld4(rs, foff(F_GO + (lane & 31))); }
            t = rt32();
            float last = (float)(int)(go - t);
            last = fmaxf(last, dpp<0xB1>(last)); last = fmaxf(last, dpp<0x4E>(last)); last = fmaxf(last, dpp<0x124>(last)); last = fmaxf(last, dpp<0x128>(last));
            t += uu((u32)min(max(CH_FIRST + (int)max16(last), CH_LEAD), CH_FIRST)); tn = t + PER0 / 2 + CH_REC;
            st16(rs, lane < 2 ? hint + (u32)lane * (MB / 4) : OOB, (v4u){lane == 0 ? t : tn, PER0, (lane == 0 ? t : tn) + (CH_W0 + CH_MW + CH_REC), 0u});
        } else {
            for (int n = 0; n < SPIN_CAP && !dead; ++n) {
                CFENCE();
                const u32 hv = ld4(rs, hvo[1]);
                t = rdl(hv, 0);
                if (rdl(hv, 1) < 400u) break;
                __builtin_amdgcn_s_sleep(1);
            }
        }
        hso[0] = (lead && lane == 0) ? hint : OOB; hso[1] = hso[0] + MB / 4;
        if (!WARM1) sn[1] = 0;
        rep = 0xFFFFu; dw[0] = dw[1] = CH_W0 + CH_MW + CH_REC;
        tw1 = t - 1u;
    }
    if (sn[0] + 1 < NSEG || sn[1] + 1 < NSEG) st4s(rs, tid == 0 ? foff(F_ERR) : OOB, 0, 1u);
}

__global__ void __launch_bounds__(256, 1) k_main(Args args) {
    Params p;
    (Args&)p = args;
    lptr lds = (lptr)(u32)16 - 16;
    if (blockIdx.x == 0) LST(256, rt32());
    asm volatile(".Lk_main_here:");
    const u32 code = code_fetch();
    asm volatile("" :: "s"(p.x), "s"(p.Wo), "s"(p.ws) : "memory");
    if (threadIdx.x == 0) {
        const v4u e = ld16s(mk_rsrc(p.ws, W_END), foff(F_EP), 0);
        const unsigned long long tag = (unsigned long long)e.y << 32 | (e.x + 1u);
        u32 role = ((u32)__builtin_amdgcn_s_getreg(20 | (3 << 11)) & 7u) * 32u + ((blockIdx.x >> 3) & 31u);
#pragma unroll 1
        for (int n = 0; n < 256; ++n, role = (role + 1u) & 255u)
            if (__hip_atomic_exchange((GAS unsigned long long*)(p.ws + W_SLOT) + role * 8u, tag, __ATOMIC_RELAXED, __HIP_MEMORY_SCOPE_AGENT) != tag) break;
        lds_w16(lds, (v4u){e.x + 1u, e.y, e.z, role});
    }
    __syncthreads();
    const v4u boot = lds_r16(lds);
    p.e0 = uu(boot.x); p.e1 = uu(boot.y); p.e2 = uu(boot.z);
    const int role = uni((int)boot.w), x = role >> 5, i = role & 31;
    asm volatile("s_waitcnt vmcnt(0)" :: "v"(code));
    __syncthreads();
    LST(8192 + blockIdx.x * 16, role);
    if (x < 2) { role_head(p, lds, i >> 4, x * 16 + (i & 15)); __builtin_amdgcn_endpgm(); }
    if (x == 2) { role_wo(p, lds, i); __builtin_amdgcn_endpgm(); }
    const int no = x == 3 ? 30 : x < 6 ? 5 : 0;
    if (x > 3 && i >= 8) { role_qkv(p, lds, (x - 4) * 24 + i - 8); __builtin_amdgcn_endpgm(); }
    const bool isout = i < no;
    const int og = x == 3 ? i / 5 : x + 2, ng = i % 5, L = og >> 2, kq = og & 3;
    const int k = i - no, w = uni(threadIdx.x >> 6);
    const int j = isout ? (L == 1 ? 0 : 44) + kq * 5 + ng : x == 3 ? 36 + k : k < 2 ? 12 + 2 * x + k : x < 6 ? 24 + x : k < 5 ? 10 + 3 * x + k : 15 + 3 * x + k;
    const rsrc_t rs = mk_rsrc(p.ws, W_END);
    wait_until(rt32() + (j < NKV ? ST_KV : ST_OUT));
    if (j < NKV) {
        const int nt = j < 320 - 7 * NKV ? 8 : 7;
#pragma unroll 1
        for (int q = 0; q < 4; ++q) kv_fetch(p, j + NKV * q, uu(q * KT_BYTES));
        Filt f;
        u32 warm[3], code2 = 0;
#pragma unroll 1
        for (int q = 0; q < nt; ++q) {
            VMW(15); BAR();
            if (q == 0) EV(2);
            if (q == 5) code2 = code_fetch();
            if (q == 4) boot_set(p, rs, threadIdx.x == 0 ? foff(F_KV + j) : OOB, 1u);
            kv_fetch(p, j + NKV * (q + 4), uu(((q + 4) & 7) * KT_BYTES));
            kv_tile(p, rs, lds + q * KT_BYTES, lds + (q == 0 ? 8 : q - 1) * KT_BYTES, j + NKV * q);
        }
        const int g = j < 20 ? j : j < 28 ? j + 16 : j - 8;
        filt_load<5, 0>(p, g, f);
        VMW(10); BAR(); USE(code2);
        boot_set(p, rs, threadIdx.x == 0 ? foff(F_KV + j) : OOB, 2u);
        EV(4);
        { bool dead = false; boot_wait(p, rs, pol_off(), 0u, dead); }
#pragma unroll
        for (int q = 0; q < 3; ++q)
            warm[q] = __builtin_amdgcn_raw_buffer_load_b32(mk_rsrc(p.ssmk, ACT), 176u * 4096u + ((u32)((q * NKV + j) * 256) + threadIdx.x) * 64u, 0, 0);
        if (g >= 36) {
            bool dead = false;
            boot_wait(p, rs, (threadIdx.x & 63) < 40 ? foff(F_KV + (threadIdx.x & 63)) : OOB, 1u, dead);
            f.a[4] = ld16s(rs, (threadIdx.x & 32) == 0 ? (threadIdx.x & 31) * 16u : OOB, W_KVT + (u32)chan(g, w) * (SEQ * 2));
            conv_job<4>(p, rs, lds, g, f, 0);
        } else conv_job<0>(p, rs, lds, g, f, 0);
        asm volatile("" :: "v"(warm[0]), "v"(warm[1]), "v"(warm[2]));
        const int g0 = j < 20 ? 36 + j : j < 36 ? 28 + j : 44 + j, g1 = j < 20 ? g0 + 1 : j < 36 ? 80 : 160;
        if (g >= 20 && g < 28) {
            filt_load<3, 0>(p, g + 16, f);
            conv_job<1>(p, rs, lds, g + 16, f, 0, threadIdx.x == 0 ? foff(F_CV + g) : OOB, 2u);
        }
        if (g < 36) filt_load<5, 0>(p, g0, f);
        DRAIN(); BAR();
        st4s(rs, threadIdx.x != 0 ? OOB : g >= 36 ? foff(F_GO - 60 + g) : foff(F_CV + (g >= 20 && g < 28 ? g + 16 : g)), 0, 2u);
        if (g < 36) {
#pragma unroll 1
            for (int n = g0; n < g1; n += 8) conv_job<2>(p, rs, lds, n, f, min(n + 8, 159));
            DRAIN(); BAR();
            st4s(rs, threadIdx.x == 0 ? foff(F_CV + g) : OOB, 0, 3u);
            if (j >= 36) { EV(15); __builtin_amdgcn_endpgm(); }
        }
    }
    if (!isout) { if (j < 28) role_row<0>(p, lds, (j - 20) * 4 + w); else role_row<1>(p, lds, (j - 28) * 4 + w); __builtin_amdgcn_endpgm(); }
    role_out(p, lds, L, kq, ng);
}

__global__ void k_end() { asm volatile(".Lk_end_here:"); }

extern "C" void kernel_launch(void* const* d_in, const int* in_sizes, int n_in, void* d_out, int out_size, void* d_ws, size_t ws_size,
                              hipStream_t stream) {
    Args p;
    p.x = (const u16*)d_in[0]; p.mem0 = (const u16*)d_in[1]; p.Wq = (const u16*)d_in[2]; p.Wk = (const u16*)d_in[3];
    p.Wv = (const u16*)d_in[4]; p.Wo = (const u16*)d_in[5];
    p.bk = (const u16*)d_in[8]; p.bv = (const u16*)d_in[9]; p.shift = (const u16*)d_in[10]; p.ssmk = (const u16*)d_in[11]; p.ssmD = (const u16*)d_in[12];
    p.out = (u16*)d_out;
    p.ws = (unsigned char*)(((size_t)d_ws + 2 * MB - 1) & ~(size_t)(2 * MB - 1));
    (void)hipFuncSetAttribute((const void*)k_main, hipFuncAttributeMaxDynamicSharedMemorySize, LDS_BYTES);
    hipLaunchKernelGGL(k_main, dim3(256), dim3(256), LDS_BYTES, stream, p);
}
```

```cpp
#include <hip/hip_runtime.h>
#include <hip/hip_bf16.h>

typedef unsigned short u16;
typedef unsigned int u32;
typedef u32 v2u __attribute__((ext_vector_type(2)));
typedef u32 v3u __attribute__((ext_vector_type(3)));
typedef u32 v4u __attribute__((ext_vector_type(4)));
typedef u32 v8u __attribute__((ext_vector_type(8)));
typedef float f32x2 __attribute__((ext_vector_type(2)));
typedef float f32x4 __attribute__((ext_vector_type(4)));
typedef float f32x16 __attribute__((ext_vector_type(16)));
typedef __bf16 bf16x2 __attribute__((ext_vector_type(2)));
typedef __bf16 bf16x8 __attribute__((ext_vector_type(8)));
typedef short s16x4 __attribute__((ext_vector_type(4)));
#define LAS __attribute__((address_space(3)))
#define GAS __attribute__((address_space(1)))
typedef LAS unsigned char* lptr;
#define DEV __device__ __forceinline__

constexpr int SEQ = 2048, DM = 640, NH = 32, DH = 128, NSEG = 64, NQ = NH * DH;

constexpr u32 MB = 1u << 20;
constexpr u32 W_ORING = 0;
constexpr u32 W_MRING = MB;
constexpr u32 W_CTRL = 4 * MB;
constexpr u32 ACT = SEQ * DM * 2;
constexpr u32 H_TILE = 128 * 1024;
constexpr u32 W_H2 = 8 * MB;
constexpr u32 W_KQ = 16 * MB;
constexpr u32 W_VO = W_KQ + 64 * MB;
constexpr u32 W_P = W_VO + 64 * MB;
constexpr u32 P_ITEM = 512 * 1024;
constexpr u32 W_H1 = W_P + 44 * MB, W_Y = W_H1 + 8 * MB, W_X2 = W_Y + ACT;
constexpr u32 W_KVT = W_X2 + ACT;
constexpr u32 W_END = W_KVT + 3 * MB;
constexpr u32 NREG = 12;
static_assert((size_t)W_END + 2 * MB <= 256ull * MB && W_H1 >= (NREG - 1) * 16 * MB + 2 * MB && W_H1 <= NREG * 16 * MB, "workspace");
constexpr u32 F_EP = 0, F_RG = 32, F_KV = 64, F_LN = 128,
              F_QKV = 320, F_O = F_QKV + 192, F_P = F_O + 64, F_H2 = F_P + 64, F_GO = F_H2 + 32, F_CV = F_H2 + 64, F_ERR = F_H2 + 16, F_N = F_CV + 192;
constexpr u32 W_SLOT = 3 * MB;
DEV u32 foff(u32 idx) { return W_CTRL + (idx >> 6) * 16384u + (idx & 63u) * 128u; }
DEV u32 near_(u32 o, u32 side, u32 pol) { return o + ((side ^ (pol >> (o >> 24)) ^ (o >> 21)) & 1u) * 8192u; }
DEV u32 p_item(int s) { return W_P + (u32)s * P_ITEM; }

constexpr u32 OOB = 0x80000000u;
constexpr u32 PSENT = 0x7F817F81u;
constexpr int SPIN_CAP = 40000;
constexpr int LDS_BYTES = 160 * 1024;
#define CALM 8
#define CALM_L 1
#define ST_KV 0
#define ST_OUT 0

#define EV(k) do {} while (0)
#define LST(idx, v) do {} while (0)
#define LS2(cond, idx) do {} while (0)
struct Args {
    const u16 *x, *mem0, *Wq, *Wk, *Wv, *Wo, *bk, *bv, *shift, *ssmk, *ssmD;
    u16* out;
    unsigned char* ws;
};
struct Params : Args { u32 e0, e1, e2; };

typedef __amdgpu_buffer_rsrc_t rsrc_t;
DEV rsrc_t mk_rsrc(const void* p, u32 bytes) { return __builtin_amdgcn_make_buffer_rsrc((void*)p, 0, bytes, 0x00020000); }
DEV v4u ld16s(rsrc_t r, u32 off, u32 so) { return __builtin_amdgcn_raw_buffer_load_b128(r, off, so, 16); }
DEV v2u ld8s(rsrc_t r, u32 off, u32 so) { return __builtin_amdgcn_raw_buffer_load_b64(r, off, so, 16); }
DEV v4u ldin16(rsrc_t r, u32 off, u32 so) { return __builtin_amdgcn_raw_buffer_load_b128(r, off, so, 0); }
DEV v2u ldin8(rsrc_t r, u32 off, u32 so) { return __builtin_amdgcn_raw_buffer_load_b64(r, off, so, 0); }
DEV u32 ld4(rsrc_t r, u32 off) { return __builtin_amdgcn_raw_buffer_load_b32(r, off, 0, 16); }
DEV void st16(rsrc_t r, u32 off, v4u v) { __builtin_amdgcn_raw_buffer_store_b128(v, r, off, 0, 16); }
DEV void st8s(rsrc_t r, u32 off, u32 so, v2u v) { __builtin_amdgcn_raw_buffer_store_b64(v, r, off, so, 16); }
DEV void st4s(rsrc_t r, u32 off, u32 so, u32 v) { __builtin_amdgcn_raw_buffer_store_b32(v, r, off, so, 16); }
#define DRAIN() asm volatile("s_waitcnt vmcnt(0)" ::: "memory")
#define VMW(n) asm volatile("s_waitcnt vmcnt(%0)" :: "i"(n) : "memory")
#define BAR() asm volatile("s_waitcnt lgkmcnt(0)\n\ts_barrier" ::: "memory")
#define SCHED() __builtin_amdgcn_sched_barrier(0)
#define CFENCE() asm volatile("" ::: "memory")
#define NOPS(n) asm volatile("s_nop %0" :: "i"(n))
#define PIN(x) asm volatile("" : "+v"(x))
#define PINS(x) asm volatile("" : "+s"(x))
#define USE(x) asm volatile("" :: "v"(x))

template <int V> struct IC { static constexpr int value = V; };
template <int I, int N, class F> DEV void static_for(F&& f) { if constexpr (I < N) { f(IC<I>{}); static_for<I + 1, N>(f); } }
#define SFOR(var, n) static_for<0, n>([&](auto ic_) __attribute__((always_inline)) { constexpr int var = decltype(ic_)::value;
#define SEND });

DEV u32 pk(float a, float b) { bf16x2 v = {(__bf16)a, (__bf16)b}; return __builtin_bit_cast(u32, v); }
DEV float lo_f(u32 u) { return __builtin_bit_cast(float, u << 16); }
DEV float hi_f(u32 u) { return __builtin_bit_cast(float, u & 0xffff0000u); }
DEV f32x2 up2(u32 u) { return (f32x2){lo_f(u), hi_f(u)}; }
DEV u32 pk2(f32x2 v) { return pk(v.x, v.y); }
DEV f32x2 rb2(f32x2 a) { f32x2 r; asm("v_cvt_pk_bf16_f32 %0, 0, %1" : "=v"(r.x) : "v"(a.x)); asm("v_cvt_pk_bf16_f32 %0, 0, %1" : "=v"(r.y) : "v"(a.y)); return r; }
DEV float rbf(float a) { return (float)(__bf16)a; }
DEV v4u lds_r16(lptr p) { return *(LAS v4u*)p; }
DEV void lds_w16(lptr p, v4u v) { *(LAS v4u*)p = v; }
DEV v2u lds_tr(lptr p) { return __builtin_bit_cast(v2u, __builtin_amdgcn_ds_read_tr16_b64_v4i16((LAS s16x4*)p)); }
DEV v4u tr2(lptr p0, lptr p1) { const v2u a = lds_tr(p0), b = lds_tr(p1); return (v4u){a.x, a.y, b.x, b.y}; }
DEV v8u cat8(v4u a, v4u b) { return __builtin_shufflevector(a, b, 0, 1, 2, 3, 4, 5, 6, 7); }
DEV f32x4 mfma16(v4u a, v4u b, f32x4 c) { return __builtin_amdgcn_mfma_f32_16x16x32_bf16(__builtin_bit_cast(bf16x8, a), __builtin_bit_cast(bf16x8, b), c, 0, 0, 0); }
DEV f32x16 mfma32(v4u a, v4u b, f32x16 c) { return __builtin_amdgcn_mfma_f32_32x32x16_bf16(__builtin_bit_cast(bf16x8, a), __builtin_bit_cast(bf16x8, b), c, 0, 0, 0); }

DEV float f_(u32 u) { return __builtin_bit_cast(float, u); }
DEV u32 u_(float f) { return __builtin_bit_cast(u32, f); }
template <int CTRL> DEV float dpp(float v) { return f_((u32)__builtin_amdgcn_update_dpp(0, (int)u_(v), CTRL, 0xf, 0xf, false)); }
DEV float max16(float v) { auto r = __builtin_amdgcn_permlane16_swap(u_(v), u_(v), false, false); return fmaxf(f_(r[0]), f_(r[1])); }
DEV float max32(float v) { auto r = __builtin_amdgcn_permlane32_swap(u_(v), u_(v), false, false); return fmaxf(f_(r[0]), f_(r[1])); }
DEV float add16(float v) { auto r = __builtin_amdgcn_permlane16_swap(u_(v), u_(v), false, false); return f_(r[0]) + f_(r[1]); }
DEV float add32(float v) { auto r = __builtin_amdgcn_permlane32_swap(u_(v), u_(v), false, false); return f_(r[0]) + f_(r[1]); }
DEV float wave_sum(float v) {
    v += dpp<0xB1>(v);
    v += dpp<0x4E>(v);
    v += dpp<0x124>(v);
    v += dpp<0x128>(v);
    return add32(add16(v));
}
DEV bool all_(bool c) { return __builtin_amdgcn_ballot_w64(!c) == 0ull; }
DEV int uni(int v) { return __builtin_amdgcn_readfirstlane(v); }
DEV u32 uu(u32 v) { return (u32)__builtin_amdgcn_readfirstlane((int)v); }
DEV u32 rdl(u32 v, int l) { return (u32)__builtin_amdgcn_readlane((int)v, l); }
DEV u32 rt32() { return (u32)__builtin_amdgcn_s_memrealtime(); }

DEV void wait_flags(rsrc_t rs, u32 off, u32 want, bool& dead, bool calm = false) {
    if (dead) return;
#pragma unroll 1
    for (int n = 0;; ++n) {
        CFENCE();
        const u32 v = ld4(rs, off);
        if (all_(off >= OOB || v >= want)) break;
        __builtin_amdgcn_s_sleep(1);
        if (calm) __builtin_amdgcn_s_sleep(CALM);
        if ((n & 63) == 63) { if (uni((int)ld4(rs, foff(F_ERR))) != 0) { dead = true; break; } }
        if (n > SPIN_CAP) { st4s(rs, foff(F_ERR), 0, 1u); dead = true; break; }
    }
    CFENCE();
}

DEV bool boot_ok(const Params& p, u32 off, v4u v, u32 want) { return all_(off >= OOB || (v.x == p.e0 && v.y == p.e1 && v.z == p.e2 && v.w >= want)); }
DEV void boot_set(const Params& p, rsrc_t rs, u32 off, u32 v) { st16(rs, off, (v4u){p.e0, p.e1, p.e2, v}); }
DEV u32 boot_wait(const Params& p, rsrc_t rs, u32 off, u32 want, bool& dead) {
    v4u v;
#pragma unroll 1
    for (int n = 0;; ++n) {
        CFENCE();
        v = ld16s(rs, off, 0);
        if (boot_ok(p, off, v, want) || dead) break;
        __builtin_amdgcn_s_sleep(1);
        if (n > SPIN_CAP) { st4s(rs, foff(F_ERR), 0, 1u); dead = true; }
    }
    CFENCE();
    return v.w;
}

DEV u32 pol_off() { const int lane = threadIdx.x & 63; return lane < 32 ? foff(F_RG + lane) : OOB; }
DEV u32 pol_of(u32 raw) {
    const int lane = threadIdx.x & 63;
    const float v = add16(__builtin_amdgcn_fmed3f((float)((int)raw - (1 << 20)), -250.f, 250.f));
    const float lo = dpp<0x121>(v), hi = dpp<0x12F>(v), ex = dpp<0x122>(v) + dpp<0x123>(v) + dpp<0x124>(v);
    const float sum = v + lo + ((lane & 15) == 11 ? 0.f : hi) + ((lane & 15) == 0 ? ex : 0.f);
    return (u32)__builtin_amdgcn_ballot_w64(sum >= 0.f) & 0xFFFFu;
}
DEV u32 get_pol(const Params& p, rsrc_t rs, bool& dead) { return pol_of(boot_wait(p, rs, pol_off(), 0u, dead)); }
DEV u32 hblk(u32 base, int ls, int h, u32 side, u32 pol) { return near_(base + (u32)(ls * NH + h) * 16384u, side, pol); }
DEV f32x2 ln_stats(const f32x2 (&x)[5]) {
    const float c = f_(uu(u_(x[0].x)));
    f32x2 d[5];
#pragma unroll
    for (int i = 0; i < 5; ++i) d[i] = x[i] - c;
    const f32x2 s2 = (d[0] + d[1]) + (d[2] + d[3]) + d[4], q = (d[0] * d[0] + d[1] * d[1]) + (d[2] * d[2] + (d[3] * d[3] + d[4] * d[4]));
    const float dm = wave_sum(s2.x + s2.y) * (1.0f / DM), mean = c + dm;
    const float var = fmaxf(wave_sum(q.x + q.y) * (1.0f / DM) - dm * dm, 0.f);
    const float sd = rbf(__builtin_amdgcn_sqrtf(rbf(rbf(var) + 1e-5f)));
    return (f32x2){rbf(mean), __builtin_amdgcn_rcpf(sd)};
}
DEV void store_h(rsrc_t rs, u32 tile, int row, const u32 (&hp)[5], u32 off = 0) {
    const u32 lane = threadIdx.x & 63;
    const u32 sw = (u32)(row & 15) * 16u | off, o8 = (lane >> 5) * 16384u + (((lane & 31u) * 8u) ^ sw), hb = tile + (u32)(row & 31) * 256u;
    st8s(rs, o8, hb, (v2u){hp[0], hp[1]});
    st8s(rs, o8, hb + 32768, (v2u){hp[2], hp[3]});
    st4s(rs, (lane * 4) ^ sw, hb + 65536, hp[4]);
}

DEV void ln_row(rsrc_t rs, int row, v2u a, v2u b, u32 c) {
    const f32x2 x[5] = {up2(a.x), up2(a.y), up2(b.x), up2(b.y), up2(c)};
    const f32x2 st = ln_stats(x);
    u32 hp[5];
#pragma unroll
    for (int q = 0; q < 5; ++q) hp[q] = pk2(up2(pk2(x[q] - st.x)) * st.y);
    store_h(rs, W_H1 + (u32)(row >> 5) * H_TILE, row, hp);
}

#define DMA16(ldsb, vo, so) asm volatile("s_mov_b32 m0, %0\n\ts_nop 0\n\tbuffer_load_dwordx4 %1, %2, %3 offen sc1 lds" :: "s"(ldsb), "v"(vo), "s"(srd), "s"(so) : "memory")
#define DMA16I(ldsb, lo, vo, so, go) do { u32 so_; asm volatile("s_add_u32 m0, %1, %5\n\ts_add_u32 %0, %4, %6\n\tbuffer_load_dwordx4 %2, %3, %0 offen sc1 lds" \
                         : "=&s"(so_) : "s"(ldsb), "v"(vo), "s"(srd), "s"(so), "i"(lo), "i"(go) : "memory", "scc"); } while (0)
#define DMA32(ldsb, vo, so) asm volatile("s_mov_b32 m0, %0\n\ts_nop 0\n\tbuffer_load_dwordx4 %1, %2, %3 offen sc1 lds\n\tbuffer_load_dwordx4 %1, %2, %3 offen offset:1024 sc1 lds" \
                         :: "s"(ldsb), "v"(vo), "s"(srd), "s"(so) : "memory")
#define INPOL "nt"
#define OUTAUX 16
#define INAUX 2
#define DMANT(sr, ldsb, vo, so) asm volatile("s_mov_b32 m0, %0\n\ts_nop 0\n\tbuffer_load_dwordx4 %1, %2, %3 offen " INPOL " lds" :: "s"(ldsb), "v"(vo), "s"(sr), "s"(so) : "memory")
#define DMAIN(sr, ldsb, vo, so) asm volatile("s_mov_b32 m0, %0\n\ts_nop 0\n\tbuffer_load_dwordx4 %1, %2, %3 offen lds" :: "s"(ldsb), "v"(vo), "s"(sr), "s"(so) : "memory")
DEV const u16* sel(bool c, const u16* a, const u16* b) { PINS(a); PINS(b); return c ? a : b; }
DEV v4u mk_srd(const void* q, u32 bytes) { return (v4u){uu((u32)(size_t)q), uu((u32)((size_t)q >> 32) & 0xffffu), bytes, 0x00020000u}; }
#define MK_SRD() const v4u srd = mk_srd(p.ws, W_END)
DEV u32 code_fetch() {
    unsigned long long pc; int back, len; u32 d;
    asm volatile("s_getpc_b64 %0\n9:\n\ts_mov_b32 %1, 9b-.Lk_main_here\n\ts_mov_b32 %2, .Lk_end_here-.Lk_main_here" : "=s"(pc), "=s"(back), "=s"(len));
    asm volatile("buffer_load_dword %0, %1, %2, 0 offen" : "=v"(d) : "v"((((blockIdx.x >> 3) & 31u) * 256u + threadIdx.x) * 16u), "s"(mk_srd((const void*)(pc - back), (u32)max(len, 0))) : "memory");
    return d;
}

constexpr int CV_PAD = 40;
constexpr int CV_PL = 4416;
constexpr int CV_F = 8 * CV_PL;
constexpr int CV_KB = 80;
constexpr int CV_KV = 65 * CV_KB;
constexpr int CV_CH = CV_F + CV_KV;
static_assert(CV_CH % 16 == 0 && 4 * CV_CH <= LDS_BYTES, "conv lds");

struct Filt { v4u a[5], b[5]; };
DEV int chan(int grp, int w) { return 16 * (grp >> 2) + (grp & 3) + 4 * w; }
template <int N = 5, int AUX = INAUX> DEV void filt_load(const Params& p, int grp, Filt& f) {
    const int tid = threadIdx.x, lane = tid & 63, d = chan(grp, tid >> 6);
    const rsrc_t rf = mk_rsrc(p.ssmk, ACT);
#pragma unroll
    for (int i = 0; i < N; ++i) {
        const u32 s0 = (u32)(8 * (lane + 64 * i) - CV_PAD);
        f.a[i] = __builtin_amdgcn_raw_buffer_load_b128(rf, s0 < SEQ ? (u32)(d * SEQ + s0) * 2u : OOB, 0, AUX);
        f.b[i] = __builtin_amdgcn_raw_buffer_load_b128(rf, s0 + 8 < SEQ ? (u32)(d * SEQ + s0 + 8) * 2u : OOB, 0, AUX);
    }
}
constexpr int KT_BYTES = 18 * 1024, KT_X = 64 * 144, NKV = 44, KT_DUMP = 8 * KT_BYTES + KT_X;
DEV void kv_fetch(const Params& p, int tile, u32 dst) {
    const int lane = threadIdx.x & 63, w = uni(threadIdx.x >> 6);
    const v4u sk = mk_srd(p.bk, ACT), sv = mk_srd(p.bv, ACT), ss = mk_srd(sel(w == 0, p.bk, p.shift), w == 0 ? ACT : DM * 8);
    const u32 vo = tile < 320 ? (u32)((lane >> 3) * DM + (tile % 10) * 64 + (lane & 7) * 8) * 2u : OOB, r0 = uu((u32)((tile / 10) * 64 + 8 * w) * (DM * 2)), d = uu(dst + w * 1024);
    DMANT(sk, d + 1024, vo, r0); DMANT(sk, d + 5120, vo, r0 + 32 * DM * 2); DMANT(sv, d + 9216, vo, r0); DMANT(sv, d + 13312, vo, r0 + 32 * DM * 2);
    DMAIN(ss, uu(w == 0 ? dst : w == 1 ? dst + 17408 : KT_DUMP), w == 0 ? (tile >= 10 ? vo : OOB) : w == 1 && lane < 32 ? (u32)lane * 16u : OOB,
          uu(w == 0 ? (tile >= 10 ? r0 - 8 * DM * 2 : 0u) : (u32)(tile % 10) * 512u));
}
DEV void kv_tile(const Params& p, rsrc_t rs, lptr src, lptr xt, int tile) {
    const int tid = threadIdx.x;
    const v4u sv = lds_r16(src + 17408 + (tid & 31) * 16);
    lptr kp = src + ((tid >> 5) * 8 + 5) * 128 + (tid & 31) * 4;
    f32x2 kf[11];
#pragma unroll
    for (int i = 0; i < 11; ++i) kf[i] = up2(*(LAS u32*)(kp + i * 128));
    const f32x2 sh[4] = {{lo_f(sv.x), lo_f(sv.z)}, {hi_f(sv.x), hi_f(sv.z)}, {lo_f(sv.y), lo_f(sv.w)}, {hi_f(sv.y), hi_f(sv.w)}};
    u32 kv[8];
#pragma unroll
    for (int i = 0; i < 8; ++i) {
        const f32x2 acc = rb2(rb2(rb2(rb2(kf[i] * sh[3]) + rb2(kf[i + 1] * sh[2])) + rb2(kf[i + 2] * sh[1])) + rb2(kf[i + 3] * sh[0]));
        kv[i] = pk2(acc * up2(*(LAS u32*)(kp + 9216 + (i - 5) * 128)));
    }
    lds_w16(xt + (2 * (tid & 31)) * 144 + (tid >> 5) * 16, (v4u){__builtin_amdgcn_perm(kv[6], kv[7], 0x05040100u), __builtin_amdgcn_perm(kv[4], kv[5], 0x05040100u),
                                                             __builtin_amdgcn_perm(kv[2], kv[3], 0x05040100u), __builtin_amdgcn_perm(kv[0], kv[1], 0x05040100u)});
    lds_w16(xt + (2 * (tid & 31) + 1) * 144 + (tid >> 5) * 16, (v4u){__builtin_amdgcn_perm(kv[6], kv[7], 0x07060302u), __builtin_amdgcn_perm(kv[4], kv[5], 0x07060302u),
                                                                 __builtin_amdgcn_perm(kv[2], kv[3], 0x07060302u), __builtin_amdgcn_perm(kv[0], kv[1], 0x07060302u)});
    BAR();
#pragma unroll
    for (int i = 0; i < 2; ++i) {
        const int ch = i * 32 + (tid >> 3);
        st16(rs, W_KVT + (u32)(((tile % 10) * 64 + ch) * SEQ + (tile / 10) * 64 + (tid & 7) * 8) * 2u, lds_r16(xt + ch * 144 + (tid & 7) * 16));
    }
}
template <int MODE> DEV void conv_job(const Params& p, rsrc_t rs, lptr lds, int grp, Filt& f, int next, u32 fo2 = OOB, u32 fv2 = 0) {
    const int tid = threadIdx.x, lane = tid & 63, w = uni(tid >> 6), bq = lane & 31, hh = lane >> 5;
    constexpr bool LO = MODE & 1, EARLY = MODE == 0 || MODE == 3, MINI = MODE == 4;
    constexpr int CV_PL = MINI ? 1088 : ::CV_PL, CV_F = 8 * CV_PL, CV_X = MINI ? CV_F + 17 * CV_KB : 3072, CV_CH = MINI ? CV_X + 1024 : ::CV_CH;
    lptr F = lds + w * CV_CH;
    lptr fa = F + (bq & 7) * CV_PL + ((bq >> 3) - hh + 4) * 16;
    lptr kb = F + CV_F + CV_KB + hh * 16, kh = kb + (bq + 32) * CV_KB;
    bool dead = false;
    v4u kvr[4];
    const u32 D = p.ssmD[chan(grp, w)];
    auto kv_load = [&]() {
        if (EARLY) boot_wait(p, rs, lane < NKV ? foff(F_KV + lane) : OOB, 2u, dead);
#pragma unroll
        for (int i = 0; i < (LO ? 2 : 4); ++i) kvr[i] = ld16s(rs, (u32)lane * 16u, W_KVT + (u32)(chan(grp, w) * SEQ + i * 512) * 2u);
    };
    auto windows = [&](int i) {
        if (i < 4 || lane < 16) {
            const u32 e[8] = {f.a[i].x, f.a[i].y, f.a[i].z, f.a[i].w, f.b[i].x, f.b[i].y, f.b[i].z, f.b[i].w};
            u32 o[7];
#pragma unroll
            for (int q = 0; q < 7; ++q) o[q] = __builtin_amdgcn_alignbit(e[q + 1], e[q], 16);
#pragma unroll
            for (int q = 0; q < 4; ++q) {
                lds_w16(F + (2 * q) * CV_PL + (64 * i + lane) * 16, (v4u){o[q], o[q + 1], o[q + 2], o[q + 3]});
                lds_w16(F + (2 * q + 1) * CV_PL + (64 * i + lane) * 16, (v4u){e[q + 1], e[q + 2], e[q + 3], e[q + 4]});
            }
        }
    };
    lptr yw = lds + (lane >> 4) * CV_CH + CV_X + (lane & 14) * 64 + w * 32 + (lane & 1) * 16, yr = F + CV_X + lane * 16;
    const u32 yo = W_Y + (u32)((((w & 1) * 8 + (lane >> 3)) * 8 + (w >> 1)) * DM + (grp & 3) * 160 + 4 * (grp >> 2)) * 32u + (u32)(lane & 7) * 16u;
    v4u dg[2];
    auto mf = [](v4u a, v4u b, f32x16 c) {
        if (!MINI) return mfma32(a, b, c);
        asm volatile("s_nop 1\n\tv_mfma_f32_32x32x16_bf16 %0, %1, %2, %0" : "+v"(c) : "v"(a), "v"(b));
        return c;
    };
    auto skip = [&](lptr k) { const f32x16 z = {}; return mf(lds_r16(k + 32), dg[1], mf(lds_r16(k), dg[0], z)); };
    auto pack = [&](const f32x16& ac, const f32x16& sk, int q) {
        const int r = 4 * (q & 3);
        u32 v[4];
#pragma unroll
        for (int i = 0; i < 4; ++i) v[i] = pk2(rb2((f32x2){ac[r + 2 * i], ac[r + 2 * i + 1]}) + rb2((f32x2){sk[r + 2 * i], sk[r + 2 * i + 1]}));
        const auto x = __builtin_amdgcn_permlane32_swap(v[0], v[2], false, false), y = __builtin_amdgcn_permlane32_swap(v[1], v[3], false, false);
        return (v4u){x[0], y[0], x[1], y[1]};
    };
    auto first = [&]() { asm volatile("s_waitcnt vmcnt(0)\n\ts_barrier" ::: "memory"); st4s(rs, EARLY ? (tid == 0 ? foff(F_CV + grp) : OOB) : fo2, 0, EARLY ? 1u : fv2); EV(12); };
    if (MODE != 2 && !MINI) EV(8);
    if (MINI) kvr[0] = f.a[4]; else if (!EARLY) kv_load();
    if (lane < CV_KB / 16) lds_w16(F + CV_F + lane * 16, (v4u){0u, 0u, 0u, 0u});
    windows(0);
    if (!MINI) { windows(1); windows(2); }
    if (!LO && !MINI) windows(4);
    if (EARLY) kv_load();
    if (MODE == 2) windows(3);
    if (MODE != 2 && !MINI) EV(9);
#pragma unroll
    for (int i = 0; i < (MINI ? 1 : LO ? 2 : 4); ++i) lds_w16(F + CV_F + (16 * i + (lane >> 2) + 1) * CV_KB + (lane & 3) * 16, kvr[i]);
#pragma unroll
    for (int ks = 0; ks < 2; ++ks)
#pragma unroll
        for (int q = 0; q < 4; ++q) { const int j = 16 * ks + 8 * hh + 7 - bq; dg[ks][q] = j == 2 * q ? D : j == 2 * q + 1 ? D << 16 : 0u; }
    f32x16 acc0 = {}, acc1 = {};
    if (MODE == 1) first();
    if (EARLY || MINI) {
        const int bk = MINI ? bq & 15 : bq;
        v4u fr[2][4];
        auto rd = [&](int m) { lptr kl = kb + max(bk - m, -1) * CV_KB; fr[m & 1][0] = lds_r16(kl); fr[m & 1][1] = lds_r16(fa + m * 64); fr[m & 1][2] = lds_r16(kl + 32); fr[m & 1][3] = lds_r16(fa + m * 64 - 32); };
        rd(0); rd(1);
        f32x16 sk = skip(kb + bk * CV_KB);
#pragma unroll
        for (int m = 0; m < 8; ++m) {
            acc0 = mf(fr[m & 1][0], fr[m & 1][1], acc0); acc0 = mf(fr[m & 1][2], fr[m & 1][3], acc0);
            if (m < 6) rd(m + 2);
            SCHED();
        }
        if (MINI) asm volatile("s_nop 11" : "+v"(acc0), "+v"(sk));
        lds_w16(yw, pack(acc0, sk, 0));
        BAR();
        st16(rs, w < 2 ? yo : OOB, lds_r16(yr));
        EV(11);
        if (MINI) { BAR(); return; }
    }
    if (LO) {
        constexpr int M0 = EARLY ? 8 : 0;
        v4u a[2][2], l[2][2];
#pragma unroll
        for (int u = 0; u < 2; ++u) { lptr kl = kb + max(bq - M0 - u, -1) * CV_KB; a[u][0] = lds_r16(fa + (M0 + u) * 64); l[u][0] = lds_r16(kl); a[u][1] = lds_r16(fa + (M0 + u) * 64 - 32); l[u][1] = lds_r16(kl + 32); }
#pragma unroll 1
        for (int m = M0; m < 32; m += 2) {
            if (MODE == 3 && m == 20) first();
#pragma unroll
            for (int u = 0; u < 2; ++u) {
                lptr kl = kb + max(bq - m - u - 2, -1) * CV_KB;
                acc0 = mfma32(l[u][0], a[u][0], acc0); a[u][0] = lds_r16(fa + (m + u + 2) * 64); l[u][0] = lds_r16(kl); SCHED();
                acc0 = mfma32(l[u][1], a[u][1], acc0); a[u][1] = lds_r16(fa + (m + u + 2) * 64 - 32); l[u][1] = lds_r16(kl + 32); SCHED();
            }
        }
    } else {
        if (MODE == 0) windows(3); else filt_load<5, 0>(p, next, f);
        v4u a0 = lds_r16(fa), a1 = lds_r16(fa - 32), c0 = lds_r16(fa + 2048), c1 = lds_r16(fa + 2048 - 32);
        v4u h0 = lds_r16(kh), h1 = lds_r16(kh + 32), l0 = lds_r16(kb + bq * CV_KB), l1 = lds_r16(kb + bq * CV_KB + 32);
#pragma unroll 2
        for (int m = 0; m < (MODE == 0 ? 8 : 32); ++m) {
            lptr kl = kb + max(bq - m - 1, -1) * CV_KB;
            if (MODE == 0 && m == 4) first();
            acc1 = mfma32(h0, a0, acc1); h0 = lds_r16(kh - (m + 1) * CV_KB); a0 = lds_r16(fa + (m + 1) * 64); SCHED();
            acc1 = mfma32(l0, c0, acc1); c0 = lds_r16(fa + (m + 1) * 64 + 2048); l0 = lds_r16(kl); SCHED();
            acc1 = mfma32(h1, a1, acc1); h1 = lds_r16(kh - (m + 1) * CV_KB + 32); a1 = lds_r16(fa + (m + 1) * 64 - 32); SCHED();
            acc1 = mfma32(l1, c1, acc1); c1 = lds_r16(fa + (m + 1) * 64 + 2048 - 32); l1 = lds_r16(kl + 32); SCHED();
        }
        if (MODE == 0) {
#pragma unroll 2
            for (int m = 8; m < 32; ++m) {
                lptr kl = kb + max(bq - m - 1, -1) * CV_KB;
                acc1 = mfma32(h0, a0, acc1); h0 = lds_r16(kh - (m + 1) * CV_KB); SCHED();
                acc0 = mfma32(l0, a0, acc0); a0 = lds_r16(fa + (m + 1) * 64); SCHED();
                acc1 = mfma32(l0, c0, acc1); c0 = lds_r16(fa + (m + 1) * 64 + 2048); l0 = lds_r16(kl); SCHED();
                acc1 = mfma32(h1, a1, acc1); h1 = lds_r16(kh - (m + 1) * CV_KB + 32); SCHED();
                acc0 = mfma32(l1, a1, acc0); a1 = lds_r16(fa + (m + 1) * 64 - 32); SCHED();
                acc1 = mfma32(l1, c1, acc1); c1 = lds_r16(fa + (m + 1) * 64 + 2048 - 32); l1 = lds_r16(kl + 32); SCHED();
            }
        }
    }
    if (MODE != 2) EV(13);
    v4u yy[4];
    if (MODE != 2) { const f32x16 sk = skip(kb + bq * CV_KB); yy[0] = pack(acc0, sk, 0); yy[1] = pack(acc0, sk, 2); }
    if (!LO) { const f32x16 sk = skip(kh); yy[2] = pack(acc1, sk, 0); yy[3] = pack(acc1, sk, 2); BAR(); }
#pragma unroll
    for (int q = (MODE == 2 ? 2 : 0); q < (LO ? 2 : 4); ++q) lds_w16(yw + q * CV_PL, yy[q]);
    BAR();
#pragma unroll
    for (int q = (MODE == 2 ? 2 : 0); q < (LO ? 2 : 4); ++q) st16(rs, q == 0 && w < 2 ? OOB : yo + (u32)q * (2 * DM * 32), lds_r16(yr + q * CV_PL));
    BAR();
    if (MODE != 2) EV(14);
}

#define NPROBE 6
DEV int probe(rsrc_t rs, int c) {
    const int lane = threadIdx.x & 63;
    const u32 k = (u32)(c & 15) < NREG ? (u32)(c & 15) : 0u;
    const u32 off0 = k * (16 * MB) + 2 * MB - 65536 + ((u32)(c & 15) >= NREG ? (u32)(c & 3) + 1u : 0u) * 256 + (u32)(c >> 4) * 128;
    st4s(rs, lane == 0 ? off0 : OOB, 0, 0u); DRAIN();
    int d0 = 1 << 16, d1 = 1 << 16;
#pragma unroll 1
    for (int q = 0; q < NPROBE; ++q) {
        const u32 o = lane == 0 ? off0 + (u32)(q & 1) * 8192 + (u32)(q >> 1) * 18432 : OOB;
        const unsigned long long t0 = __builtin_amdgcn_s_memtime();
        st4s(rs, o, 0, 0u); DRAIN();
        const int dt = (int)(__builtin_amdgcn_s_memtime() - t0);
        if (q & 1) d1 = min(d1, dt); else d0 = min(d0, dt);
    }
    const u32 xcc = (u32)__builtin_amdgcn_s_getreg(20 | (3 << 11)) & 0xfu;
    return (d0 - d1) * ((xcc & 4u) ? -1 : 1);
}
DEV int pre_rings(const Params& p, rsrc_t rs, int j) {
    const u32 gt = (u32)(j * 256 + threadIdx.x) * 16u, n = ((gt >> 14) << 9) + ((gt & 8191u) >> 4);
    const v4u ps = {PSENT, PSENT, PSENT, PSENT};
    v4u m = ps;
    if (n < 640u) m = *(const v4u*)(p.mem0 + (2 * ((n >> 2) & 3) + (n & 1)) * DM + (n >> 4) * 16 + (n & 2) * 4);
    int vote = 0;
    if (uni(threadIdx.x >> 6) == 0) vote = probe(rs, j);
    BAR();
    EV(2);
#pragma unroll
    for (u32 i = 0; i < 8; ++i) st16(rs, W_ORING + gt + i * 131072u, ps);
#pragma unroll
    for (u32 i = 0; i < 2; ++i) st16(rs, gt < 16384u ? W_MRING + i * (MB / 4) + 131072u + gt : OOB, ps);
    if (n < 640u) {
        m = (v4u){__builtin_amdgcn_perm(m.z, m.x, 0x05040100u), __builtin_amdgcn_perm(m.z, m.x, 0x07060302u), __builtin_amdgcn_perm(m.w, m.y, 0x05040100u), __builtin_amdgcn_perm(m.w, m.y, 0x07060302u)};
#pragma unroll
        for (int i = 0; i < 4; ++i) m[i] = m[i] == PSENT ? 0x7FC17FC1u : m[i];
    }
    st16(rs, W_MRING + gt, m);
    st16(rs, W_MRING + MB / 4 + gt, m);
    static_assert(2 * (F_N - F_QKV) == 32 * 36, "clear");
    st4s(rs, threadIdx.x < 36 ? foff(F_QKV) + (u32)(j * 36 + threadIdx.x) * 128u : OOB, 0, 0u);
    return vote;
}

#define M32A(first, d, a, b) do { if (first) asm volatile("v_mfma_f32_32x32x16_bf16 %0, %1, %2, 0" : "=&v"(d) : "a"(a), "v"(b)); \
                                  else asm volatile("v_mfma_f32_32x32x16_bf16 %0, %1, %2, %0" : "+v"(d) : "a"(a), "v"(b)); } while (0)
#define M32B(first, d, a, b) do { if (first) asm volatile("v_mfma_f32_32x32x16_bf16 %0, %1, %2, 0" : "=&v"(d) : "v"(a), "a"(b)); \
                                  else asm volatile("v_mfma_f32_32x32x16_bf16 %0, %1, %2, %0" : "+v"(d) : "v"(a), "a"(b)); } while (0)
#define VCVT(d, a, b) asm volatile("v_cvt_pk_bf16_f32 %0, %1, %2" : "=v"(d) : "v"(a), "v"(b))
#define LD4A(d, vo, so) asm volatile("buffer_load_dword %0, %1, %2, %3 offen sc1" : "=v"(d) : "v"(vo), "s"(srd), "s"(so) : "memory")
DEV u32 wst_src(int lane, int ld) { return (u32)((lane >> 4) * ld + 8 * ((lane & 15) ^ ((lane >> 4) << 2))) * 2u; }
DEV u32 wst_rd(int lane, int w) { const int q4 = (lane >> 2) & 3; return (u32)(q4 * 256 + ((w ^ q4) * 64) + ((lane >> 4) & 1) * 32 + (lane & 3) * 8); }

constexpr int QK_A = 65536;
#define QLEAD 3
#define QFIRST 2
static_assert(2 * QFIRST <= 12, "the first rounds stay within the first LN rows");
static_assert(QFIRST < QLEAD, "a round of the lead carries the flag of the convolution");
#define QW0 10
#define QW1 3
#define QSAMP 72
#define QPK1(d, a, a_, q_) do { if constexpr (V) VCVT(d[q_], a[4 * (a_) + 8 * ((q_) >> 1) + 2 * ((q_) & 1)], a[4 * (a_) + 8 * ((q_) >> 1) + 2 * ((q_) & 1) + 1]); else VCVT(d[q_], a[4 * (a_) + (q_)], a[4 * (a_) + (q_) + 8]); } while (0)
#define QSC(a, i) asm volatile("v_mul_f32 %0, %2, %0\n\tv_mul_f32 %1, %2, %1" : "+v"(a[i]), "+v"(a[(i) + 8]) : "s"(ksc))
#define QPK(d, a, a_, q_) do { if constexpr (!V) { QSC(a, 4 * (a_) + (q_)); QSC(a, 4 * (a_) + (q_) + 1); } QPK1(d, a, a_, q_); QPK1(d, a, a_, (q_) + 1); } while (0)
DEV void role_qkv(const Params& p, lptr lds, int c) {
    const int grp = 64 + c;
    const int tid = threadIdx.x, lane = tid & 63, w = uni(tid >> 6), hh = lane >> 5, r32 = lane & 31;
    const int m = c >> 5, h = c & 31;
    const rsrc_t rs = mk_rsrc(p.ws, W_END);
    v4u wt[40];
    Filt flt;
    bool dead = false;
    const u32 ksc = m == 1 ? 0x3e0293eeu : 0x3f800000u;
    u32 pol, h2flag, fo, ol, ar[8];
    const u32 ds = uu(w * 1024), dx = ds + QK_A;
#define XRD(ks) lds_r16(lds + ar[(ks) & 7] + ((ks) >> 3) * 8192)
    const u32 t16 = (u32)tid * 16u, lnf = tid < 96 ? foff(F_LN + tid) : OOB;
    MK_SRD();
#define QDMA(i, dst, vo, so) DMA16I(dst, (i) * 4096, vo, so, ((i) >> 1) * 16384 + ((i) & 1) * 4096)
    {
        const v4u sw = mk_srd(sel(m == 0, p.Wq, sel(m == 1, p.Wk, p.Wv)), DM * NQ * 2), sx = mk_srd(p.x, ACT);
        const u32 vo = wst_src(lane, NQ) + (u32)h * (DH * 2), x8 = (u32)lane * 8u, x4 = (u32)lane * 4u;
        v2u xa[6], xb[6]; u32 xc[6];
#pragma unroll
        for (int i = 0; i < 6; ++i)
            asm volatile("buffer_load_dwordx2 %0, %3, %5, %6 offen\n\tbuffer_load_dwordx2 %1, %3, %5, %6 offen offset:512\n\tbuffer_load_dword %2, %4, %5, %6 offen offset:1024"
                         : "=&v"(xa[i]), "=&v"(xb[i]), "=&v"(xc[i]) : "v"(x8), "v"(x4), "s"(sx), "s"(uu(min(4 * c + w + 384 * i, SEQ - 1) * (DM * 2))) : "memory");
        EV(1);
        auto wfill = [&](int i0, int n) {
#pragma unroll 1
            for (int i = i0; i < i0 + n; ++i) DMAIN(sw, uu((4 * i + w) * 1024), vo, uu((16 * i + 4 * w) * (NQ * 2)));
        };
        const int bl = (int)((sw.x >> 8) + h) & 31, hn = (bl + 2) & 3;
        const u32 va = hn == 3 || bl == 31 ? OOB : vo + (u32)((((h + 3 - hn) & 3) - (h & 3)) * 256), vb = hn == 3 || (bl >> 2) != 6 ? OOB : vo + (u32)((31 - bl) * 256);
        wfill(0, QW0);
        asm volatile("s_waitcnt vmcnt(%0) ; HZCLR" :: "i"(QW0) : "memory");
        EV(2);
#pragma unroll
        for (int i = 0; i < 6; ++i) {
            wfill(QW0 + QW1 * i, QW1);
            DMAIN(sw, uu((4 * 39 + w) * 1024), i < 4 ? va : vb, uu((16 * (39 - 3 * (i & 3) - hn) + 4 * w) * (NQ * 2)));
            if (i == 2) { VMW(2 * QW1 + 5); BAR(); boot_set(p, rs, tid == 0 ? foff(F_LN + c) : OOB, 1u); }
            asm volatile("" : "+v"(xa[i]), "+v"(xb[i]), "+v"(xc[i]));
            const int row = 4 * c + w + 384 * i;
            if (i < 5 || row < SEQ) {
                ln_row(rs, row, xa[i], xb[i], xc[i]);
            }
        }
        EV(3);
        constexpr int K0 = QW0 + 6 * QW1;
        static_assert(K0 * 4096 >= QK_A + 40960, "the first tile's place is free");
        v4u fv, praw;
        asm volatile("buffer_load_dwordx4 %0, %2, %4, 0 offen sc1\n\tbuffer_load_dwordx4 %1, %3, %4, 0 offen sc1" : "=&v"(praw), "=&v"(fv) : "v"(pol_off()), "v"(lnf), "s"(srd) : "memory");
        const u32 code = code_fetch();
        wfill(K0, 40 - K0);
        asm volatile("s_waitcnt vmcnt(%2) ; HZCLR\n\ts_barrier" : "+v"(praw), "+v"(fv) : "i"(40 - K0), "v"(code) : "memory");
        boot_set(p, rs, tid == 0 ? foff(F_LN + c) : OOB, 2u);
        EV(4);
        lptr tb = lds + hh * 2048 + wst_rd(lane, w);
#define WCV(k) do { lptr q_ = tb + ((k) >> 3) * 32768 + ((k) & 7) * 4096; wt[k] = tr2(q_, q_ + 1024); } while (0)
        SFOR(k, K0) WCV(k); asm volatile("" : "+a"(wt[k])); SCHED(); SEND
        if (!boot_ok(p, lnf, fv, 1u)) boot_wait(p, rs, lnf, 1u, dead);
        if (!boot_ok(p, pol_off(), praw, 0u)) praw.w = boot_wait(p, rs, pol_off(), 0u, dead);
        BAR();
        EV(5);
        { const u32 so = W_H1; SFOR(i, 10) QDMA(i, ds, t16, so); SEND }
        { const u32 so = W_H1 + H_TILE; SFOR(i, 10) QDMA(i, dx, t16, so); SEND }
        pol = pol_of(praw.w);
        h2flag = tid < 8 ? near_(foff(F_H2 + lane), 1u, pol) : OOB;
        ol = (m == 2 ? (u32)(2 * w + (r32 >> 4)) * 1024u : (u32)((r32 >> 4) * 4 + w) * 1024u) + (u32)(hh * 16 + (r32 & 15)) * 16u;
        fo = near_(foff(F_QKV + m * 64 + h), 0u, pol);
        u32 oa = ol + near_((m == 2 ? W_VO : W_KQ) + (u32)h * 16384u, m == 1 ? 0u : 1u, pol);
#pragma unroll
        for (int j = 0; j < 8; ++j) { ar[j] = (u32)(r32 * 256 + ((2 * j + hh) ^ (r32 & 15)) * 16); PIN(ar[j]); }
        PIN(h2flag); PIN(ol); PIN(oa); PINS(fo); PINS(pol);
        VMW(10); BAR();
        EV(6);
        auto first = [&](auto isv) __attribute__((always_inline)) {
            constexpr bool V = decltype(isv)::value;
            v4u x[8], pp, pq; f32x16 a;
#pragma unroll
            for (int ks = 0; ks < 5; ++ks) x[ks] = XRD(ks);
            SFOR(ks, 40)
                if constexpr (V) M32B(ks == 0, a, x[ks & 7], wt[ks]); else M32A(ks == 0, a, wt[ks], x[ks & 7]);
                if constexpr (ks < 35) x[(ks + 5) & 7] = XRD(ks + 5);
                if constexpr (ks % 2 == 0 && K0 + ks / 2 < 40) WCV(K0 + ks / 2);
                SCHED();
                if constexpr (ks >= 3 && ks % 2 == 1 && K0 + (ks - 3) / 2 < 40) asm volatile("" : "+a"(wt[K0 + (ks - 3) / 2]));
                if constexpr (ks >= 27 && ks < 35) asm volatile("v_xor_b32 %0, 0x10000, %0" : "+v"(ar[(ks + 5) & 7]));
                SCHED();
            SEND
            NOPS(11);
            QPK(pp, a, 0, 0); QPK(pp, a, 0, 2); st16(rs, oa, pp);
            QPK(pq, a, 1, 0); QPK(pq, a, 1, 2); st16(rs, oa + 512, pq);
        };
        if (m == 2) first(IC<1>{}); else first(IC<0>{});
    }
    int n0 = 0, n1 = -QLEAD;
    const u32 bfo = tid < 192 ? lnf : tid - 192u < NKV ? foff(F_KV - 192 + tid) : OOB;
    v4u brk = {};
    auto run = [&](auto isv) __attribute__((always_inline)) {
      constexpr bool V = decltype(isv)::value;
      v4u x[8] = {};
      f32x16 a0 = {};
      bool fresh = true;
      u32 fv = 0, fo0 = tid == 192 ? fo : n1 > -QLEAD && tid == 194 ? foff(F_CV + grp) : OOB, fo1 = tid == 192 ? fo : tid == 193 ? fo + 32 * 128 : OOB;
      PIN(fo0); PIN(fo1);
      if (n1 == -QLEAD) { DRAIN(); BAR(); st4s(rs, fo0, 0, 1u); EV(7); }
#define QS(k) do {} while (0)
#pragma unroll 1
      for (const int end = n1 == -QLEAD ? QFIRST - QLEAD : NSEG; n1 < end; ++n1) {
        const int cur = n1 < 0 ? n0 + 1 : 64 | n1, nxt = n1 < 0 ? n0 + 2 : n0 + 1;
        u32 xs = n1 < 0 ? W_H1 + (u32)cur * H_TILE : near_(W_H2 + (u32)n1 * H_TILE, 1u, pol);
        PINS(xs);
        QS(0);
        if (w == 0 && n1 >= 0 && ((u32)__builtin_amdgcn_ballot_w64(fv <= (u32)n1) & 255u) != 0u) wait_flags(rs, h2flag, (u32)n1 + 1, dead);
        BAR();
        QS(1);
        f32x16 a1;
        u32 ss, sv, hb, oa, qfo, qfv;
        v4u pp, pq;
        SCHED();
        auto slot = [&](auto zc) __attribute__((always_inline)) {
            constexpr int z = decltype(zc)::value, ks = (z + 20) % 40;
            if constexpr (z < 20) { if constexpr (V) M32B(false, a0, x[ks & 7], wt[ks]); else M32A(false, a0, wt[ks], x[ks & 7]); }
            else if constexpr (z < 60) { if constexpr (V) M32B(z == 20, a1, x[ks & 7], wt[ks]); else M32A(z == 20, a1, wt[ks], x[ks & 7]); }
            else if constexpr (z == 60) {
                if constexpr (V) asm volatile("v_mfma_f32_32x32x16_bf16 %0, %1, %2, 0" : "+v"(a0) : "v"(x[ks & 7]), "a"(wt[ks]));
                else asm volatile("v_mfma_f32_32x32x16_bf16 %0, %1, %2, 0" : "+v"(a0) : "a"(wt[ks]), "v"(x[ks & 7]));
            } else { if constexpr (V) M32B(false, a0, x[ks & 7], wt[ks]); else M32A(false, a0, wt[ks], x[ks & 7]); }
            if constexpr (z == 18) { QS(2); asm volatile("s_waitcnt vmcnt(6) lgkmcnt(0)\n\ts_barrier" ::: "memory"); QS(3); }
            if constexpr (z < 15 || z > 20) x[(ks + 5) & 7] = XRD((ks + 5) % 40);
            if constexpr (z >= 18 && z <= 20) { x[2 * (z - 18)] = XRD(2 * (z - 18)); x[2 * (z - 18) + 1] = XRD(2 * (z - 18) + 1); }
            SCHED();
            if constexpr (z < 4) QDMA(z, dx, t16, xs);
            if constexpr (z >= 5 && z < 17 && (z & 1)) QDMA((z + 3) >> 1, dx, t16, xs);
            if constexpr ((z >= 7 && z < 15) || (z >= 47 && z < 55)) asm volatile("v_xor_b32 %0, 0x10000, %0" : "+v"(ar[(ks + 5) & 7]));
            if constexpr (z == 16) { ss = W_H1 + (u32)nxt * H_TILE; PINS(ss); }
            if constexpr (z == 17) { sv = t16 | (nxt >= NSEG ? OOB : 0u); PIN(sv); }
            if constexpr (z >= 21 && z < 41 && (z & 1)) QDMA((z - 21) >> 1, ds, sv, ss);
            if constexpr (z == 22 || z == 24) QPK(pp, a0, 0, z - 22);
            if constexpr (z == 26 || z == 28) QPK(pq, a0, 1, z - 26);
            if constexpr (z == 30) { QS(4); asm volatile("s_waitcnt vmcnt(5)\n\ts_barrier" ::: "memory"); QS(5); }
            if constexpr (z == 32 || z == 44) { hb = (m == 2 ? W_VO : W_KQ) + (u32)((z == 32 ? n0 : cur) * NH + h) * 16384u; PINS(hb); }
            if constexpr (z == 34 || z == 45) { hb = near_(hb, m == 1 ? 0u : 1u, pol); PINS(hb); }
            if constexpr (z == 36) { hb = n0 < NSEG && !fresh ? hb : OOB; PINS(hb); }
            if constexpr (z == 38 || z == 46) { oa = ol + hb; PIN(oa); }
            if constexpr (z == 41) st16(rs, oa, pp);
            if constexpr (z == 43) st16(rs, oa + 512, pq);
            if constexpr (z == 54) { QS(6); asm volatile("s_waitcnt vmcnt(4)\n\ts_barrier" ::: "memory"); QS(7); }
            if constexpr (z == 56) { if (__builtin_expect(n1 == QFIRST - QLEAD - 1, 0)) { filt_load<3, 0>(p, grp, flt); brk = ld16s(rs, bfo, 0); } }
            if constexpr (z == 60) NOPS(5);
            if constexpr (z == 63) st16(rs, oa, pp);
            if constexpr (z == 65) st16(rs, oa + 512, pq);
            if constexpr (z == 61 || z == 62) QPK(pp, a1, 0, 2 * (z - 61));
            if constexpr (z == 63 || z == 64) QPK(pq, a1, 1, 2 * (z - 63));
            if constexpr (z == 67) { qfo = n1 >= 0 ? fo1 : fo0; PIN(qfo); }
            if constexpr (z == 68) { qfv = tid == 193 ? (u32)n1 + 1u : (u32)min(nxt, NSEG); PIN(qfv); }
            if constexpr (z == QSAMP) LD4A(fv, h2flag, 0);
            SCHED();
        };
        if (__builtin_expect(!fresh, 1)) static_for<0, 16>(slot);
        static_for<16, 80>(slot);
        QS(8);
        asm volatile("s_waitcnt vmcnt(1)\n\ts_barrier" ::: "memory");
        st4s(rs, qfo, 0, qfv);
        QS(9);
        asm volatile("s_waitcnt vmcnt(1)" : "+v"(fv) :: "memory");
        n0 = nxt; fresh = false;
      }
      if (n1 < 0) {
        SFOR(z, 20)
            if constexpr (V) M32B(false, a0, x[(z + 20) & 7], wt[z + 20]); else M32A(false, a0, wt[z + 20], x[(z + 20) & 7]);
            if constexpr (z < 15) x[(z + 25) & 7] = XRD(z + 25);
            if constexpr (z >= 7 && z < 15) asm volatile("v_xor_b32 %0, 0x10000, %0" : "+v"(ar[(z + 25) & 7]));
            SCHED();
        SEND
        u32 oa = ol + near_((m == 2 ? W_VO : W_KQ) + (u32)(n0 * NH + h) * 16384u, m == 1 ? 0u : 1u, pol);
        v4u pp, pq;
        asm volatile("s_nop 11" : "+v"(oa));
        QPK(pp, a0, 0, 0); QPK(pp, a0, 0, 2); st16(rs, oa, pp);
        QPK(pq, a0, 1, 0); QPK(pq, a0, 1, 2); st16(rs, oa + 512, pq);
      }
    };
#pragma unroll 1
    for (;;) {
        if (m == 2) run(IC<1>{}); else run(IC<0>{});
        if (n1 > 0) break;
        if (!boot_ok(p, bfo, brk, 2u)) boot_wait(p, rs, bfo, 2u, dead);
        BAR();
        conv_job<1>(p, rs, lds, grp, flt, 0, tid == 1 ? fo : OOB, (u32)n0 + 1u);
        const u32 so = uu(W_H1 + (u32)(n0 + 1) * H_TILE);
        SFOR(i, 10) QDMA(i, dx, t16, so); SEND
    }
}

constexpr int OU_SEL = 131072;
#define OSAMP 46

DEV void role_out(const Params& p, lptr lds, int L, int kq, int ng) {
    const int tid = threadIdx.x, lane = tid & 63, w = uni(tid >> 6), hh = lane >> 5, r32 = lane & 31;
    const rsrc_t rs = mk_rsrc(p.ws, W_END);
    v4u wo[64];
    u32 code;
    Filt flt;
    bool dead = false;
    {
        const v4u sw = mk_srd(p.Wo, DM * NQ * 2);
        const u32 vo = wst_src(lane, DM) + (ng == 4 ? 1024u : (u32)((ng >> 1) * 512 + (ng & 1) * 128 + ((lane ^ (lane >> 2)) & 8) * 16));
        auto fill = [&](int b) {
#pragma unroll 1
            for (int i = 0; i < 8; ++i) DMAIN(sw, uu((b % 5) * 32768 + (4 * i + w) * 1024), vo, uu((kq * 1024 + b * 128 + 16 * i + 4 * w) * (DM * 2)));
        };
#pragma unroll 1
        for (int b = 0; b < 5; ++b) fill(b);
        lptr tb = lds + hh * 1024 + wst_rd(lane, w);
        SFOR(b, 8)
            VMW(b < 3 ? 32 : 63 - 8 * b); BAR();
            lptr q = tb + (b % 5) * 32768;
#pragma unroll
            for (int k = 0; k < 8; ++k) { lptr a = q + (k >> 1) * 8192 + (k & 1) * 2048; wo[8 * b + k] = tr2(a, a + 4096); if (b < 7) asm volatile("" : "+a"(wo[8 * b + k])); else PIN(wo[8 * b + k]); }
            if constexpr (b < 3) { BAR(); fill(b + 5); }
            if constexpr (b == 2) filt_load<3, 0>(p, L == 0 ? 44 + kq * 5 + ng : 1 << 12, flt);
            if constexpr (b == 2) code = code_fetch();
        SEND
    }
    EV(6);
    const u32 pol = get_pol(p, rs, dead);
    USE(code);
    if (L == 0) {
        BAR();
        conv_job<3>(p, rs, lds, 44 + kq * 5 + ng, flt, 0);
        DRAIN(); BAR();
        st4s(rs, tid == 0 ? foff(F_CV + 44 + kq * 5 + ng) : OOB, 0, 2u);
    }
    EV(15);
    MK_SRD();
    const u32 ofl = (lane < 8 && w == 0) ? near_(foff(F_O + L * 32 + kq * 8 + lane), 0u, pol) : OOB;
    const u32 l16 = (u32)lane * 16u;
    const u32 fo = (tid == 192) ? near_(foff(F_P + L * 32 + kq * 5 + ng), 1u, pol) : OOB;
    auto osrc = [&](int s) { return uu(hblk(W_VO, L * NSEG + s, kq * 8, 0u, pol) + w * 1024); };
#define ODMA(i, dst, vo, so) DMA16I(dst, (i) * 4096, vo, so, ((i) >> 1) * 16384 + ((i) & 1) * 4096)
    const u32 pvo = (u32)(kq * 5 + ng) * 16384u + (u32)w * 2048u + l16;
    lptr tw = lds + OU_SEL + 256 + r32 * 272 + (ng == 4 ? hh * 32 + w * 4 : (w & 1) * 128 + hh * 16 + (w >> 1) * 8), tr = lds + OU_SEL + 256 + (8 * w + (lane >> 4)) * 272 + (lane & 15) * 16;
    lptr selw = lds + OU_SEL + (w == 0 ? 0 : 64);
    u32 pfo = OOB;
    u32 buf = 0;
    if (w == 0) wait_flags(rs, ofl, 1u, dead);
    BAR();
    for (int s = 0; s < NSEG; ++s) {
        u32 src = osrc(s), dst = uu(buf + w * 1024);
        lptr A = lds + buf + l16;
        asm volatile("" : "+s"(src), "+s"(dst));
#define OS(k) do {} while (0)
        OS(0);
        OS(1);
        SFOR(i, 10) ODMA(i, dst, l16, src); SEND
        OS(2);
        asm volatile("s_waitcnt vmcnt(7)\n\ts_barrier" ::: "memory");
        OS(3);
        LS2(L == 0 && w == 1, 96 + kq * 5 + ng);
        v4u o[64];
#pragma unroll
        for (int k = 0; k < 3; ++k) o[k] = lds_r16(A + k * 1024);
        st4s(rs, pfo, 0, (u32)s);
        u32 fv, pa; int rdy;
        f32x16 acc;
        SCHED();
        SFOR(k, 64)
            if constexpr (k < 56) M32A(k == 0, acc, wo[k], o[k]); else asm volatile("v_mfma_f32_32x32x16_bf16 %0, %1, %2, %0" : "+v"(acc) : "v"(wo[k]), "v"(o[k]));
            if constexpr (k < 61) o[k + 3] = lds_r16(A + (k + 3) * 1024);
            SCHED();
            if constexpr (k < 12 && (k & 1) == 0) ODMA(10 + (k >> 1), dst, l16, src);
            if constexpr (k == 7) OS(4);
            if constexpr (k == 8) OS(5);
            if constexpr (k == 23) OS(6);
            if constexpr (k == 24) OS(7);
            if constexpr (k == 43) OS(8);
            if constexpr (k == 44) OS(9);
            if constexpr (k == 59) OS(10);
            if constexpr (k == 63) OS(11);
            if constexpr (k == 7) asm volatile("s_waitcnt vmcnt(8)\n\ts_barrier" ::: "memory");
            if constexpr (k == 23) asm volatile("s_waitcnt vmcnt(4)\n\ts_barrier" ::: "memory");
            if constexpr (k == 30) { pa = pvo + near_(p_item(s), (u32)L, pol); PIN(pa); }
            if constexpr (k == 43) asm volatile("s_waitcnt vmcnt(0)\n\ts_barrier" ::: "memory");
            if constexpr (k == OSAMP) LD4A(fv, ofl, 0);
            if constexpr (k == 62) {
                asm volatile("s_waitcnt vmcnt(0)" : "+v"(fv) :: "memory");
                *(LAS int*)selw = (int)((u32)__builtin_amdgcn_ballot_w64(fv < (u32)s + 2) & 255u);
            }
            SCHED();
        SEND
        asm volatile("s_nop 11" : "+v"(acc));
        u32 pc[8];
#pragma unroll
        for (int i = 0; i < 8; ++i) pc[i] = pk(acc[2 * i], acc[2 * i + 1]);
        if (ng != 4) {
#pragma unroll
            for (int i = 0; i < 4; ++i) *(LAS v2u*)(tw + i * 32) = (v2u){pc[2 * i], pc[2 * i + 1]};
        } else {
#pragma unroll
            for (int i = 0; i < 8; ++i) *(LAS u32*)(tw + (i >> 1) * 64 + (i & 1) * 16) = pc[i];
        }
        BAR();
        const v4u p0 = lds_r16(tr), p1 = lds_r16(tr + 4 * 272);
        rdy = *(LAS int*)(lds + OU_SEL);
        st16(rs, pa, p0); st16(rs, pa + 1024, p1);
        const bool ready = uni(rdy) == 0;
        SCHED();
        OS(12);
        if (ready) pfo = fo;
        else {
            LD4A(fv, ofl, 0);
            asm volatile("s_waitcnt vmcnt(1)\n\ts_barrier" ::: "memory");
            st4s(rs, fo, 0, (u32)s + 1); pfo = OOB;
            OS(14);
            LS2(L == 0 && w == 1, 160 + kq * 5 + ng);
            asm volatile("s_waitcnt vmcnt(1)" : "+v"(fv) :: "memory");
            if (w == 0 && s + 1 < NSEG && ((u32)__builtin_amdgcn_ballot_w64(fv < (u32)s + 2) & 255u) != 0u) wait_flags(rs, ofl, (u32)s + 2, dead, L >= CALM_L && s + 2 < NSEG);
            BAR();
        }
        OS(13);
        buf ^= 65536u;
    }
}

template <int L> DEV void role_row(const Params& p, lptr lds, int ri) {
    const int lane = threadIdx.x & 63, w = uni(threadIdx.x >> 6);
    const rsrc_t rs = mk_rsrc(p.ws, W_END);
    bool dead = false;
    EV(3);
    const u32 pol = get_pol(p, rs, dead);
    const u32 fp = threadIdx.x < 20 ? near_(foff(F_P + L * 32 + lane), 1u, pol) : OOB, fo = threadIdx.x != 192 ? OOB : L == 0 ? near_(foff(F_H2 + (ri >> 2)), 1u, pol) : ri == 3 ? foff(F_EP) : OOB;
    u32 fv = 0;
    MK_SRD();
    auto yfill = [&](int b, int i) { DMA32(uu((b & 1) * 40960 + w * 10240 + i * 2048), (u32)lane * 16u, uu(W_Y + (u32)((ri >> 1) * 8 + b) * 20480u + (w & 1) * 10240 + i * 2048)); };
    auto ywait = [&](u32 v) {
        wait_flags(rs, threadIdx.x < (v == 1u ? 96u : v == 2u ? 160u : 36u) ? foff(v == 1u && threadIdx.x - 36u < 8u ? F_GO - 60 + threadIdx.x : F_CV + threadIdx.x + (v == 1u && w == 1 ? 96u : 0u)) : OOB, v, dead);
    };
    v4u y8[10] = {};
    v2u xa, xb; u32 xc;
    const v4u sx = L == 0 ? mk_srd(p.x, ACT) : mk_srd(p.ws + W_X2, ACT);
    auto xload = [&](int s) {
        asm volatile("buffer_load_dwordx2 %0, %3, %5, %6 offen sc1\n\tbuffer_load_dwordx2 %1, %3, %5, %6 offen offset:512 sc1\n\tbuffer_load_dword %2, %4, %5, %6 offen offset:1024 sc1"
                     : "=&v"(xa), "=&v"(xb), "=&v"(xc) : "v"((u32)lane * 8u), "v"((u32)lane * 4u), "s"(sx), "s"(uu((u32)(min(s, NSEG - 1) * 32 + ri) * (DM * 2))) : "memory");
        SCHED();
    };
    if (L == 1) wait_flags(rs, lane == 0 ? near_(foff(F_H2 + (ri >> 2)), 1u, pol) : OOB, 1u, dead);
    EV(5);
    xa = xb = (v2u){0u, 0u}; xc = 0u;
    for (int s = -1; s < NSEG; ++s) {
        const u32 off = s < 0 ? OOB : 0u;
        u32 xi[5] = {xa.x, xa.y, xb.x, xb.y, xc};
        u32 pa = (near_(p_item(s), (u32)L, pol) + (u32)ri * 256u) | off, l8 = (u32)lane * 8u | off, l4 = (u32)lane * 4u | off, pc = pa + 65536u + (u32)(lane >> 4) * 81920u + (u32)(lane & 15) * 16u;
        pa += (u32)(lane >> 5) * 81920u + (u32)((lane >> 4) & 1) * 16384u + (u32)(lane & 15) * 16u;
        u32 yf = s == 2 ? foff(F_CV + min(threadIdx.x, 159u)) : s == 26 ? foff(F_CV + min(lane, 35)) : OOB;
        asm volatile("" : "+v"(xi[0]), "+v"(xi[1]), "+v"(xi[2]), "+v"(xi[3]), "+v"(xi[4]), "+v"(pa), "+v"(pc), "+v"(yf), "+v"(l8), "+v"(l4));
#define RS(k) do {} while (0)
        RS(0);
        if (w == 0 && ((u32)__builtin_amdgcn_ballot_w64(fv < (u32)(s + 1)) & 0xFFFFFu) != 0u) wait_flags(rs, fp, (u32)s + 1, dead);
        BAR();
        RS(1);
        v4u d[5];
        asm volatile("buffer_load_dwordx4 %0, %5, %7, 0 offen sc1\n\tbuffer_load_dwordx4 %1, %5, %7, %8 offen sc1\n\tbuffer_load_dwordx4 %2, %5, %7, %9 offen sc1\n\t"
                     "buffer_load_dwordx4 %3, %5, %7, %10 offen sc1\n\tbuffer_load_dwordx4 %4, %6, %7, 0 offen sc1"
                     : "=&v"(d[0]), "=&v"(d[1]), "=&v"(d[2]), "=&v"(d[3]), "=&v"(d[4]) : "v"(pa), "v"(pc), "s"(srd), "s"(163840), "s"(32768), "s"(196608) : "memory");
        RS(2);
        LS2(L == 0, 192 + ri);
        SCHED();
        f32x2 y[5];
        {
            if ((s & 7) == 0 || s < 0) {
                lptr ya = lds + ((s >> 3) & 1) * 40960 + (w >> 1) * 20480 + (w & 1) * 16, yc = ya + 4096 + (lane >> 1) * 32 + (lane & 1) * 10240;
#pragma unroll
                for (int i = 0; i < 8; ++i) y8[i] = lds_r16(ya + lane * 32 + (i >> 2) * 2048 + (i & 3) * 5120);
                y8[8] = lds_r16(yc); y8[9] = lds_r16(yc + 5120);
            }
#pragma unroll
            for (int i = 0; i < 10; ++i) {
                y[i >> 1][i & 1] = lo_f(y8[i].x);
                y8[i] = (v4u){__builtin_amdgcn_alignbit(y8[i].y, y8[i].x, 16), __builtin_amdgcn_alignbit(y8[i].z, y8[i].y, 16), __builtin_amdgcn_alignbit(y8[i].w, y8[i].z, 16), y8[i].w >> 16};
                PIN(y8[i]);
            }
            asm volatile("" : "+v"(y[0]), "+v"(y[1]), "+v"(y[2]), "+v"(y[3]), "+v"(y[4]));
        }
        u32 yv;
        {
            xload(s + 1);
            const bool yy = (s & 7) >= 3 && (u32)s < NSEG - 8;
            u32 yo = yy ? (u32)lane * 16u : OOB;
            DMA32(uu(yy ? (((s >> 3) + 1) & 1) * 40960 + w * 10240 + ((s & 7) - 3) * 2048 : 81920), yo, uu(W_Y + (u32)((ri >> 1) * 8 + (s >> 3) + 1) * 20480u + (w & 1) * 10240 + ((s & 7) - 3) * 2048));
            LD4A(yv, yf, 0);
            asm volatile("s_waitcnt vmcnt(6)" : "+v"(d[0]), "+v"(d[1]), "+v"(d[2]), "+v"(d[3]), "+v"(d[4]));
        }
        f32x2 o[5];
        {
            f32x2 t[5][2];
#pragma unroll
            for (int i = 0; i < 5; ++i) {
                const auto e = __builtin_amdgcn_permlane32_swap(d[i].x, d[i].z, false, false), f = __builtin_amdgcn_permlane32_swap(d[i].y, d[i].w, false, false);
                t[i][0] = up2(e[0]) + up2(e[1]); t[i][1] = up2(f[0]) + up2(f[1]);
            }
            o[0] = t[0][0] + t[1][0]; o[1] = t[0][1] + t[1][1]; o[2] = t[2][0] + t[3][0]; o[3] = t[2][1] + t[3][1];
            const auto e = __builtin_amdgcn_permlane16_swap(u_(t[4][0].x), u_(t[4][1].x), false, false), f = __builtin_amdgcn_permlane16_swap(u_(t[4][0].y), u_(t[4][1].y), false, false);
            o[4] = (f32x2){f_(e[0]) + f_(e[1]), f_(f[0]) + f_(f[1])};
        }
        f32x2 x1[5];
#pragma unroll
        for (int i = 0; i < 5; ++i) x1[i] = up2(pk2(up2(xi[i]) + up2(pk2(o[i]))));
        asm volatile("" : "+v"(x1[0]), "+v"(x1[1]), "+v"(x1[2]), "+v"(x1[3]), "+v"(x1[4]));
        RS(3);
        if (s < 0) EV(6);
        const f32x2 st1 = ln_stats(x1);
        u32 xp[5];
#pragma unroll
        for (int i = 0; i < 5; ++i) xp[i] = pk2(x1[i] + ((x1[i] - st1.x) * st1.y) * y[i]);
        const int row = max(s, 0) * 32 + ri;
        RS(4);
        if (L == 0) {
            const u32 ro = (u32)row * (DM * 2);
            f32x2 x2[5];
#pragma unroll
            for (int i = 0; i < 5; ++i) x2[i] = up2(xp[i]);
            st8s(rs, l8, W_X2 + ro, (v2u){xp[0], xp[1]});
            st8s(rs, l8 + 512, W_X2 + ro, (v2u){xp[2], xp[3]});
            st4s(rs, l4 + 1024, W_X2 + ro, xp[4]);
            SCHED();
            const f32x2 st2 = ln_stats(x2);
            u32 hp[5];
#pragma unroll
            for (int i = 0; i < 5; ++i) hp[i] = pk2((x2[i] - st2.x) * st2.y);
            store_h(rs, near_(W_H2 + (u32)s * H_TILE, 1u, pol), row, hp, off);
        } else {
            const rsrc_t ro = mk_rsrc(p.out, ACT);
            __builtin_amdgcn_raw_buffer_store_b64((v2u){xp[0], xp[1]}, ro, l8, (u32)row * (DM * 2), OUTAUX);
            __builtin_amdgcn_raw_buffer_store_b64((v2u){xp[2], xp[3]}, ro, l8 + 512u, (u32)row * (DM * 2), OUTAUX);
            __builtin_amdgcn_raw_buffer_store_b32(xp[4], ro, l4 + 1024u, (u32)row * (DM * 2), OUTAUX);
        }
        RS(5);
        if (L == 1 && s == NSEG - 1) __builtin_amdgcn_endpgm();
        LD4A(fv, fp, 0);
        if (L == 0) asm volatile("s_waitcnt vmcnt(1)\n\ts_barrier" ::: "memory");
        st4s(rs, L == 0 || s == 40 ? fo : OOB, 0, L == 0 ? (u32)s + 1 : p.e0);
        RS(6);
        VMW(1); SCHED();
        asm volatile("" : "+v"(fv), "+v"(yv), "+v"(xa), "+v"(xb), "+v"(xc));
        if (__builtin_expect(s == 2 || s == 26, 0) && !all_(yv >= (s == 2 ? 2u : 3u))) ywait(s == 2 ? 2u : 3u);
        if (__builtin_expect(s < 0, 0)) {
            EV(7);
            ywait(1u);
            BAR();
            EV(15);
#pragma unroll 1
            for (int i = 0; i < 5; ++i) yfill(0, i);
            fv = ld4(rs, fp);
            DRAIN();
        }
        RS(7);
        if (ri == 0 && s >= 0) LST(320 + L * 64 + s, rt32());
        LS2(L == 0, 224 + ri);
    }
}

constexpr float SCALE = 0.08838834764831845f, LOG2E = 1.4426950408889634f;
constexpr int C_K = 0, C_V = 8192, C_QX = 16384, C_MEM = 24576, C_MASK = C_MEM + 10240,
              C_Q = C_MASK + 1024, C_ZERO = C_Q + 2304, C_KM = C_Q + 4096, C_VM = C_KM + 4096, C_DUMP = C_VM + 2048, C_WL = C_DUMP + 1024;
constexpr int NLF = 14;
static_assert(C_WL % 16 == 0 && C_WL + 4 * NLF * 2048 <= LDS_BYTES, "head lds");
#define CH_H0 98
#define CH_W0 80
#define CH_MH 11
#define CH_MW 17
#define CH_REC 16
#define CH_LATE 30
#define CH_SOLO 6
#define CH_KV 12
#define CH_KV0 44
#define CH_M0 3
#define CH_DEC 2u
#define CH_EARLY 16
#define CH_NEAR 4
#define CH_FIRST 160
#define CH_ATT 2
#define CH_CLIP 12u
#define CH_LEAD 70
#define CH_UP 3
#define CH_WX 6
DEV void wait_until(u32 t) {
    unsigned long long c;
    asm volatile("1:\n\ts_memrealtime %0\n\ts_waitcnt lgkmcnt(0)\n\ts_mov_b64 vcc, %0\n\ts_sub_u32 vcc_lo, %1, vcc_lo\n\ts_cmpk_lt_u32 vcc_lo, 400\n\ts_cbranch_scc1 1b" : "=&s"(c) : "s"(uu(t - 1u)) : "vcc", "scc", "memory");
}
#define CH_FD 14
#define CH_FC 34
#define FINE_T1(t) uu((t) - (CH_FD + 1u))
#define FINE_T4(t) uu(4u * (t) - CH_FC)
#define NOP8 "s_nop 0\n\ts_nop 0\n\ts_nop 0\n\ts_nop 0\n\ts_nop 0\n\ts_nop 0\n\ts_nop 0\n\ts_nop 0\n\t"
#define LOOP_TXT(tmp, t1) "s_memrealtime vcc\n1:\n\ts_waitcnt lgkmcnt(0)\n\ts_sub_u32 " tmp ", " t1 ", vcc_lo\n\ts_memrealtime vcc\n\ts_cmpk_lt_u32 " tmp ", 400\n\ts_cbranch_scc1 1b\n\t"
#define WAIT_TXT(tmp, t1, t4) LOOP_TXT(tmp, t1) \
    "s_nop 1\n\ts_memrealtime s[90:91]\n\ts_nop 3\n\ts_memrealtime s[92:93]\n\ts_nop 3\n\ts_memrealtime s[94:95]\n\ts_getpc_b64 s[96:97]\n2:\n\ts_waitcnt lgkmcnt(0)\n\t" \
    "s_add_u32 s90, s90, vcc_lo\n\ts_add_u32 s92, s92, s94\n\ts_sub_u32 s90, " t4 ", s90\n\ts_sub_u32 s90, s90, s92\n\ts_max_i32 s90, s90, 0\n\ts_min_i32 s90, s90, 29\n\t" \
    "s_mul_i32 s90, s90, 87\n\ts_lshr_b32 s90, s90, 4\n\ts_andn2_b32 s90, s90, 3\n\ts_sub_u32 s90, 3f-2b, s90\n\ts_add_u32 s96, s96, s90\n\ts_addc_u32 s97, s97, 0\n\t" \
    "s_setpc_b64 s[96:97]\n\t" NOP8 NOP8 NOP8 NOP8 NOP8 "3:\n\t"
#define WAIT_CLOB "vcc", "scc", "s90", "s91", "s92", "s93", "s94", "s95", "s96", "s97", "memory"
#define SMF_A(acc, a, b) asm volatile("v_smfmac_f32_16x16x64_bf16 %0, %1, %2, %3" : "+v"(acc) : "v"(a), "a"(b), "v"(idx))
#define SMF_V(acc, a, b) asm volatile("v_smfmac_f32_16x16x64_bf16 %0, %1, %2, %3" : "+v"(acc) : "v"(a), "v"(b), "v"(idx))
#define MF0(d, a, b) asm volatile("v_mfma_f32_16x16x32_bf16 %0, %1, %2, 0" : "=&v"(d) : "v"(a), "v"(b))
#define MF1(d, a, b) asm volatile("v_mfma_f32_16x16x32_bf16 %0, %1, %2, %0" : "+v"(d) : "v"(a), "v"(b))
#define MF0H(d, a, b) asm volatile("v_mfma_f32_16x16x16_bf16 %0, %1, %2, 0" : "=&v"(d) : "v"(a), "v"(b))
#define MF1H(d, a, b) asm volatile("v_mfma_f32_16x16x16_bf16 %0, %1, %2, %0" : "+v"(d) : "v"(a), "v"(b))
DEV v8u tr_frag(lptr p0, lptr p1, lptr p2, lptr p3) { return cat8(tr2(p0, p1), tr2(p2, p3)); }
constexpr int qslot(int ks) { return (ks & 1) && ks < 8 ? ks >> 1 : -1; }
constexpr int kslot(int ks) { return ks < 4 ? 4 + ks : -1; }
constexpr int vslot(int ks) { return ks >= 4 ? 4 + ks : -1; }
DEV u32 ring_map(u32 off, u32 half) { return ((off >> 13) << 14) + (half << 13) + (off & 8191u); }
#define ZRD(x, i) x = __builtin_bit_cast(f32x4, lds_r16(ZP + 16 * (i)))
#define VMAX(d, a, b) asm volatile("v_max_f32 %0, %1, %2" : "=v"(d) : "v"(a), "v"(b))
#define VMAX3(d, a, b, c_) asm volatile("v_max3_f32 %0, %1, %2, %3" : "=v"(d) : "v"(a), "v"(b), "v"(c_))
#define VADD(d, a, b) asm volatile("v_add_f32 %0, %1, %2" : "=v"(d) : "v"(a), "v"(b))
#define VEXP(d, a) asm volatile("v_exp_f32 %0, %1" : "=v"(d) : "v"(a))
#define VFMC(d, a, b) asm volatile("v_fmamk_f32 %0, %1, 0x3e0293ee, %2" : "=v"(d) : "v"(a), "v"(b))
#define VMULC(d, a) asm volatile("v_mul_f32 %0, 0x3e0293ee, %1" : "=v"(d) : "v"(a))
#define SEL(d, a, b) asm volatile("v_cndmask_b32 %0, %1, %2, %3" : "=v"(d) : "v"(a), "v"(b), "s"(himask))
#define QPERM(d, a, q) asm volatile("v_mov_b32_dpp %0, %1 quad_perm:" q " row_mask:0xf bank_mask:0xf" : "=v"(d) : "v"(a))

#define SUMRANGE 0x28000000u
#define FL_EARLY 1
#define HST2 2
#define HGAP 7
#define HSAMP 71
DEV void role_head(const Params& p, lptr lds, int L, int h) {
    const int tid = threadIdx.x, lane = tid & 63, w = uni(tid >> 6), g = lane >> 4, r = lane & 15;
    const rsrc_t rs = mk_rsrc(p.ws, W_END);
    v8u wq0[10], wq1[10], wk0[10], wk1[10], wv0[10], wv1[10];
    lptr WL = lds + C_WL + w * (NLF * 2048) + lane * 16;
    MK_SRD();
    bool dead = false, have;
    const u32 l16 = (u32)lane * 16u;
    u32 pol, pol0, mring, hint, fkh, ksrc, vsrc, kb = uu(W_KQ + (u32)(L * NSEG * NH + h) * 16384u + w * 2048);
    unsigned long long kmask, vmask;
    u32 dk = uu(C_K + w * 2048), dv = dk + (C_V - C_K), dq = dk + (C_QX - C_K), dm = uu(C_MEM + w * 1024), dm2 = uu(w < 2 ? C_MEM + 8192 + w * 1024 : C_DUMP);
    {
        constexpr int GS = 1056, CS = 48 * GS;
        const v4u sq = mk_srd(p.Wq, DM * NQ * 2), sk = mk_srd(p.Wk, DM * NQ * 2), sv = mk_srd(p.Wv, DM * NQ * 2);
        const u32 vo = (u32)(g * NQ + h * DH + 8 * (r ^ ((g & 2) << 2))) * 2u;
        auto fill = [&](int ch) {
#pragma unroll 1
            for (int i = 0; i < 4; ++i) {
                const u32 dst = uu((ch % 3) * CS + (4 * i + w) * GS), so = uu((ch * 64 + 16 * i + 4 * w) * (NQ * 2));
                DMAIN(sq, dst, vo, so); DMAIN(sk, dst + 16 * GS, vo, so); DMAIN(sv, dst + 32 * GS, vo, so);
            }
        };
        const int vote = L == 1 ? pre_rings(p, rs, h) : 0;
        fill(0);
        if (L == 1) { VMW(12); BAR(); EV(3); }
        boot_set(p, rs, tid == 0 && L == 1 ? foff(F_RG + h) : OOB, (u32)((1 << 20) + vote));
        fill(1); fill(2);
        const int q4 = r >> 2;
        lptr tb = lds + (4 * (g >> 1) + q4) * GS + (g & 1) * 512 + ((w * 64 + (r & 3) * 8) ^ ((g & 1) << 7));
        v4u praw; u32 f0, code;
        SFOR(ch, 10)
            asm volatile("s_waitcnt vmcnt(%0) ; HZCLR" :: "i"(ch == 0 || (ch >= 4 && ch < 7) ? 25 : ch < 7 ? 24 : ch == 7 ? 25 : ch == 8 ? 13 : 2) : "memory");
            BAR();
            auto frag = [&](int m, int t) { lptr b = tb + (ch % 3) * CS + m * 16 * GS + t * 32; return tr_frag(b, b + 256, b + 8 * GS, b + 8 * GS + 256); };
            wq0[ch] = frag(0, 0); wq1[ch] = frag(0, 1); wk0[ch] = frag(1, 0); wk1[ch] = frag(1, 1); wv0[ch] = frag(2, 0); wv1[ch] = frag(2, 1);
            asm volatile("" : "+a"(wq0[ch]), "+a"(wk0[ch]), "+v"(wv0[ch]), "+v"(wv1[ch]));
            if constexpr (qslot(ch) < 0) asm volatile("" : "+a"(wq1[ch])); else asm volatile("" : "+v"(wq1[ch]));
            if constexpr (kslot(ch) < 0) asm volatile("" : "+a"(wk1[ch])); else asm volatile("" : "+v"(wk1[ch]));
            BAR();
            if constexpr (ch < 7) fill(ch + 3);
            if constexpr (ch == 3) asm volatile("buffer_load_dwordx4 %0, %1, %2, 0 offen sc1" : "=v"(praw) : "v"(pol_off()), "s"(srd) : "memory");
            if constexpr (ch == 6) code = code_fetch();
            if constexpr (ch == 7) {
                PIN(praw);
                if (!boot_ok(p, pol_off(), praw, 0u)) praw.w = boot_wait(p, rs, pol_off(), 0u, dead);
                pol = pol_of(praw.w); pol0 = pol & 1u;
                mring = W_MRING + (u32)L * (MB / 4) + pol0 * 8192u; hint = mring + 4 * 32768u;
                fkh = lane < 3 ? near_(foff(F_QKV + lane * 64 + L * 32 + h), 0u, pol) : lane < 6 ? hint + (u32)(lane - 3) * 4u : OOB;
                kmask = __builtin_amdgcn_ballot_w64((hblk(W_KQ, L * NSEG + lane, h, 0u, pol) & 8192u) != 0u);
                vmask = __builtin_amdgcn_ballot_w64((hblk(W_VO, L * NSEG + lane, h, 1u, pol) & 8192u) != 0u);
                ksrc = kb + ((u32)kmask & 1u) * 8192u; vsrc = kb + (W_VO - W_KQ) + ((u32)vmask & 1u) * 8192u;
                PIN(fkh);
                asm volatile("" : "+s"(pol), "+s"(kmask), "+s"(vmask), "+s"(ksrc), "+s"(vsrc), "+s"(dk), "+s"(dv), "+s"(dq), "+s"(dm), "+s"(dm2));
            }
            if constexpr (ch == 8) LD4A(f0, fkh, 0);
            SCHED();
        SEND
        VMW(0); SCHED(); PIN(f0); USE(code);
        have = ((u32)__builtin_amdgcn_ballot_w64(f0 == 0u) & 7u) == 0u;
        if (have) { DMA32(dv, l16, vsrc); DMA32(dk, l16, ksrc); }
#pragma unroll
        for (int ch = 0; ch < 10; ++ch) {
            if (qslot(ch) >= 0) { lds_w16(WL + qslot(ch) * 2048, wq1[ch].lo); lds_w16(WL + qslot(ch) * 2048 + 1024, wq1[ch].hi); }
            if (kslot(ch) >= 0) { lds_w16(WL + kslot(ch) * 2048, wk1[ch].lo); lds_w16(WL + kslot(ch) * 2048 + 1024, wk1[ch].hi); }
            if (vslot(ch) >= 0) { lds_w16(WL + vslot(ch) * 2048, wv1[ch].lo); lds_w16(WL + vslot(ch) * 2048 + 1024, wv1[ch].hi); }
        }
        CFENCE();
    }
    const u32 idx = (r & 2) ? 0xEEEEu : 0x4444u;
    const unsigned long long himask = 0xCCCCCCCCCCCCCCCCull;
    EV(6);
    if (L == 1) {
        static_assert(4 * (8 * 1088 + 17 * CV_KB + 1024) <= C_WL, "conv in the head's buffers");
        Filt f[3];
#pragma unroll
        for (int i = 0; i < 3; ++i) filt_load<1, 0>(p, 64 + 3 * h + i, f[i]);
        boot_wait(p, rs, lane < 40 ? foff(F_KV + lane) : OOB, 1u, dead);
#pragma unroll
        for (int i = 0; i < 3; ++i) f[i].a[4] = ld16s(rs, lane < 32 ? l16 : OOB, W_KVT + (u32)chan(64 + 3 * h + i, w) * (SEQ * 2));
#pragma unroll 1
        for (int i = 0; i < 3; ++i) {
            conv_job<4>(p, rs, lds, 64 + 3 * h + i, f[0], 0);
            f[0].a[0] = f[1].a[0]; f[0].b[0] = f[1].b[0]; f[0].a[4] = f[1].a[4]; f[1].a[0] = f[2].a[0]; f[1].b[0] = f[2].b[0]; f[1].a[4] = f[2].a[4];
        }
        DRAIN(); BAR();
        st4s(rs, tid == 0 ? foff(F_CV + 160 + h) : OOB, 0, 1u);
        EV(12);
    }
    u32 tm = 0;
    lptr qw = lds + C_Q + (2 * g) * 288 + (32 * w + 2 * r) * 2;
    lptr kw = qw + (C_KM - C_Q);
    lptr qr = lds + C_Q + (2 * (r >> 2) + (r & 1)) * 288 + g * 16;
    lptr LB = lds + l16, VX = lds + C_V + w * 2048 + l16, QX = lds + C_QX + (w & 1) * 4096 + l16;
    if (tid >= 64 && tid < 70) { const u32 z = tid == 68 ? 0x3F803F80u : tid == 69 ? PSENT : 0u; lds_w16(lds + C_ZERO + (tid - 64) * 16, (v4u){z, z, z, z}); }
    if (w < 3) {
        const u32 ng = u_(-1000000000.0f * LOG2E);
        v4u mk;
#pragma unroll
        for (int i = 0; i < 4; ++i) mk[i] = w == 0 || (w == 1 && 4 * g + i > r) ? ng : 0u;
        lds_w16(lds + C_MASK + w * 4096 + l16, mk);
    }
    u32 zaddr = 0; PIN(zaddr);
    lptr ZP = lds + zaddr + C_ZERO;
#define LF_RD(slot, hf) lf[slot][hf] = lds_r16(WL + (slot) * 2048 + (hf) * 1024)
    const u32 ofl = tid == 192 ? near_(foff(F_O + L * 32 + h), 0u, pol) : tid == 193 && (h & 15) == 0 ? hint + 128u + (u32)h * 8u : OOB;
    u32 oso = (g & 1) ? OOB : W_ORING + (u32)L * (MB / 2) + ring_map((u32)((r >> 3) * 32768 + h * 1024 + w * 256 + (g >> 1) * 128 + (r & 7) * 16), pol0);
    u32 missed = 0, dirty = 1, rep = PSENT;
    const u32 early = L < FL_EARLY ? 0u : OOB;
    asm volatile("" : "+v"(oso), "+v"(qw), "+v"(kw), "+v"(qr), "+v"(VX), "+v"(QX));
    if (!have) { wait_flags(rs, lane < 3 ? fkh : OOB, 1u, dead); DMA32(dv, l16, vsrc); DMA32(dk, l16, ksrc); }
    EV(7);
    st4s(rs, L == 0 && tid == 192 ? foff(F_GO + h) : OOB, 0, rt32() | 1u);
    EV(8);
    auto next_slot = [&](u32 now) {
        const u32 hv = ld4(rs, fkh), per = rdl(hv, 4);
        if (per < 400u) { tm = rdl(hv, 5) - per; for (int n = 0; n < 3 && (int)(tm - now) < 0; ++n) tm += per; }
    };
    if (L == 1) { next_slot(rt32() + CH_KV0); tm -= 24u; }
#define C2(k) do {} while (0)
    for (int s = 0; s < NSEG;) {
        {
            *(LAS u32*)(lds + C_DUMP + 128 + w * 4) = tm;
            BAR();
            const v4u c = lds_r16(lds + C_DUMP + 128) - tm;
            const u32 add = uu((u32)max(max((int)c.x, (int)c.y), max((int)c.z, (int)c.w)));
            tm += add; dirty |= add;
        }
        u32 ms = mring + (u32)(s & 3) * 32768u + w * 1024, ms1 = ms + 4096u, ms2 = mring + (u32)(s & 3) * 32768u + 16384u + (w & 1) * 1024;
        u32 l3 = w < 2 ? l16 : OOB;
        asm volatile("" : "+s"(ms), "+s"(ms1), "+s"(ms2)); PIN(l3);
        f32x4 aq0, aq1, ak0, ak1;
        v4u lf[NLF][2];
        u32 tw1 = FINE_T1(tm), tw4 = FINE_T4(tm), fv = (l16 & 16u) ? rep : (u32)s;
        asm volatile("" : "+s"(tw1), "+s"(tw4)); PIN(fv);
        SCHED();
        C2(4);
        asm volatile("s_waitcnt vmcnt(5)\n\ts_barrier" ::: "memory");
        C2(5);
        st4s(rs, ofl | early, 0, fv);
        if (__builtin_expect(dirty != 0u, 0)) {
            wait_until(tm - 44u);
            st16(rs, oso, lds_r16(ZP + 80));
        }
        SCHED();
        v4u A[10];
#define HT(k) do {} while (0)
        v4u kx[8], qf[4], kmf[4], vx[2], ones, P, tq[4];
        float fa, fb, fc, fd, sx[4], t[4], sm0, sm1, tm0, tm1, inv; u32 pa, pb, fl, pmm, tmp = 0; unsigned long long odd;
        f32x4 av0, av1, s0, s1, sm, oa, ob, rsum;
        v2u vm, pm, vh, vl, on[2];
        f32x2 zz[4];
#define LW(sl) cat8(lf[sl][0], lf[sl][1])
#define PQ(ks, tl) do { if constexpr (!(tl)) SMF_A(aq0, A[ks], wq0[ks]); else if constexpr (qslot(ks) < 0) SMF_A(aq1, A[ks], wq1[ks]); else { const v8u lw_ = LW(qslot(ks)); SMF_V(aq1, A[ks], lw_); } } while (0)
#define PK(ks, tl) do { if constexpr (!(tl)) SMF_A(ak0, A[ks], wk0[ks]); else if constexpr (kslot(ks) < 0) SMF_A(ak1, A[ks], wk1[ks]); else { const v8u lw_ = LW(kslot(ks)); SMF_V(ak1, A[ks], lw_); } } while (0)
#define PV(ks, tl) do { if constexpr (!(tl)) SMF_V(av0, A[ks], wv0[ks]); else if constexpr (vslot(ks) < 0) SMF_V(av1, A[ks], wv1[ks]); else { const v8u lw_ = LW(vslot(ks)); SMF_V(av1, A[ks], lw_); } } while (0)
#define KXR(i) kx[i] = lds_r16(LB + C_K + ((i) & 1) * 4096 + ((i) >> 1) * 1024)
#define XW(q_) do { *(LAS u32*)(q_) = pa; *(LAS u32*)((q_) + 288) = pb; } while (0)
        auto slot = [&](auto zc) __attribute__((always_inline)) {
            constexpr int z = decltype(zc)::value;
            constexpr int vs[] = {0, 8, 1, 10, 9, 2, 11, -1, -1, -1, -1, 4, 6, 12, -1, -1, -1, 13, 14, -1, 16, 18, 15, 3, 5, 17, 7, 19};
            if constexpr (z < 12) PQ(z >> 1, z & 1);
            else if constexpr (z < 20) PQ(6 + (z & 3), z >> 4);
            else if constexpr (z < 32) PK((z - 20) >> 1, z & 1);
            else if constexpr (z < 40) PK(6 + (z & 3), (z >> 2) & 1);
            else if constexpr (z < 42) { if constexpr (z == 40) MF0(s0, kx[0], qf[0]); else MF0(s1, kx[1], qf[0]); }
            else if constexpr (z < 48) { if constexpr (z & 1) MF1(s1, kx[z - 40], qf[(z - 40) >> 1]); else MF1(s0, kx[z - 40], qf[(z - 40) >> 1]); }
            else if constexpr (z == 55) MF0(sm, kmf[0], qf[0]);
            else if constexpr (z > 55 && z < 59) MF1(sm, kmf[z - 55], qf[z - 55]);
            else if constexpr (z == 62) MF0(rsum, ones, P);
            else if constexpr (z == 63) MF0(oa, vx[0], P);
            else if constexpr (z == 64) MF0(ob, vx[1], P);
            else if constexpr (z == 67) { const v2u ones2 = ones.lo; MF1H(rsum, ones2, pm); }
            else PV(vs[z - 48] >> 1, vs[z - 48] & 1);
            if constexpr (z == 20 || z == 24 || z == 26) USE(lf[4 + ((z - 20) >> 1)][1]);
            if constexpr (z == 41) USE(qf[3]);
            if constexpr (z == 45 || z == 46) USE(kx[z - 39]);
            if constexpr (z == 53) USE(lf[9][1]);
            if constexpr (z >= 54 && z < 58) USE(kmf[z - 54]);
            if constexpr (z == 61) USE(vl);
            if constexpr (z == 64) USE(lf[10][1]);
            if constexpr (z == 69) USE(lf[11][1]);
            if constexpr (z == 72) USE(lf[12][1]);
            if constexpr (z == 74) USE(lf[13][1]);
            if constexpr (z < 2) A[2 + z] = lds_r16(LB + C_MEM + (2 + z) * 1024);
            if constexpr (z == HST2) asm volatile("s_waitcnt vmcnt(3)\n\ts_barrier" ::: "memory");
            if constexpr (z > HST2 && z < HST2 + 7) A[3 + z - HST2] = lds_r16(LB + C_MEM + (3 + z - HST2) * 1024);
            if constexpr (z >= 15 && z < 23) LF_RD(4 + ((z - 15) >> 1), (z - 15) & 1);
            if constexpr (z >= 24 && z < 27) KXR(z - 24);
            if constexpr (z == 27) XW(qw);
            if constexpr (z >= 28 && z < 31) KXR(z - 25);
            if constexpr (z == 32) asm volatile("s_waitcnt lgkmcnt(3)\n\ts_barrier" ::: "memory");
            if constexpr (z >= 33 && z < 37) qf[z - 33] = lds_r16(qr + (z - 33) * 64);
            if constexpr (z == 37 || z == 38) LF_RD(8, z - 37);
            if constexpr (z == 39 || z == 41) KXR(6 + (z == 41));
            if constexpr (z == 44) XW(kw);
            if constexpr (z == 45 || z == 46) LF_RD(9, z - 45);
            if constexpr (z == 47) vx[0] = lds_r16(VX);
            if constexpr (z == 48) { VMW(1); vh = *(LAS v2u*)(VX + 1032); }
            if constexpr (z == 54) { vl = *(LAS v2u*)(VX + 1024); vx[1] = (v4u){vh.x, vh.y, vl.x, vl.y}; }
            if constexpr (z == 49) { USE(lf[9][0]); asm volatile("s_barrier" ::: "memory"); }
            if constexpr (z >= 50 && z < 54) kmf[z - 50] = lds_r16(qr + (C_KM - C_Q) + (z - 50) * 64);
            if constexpr (z == 59 || z == 60) LF_RD(10, z - 59);
            if constexpr (z == 61 || z == 65) LF_RD(11, z == 65);
            if constexpr (z == 66 || z == 68) LF_RD(12, z == 68);
            if constexpr (z == 69 || z == 70) LF_RD(13, z - 69);
            if constexpr (z >= 72) tq[z - 72] = lds_r16(QX + (z - 72) * 1024);
            if constexpr (z == HSAMP) LD4A(fl, fkh, 0);
            SCHED();
            if constexpr (z == 19) HT(2);
            if constexpr (z == 39) HT(3);
            if constexpr (z == 51) HT(4);
            if constexpr (z == 64) HT(5);
            if constexpr (z == 20) VADD(fa, aq0[0], aq0[2]);
            if constexpr (z == 21) VADD(fb, aq0[1], aq0[3]);
            if constexpr (z == 23) VADD(fc, aq1[0], aq1[2]);
            if constexpr (z == 24) VADD(fd, aq1[1], aq1[3]);
            if constexpr (z == 25) VCVT(pa, fa, fc);
            if constexpr (z == 26) VCVT(pb, fb, fd);
            if constexpr (z >= 44 && z < 48) asm volatile("v_mov_b64 %0, 0" : "=v"(zz[z - 44]));
            if constexpr (z == 47) { av0 = __builtin_shufflevector(zz[0], zz[1], 0, 1, 2, 3); av1 = __builtin_shufflevector(zz[2], zz[3], 0, 1, 2, 3); }
            if constexpr (z == 48 || z == 49) asm volatile("v_mov_b64 %0, %1" : "=v"(on[z - 48]) : "s"(0x3F803F803F803F80ull));
            if constexpr (z == 49) ones = __builtin_shufflevector(on[0], on[1], 0, 1, 2, 3);
            if constexpr (z == 40) { VADD(fa, ak0[0], ak0[2]); VADD(fb, ak0[1], ak0[3]); }
            if constexpr (z == 42) { VADD(fd, ak1[1], ak1[3]); VADD(fc, ak1[0], ak1[2]); }
            if constexpr (z == 43) { VCVT(pb, fb, fd); VCVT(pa, fa, fc); }
            if constexpr (z >= 50 && z < 54) SEL(sx[z - 50], s0[z - 50], s1[z - 50]);
            if constexpr (z >= 54 && z < 58) VEXP(t[z - 54], sx[z - 54]);
            if constexpr (z == 58) asm volatile("v_cvt_pk_bf16_f32 %0, %2, %3\n\tv_cvt_pk_bf16_f32 %1, %4, %5" : "=&v"(P.x), "=v"(P.y) : "v"(t[0]), "v"(t[1]), "v"(t[2]), "v"(t[3]));
            if constexpr (z == 59) QPERM(P.z, P.x, "[2,3,0,1]");
            if constexpr (z == 60) QPERM(P.w, P.y, "[2,3,0,1]");
            if constexpr (z == 61) { USE(sm); VMULC(sm0, sm[0]); }
            if constexpr (z == 62) { VEXP(tm0, sm0); VMULC(sm1, sm[1]); }
            if constexpr (z == 63) VEXP(tm1, sm1);
            if constexpr (z == 64) asm volatile("v_cvt_pk_bf16_f32 %0, %2, %3\n\tv_cndmask_b32 %1, %0, 0, %4" : "=&v"(pmm), "=v"(pm.x) : "v"(tm0), "v"(tm1), "s"(himask));
            if constexpr (z == 65) asm volatile("v_cndmask_b32 %0, 0, %1, %2" : "=v"(pm.y) : "v"(pmm), "s"(himask));
            if constexpr (z == 67) { SEL(oa[0], oa[0], ob[0]); SEL(oa[1], oa[1], ob[1]); }
            if constexpr (z == 68 || z == 69) SEL(oa[z - 66], oa[z - 66], ob[z - 66]);
            if constexpr (z == 70) asm volatile("v_sub_u32 %0, %1, %2" : "=v"(pa) : "v"(rsum[0]), "s"(0x2B800000u), "v"(rsum));
            if constexpr (z == 71) asm volatile("v_cmp_le_u32 %0, %1, %2" : "=s"(odd) : "s"(SUMRANGE), "v"(pa));
            if constexpr (z == 72) VADD(fa, av0[0], av0[2]);
            if constexpr (z == 73) VADD(fb, av0[1], av0[3]);
            if constexpr (z == 74) VCVT(vm.x, fa, fb);
            SCHED();
        };
        asm volatile("s_mov_b32 m0, %2\n\t" WAIT_TXT("%0", "%1", "%9") "buffer_load_dwordx4 %4, %5, %6 offen sc1 lds\n\ts_add_u32 m0, %2, 4096\n\ts_nop %11\n\tbuffer_load_dwordx4 %4, %5, %10 offen sc1 lds\n\t"
                     "s_mov_b32 m0, %3\n\ts_nop 0\n\tbuffer_load_dwordx4 %8, %5, %7 offen sc1 lds"
                     : "=&s"(tmp) : "s"(tw1), "s"(dm), "s"(dm2), "v"(l16), "s"(srd), "s"(ms), "s"(ms2), "v"(l3), "s"(tw4), "s"(ms1), "i"(HGAP) : WAIT_CLOB);
        HT(0);
        ZRD(aq0, 0); ZRD(aq1, 1); ZRD(ak0, 2); ZRD(ak1, 3);
#pragma unroll
        for (int i = 0; i < 4; ++i) { LF_RD(i, 0); LF_RD(i, 1); }
        u32 ob_ = (vsrc ^ 8192u) + (u32)((w & 1) * 256); PINS(ob_);
        USE(lf[3][1]);
        DMA32(dq, l16, ksrc ^ 8192u);
        st4s(rs, ofl | (early ^ OOB), 0, fv);
        asm volatile("s_waitcnt vmcnt(5)\n\ts_barrier" ::: "memory");
        HT(1);
#pragma unroll
        for (int ks = 0; ks < 2; ++ks) A[ks] = lds_r16(LB + C_MEM + ks * 1024);
        SCHED();
        static_for<0, 76>(slot);
        u32 fail = 0;
        if (__builtin_expect(odd != 0ull, 0)) {
            u32 mn = ~0u;
#pragma unroll 5
            for (int i = 0; i < 10; ++i) { const v4u e = lds_r16(LB + C_MEM + i * 1024); mn = min(min(min(mn, e.x ^ PSENT), e.y ^ PSENT), min(e.z ^ PSENT, e.w ^ PSENT)); }
            fail = missed < SPIN_CAP && !all_(mn != 0u) ? OOB : 0u;
            float mx = fmaxf(fmaxf(fmaxf(sx[0], sx[1]), fmaxf(sx[2], sx[3])), fmaxf(sm0, sm1));
            mx = max32(max16(fmaxf(mx, dpp<0x4E>(mx))));
#pragma unroll
            for (int i = 0; i < 4; ++i) t[i] = __builtin_amdgcn_exp2f(sx[i] - mx);
            P.x = pk(t[0], t[1]); P.y = pk(t[2], t[3]); P.z = u_(dpp<0x4E>(f_(P.x))); P.w = u_(dpp<0x4E>(f_(P.y)));
            pmm = pk(__builtin_amdgcn_exp2f(sm0 - mx), __builtin_amdgcn_exp2f(sm1 - mx));
            pm = (r & 2) ? (v2u){0u, pmm} : (v2u){pmm, 0u};
            asm volatile("s_nop 1" : "+v"(P), "+v"(pm));
            MF0(rsum, ones, P); MF0(oa, vx[0], P); MF0(ob, vx[1], P);
            NOPS(7);
            { const v2u ones2 = ones.lo; MF1H(rsum, ones2, pm); }
#pragma unroll
            for (int i = 0; i < 4; ++i) SEL(oa[i], oa[i], ob[i]);
            asm volatile("s_nop 4" :: "v"(rsum));
            oso |= fail;
        }
        v4u tk[8];
        f32x4 c0, c1;
#define TKR(i) tk[i] = lds_r16(LB + C_K + ((i) >> 2) * 4096 + ((i) & 3) * 1024)
        asm volatile("v_rcp_f32 %0, %3\n\ts_nop 2\n\tv_add_f32 %1, %4, %6\n\tv_add_f32 %2, %5, %7\n\tv_cvt_pk_bf16_f32 %1, %1, %2\n\ts_nop 0"
                     : "=&v"(inv), "=&v"(vm.y), "=&v"(fd) : "v"(rsum[0]), "v"(av1[0]), "v"(av1[1]), "v"(av1[2]), "v"(av1[3]));
        MF1H(oa, vm, pm);
        SCHED();
        c0 = __builtin_bit_cast(f32x4, lds_r16(QX + (C_MASK + 4096 - C_QX)));
        SCHED();
        {
            v4u oc; float u0, u1, u2, u3;
            asm volatile("s_nop 6\n\tv_mul_f32 %4, %8, %12\n\tv_mul_f32 %5, %9, %12\n\tv_mul_f32 %6, %10, %12\n\tv_mul_f32 %7, %11, %12\n\t"
                         "v_cvt_pk_bf16_f32 %0, %4, %5\n\tv_cvt_pk_bf16_f32 %2, %4, %5\n\tv_cvt_pk_bf16_f32 %1, %6, %7\n\tv_cvt_pk_bf16_f32 %3, %6, %7\n\t"
                         "v_permlane16_swap_b32 %0, %2\n\ts_nop 0\n\tv_permlane16_swap_b32 %1, %3"
                         : "=&v"(oc.x), "=&v"(oc.y), "=&v"(oc.z), "=&v"(oc.w), "=&v"(u0), "=&v"(u1), "=&v"(u2), "=&v"(u3) : "v"(oa[0]), "v"(oa[1]), "v"(oa[2]), "v"(oa[3]), "v"(inv));
            st16(rs, oso, oc);
        }
        SCHED();
        unsigned long long clk;
        asm volatile("s_memrealtime %0" : "=s"(clk));
        if (__builtin_expect(fail == 0u, 1)) {
        HT(6);
        C2(0);
        v4u tv[2], px, pv;
        f32x4 cm, o[4], sum;
        float ex[10], cs0, cs1;
        v2u p0, p1, tvm;
        u32 oo, kvo, oz; bool kvok;
        u32 ln = l16; PIN(ln);
        lptr VO, VMX, MK = QX + (C_MASK - C_QX);
        TKR(0);
        SCHED();
        SFOR(i, 12)
            if constexpr (i == 0) MF0(cm, kmf[0], tq[0]); else if constexpr (i < 4) MF1(cm, kmf[i], tq[i]);
            else if constexpr (i < 8) MF1(c0, tk[i - 4], tq[i - 4]); else MF1(c1, tk[i - 4], tq[i - 8]);
            if constexpr (i == 0) USE(tq[3]);
            if constexpr (i == 3) USE(tk[0]);
            if constexpr (i == 4) USE(tk[1]);
            if constexpr (i == 6) USE(tk[3]);
            if constexpr (i >= 8 && i < 11) USE(tk[i - 3]);
            if constexpr (i < 3) TKR(i + 1);
            if constexpr (i == 3) { c1 = __builtin_bit_cast(f32x4, lds_r16(MK)); TKR(4); }
            if constexpr (i >= 4 && i < 7) TKR(i + 1);
            if constexpr (i == 5) *(LAS v2u*)VMX = vm;
            if constexpr (i == 7) { vx[1] = lds_r16(VX + 1024); tv[0] = lds_r16(VO); }
            if constexpr (i == 8) tv[1] = lds_r16(VO + 1024);
            SCHED();
            if constexpr (i == 0) { VMX = lds + (ln >> 1); PIN(VMX); }
            if constexpr (i == 1) { VMX += C_VM + w * 512; PIN(VMX); }
            if constexpr (i == 2) { VO = lds + C_V + (w ^ 1) * 2048 + ln; PIN(VO); }
            if constexpr (i == 6) { USE(cm); VMULC(cs0, cm[0]); }
            if constexpr (i == 8) VMULC(cs1, cm[1]);
            if constexpr (i == 9) VEXP(ex[8], cs0);
            if constexpr (i == 10) { VEXP(ex[9], cs1); VEXP(ex[0], c0[0]); }
            if constexpr (i == 11) { VEXP(ex[1], c0[1]); VEXP(ex[2], c0[2]); VEXP(ex[3], c0[3]); }
            SCHED();
        SEND
        asm volatile("v_cvt_pk_bf16_f32 %0, %6, %7\n\tv_mov_b32 %1, 0\n\tv_mov_b32 %2, 0\n\tv_cvt_pk_bf16_f32 %3, %6, %7\n\tv_cvt_pk_bf16_f32 %4, %8, %9\n\tv_cvt_pk_bf16_f32 %5, %10, %11"
                     : "=&v"(p0.x), "=&v"(p0.y), "=&v"(p1.x), "=&v"(p1.y), "=&v"(px.x), "=&v"(px.y) : "v"(ex[8]), "v"(ex[9]), "v"(ex[0]), "v"(ex[1]), "v"(ex[2]), "v"(ex[3]));
        asm volatile("s_waitcnt lgkmcnt(0)\n\ts_barrier" : "+s"(clk) :: "memory");
        tvm = *(LAS v2u*)(VMX + (512 - (w & 1) * 1024));
        pv = lds_r16(ZP + 80);
        SCHED();
#pragma unroll
        for (int i = 0; i < 4; ++i) VEXP(ex[4 + i], c1[i]);
        asm volatile("v_cvt_pk_bf16_f32 %0, %2, %3\n\tv_cvt_pk_bf16_f32 %1, %4, %5\n\ts_nop 1" : "=&v"(px.z), "=&v"(px.w) : "v"(ex[4]), "v"(ex[5]), "v"(ex[6]), "v"(ex[7]));
        {
            const v2u ones2 = ones.lo;
            const u32 now = (u32)clk;
            auto pvm = [&](auto first) __attribute__((always_inline)) {
                SCHED();
                MF0(sum, ones, px); SCHED(); if constexpr (decltype(first)::value) { oo = ln + (ln & 0x300u) + ob_; PIN(oo); } SCHED();
                MF0(o[0], vx[0], px); NOPS(1);
                MF0(o[1], vx[1], px); NOPS(1);
                MF0(o[2], tv[0], px);
                MF1H(sum, ones2, p0); NOPS(0);
                MF1H(o[0], vm, p0); NOPS(1);
                MF1H(o[1], vm, p1); NOPS(1);
                MF0(o[3], tv[1], px);
                asm volatile("v_sub_u32 %0, %2, %3\n\tv_rcp_f32 %1, %2" : "=&v"(pa), "=&v"(inv) : "v"(sum[0]), "s"(0x2B800000u), "v"(sum));
                MF1H(o[2], tvm, p0); NOPS(2);
                MF1H(o[3], tvm, p1);
                SCHED();
            };
            pvm(IC<1>{});
            if (__builtin_expect(__builtin_amdgcn_ballot_w64(pa >= SUMRANGE) != 0ull, 0)) {
                const f32x4 m0 = __builtin_bit_cast(f32x4, lds_r16(MK + 4096)), m1 = __builtin_bit_cast(f32x4, lds_r16(MK));
                float mx = fmaxf(cs0, cs1);
#pragma unroll
                for (int i = 0; i < 4; ++i) { ex[i] = m0[i] == 0.f ? c0[i] : m0[i]; ex[4 + i] = m1[i] == 0.f ? c1[i] : m1[i]; mx = fmaxf(mx, fmaxf(ex[i], ex[4 + i])); }
                mx = max32(max16(mx));
#pragma unroll
                for (int i = 0; i < 4; ++i) px[i] = pk(__builtin_amdgcn_exp2f(ex[2 * i] - mx), __builtin_amdgcn_exp2f(ex[2 * i + 1] - mx));
                p0.x = p1.y = pk(__builtin_amdgcn_exp2f(cs0 - mx), __builtin_amdgcn_exp2f(cs1 - mx));
                asm volatile("s_nop 7" : "+v"(px), "+v"(p0), "+v"(p1));
                pvm(IC<0>{});
                NOPS(3);
            }
            asm volatile("" : "+v"(o[0]), "+v"(o[1]));
            st16(rs, oo, (v4u){pk2(o[0].lo * inv), pk2(o[0].hi * inv), pk2(o[1].lo * inv), pk2(o[1].hi * inv)});
            asm volatile("" : "+v"(o[2]), "+v"(o[3]));
            st16(rs, oo ^ 2048u, (v4u){pk2(o[2].lo * inv), pk2(o[2].hi * inv), pk2(o[3].lo * inv), pk2(o[3].hi * inv)});
            SCHED();
            C2(1);
            kb += NH * 16384;
            ksrc = kb + (((kmask >> ((s + 1) & 63)) & 1ull) ? 8192u : 0u);
            vsrc = kb + (((vmask >> ((s + 1) & 63)) & 1ull) ? W_VO - W_KQ + 8192u : W_VO - W_KQ);
            oz = (oso & ~(3u << 17)) | (u32)((s + 3) & 3) << 17; oso = (oso & ~(3u << 17)) | (u32)((s + 1) & 3) << 17;
            VMW(3); SCHED();
            C2(2);
            PIN(fl); kvok = ((u32)__builtin_amdgcn_ballot_w64(fl < (u32)s + 2) & 7u) == 0u;
            kvo = l16 | (kvok ? 0u : OOB);
            DMA32(dv, kvo, vsrc); DMA32(dk, kvo, ksrc);
            st16(rs, oz, pv);
            SCHED();
            C2(3);
            rep = (u32)s << 16 | min(missed, 1u) << 14 | min(now - tm, 0x3FFFu);
            if (h == 0 && w == 3) { LST(L * 64 + s, now); LST(128 + L * 64 + s, ((int)(rdl(fl, 3) - now) >= 4) | ((int)kvok << 1) | ((int)min(missed, 3u) << 2)); LST(640 + L * 64 + s, tm); LST(1728 + L * 64 + s, __builtin_amdgcn_s_memtime()); }
            tm = rdl(fl, 5);
            missed = missed == SPIN_CAP ? missed : 0u;
            dirty = 0;
            if (__builtin_expect(rdl(fl, 3) - now + CH_LATE > 4000u && s + 1 < NSEG, 0)) {
                dirty = 1;
#pragma unroll 1
                for (int n = 0; n < 64; ++n) {
                    CFENCE();
                    const u32 hv = ld4(rs, fkh);
                    tm = rdl(hv, 5);
                    if (rdl(hv, 3) - now + CH_LATE <= 4000u) break;
                }
            }
        }
        LS2(L == 0 && w == 0, h);
        if (__builtin_expect(!kvok, 0) && s + 1 < NSEG) {
            dirty = 1;
            wait_flags(rs, ln < 48u ? fkh : OOB, (u32)s + 2, dead);
            DMA32(dv, l16, vsrc); DMA32(dk, l16, ksrc);
            const u32 now = rt32() + CH_KV;
            if ((int)(tm - now) < 0) next_slot(now);
        }
        ++s;
        } else {
            u32 ln = l16; PIN(ln);
            oso = (ln & 256u) ? oso : oso & ~OOB;
            asm volatile("s_waitcnt vmcnt(1) lgkmcnt(0)" :: "s"(clk) : "memory"); SCHED(); PIN(fl);
            const u32 was = tm - tmp;
            tm = rdl(fl, 5); dirty = 1;
            if ((int)(tm - rdl(fl, 4) - was) > 24) tm -= rdl(fl, 4);
            if ((++missed & 7u) == 0u) {
                *(LAS u32*)(lds + C_DUMP + (w == 0 ? 0 : 64)) = ld4(rs, foff(F_ERR));
                BAR();
                missed = uu(*(LAS u32*)(lds + C_DUMP)) != 0u ? SPIN_CAP : missed;
                st4s(rs, missed == SPIN_CAP ? foff(F_ERR) : OOB, 0, 1u);
            }
        }
    }
    DRAIN(); BAR();
    st4s(rs, ofl, 0, (l16 & 16u) ? rep : NSEG);
}

#define WARM1 0
#define WO_FINE 0
#define WO_NI 4
#define WROT 0
DEV void role_wo(const Params& p, lptr lds, int j) {
    const int tid = threadIdx.x, lane = tid & 63, w = uni(tid >> 6), g = lane >> 4, r = lane & 15;
    const int rg = j & 1, cg = j >> 1, rot = cg & WROT;
    const rsrc_t rs = mk_rsrc(p.ws, W_END);
    MK_SRD();
    v4u praw; u32 code;
    v8u wo[8][5];
    {
        const v4u so_ = mk_srd(p.Wo, DM * NQ * 2);
        const int one = (0x3F2FF1F0u >> (4 * (cg & 7))) & 15;
        u32 vo[3];
#pragma unroll
        for (int i = 0; i < 3; ++i) { const int n = 64 * i + lane; vo[i] = n % 6 < 5 ? (u32)((n / 6) * (DM * 2) + cg * 80 + (n % 6) * 16) : ((n / 6) & 3) == one ? (u32)((n / 6) * (DM * 2) + (cg >> 3) * 640 + 304 + rg * 16) : OOB; }
        auto fill = [&](int b) {
            const u32 dst = uu(w * 36864 + (b % 3) * 12288), src = uu((8 * w + ((b + rot) & 7)) * (DH * DM * 2));
#pragma unroll
            for (int i = 0; i < 12; ++i) DMAIN(so_, dst + i * 1024, vo[i % 3], src + (i / 3) * (32 * DM * 2));
        };
#pragma unroll 1
        for (int b = 0; b < 3; ++b) fill(b);
        const int q4 = r >> 2;
        lptr tb = lds + w * 36864 + (32 * (g >> 1) + 16 * (q4 >> 1) + 8 * ((r >> 1) & 1) + 4 * (g & 1) + (q4 & 1)) * 96 + (r & 1) * 8;
        SFOR(b, 8)
            asm volatile("s_waitcnt vmcnt(%0) ; HZCLR" :: "i"(b < 5 ? 24 : 86 - 12 * b) : "memory");
#pragma unroll
            for (int t = 0; t < 5; ++t) {
                lptr q = tb + (b % 3) * 12288 + (t == 0 ? 4 : (w + t) & 3) * 16;
                wo[b][t] = tr_frag(q, q + 192, q + 64 * 96, q + 64 * 96 + 192);
                if (t < 4) asm volatile("" : "+a"(wo[b][t])); else asm volatile("" : "+v"(wo[b][t]));
            }
            CFENCE();
            if constexpr (b < 5) fill(b + 3);
            if constexpr (b == 4) { asm volatile("buffer_load_dwordx4 %0, %1, %2, 0 offen sc1" : "=v"(praw) : "v"(pol_off()), "s"(srd) : "memory"); code = code_fetch(); }
            SCHED();
        SEND
    }
    VMW(0); BAR(); SCHED(); PIN(praw); USE(code);
    EV(6);
    const u32 idx = (r & 2) ? 0xEEEEu : 0x4444u;
    bool dead = false;
    if (!boot_ok(p, pol_off(), praw, 0u)) praw.w = boot_wait(p, rs, pol_off(), 0u, dead);
    const u32 pol0 = pol_of(praw.w) & 1u;
    const u32 l16 = (u32)lane * 16u;
    u32 oo[8];
#pragma unroll
    for (int i = 0; i < 8; ++i) oo[i] = uu(W_ORING + ring_map((u32)(rg * 32768 + (8 * w + ((i + rot) & 7)) * 1024), pol0));
    const u32 hint = W_MRING + 4 * 32768u + pol0 * 8192u;
    u32 hvo[2];
#pragma unroll
    for (int l = 0; l < 2; ++l) hvo[l] = lane < 2 ? hint + (u32)(l ^ 1) * (MB / 4) + (u32)lane * 4u : lane == 2 ? hint + (u32)l * (MB / 4) + 128u : lane == 3 ? foff(F_ERR) : lane == 4 ? hint + (u32)l * (MB / 4) + 256u : OOB;
    const int xo = g * 128 + (r & 7) * 16, O = 5 * cg + (r < 8 ? w : 4), q2 = 2 + (r >> 3);
    lptr xw0 = lds + (r < 8 ? 2048 + w * 4 : ((w + 1) & 3) * 512 + 4) + xo, xw1 = lds + ((w + q2) & 3) * 512 + q2 * 4 + xo;
    lptr xr = lds + (r < 8 ? w : 4) * 512 + xo;
    if (r < 8) { *(LAS u32*)xr = 0u; *(LAS u32*)(xr + 8192) = 0u; }
    const u32 mpo = (r & 4) == 0 && (r < 8 || w == 0) ? W_MRING + ring_map((u32)((O >> 1) * 256 + rg * 128 + (4 * (g & 1) + 2 * (O & 1) + (g >> 1)) * 16 + (r & 3) * 4), pol0) : OOB;
    constexpr u32 PER0 = CH_W0 + CH_MW + CH_H0 + CH_MH;
    const bool lead = j == 0 && w == 3;
    u32 hso[2] = {OOB, OOB};
    u32 t = 0, tn = 0;
    int sn[2] = {0, WARM1 ? 0 : NSEG};
    u32 hav = 16 * CH_H0, wav = 16 * CH_W0, mx = 16 * CH_M0, ok[2] = {0u, 0u}, dw[2] = {0u, 0u};
    u32 zero = 0, rep = 0xFFFFu, failed = 1, pmp = OOB, mp = OOB, mq = OOB, cw = 0, vo = l16, tp = 0, tw1 = 0, stop = 0;
    f32x4 acc[5] = {};
    if (tid < 20) *(LAS u32*)(lds + 5632 + tid * 4) = 0u;
    unsigned long long clk = 0;
    const u32 psent = PSENT;
    lptr sw = lds + 5120 + w * 4;
    asm volatile("" : "+v"(zero), "+v"(sw), "+v"(xw0), "+v"(xw1), "+v"(xr));
    auto turn = [&](auto lc) __attribute__((always_inline)) {
        constexpr int L = decltype(lc)::value, M = L ^ 1;
        u32 done, rd, dh, dn, nt, za, zn, pdur, hn; v3u hw;
        t = uu(t); tn = uu(tn); tp = uu(tp); dw[L] = uu(dw[L]); failed = uu(failed); rep = uu(rep);
        const int sc = sn[L];
        auto settle = [&](auto ic) __attribute__((always_inline)) {
            constexpr int i = decltype(ic)::value;
            if constexpr (i == 0) { pdur = (u32)clk - tp; dw[L] += max((int)((u32)clk + CH_WX - t), 0); PINS(pdur); PINS(dw[L]); }
            if constexpr (i == 1) {
                asm volatile("v_or_b32 %0, %2, %3\n\tv_cmp_ne_u32 vcc, %4, %5\n\tv_or_b32 %1, %2, %5\n\tv_cndmask_b32 %1, %6, %1, vcc" : "=&v"(za), "=&v"(zn) : "s"(failed << 31), "v"(pmp), "v"(mq), "v"(mp), "v"(OOB) : "vcc");
                done = failed ^ 1u; PINS(done);
            }
            SCHED();
        };
        auto ahead = [&]() __attribute__((always_inline)) {
            u32 h0 = rdl(hn, 0), h1 = rdl(hn, 1), h2 = rdl(hn, 2), h3 = rdl(hn, 3), h4 = rdl(hn, 4);
            asm volatile("" : "+s"(h0), "+s"(h1), "+s"(h2), "+s"(h3), "+s"(h4));
            tp = t; t = lead ? tn : (int)(h0 - t) < 0 ? h0 + h1 : h0;
            tn = nt; stop |= h3; rep = max(h2, h4); tw1 = t - 1u;
            asm volatile("" : "+s"(tp), "+s"(t), "+s"(tn), "+s"(stop), "+s"(rep), "+s"(tw1));
            SCHED();
        };
        auto books = [&](auto ic) __attribute__((always_inline)) {
            constexpr int i = decltype(ic)::value;
            if constexpr (i == 0) { rd = rep & 0xFFFFu; PINS(rd); }
            if constexpr (i == 1) { dn = wav >> 4; PINS(dn); pdur = min(pdur, dn + CH_CLIP); PINS(pdur); }
            if constexpr (i == 2) { pdur = (done & ok[M]) ? pdur - dn : 0u; PINS(pdur); }
            if constexpr (i == 3) { wav += (u32)max((int)pdur, (int)(pdur << CH_ATT)); PINS(wav); }
            if constexpr (i == 4) { dh = hav >> 4; PINS(dh); nt = min(rd, dh + CH_CLIP); PINS(nt); }
            if constexpr (i == 5) { nt = rd < 0x3FFFu ? nt - dh : 0u; PINS(nt); }
            if constexpr (i == 6) { hav += (u32)max((int)nt, (int)(nt << CH_ATT)); PINS(hav); }
            if constexpr (i == 7) { rd = (ok[M] & ~done) * (CH_UP * 16u); PINS(rd); mx = (u32)max((int)(mx - CH_DEC), (int)rd); ok[M] = done; PINS(mx); }
            if constexpr (i == 8) { dh = (hav + mx) / 16u + (sn[1] < 2 && sn[0] >= CH_NEAR ? CH_MH + CH_SOLO : CH_MH); PINS(dh); }
            if constexpr (i == 9) { dn = (wav + mx) / 16u + (sn[0] < CH_NEAR ? CH_MW + CH_EARLY : CH_MW); PINS(dn); }
            if constexpr (i == 10) { nt = dw[L] + dh; dh += dn; dw[L] = dn; sn[M] += (int)done; PINS(nt); PINS(dh); PINS(sn[M]); }
            if constexpr (i == 11) { rd = tn - t + (dh >> 1); PINS(rd); }
            if constexpr (i == 12) { rd = sn[M] + 1 < NSEG ? rd : 0u; PINS(rd); }
            if constexpr (i == 13) { nt = t + (u32)max((int)nt, (int)rd); PINS(nt); }
            if constexpr (i == 14) asm volatile("v_mov_b32 %0, %1" : "=v"(hw.x) : "s"(nt));
            if constexpr (i == 15) asm volatile("v_mov_b32 %0, %1" : "=v"(hw.y) : "s"(dh));
            if constexpr (i == 16) asm volatile("v_mov_b32 %0, %1" : "=v"(hw.z) : "s"(nt + dn));
            SCHED();
        };
        if (__builtin_expect(sc + 1 < NSEG, 1)) {
            u32 zb;
#define WT(k) do {} while (0)
            v4u d[8];
            {
                u32 tmp;
                asm volatile(LOOP_TXT("%1", "%2") "buffer_load_dwordx4 %0, %3, %4, %5 offen sc1\n\ts_waitcnt lgkmcnt(0)"
                             : "=&v"(d[0]), "=&s"(tmp) : "s"(uu(tw1)), "v"(vo), "s"(srd), "s"(oo[0]) : "vcc", "scc", "memory");
            }
            WT(0);
            SFOR(i, 7)
                asm volatile("buffer_load_dwordx4 %0, %1, %2, %3 offen sc1" : "=v"(d[i + 1]) : "v"(vo), "s"(srd), "s"(oo[i + 1]) : "memory");
                SCHED();
                if constexpr (i == 0) { settle(IC<0>{}); static_for<0, 2>(books); }
                if constexpr (i == 1) { settle(IC<1>{}); books(IC<2>{}); }
                if constexpr (i == 2) {
                    mp = mpo + (L * (MB / 4) + (u32)((sc + 1) & 3) * 32768u); pmp = mpo + (L * (MB / 4) + (u32)(sc & 3) * 32768u);
                    zb = mp | ok[L] << 31;
                    PIN(mp); PIN(pmp); PIN(zb);
                }
                if constexpr (i == 3) static_for<3, 7>(books);
                if constexpr (i == 4) { books(IC<7>{}); books(IC<9>{}); }
                if constexpr (i == 5) { books(IC<8>{}); books(IC<10>{}); }
                SCHED();
            SEND
            LD4A(hn, hvo[L], 0);
            *(LAS u32*)(sw + L * 8192) = zero;
            SCHED();
            WT(1);
            float f0[5], f1[5], xs; unsigned long long bad; v4u sv;
#define WSW(o_) asm volatile("v_permlane32_swap_b32 %0, %1" : "+v"(f0[o_]), "+v"(f1[o_]))
#define WAD(o_) asm volatile("v_add_f32_dpp %0, %1, %0 row_ror:8 row_mask:0xf bank_mask:0xf\n\tv_add_f32_dpp %0, %2, %3 row_ror:8 row_mask:0xf bank_mask:0xc" : "+v"(f0[o_]) : "v"(f1[o_]), "v"(f0[o_ + 1]), "v"(f1[o_ + 1]))
#define WFO(o_) do { VADD(f0[o_], acc[o_][0], acc[o_][2]); VADD(f1[o_], acc[o_][1], acc[o_][3]); } while (0)
#define WOWN(k) do { if constexpr (WO_NI + (k) < 8) SMF_V(acc[4], d[(WO_NI + (k)) & 7], wo[(WO_NI + (k)) & 7][4]); SCHED(); } while (0)
            SFOR(i, 40)
                constexpr int pc = i / 5, oc = i % 5;
                if constexpr (oc == 0) VMW((pc == 0 ? 8 : 11) - pc);
                if constexpr (oc < 4) SMF_A(acc[oc], d[pc], wo[pc][oc]); else if constexpr (pc < WO_NI) SMF_V(acc[4], d[pc], wo[pc][4]);
                SCHED();
                if constexpr (i == 0) WT(2);
                if constexpr (i == 20) WT(3);
                if constexpr (i == 35) WT(4);
                if constexpr (i == 0) st4s(rs, zb, 0, psent);
                if constexpr (i == 1) st4s(rs, za, 0, psent);
                if constexpr (i == 2) st4s(rs, zn, 0, cw);
                if constexpr (i >= 5 && i < 8) books(IC<6 + i>{});
                if constexpr (i >= 10 && i < 13) books(IC<4 + i>{});
                if constexpr (i == 13) { xs = 0.f; PIN(xs); }
                if constexpr (i == 15) { vo = l16 + (M * (MB / 2) + (u32)(sn[M] & 3) * 131072u); PIN(vo); }
                if constexpr (i == 36) NOPS(1);
                if constexpr (i == 37) NOPS(2);
                SCHED();
            SEND
            WFO(0); WFO(1);
            asm volatile("v_cmp_u_f32 %0, %1, %2" : "=s"(bad) : "v"(f0[0]), "v"(f1[0]));
            WSW(0); WSW(1); WFO(2); WAD(0); WFO(3);
            *(LAS float*)(xw0 + L * 8192) = f0[0]; SCHED();
            WSW(2); WSW(3);
            if (__builtin_expect(bad != 0ull, 0)) {
                u32 mn = ~0u;
#pragma unroll
                for (int i = 0; i < 8; ++i) { u32 e = d[i].x; PIN(e); mn = min(mn, e ^ PSENT); }
                if (all_(mn != 0u)) {
#pragma unroll
                    for (int i = 0; i < 8; ++i) { v4u e = d[i]; PIN(e); mn = min(min(mn, e.y ^ PSENT), min(e.z ^ PSENT, e.w ^ PSENT)); }
                }
                *(LAS u32*)(sw + L * 8192) = all_(mn != 0u) ? 0u : 1u;
            }
            WAD(2);
            *(LAS float*)(xw1 + L * 8192) = f0[2]; SCHED();
            WOWN(0); WOWN(1); WOWN(2); WOWN(3);
            WT(5);
            f32x4 xa; float nb;
            asm volatile("s_waitcnt lgkmcnt(0)\n\ts_barrier\n\tds_read_b128 %0, %2 offset:%4\n\tds_read_b128 %1, %3 offset:%4+5120\n\ts_nop 3"
                         : "=&v"(xa), "=&v"(sv) : "v"(xr), "v"(zero), "i"(L * 8192) : "memory");
            WT(6);
            WFO(4);
            asm volatile("s_nop 0\n\tv_permlane32_swap_b32 %1, %2\n\ts_nop 1\n\tv_add_f32_dpp %0, %2, %1 row_ror:8 row_mask:0xf bank_mask:0x3\n\ts_waitcnt lgkmcnt(1)" : "+v"(xs), "+v"(f0[4]), "+v"(f1[4]));
            asm volatile("v_add_f32 %0, %0, %4\n\tv_add_f32 %1, %5, %6\n\tv_add_f32 %0, %0, %7\n\tv_add_f32 %0, %0, %1\n\tv_cmp_u_f32 vcc, %0, %0\n\ts_nop 0\n\t"
                         "v_mov_b32_dpp %1, %0 row_shl:4 row_mask:0xf bank_mask:0xf\n\tv_cndmask_b32 %3, %8, %9, vcc\n\tv_cvt_pk_bf16_f32 %2, %0, %1"
                         : "+v"(xs), "=&v"(nb), "=&v"(cw), "=&v"(mq) : "v"(xa[0]), "v"(xa[1]), "v"(xa[2]), "v"(xa[3]), "v"(mp), "v"(OOB) : "vcc");
            st4s(rs, mq, 0, cw);
            SCHED();
            WT(7);
            asm volatile("s_memrealtime %0" : "=s"(clk));
            __builtin_amdgcn_raw_buffer_store_b96(hw, rs, hso[L], 0, 16);
#pragma unroll
            for (int q = 0; q < 5; ++q) acc[q] = __builtin_bit_cast(f32x4, lds_r16(lds + zero + (5632 + 16 * q)));
            VMW(5); SCHED(); PIN(hn);
            asm volatile("s_waitcnt lgkmcnt(5)" ::: "memory");
            SCHED(); PIN(sv);
            failed = uu(sv.x | sv.y | sv.z | sv.w);
            ahead();
            asm volatile("s_waitcnt lgkmcnt(0)" : "+s"(clk) :: "memory");
        } else {
            BAR();
            static_for<0, 2>(settle);
            st4s(rs, za, 0, psent);
            st4s(rs, zn, 0, cw);
            static_for<0, 14>(books);
            failed = 1u;
            hn = ld4(rs, hvo[L]);
            vo = l16 + (M * (MB / 2) + (u32)(sn[M] & 3) * 131072u);
            ahead();
            clk = __builtin_amdgcn_s_memrealtime();
        }
    };
#pragma unroll 1
    for (int k = 0; k < 2 * SPIN_CAP && !stop && (sn[0] + 1 < NSEG || sn[1] + 1 < NSEG); ++k) {
        turn(IC<0>{}); turn(IC<1>{});
        if (__builtin_expect(k != 0, 1)) continue;
        if (lead) {
            u32 go = 0;
#pragma unroll 1
            for (int n = 0; n < SPIN_CAP && !all_(go != 0u); ++n) { CFENCE(); go = ld4(rs, foff(F_GO + (lane & 31))); }
            t = rt32();
            float last = (float)(int)(go - t);
            last = fmaxf(last, dpp<0xB1>(last)); last = fmaxf(last, dpp<0x4E>(last)); last = fmaxf(last, dpp<0x124>(last)); last = fmaxf(last, dpp<0x128>(last));
            t += uu((u32)min(max(CH_FIRST + (int)max16(last), CH_LEAD), CH_FIRST)); tn = t + PER0 / 2 + CH_REC;
            st16(rs, lane < 2 ? hint + (u32)lane * (MB / 4) : OOB, (v4u){lane == 0 ? t : tn, PER0, (lane == 0 ? t : tn) + (CH_W0 + CH_MW + CH_REC), 0u});
        } else {
            for (int n = 0; n < SPIN_CAP && !dead; ++n) {
                CFENCE();
                const u32 hv = ld4(rs, hvo[1]);
                t = rdl(hv, 0);
                if (rdl(hv, 1) < 400u) break;
                __builtin_amdgcn_s_sleep(1);
            }
        }
        hso[0] = (lead && lane == 0) ? hint : OOB; hso[1] = hso[0] + MB / 4;
        if (!WARM1) sn[1] = 0;
        rep = 0xFFFFu; dw[0] = dw[1] = CH_W0 + CH_MW + CH_REC;
        tw1 = t - 1u;
    }
    if (sn[0] + 1 < NSEG || sn[1] + 1 < NSEG) st4s(rs, tid == 0 ? foff(F_ERR) : OOB, 0, 1u);
}

__global__ void __launch_bounds__(256, 1) k_main(Args args) {
    Params p;
    (Args&)p = args;
    lptr lds = (lptr)(u32)16 - 16;
    if (blockIdx.x == 0) LST(256, rt32());
    asm volatile(".Lk_main_here:");
    const u32 code = code_fetch();
    asm volatile("" :: "s"(p.x), "s"(p.Wo), "s"(p.ws) : "memory");
    if (threadIdx.x == 0) {
        const v4u e = ld16s(mk_rsrc(p.ws, W_END), foff(F_EP), 0);
        const unsigned long long tag = (unsigned long long)e.y << 32 | (e.x + 1u);
        u32 role = ((u32)__builtin_amdgcn_s_getreg(20 | (3 << 11)) & 7u) * 32u + ((blockIdx.x >> 3) & 31u);
#pragma unroll 1
        for (int n = 0; n < 256; ++n, role = (role + 1u) & 255u)
            if (__hip_atomic_exchange((GAS unsigned long long*)(p.ws + W_SLOT) + role * 8u, tag, __ATOMIC_RELAXED, __HIP_MEMORY_SCOPE_AGENT) != tag) break;
        lds_w16(lds, (v4u){e.x + 1u, e.y, e.z, role});
    }
    __syncthreads();
    const v4u boot = lds_r16(lds);
    p.e0 = uu(boot.x); p.e1 = uu(boot.y); p.e2 = uu(boot.z);
    const int role = uni((int)boot.w), x = role >> 5, i = role & 31;
    asm volatile("s_waitcnt vmcnt(0)" :: "v"(code));
    __syncthreads();
    LST(8192 + blockIdx.x * 16, role);
    if (x < 2) { role_head(p, lds, i >> 4, x * 16 + (i & 15)); __builtin_amdgcn_endpgm(); }
    if (x == 2) { role_wo(p, lds, i); __builtin_amdgcn_endpgm(); }
    const int no = x == 3 ? 30 : x < 6 ? 5 : 0;
    if (x > 3 && i >= 8) { role_qkv(p, lds, (x - 4) * 24 + i - 8); __builtin_amdgcn_endpgm(); }
    const bool isout = i < no;
    const int og = x == 3 ? i / 5 : x + 2, ng = i % 5, L = og >> 2, kq = og & 3;
    const int k = i - no, w = uni(threadIdx.x >> 6);
    const int j = isout ? (L == 1 ? 0 : 44) + kq * 5 + ng : x == 3 ? 36 + k : k < 2 ? 12 + 2 * x + k : x < 6 ? 24 + x : k < 5 ? 10 + 3 * x + k : 15 + 3 * x + k;
    const rsrc_t rs = mk_rsrc(p.ws, W_END);
    wait_until(rt32() + (j < NKV ? ST_KV : ST_OUT));
    if (j < NKV) {
        const int nt = j < 320 - 7 * NKV ? 8 : 7;
#pragma unroll 1
        for (int q = 0; q < 4; ++q) kv_fetch(p, j + NKV * q, uu(q * KT_BYTES));
        Filt f;
        u32 warm[3], code2 = 0;
#pragma unroll 1
        for (int q = 0; q < nt; ++q) {
            VMW(15); BAR();
            if (q == 0) EV(2);
            if (q == 5) code2 = code_fetch();
            if (q == 4) boot_set(p, rs, threadIdx.x == 0 ? foff(F_KV + j) : OOB, 1u);
            kv_fetch(p, j + NKV * (q + 4), uu(((q + 4) & 7) * KT_BYTES));
            kv_tile(p, rs, lds + q * KT_BYTES, lds + (q == 0 ? 8 : q - 1) * KT_BYTES, j + NKV * q);
        }
        const int g = j < 20 ? j : j < 28 ? j + 16 : j - 8;
        filt_load<5, 0>(p, g, f);
        VMW(10); BAR(); USE(code2);
        boot_set(p, rs, threadIdx.x == 0 ? foff(F_KV + j) : OOB, 2u);
        EV(4);
        { bool dead = false; boot_wait(p, rs, pol_off(), 0u, dead); }
#pragma unroll
        for (int q = 0; q < 3; ++q)
            warm[q] = __builtin_amdgcn_raw_buffer_load_b32(mk_rsrc(p.ssmk, ACT), 176u * 4096u + ((u32)((q * NKV + j) * 256) + threadIdx.x) * 64u, 0, 0);
        if (g >= 36) {
            bool dead = false;
            boot_wait(p, rs, (threadIdx.x & 63) < 40 ? foff(F_KV + (threadIdx.x & 63)) : OOB, 1u, dead);
            f.a[4] = ld16s(rs, (threadIdx.x & 32) == 0 ? (threadIdx.x & 31) * 16u : OOB, W_KVT + (u32)chan(g, w) * (SEQ * 2));
            conv_job<4>(p, rs, lds, g, f, 0);
        } else conv_job<0>(p, rs, lds, g, f, 0);
        asm volatile("" :: "v"(warm[0]), "v"(warm[1]), "v"(warm[2]));
        const int g0 = j < 20 ? 36 + j : j < 36 ? 28 + j : 44 + j, g1 = j < 20 ? g0 + 1 : j < 36 ? 80 : 160;
        if (g >= 20 && g < 28) {
            filt_load<3, 0>(p, g + 16, f);
            conv_job<1>(p, rs, lds, g + 16, f, 0, threadIdx.x == 0 ? foff(F_CV + g) : OOB, 2u);
        }
        if (g < 36) filt_load<5, 0>(p, g0, f);
        DRAIN(); BAR();
        st4s(rs, threadIdx.x != 0 ? OOB : g >= 36 ? foff(F_GO - 60 + g) : foff(F_CV + (g >= 20 && g < 28 ? g + 16 : g)), 0, 2u);
        if (g < 36) {
#pragma unroll 1
            for (int n = g0; n < g1; n += 8) conv_job<2>(p, rs, lds, n, f, min(n + 8, 159));
            DRAIN(); BAR();
            st4s(rs, threadIdx.x == 0 ? foff(F_CV + g) : OOB, 0, 3u);
            if (j >= 36) { EV(15); __builtin_amdgcn_endpgm(); }
        }
    }
    if (!isout) { if (j < 28) role_row<0>(p, lds, (j - 20) * 4 + w); else role_row<1>(p, lds, (j - 28) * 4 + w); __builtin_amdgcn_endpgm(); }
    role_out(p, lds, L, kq, ng);
}

__global__ void k_end() { asm volatile(".Lk_end_here:"); }

extern "C" void kernel_launch(void* const* d_in, const int* in_sizes, int n_in, void* d_out, int out_size, void* d_ws, size_t ws_size,
                              hipStream_t stream) {
    Args p;
    p.x = (const u16*)d_in[0]; p.mem0 = (const u16*)d_in[1]; p.Wq = (const u16*)d_in[2]; p.Wk = (const u16*)d_in[3];
    p.Wv = (const u16*)d_in[4]; p.Wo = (const u16*)d_in[5];
    p.bk = (const u16*)d_in[8]; p.bv = (const u16*)d_in[9]; p.shift = (const u16*)d_in[10]; p.ssmk = (const u16*)d_in[11]; p.ssmD = (const u16*)d_in[12];
    p.out = (u16*)d_out;
    p.ws = (unsigned char*)(((size_t)d_ws + 2 * MB - 1) & ~(size_t)(2 * MB - 1));
    (void)hipFuncSetAttribute((const void*)k_main, hipFuncAttributeMaxDynamicSharedMemorySize, LDS_BYTES);
    hipLaunchKernelGGL(k_main, dim3(256), dim3(256), LDS_BYTES, stream, p);
}
```
